```python
import torch

def _softmax(x, axis=-1):
    return torch.softmax(x, dim=axis)
def _gelu(x):
    return torch.nn.functional.gelu(x, approximate="tanh")
def _silu(x):
    return torch.nn.functional.silu(x)
def _sigmoid(x):
    return torch.sigmoid(x)
def _layernorm(x, axis=-1, eps=1e-5):
    m = x.mean(dim=axis, keepdim=True)
    v = x.var(dim=axis, keepdim=True, unbiased=False)
    return (x - m) / torch.sqrt(v + eps)
def _scatter_add(tgt, idx, src, axis=0):
    return tgt.clone().index_add_(axis, idx, src)
def _segment_sum(src, seg, n):
    out = torch.zeros((n,) + src.shape[1:], dtype=src.dtype)
    return out.index_add_(0, seg, src)
def _pad(x, pw):
    flat = tuple(v for pair in reversed(pw) for v in pair)
    return torch.nn.functional.pad(x, flat)

kernel_name = 'recurrent_scan__ssm_conv_s5__mixer_channel_mlp__prompt'

def setup_inputs():

    _w = (lambda a: (lambda t: t.to(torch.bfloat16) if t.is_floating_point() else t)(torch.as_tensor(a)))
    def _A_setup():
        B, S, D = 1, 2048, 576
        P = D
        Lambda_re = -0.5 * (lambda _x: torch.exp(torch.as_tensor(_x)))((lambda *s: torch.randn(*s, dtype=torch.bfloat16) * 0.2)(P))
        Lambda_im = (lambda *s: torch.randn(*s, dtype=torch.bfloat16) * 0.8)(P) * 1.0
        B_re = (lambda *s: torch.randn(*s, dtype=torch.bfloat16) * 0.2)(P, D) * (D ** -0.5)
        B_im = (lambda *s: torch.randn(*s, dtype=torch.bfloat16) * 0.2)(P, D) * (D ** -0.5)
        C_re = (lambda *s: torch.randn(*s, dtype=torch.bfloat16) * 0.2)(D, P) * (P ** -0.5)
        C_im = (lambda *s: torch.randn(*s, dtype=torch.bfloat16) * 0.2)(D, P) * (P ** -0.5)
        log_dt = (lambda *s: torch.randn(*s, dtype=torch.bfloat16) * 0.2)(P) * 0.3 - 2.0
        Dskip = (lambda *s: torch.randn(*s, dtype=torch.bfloat16) * 0.05)(D)
        return {
            "x": _w((lambda *s: torch.randn(*s, dtype=torch.bfloat16) * 0.05)(B, S, D)),
            "Lambda_re": _w(Lambda_re),
            "Lambda_im": _w(Lambda_im),
            "B_re": _w(B_re),
            "B_im": _w(B_im),
            "C_re": _w(C_re),
            "C_im": _w(C_im),
            "log_dt": _w(log_dt),
            "Dskip": _w(Dskip),
        }
    _A_in = _A_setup()

    def _B_setup():
        B, S, D, F = 1, 2048, 576, 4608
        return {
            "x": _w((lambda *s: torch.randn(*s, dtype=torch.bfloat16) * 0.05)(B, S, D)),
            "w1": _w((lambda *s: torch.randn(*s, dtype=torch.bfloat16) * 3.2)(D, F) * (D ** -0.5)),
            "b1": _w((lambda *s: torch.randn(*s, dtype=torch.bfloat16) * 1.6)(F)),
            "w2": _w((lambda *s: torch.randn(*s, dtype=torch.bfloat16) * 0.1)(F, D) * (F ** -0.5)),
            "b2": _w((lambda *s: torch.randn(*s, dtype=torch.bfloat16) * 0.1)(D)),
        }
    _B_in = _B_setup()

    return {
        "A_x": _A_in["x"],
        "A_Lambda_re": _A_in["Lambda_re"],
        "A_Lambda_im": _A_in["Lambda_im"],
        "A_B_re": _A_in["B_re"],
        "A_B_im": _A_in["B_im"],
        "A_C_re": _A_in["C_re"],
        "A_C_im": _A_in["C_im"],
        "A_log_dt": _A_in["log_dt"],
        "A_Dskip": _A_in["Dskip"],
        "B_x": _B_in["x"],
        "B_w1": _B_in["w1"],
        "B_b1": _B_in["b1"],
        "B_w2": _B_in["w2"],
        "B_b2": _B_in["b2"],
    }

def reference(A_x, A_Lambda_re, A_Lambda_im, A_B_re, A_B_im, A_C_re, A_C_im, A_log_dt, A_Dskip, B_x, B_w1, B_b1, B_w2, B_b2):
    def _A_ref(x, Lambda_re, Lambda_im, B_re, B_im, C_re, C_im, log_dt, Dskip):
        Bsz, L, H = x.shape
        Lam = Lambda_re + 1j * Lambda_im
        Bmat = B_re + 1j * B_im
        Cmat = C_re + 1j * C_im
        dt = torch.exp(log_dt)
        Lam_bar = torch.exp(dt * Lam)
        B_bar = ((Lam_bar - 1.0) / Lam)[:, None] * Bmat
        Bu = torch.einsum("bld,pd->blp", x + 0j, B_bar)
        h = torch.zeros_like(Bu[:, 0, :])
        hs = []
        for t in range(L):
            h = Lam_bar[None, :] * h + Bu[:, t, :]
            hs.append(h)
        hseq = torch.stack(hs, 1)
        y = 2.0 * torch.einsum("blp,dp->bld", hseq, Cmat).real + x * Dskip[None, None, :]
        return y

    def _B_ref(x, w1, b1, w2, b2):
        y = _layernorm(x, axis=-1)
        h = _gelu(torch.einsum("bsd,df->bsf", y, w1) + b1)
        return torch.einsum("bsf,fd->bsd", h, w2) + b2

    x = A_x
    xn = _layernorm(x, axis=-1)
    s = _A_ref(xn, A_Lambda_re, A_Lambda_im, A_B_re, A_B_im, A_C_re, A_C_im, A_log_dt, A_Dskip)
    g = _silu(_B_ref(xn, B_w1, B_b1, B_w2, B_b2))
    return x + s * g
```

```cpp
#include <hip/hip_runtime.h>
#include <hip/hip_bf16.h>
#include <stdint.h>

constexpr int L = 2048, D = 576, P = 576, F = 4608;
typedef unsigned short u16;
__device__ __forceinline__ float b2f(u16 v) { return __uint_as_float(((unsigned)v) << 16); }
__device__ __forceinline__ u16 f2b(float f) { unsigned u = __float_as_uint(f); return (u16)((u + 0x7FFF + ((u >> 16) & 1)) >> 16); }
__device__ __forceinline__ float rb(float f) { return b2f(f2b(f)); }

__device__ float block_sum(float v, float* sh) {
  for (int o = 32; o > 0; o >>= 1) v += __shfl_xor(v, o);
  __syncthreads();
  if ((threadIdx.x & 63) == 0) sh[threadIdx.x >> 6] = v;
  __syncthreads();
  float r = 0; for (int i = 0; i < (int)(blockDim.x >> 6); ++i) r += sh[i];
  return r;
}

__global__ void k_ln(const u16* x, u16* xn, u16* yn) {
  __shared__ float sh[16];
  int t = blockIdx.x, d = threadIdx.x;
  float v = b2f(x[t * D + d]);
  float mean = block_sum(v, sh) / D;
  float var = block_sum((v - mean) * (v - mean), sh) / D;
  float m = rb(mean), s = rb(sqrtf(rb(rb(var) + 1e-5f)));
  float a = rb(rb(v - m) / s);
  xn[t * D + d] = f2b(a);
  float mean2 = block_sum(a, sh) / D;
  float var2 = block_sum((a - mean2) * (a - mean2), sh) / D;
  float m2 = rb(mean2), s2 = rb(sqrtf(rb(rb(var2) + 1e-5f)));
  yn[t * D + d] = f2b(rb(a - m2) / s2);
}

__global__ void k_bu(const u16* xn, const u16* Bre, const u16* Bim, const u16* lre, const u16* lim, const u16* ldt, float2* Bu) {
  int t = blockIdx.x, p = threadIdx.x;
  __shared__ float xs[D];
  xs[p] = b2f(xn[t * D + p]);
  __syncthreads();
  float ur = 0, ui = 0;
  for (int d = 0; d < D; ++d) { ur += xs[d] * b2f(Bre[p * D + d]); ui += xs[d] * b2f(Bim[p * D + d]); }
  float dt = rb(expf(b2f(ldt[p])));
  float lr = b2f(lre[p]), li = b2f(lim[p]);
  float e = expf(dt * lr), sn, cs; sincosf(dt * li, &sn, &cs);
  float br = e * cs - 1.f, bi = e * sn;
  float den = lr * lr + li * li;
  float cr = (br * lr + bi * li) / den, ci = (bi * lr - br * li) / den;
  Bu[t * P + p] = make_float2(cr * ur - ci * ui, cr * ui + ci * ur);
}
__global__ void k_scan(float2* Bu, const u16* lre, const u16* lim, const u16* ldt) {
  int p = blockIdx.x * 64 + threadIdx.x;
  float dt = rb(expf(b2f(ldt[p])));
  float lr = b2f(lre[p]), li = b2f(lim[p]);
  float e = expf(dt * lr), sn, cs; sincosf(dt * li, &sn, &cs);
  float ar = e * cs, ai = e * sn, hr = 0, hi = 0;
  for (int t = 0; t < L; ++t) {
    float2 u = Bu[t * P + p];
    float nr = ar * hr - ai * hi + u.x, ni = ar * hi + ai * hr + u.y;
    hr = nr; hi = ni;
    Bu[t * P + p] = make_float2(hr, hi);
  }
}
__global__ void k_mlp1(const u16* yn, const u16* w1, const u16* b1, u16* G) {
  int t = blockIdx.x;
  __shared__ float xs[D];
  for (int d = threadIdx.x; d < D; d += blockDim.x) xs[d] = b2f(yn[t * D + d]);
  __syncthreads();
  for (int f = threadIdx.x; f < F; f += blockDim.x) {
    float a = 0;
    for (int d = 0; d < D; ++d) a += xs[d] * b2f(w1[d * F + f]);
    a = rb(rb(a) + b2f(b1[f]));
    float g = 0.5f * a * (1.f + tanhf(0.7978845608028654f * (a + 0.044715f * a * a * a)));
    G[t * F + f] = f2b(g);
  }
}
__global__ void k_final(const u16* x, const u16* xn, const float2* h, const u16* Cre, const u16* Cim, const u16* Dk,
                        const u16* G, const u16* w2, const u16* b2, float* out) {
  int t = blockIdx.x, d = threadIdx.x;
  __shared__ float gs[F];
  __shared__ float2 hs[P];
  for (int f = threadIdx.x; f < F; f += blockDim.x) gs[f] = b2f(G[t * F + f]);
  hs[d] = h[t * P + d];
  __syncthreads();
  float y = 0;
  for (int p = 0; p < P; ++p) y += hs[p].x * b2f(Cre[d * P + p]) - hs[p].y * b2f(Cim[d * P + p]);
  float s = 2.f * y + rb(b2f(xn[t * D + d]) * b2f(Dk[d]));
  float m = 0;
  for (int f = 0; f < F; ++f) m += gs[f] * b2f(w2[f * D + d]);
  m = rb(rb(m) + b2f(b2[d]));
  float g = rb(m / (1.f + expf(-m)));
  out[t * D + d] = b2f(x[t * D + d]) + s * g;
}

extern "C" void kernel_launch(void* const* d_in, const int* in_sizes, int n_in,
                              void* d_out, int out_size, void* d_ws, size_t ws_size,
                              hipStream_t stream) {
  const u16* x = (const u16*)d_in[0];
  const u16 *lre = (const u16*)d_in[1], *lim = (const u16*)d_in[2], *Bre = (const u16*)d_in[3], *Bim = (const u16*)d_in[4];
  const u16 *Cre = (const u16*)d_in[5], *Cim = (const u16*)d_in[6], *ldt = (const u16*)d_in[7], *Dk = (const u16*)d_in[8];
  const u16 *w1 = (const u16*)d_in[10], *b1 = (const u16*)d_in[11], *w2 = (const u16*)d_in[12], *b2 = (const u16*)d_in[13];
  char* ws = (char*)d_ws;
  u16* xn = (u16*)ws; ws += L * D * 2;
  u16* yn = (u16*)ws; ws += L * D * 2;
  float2* Bu = (float2*)ws; ws += (size_t)L * P * 8;
  u16* G = (u16*)ws;
  k_ln<<<L, D, 0, stream>>>(x, xn, yn);
  k_bu<<<L, P, 0, stream>>>(xn, Bre, Bim, lre, lim, ldt, Bu);
  k_scan<<<P / 64, 64, 0, stream>>>(Bu, lre, lim, ldt);
  k_mlp1<<<L, 256, 0, stream>>>(yn, w1, b1, G);
  k_final<<<L, D, 0, stream>>>(x, xn, Bu, Cre, Cim, Dk, G, w2, b2, (float*)d_out);
}
```

```cpp
#include <hip/hip_runtime.h>
#include <hip/hip_bf16.h>
#include <stdint.h>

struct Params { const void* p[13]; void* out; void* ws; };

extern "C" __global__ __launch_bounds__(256, 1) void k_fused(Params prm) {
  auto ka = __builtin_amdgcn_kernarg_segment_ptr();
  int wid = __builtin_amdgcn_readfirstlane(threadIdx.x >> 6);
  asm volatile(
    "K_BEGIN:\n"
    "s_mov_b32 s8, %1\n"
    "s_mov_b32 s9, %2\n"
    "s_load_dwordx2 s[86:87], %0, 0\n"
    "s_load_dwordx2 s[66:67], %0, 112\n"
    "s_load_dwordx4 s[88:91], %0, 24\n"
    "s_load_dwordx4 s[92:95], %0, 72\n"
    "v_mbcnt_lo_u32_b32 v0, -1, 0\n"
    "v_mbcnt_hi_u32_b32 v0, -1, v0\n"
    "s_and_b32 s40, s8, 7\n"
    "s_lshr_b32 s41, s8, 3\n"
    "s_and_b32 s42, s40, 1\n"
    "s_lshl_b32 s42, s42, 3\n"
    "s_and_b32 s43, s41, 7\n"
    "s_add_u32 s10, s42, s43\n"
    "s_lshr_b32 s42, s40, 1\n"
    "s_lshr_b32 s43, s41, 3\n"
    "s_mul_i32 s44, s42, 3\n"
    "s_add_u32 s44, s44, s43\n"
    "s_mul_i32 s45, s42, 1\n"
    "s_add_u32 s45, s45, s43\n"
    "s_sub_u32 s45, s45, 3\n"
    "s_add_u32 s46, s45, 12\n"
    "s_cmp_ge_u32 s43, 3\n"
    "s_cselect_b32 s11, s45, s44\n"
    "s_cselect_b32 s61, s46, s44\n"
    "s_cselect_b32 s55, 1, 0\n"
    "s_mul_i32 s34, s11, 11\n"
    "s_lshr_b32 s34, s34, 5\n"
    "s_mul_i32 s44, s34, 3\n"
    "s_sub_u32 s35, s11, s44\n"
    "s_lshr_b32 s12, s9, 1\n"
    "s_and_b32 s13, s9, 1\n"
    "s_and_b32 s40, s10, 7\n"
    "s_cmp_ge_u32 s40, 6\n"
    "s_cselect_b32 s41, 6, 0\n"
    "s_sub_u32 s14, s40, s41\n"
    "s_cmp_ge_u32 s10, 9\n"
    "s_cselect_b32 s41, 9, 0\n"
    "s_sub_u32 s15, s10, s41\n"
    "v_lshlrev_b32 v13, 4, v0\n"
    "s_lshl_b32 s40, s10, 7\n"
    "s_lshl_b32 s41, s61, 3\n"
    "s_add_u32 s40, s40, s41\n"
    "s_lshl_b32 s41, s9, 1\n"
    "s_add_u32 s40, s40, s41\n"
    "v_lshrrev_b32 v90, 5, v0\n"
    "v_add_u32 v91, s40, v90\n"
    "v_and_b32 v92, 31, v0\n"
    "v_mul_u32_u24 v93, 1152, v91\n"
    "v_lshl_add_u32 v93, v92, 4, v93\n"
    "v_cmp_gt_u32 s[100:101], 24, v92\n"
    "v_mov_b32 v94, 0x7fffffff\n"
    "s_nop 0\n"
    "v_cndmask_b32 v93, v94, v93, s[100:101]\n"
    "s_waitcnt lgkmcnt(0)\n"
    "s_mov_b32 s76, s86\n"
    "s_and_b32 s77, s87, 0xffff\n"
    "s_mov_b32 s78, 2359296\n"
    "s_mov_b32 s79, 0x20000\n"
    "s_add_u32 s72, s66, 0\n"
    "s_addc_u32 s73, s67, 0\n"
    "s_and_b32 s73, s73, 0xffff\n"
    "s_mov_b32 s74, 1048576\n"
    "s_mov_b32 s75, 0x20000\n"
    "buffer_load_dwordx4 v[100:103], v93, s[76:79], 0 offen offset:0\n"
    "buffer_load_dwordx4 v[104:107], v93, s[76:79], 0 offen offset:384\n"
    "buffer_load_dwordx4 v[108:111], v93, s[76:79], 0 offen offset:768\n"
    "v_mov_b32 v95, 0\n"
    "buffer_load_dwordx2 v[96:97], v95, s[72:75], 0 offen sc1\n"
    "s_getpc_b64 s[44:45]\n"
    "PF_ANCHOR:\n"
    "s_sub_u32 s44, s44, PF_ANCHOR-K_BEGIN\n"
    "s_subb_u32 s45, s45, 0\n"
    "s_mov_b32 s46, (K_END-K_BEGIN+4095)/4096\n"
    "s_lshr_b32 s47, s8, 3\n"
    "s_mul_i32 s47, s47, s46\n"
    "s_lshl_b32 s40, s9, 6\n"
    "v_add_u32 v88, s40, v0\n"
    "v_cmp_gt_u32 vcc, s46, v88\n"
    "v_add_u32 v88, s47, v88\n"
    "v_lshlrev_b32 v88, 7, v88\n"
    "v_mov_b32 v89, K_END-K_BEGIN-4\n"
    "v_min_u32 v88, v88, v89\n"
    "v_cndmask_b32 v88, 0, v88, vcc\n"
    "global_load_dword v89, v88, s[44:45]\n"
    "v_lshrrev_b32 v98, 2, v92\n"
    "v_lshlrev_b32 v98, 10, v98\n"
    "v_and_b32 v99, 3, v92\n"
    "v_lshl_add_u32 v98, v99, 8, v98\n"
    "v_lshl_add_u32 v98, v90, 4, v98\n"
    "v_cndmask_b32 v98, v94, v98, s[100:101]\n"
    "s_add_u32 s36, s66, 1048576\n"
    "s_addc_u32 s37, s67, 0\n"
    "s_and_b32 s37, s37, 0xffff\n"
    "s_mov_b32 s38, 5505024\n"
    "s_mov_b32 s39, 0x20000\n"
    "s_mul_i32 s40, s10, 147456\n"
    "s_lshr_b32 s41, s61, 2\n"
    "s_mul_i32 s41, s41, 36864\n"
    "s_add_u32 s56, s40, s41\n"
    "s_mul_i32 s41, s12, 18432\n"
    "s_add_u32 s56, s56, s41\n"
    "s_and_b32 s41, s61, 3\n"
    "s_lshl_b32 s41, s41, 6\n"
    "s_add_u32 s56, s56, s41\n"
    "s_lshl_b32 s41, s13, 5\n"
    "s_add_u32 s56, s56, s41\n"
    "s_add_u32 s56, s56, 3145728\n"
    "s_lshr_b32 s41, s61, 1\n"
    "s_mul_i32 s41, s41, 18432\n"
    "s_add_u32 s57, s40, s41\n"
    "s_and_b32 s41, s61, 1\n"
    "s_lshl_b32 s41, s41, 7\n"
    "s_add_u32 s57, s57, s41\n"
    "s_lshl_b32 s41, s9, 5\n"
    "s_add_u32 s57, s57, s41\n"
    "v_lshlrev_b32 v99, 3, v91\n"
    "v_add_u32 v99, 131072, v99\n"
    "v_cmp_eq_u32 vcc, 0, v92\n"
    "s_nop 1\n"
    "v_cndmask_b32 v99, v94, v99, vcc\n"
    "s_mov_b32 s58, 0\n"
    "s_waitcnt vmcnt(2)\n"
    "LNL_1:\n"
    "v_lshlrev_b32 v112, 16, v100\n"
    "v_and_b32 v113, 0xffff0000, v100\n"
    "v_lshlrev_b32 v114, 16, v101\n"
    "v_and_b32 v115, 0xffff0000, v101\n"
    "v_lshlrev_b32 v116, 16, v102\n"
    "v_and_b32 v117, 0xffff0000, v102\n"
    "v_lshlrev_b32 v118, 16, v103\n"
    "v_and_b32 v119, 0xffff0000, v103\n"
    "v_lshlrev_b32 v120, 16, v104\n"
    "v_and_b32 v121, 0xffff0000, v104\n"
    "v_lshlrev_b32 v122, 16, v105\n"
    "v_and_b32 v123, 0xffff0000, v105\n"
    "v_lshlrev_b32 v124, 16, v106\n"
    "v_and_b32 v125, 0xffff0000, v106\n"
    "v_lshlrev_b32 v126, 16, v107\n"
    "v_and_b32 v127, 0xffff0000, v107\n"
    "v_lshlrev_b32 v128, 16, v108\n"
    "v_and_b32 v129, 0xffff0000, v108\n"
    "v_lshlrev_b32 v130, 16, v109\n"
    "v_and_b32 v131, 0xffff0000, v109\n"
    "v_lshlrev_b32 v132, 16, v110\n"
    "v_and_b32 v133, 0xffff0000, v110\n"
    "v_lshlrev_b32 v134, 16, v111\n"
    "v_and_b32 v135, 0xffff0000, v111\n"
    "v_mov_b32 v140, 0\n"
    "v_mov_b32 v141, 0\n"
    "v_mov_b32 v142, 0\n"
    "v_mov_b32 v143, 0\n"
    "s_mov_b64 exec, s[100:101]\n"
    "v_pk_add_f32 v[140:141], v[140:141], v[112:113]\n"
    "v_pk_add_f32 v[142:143], v[142:143], v[114:115]\n"
    "v_pk_add_f32 v[140:141], v[140:141], v[116:117]\n"
    "v_pk_add_f32 v[142:143], v[142:143], v[118:119]\n"
    "v_pk_add_f32 v[140:141], v[140:141], v[120:121]\n"
    "v_pk_add_f32 v[142:143], v[142:143], v[122:123]\n"
    "v_pk_add_f32 v[140:141], v[140:141], v[124:125]\n"
    "v_pk_add_f32 v[142:143], v[142:143], v[126:127]\n"
    "v_pk_add_f32 v[140:141], v[140:141], v[128:129]\n"
    "v_pk_add_f32 v[142:143], v[142:143], v[130:131]\n"
    "v_pk_add_f32 v[140:141], v[140:141], v[132:133]\n"
    "v_pk_add_f32 v[142:143], v[142:143], v[134:135]\n"
    "s_mov_b64 exec, -1\n"
    "v_pk_add_f32 v[140:141], v[140:141], v[142:143]\n"
    "v_mov_b32 v144, 0\n"
    "v_mov_b32 v145, 0\n"
    "v_add_f32 v140, v140, v141\n"
    "v_mov_b32 v146, 0\n"
    "v_mov_b32 v147, 0\n"
    "v_add_f32_dpp v140, v140, v140 row_ror:8 row_mask:0xf bank_mask:0xf\n"
    "s_nop 1\n"
    "v_add_f32_dpp v140, v140, v140 row_ror:4 row_mask:0xf bank_mask:0xf\n"
    "s_nop 1\n"
    "v_add_f32_dpp v140, v140, v140 row_ror:2 row_mask:0xf bank_mask:0xf\n"
    "s_nop 1\n"
    "v_add_f32_dpp v140, v140, v140 row_ror:1 row_mask:0xf bank_mask:0xf\n"
    "v_mov_b32 v141, v140\n"
    "s_nop 1\n"
    "v_permlane16_swap_b32 v140, v141\n"
    "v_add_f32 v140, v140, v141\n"
    "v_mul_f32 v140, 0x3ae38e39, v140\n"
    "s_mov_b64 exec, s[100:101]\n"
    "v_pk_add_f32 v[148:149], v[112:113], v[140:141] op_sel_hi:[1,0] neg_lo:[0,1] neg_hi:[0,1]\n"
    "v_pk_fma_f32 v[144:145], v[148:149], v[148:149], v[144:145]\n"
    "v_pk_add_f32 v[150:151], v[114:115], v[140:141] op_sel_hi:[1,0] neg_lo:[0,1] neg_hi:[0,1]\n"
    "v_pk_fma_f32 v[146:147], v[150:151], v[150:151], v[146:147]\n"
    "v_pk_add_f32 v[148:149], v[116:117], v[140:141] op_sel_hi:[1,0] neg_lo:[0,1] neg_hi:[0,1]\n"
    "v_pk_fma_f32 v[144:145], v[148:149], v[148:149], v[144:145]\n"
    "v_pk_add_f32 v[150:151], v[118:119], v[140:141] op_sel_hi:[1,0] neg_lo:[0,1] neg_hi:[0,1]\n"
    "v_pk_fma_f32 v[146:147], v[150:151], v[150:151], v[146:147]\n"
    "v_pk_add_f32 v[148:149], v[120:121], v[140:141] op_sel_hi:[1,0] neg_lo:[0,1] neg_hi:[0,1]\n"
    "v_pk_fma_f32 v[144:145], v[148:149], v[148:149], v[144:145]\n"
    "v_pk_add_f32 v[150:151], v[122:123], v[140:141] op_sel_hi:[1,0] neg_lo:[0,1] neg_hi:[0,1]\n"
    "v_pk_fma_f32 v[146:147], v[150:151], v[150:151], v[146:147]\n"
    "v_pk_add_f32 v[148:149], v[124:125], v[140:141] op_sel_hi:[1,0] neg_lo:[0,1] neg_hi:[0,1]\n"
    "v_pk_fma_f32 v[144:145], v[148:149], v[148:149], v[144:145]\n"
    "v_pk_add_f32 v[150:151], v[126:127], v[140:141] op_sel_hi:[1,0] neg_lo:[0,1] neg_hi:[0,1]\n"
    "v_pk_fma_f32 v[146:147], v[150:151], v[150:151], v[146:147]\n"
    "v_pk_add_f32 v[148:149], v[128:129], v[140:141] op_sel_hi:[1,0] neg_lo:[0,1] neg_hi:[0,1]\n"
    "v_pk_fma_f32 v[144:145], v[148:149], v[148:149], v[144:145]\n"
    "v_pk_add_f32 v[150:151], v[130:131], v[140:141] op_sel_hi:[1,0] neg_lo:[0,1] neg_hi:[0,1]\n"
    "v_pk_fma_f32 v[146:147], v[150:151], v[150:151], v[146:147]\n"
    "v_pk_add_f32 v[148:149], v[132:133], v[140:141] op_sel_hi:[1,0] neg_lo:[0,1] neg_hi:[0,1]\n"
    "v_pk_fma_f32 v[144:145], v[148:149], v[148:149], v[144:145]\n"
    "v_pk_add_f32 v[150:151], v[134:135], v[140:141] op_sel_hi:[1,0] neg_lo:[0,1] neg_hi:[0,1]\n"
    "v_pk_fma_f32 v[146:147], v[150:151], v[150:151], v[146:147]\n"
    "s_mov_b64 exec, -1\n"
    "v_pk_add_f32 v[144:145], v[144:145], v[146:147]\n"
    "v_cvt_pk_bf16_f32 v142, v140, v140\n"
    "v_lshlrev_b32 v142, 16, v142\n"
    "v_add_f32 v144, v144, v145\n"
    "v_pk_add_f32 v[112:113], v[112:113], v[142:143] op_sel_hi:[1,0] neg_lo:[0,1] neg_hi:[0,1]\n"
    "s_nop 0\n"
    "v_add_f32_dpp v144, v144, v144 row_ror:8 row_mask:0xf bank_mask:0xf\n"
    "v_pk_add_f32 v[114:115], v[114:115], v[142:143] op_sel_hi:[1,0] neg_lo:[0,1] neg_hi:[0,1]\n"
    "s_nop 0\n"
    "v_add_f32_dpp v144, v144, v144 row_ror:4 row_mask:0xf bank_mask:0xf\n"
    "v_pk_add_f32 v[116:117], v[116:117], v[142:143] op_sel_hi:[1,0] neg_lo:[0,1] neg_hi:[0,1]\n"
    "s_nop 0\n"
    "v_add_f32_dpp v144, v144, v144 row_ror:2 row_mask:0xf bank_mask:0xf\n"
    "v_pk_add_f32 v[118:119], v[118:119], v[142:143] op_sel_hi:[1,0] neg_lo:[0,1] neg_hi:[0,1]\n"
    "s_nop 0\n"
    "v_add_f32_dpp v144, v144, v144 row_ror:1 row_mask:0xf bank_mask:0xf\n"
    "v_pk_add_f32 v[120:121], v[120:121], v[142:143] op_sel_hi:[1,0] neg_lo:[0,1] neg_hi:[0,1]\n"
    "v_pk_add_f32 v[122:123], v[122:123], v[142:143] op_sel_hi:[1,0] neg_lo:[0,1] neg_hi:[0,1]\n"
    "v_pk_add_f32 v[124:125], v[124:125], v[142:143] op_sel_hi:[1,0] neg_lo:[0,1] neg_hi:[0,1]\n"
    "v_pk_add_f32 v[126:127], v[126:127], v[142:143] op_sel_hi:[1,0] neg_lo:[0,1] neg_hi:[0,1]\n"
    "v_pk_add_f32 v[128:129], v[128:129], v[142:143] op_sel_hi:[1,0] neg_lo:[0,1] neg_hi:[0,1]\n"
    "v_pk_add_f32 v[130:131], v[130:131], v[142:143] op_sel_hi:[1,0] neg_lo:[0,1] neg_hi:[0,1]\n"
    "v_pk_add_f32 v[132:133], v[132:133], v[142:143] op_sel_hi:[1,0] neg_lo:[0,1] neg_hi:[0,1]\n"
    "v_pk_add_f32 v[134:135], v[134:135], v[142:143] op_sel_hi:[1,0] neg_lo:[0,1] neg_hi:[0,1]\n"
    "v_mov_b32 v145, v144\n"
    "v_cvt_pk_bf16_f32 v100, v112, v113\n"
    "v_cvt_pk_bf16_f32 v101, v114, v115\n"
    "v_permlane16_swap_b32 v144, v145\n"
    "v_cvt_pk_bf16_f32 v102, v116, v117\n"
    "v_add_f32 v144, v144, v145\n"
    "v_cvt_pk_bf16_f32 v103, v118, v119\n"
    "v_mul_f32 v144, 0x3ae38e39, v144\n"
    "v_cvt_pk_bf16_f32 v104, v120, v121\n"
    "v_cvt_pk_bf16_f32 v146, v144, v144\n"
    "v_cvt_pk_bf16_f32 v105, v122, v123\n"
    "v_lshlrev_b32 v146, 16, v146\n"
    "v_cvt_pk_bf16_f32 v106, v124, v125\n"
    "v_add_f32 v146, 0x3727c5ac, v146\n"
    "v_cvt_pk_bf16_f32 v107, v126, v127\n"
    "v_cvt_pk_bf16_f32 v146, v146, v146\n"
    "v_cvt_pk_bf16_f32 v108, v128, v129\n"
    "v_lshlrev_b32 v146, 16, v146\n"
    "v_cvt_pk_bf16_f32 v109, v130, v131\n"
    "v_sqrt_f32 v146, v146\n"
    "v_cvt_pk_bf16_f32 v110, v132, v133\n"
    "v_cvt_pk_bf16_f32 v111, v134, v135\n"
    "v_cvt_pk_bf16_f32 v146, v146, v146\n"
    "v_lshlrev_b32 v146, 16, v146\n"
    "v_lshlrev_b32 v112, 16, v100\n"
    "v_and_b32 v113, 0xffff0000, v100\n"
    "v_lshlrev_b32 v114, 16, v101\n"
    "v_and_b32 v115, 0xffff0000, v101\n"
    "v_lshlrev_b32 v116, 16, v102\n"
    "v_and_b32 v117, 0xffff0000, v102\n"
    "v_lshlrev_b32 v118, 16, v103\n"
    "v_and_b32 v119, 0xffff0000, v103\n"
    "v_lshlrev_b32 v120, 16, v104\n"
    "v_and_b32 v121, 0xffff0000, v104\n"
    "v_lshlrev_b32 v122, 16, v105\n"
    "v_and_b32 v123, 0xffff0000, v105\n"
    "v_lshlrev_b32 v124, 16, v106\n"
    "v_and_b32 v125, 0xffff0000, v106\n"
    "v_lshlrev_b32 v126, 16, v107\n"
    "v_and_b32 v127, 0xffff0000, v107\n"
    "v_lshlrev_b32 v128, 16, v108\n"
    "v_and_b32 v129, 0xffff0000, v108\n"
    "v_lshlrev_b32 v130, 16, v109\n"
    "v_and_b32 v131, 0xffff0000, v109\n"
    "v_lshlrev_b32 v132, 16, v110\n"
    "v_and_b32 v133, 0xffff0000, v110\n"
    "v_lshlrev_b32 v134, 16, v111\n"
    "v_and_b32 v135, 0xffff0000, v111\n"
    "v_rcp_f32 v143, v146\n"
    "s_nop 0\n"
    "v_pk_mul_f32 v[112:113], v[112:113], v[142:143] op_sel:[0,1] op_sel_hi:[1,1]\n"
    "v_pk_mul_f32 v[114:115], v[114:115], v[142:143] op_sel:[0,1] op_sel_hi:[1,1]\n"
    "v_pk_mul_f32 v[116:117], v[116:117], v[142:143] op_sel:[0,1] op_sel_hi:[1,1]\n"
    "v_pk_mul_f32 v[118:119], v[118:119], v[142:143] op_sel:[0,1] op_sel_hi:[1,1]\n"
    "v_pk_mul_f32 v[120:121], v[120:121], v[142:143] op_sel:[0,1] op_sel_hi:[1,1]\n"
    "v_pk_mul_f32 v[122:123], v[122:123], v[142:143] op_sel:[0,1] op_sel_hi:[1,1]\n"
    "v_pk_mul_f32 v[124:125], v[124:125], v[142:143] op_sel:[0,1] op_sel_hi:[1,1]\n"
    "v_pk_mul_f32 v[126:127], v[126:127], v[142:143] op_sel:[0,1] op_sel_hi:[1,1]\n"
    "v_pk_mul_f32 v[128:129], v[128:129], v[142:143] op_sel:[0,1] op_sel_hi:[1,1]\n"
    "v_pk_mul_f32 v[130:131], v[130:131], v[142:143] op_sel:[0,1] op_sel_hi:[1,1]\n"
    "v_pk_mul_f32 v[132:133], v[132:133], v[142:143] op_sel:[0,1] op_sel_hi:[1,1]\n"
    "v_pk_mul_f32 v[134:135], v[134:135], v[142:143] op_sel:[0,1] op_sel_hi:[1,1]\n"
    "v_cvt_pk_bf16_f32 v100, v112, v113\n"
    "v_cvt_pk_bf16_f32 v101, v114, v115\n"
    "v_cvt_pk_bf16_f32 v102, v116, v117\n"
    "v_cvt_pk_bf16_f32 v103, v118, v119\n"
    "v_cvt_pk_bf16_f32 v104, v120, v121\n"
    "v_cvt_pk_bf16_f32 v105, v122, v123\n"
    "v_cvt_pk_bf16_f32 v106, v124, v125\n"
    "v_cvt_pk_bf16_f32 v107, v126, v127\n"
    "v_cvt_pk_bf16_f32 v108, v128, v129\n"
    "v_cvt_pk_bf16_f32 v109, v130, v131\n"
    "v_cvt_pk_bf16_f32 v110, v132, v133\n"
    "v_cvt_pk_bf16_f32 v111, v134, v135\n"
    "buffer_store_dwordx4 v[100:103], v98, s[36:39], s56 offen sc1\n"
    "s_add_u32 s56, s56, 6144\n"
    "buffer_store_dwordx4 v[104:107], v98, s[36:39], s56 offen sc1\n"
    "s_add_u32 s56, s56, 6144\n"
    "buffer_store_dwordx4 v[108:111], v98, s[36:39], s56 offen sc1\n"
    "buffer_store_dwordx2 v[142:143], v99, s[72:75], 0 offen sc1\n"
    "s_mov_b32 s56, s57\n"
    "v_mov_b32 v99, v94\n"
    "s_add_u32 s58, s58, 1\n"
    "s_cmp_lt_u32 s58, 2\n"
    "s_cbranch_scc1 LNL_1\n"
    "s_waitcnt vmcnt(9)\n"
    "v_readfirstlane_b32 s84, v96\n"
    "v_readfirstlane_b32 s85, v97\n"
    "s_nop 0\n"
    "s_add_u32 s84, s84, 1\n"
    "s_addc_u32 s85, s85, 0\n"
    "s_cmp_eq_u32 s55, 1\n"
    "s_cbranch_scc1 K_SSM\n"
    "s_waitcnt vmcnt(0)\n"
    "s_lshl_b32 s40, s10, 6\n"
    "s_lshl_b32 s41, s61, 2\n"
    "s_add_u32 s40, s40, s41\n"
    "s_add_u32 s40, s40, s9\n"
    "s_lshl_b32 s40, s40, 3\n"
    "s_add_u32 s40, s40, 4096\n"
    "v_mov_b32 v95, 0x7fffffff\n"
    "v_cmp_eq_u32 vcc, 0, v0\n"
    "s_nop 1\n"
    "v_cndmask_b32 v95, v95, 0, vcc\n"
    "v_mov_b32 v96, s84\n"
    "v_mov_b32 v97, s85\n"
    "buffer_store_dwordx2 v[96:97], v95, s[72:75], s40 offen sc1\n"
    "s_mov_b32 s16, s92\n"
    "s_and_b32 s17, s93, 0xffff\n"
    "s_mov_b32 s18, 5308416\n"
    "s_mov_b32 s19, 0x20000\n"
    "s_mov_b32 s44, s94\n"
    "s_and_b32 s45, s95, 0xffff\n"
    "s_mov_b32 s46, 9216\n"
    "s_mov_b32 s47, 0x20000\n"
    "v_lshrrev_b32 v72, 3, v0\n"
    "v_mul_u32_u24 v72, 9216, v72\n"
    "v_and_b32 v73, 1, v0\n"
    "v_lshl_add_u32 v72, v73, 4, v72\n"
    "v_bfe_u32 v73, v0, 1, 2\n"
    "v_bfe_u32 v74, v0, 4, 1\n"
    "v_xor_b32 v73, v73, v74\n"
    "v_lshl_add_u32 v6, v73, 5, v72\n"
    "v_xor_b32 v73, 2, v73\n"
    "v_lshl_add_u32 v7, v73, 5, v72\n"
    "v_add_u32 v7, 72704, v7\n"
    "s_mul_i32 s49, s11, 768\n"
    "s_mul_i32 s40, s9, 1327104\n"
    "s_add_u32 s49, s49, s40\n"
    "s_add_u32 s40, s14, 0\n"
    "s_cmp_ge_u32 s40, 6\n"
    "s_cselect_b32 s41, 6, 0\n"
    "s_sub_u32 s40, s40, s41\n"
    "s_lshl_b32 s40, s40, 7\n"
    "s_mul_i32 s41, s9, 18432\n"
    "s_add_u32 m0, s41, 0\n"
    "s_add_u32 s48, s49, s40\n"
    "buffer_load_dwordx4 v6, s[16:19], s48 offen offset:0 lds\n"
    "buffer_load_dwordx4 v7, s[16:19], s48 offen offset:1024 lds\n"
    "s_add_u32 m0, m0, 2048\n"
    "s_add_u32 s48, s48, 147456\n"
    "buffer_load_dwordx4 v6, s[16:19], s48 offen offset:0 lds\n"
    "buffer_load_dwordx4 v7, s[16:19], s48 offen offset:1024 lds\n"
    "s_add_u32 m0, m0, 2048\n"
    "s_add_u32 s48, s48, 147456\n"
    "buffer_load_dwordx4 v6, s[16:19], s48 offen offset:0 lds\n"
    "buffer_load_dwordx4 v7, s[16:19], s48 offen offset:1024 lds\n"
    "s_add_u32 m0, m0, 2048\n"
    "s_add_u32 s48, s48, 147456\n"
    "buffer_load_dwordx4 v6, s[16:19], s48 offen offset:0 lds\n"
    "buffer_load_dwordx4 v7, s[16:19], s48 offen offset:1024 lds\n"
    "s_add_u32 m0, m0, 2048\n"
    "s_add_u32 s48, s48, 147456\n"
    "buffer_load_dwordx4 v6, s[16:19], s48 offen offset:0 lds\n"
    "buffer_load_dwordx4 v7, s[16:19], s48 offen offset:1024 lds\n"
    "s_add_u32 m0, m0, 2048\n"
    "s_add_u32 s48, s48, 147456\n"
    "buffer_load_dwordx4 v6, s[16:19], s48 offen offset:0 lds\n"
    "buffer_load_dwordx4 v7, s[16:19], s48 offen offset:1024 lds\n"
    "s_add_u32 m0, m0, 2048\n"
    "s_add_u32 s48, s48, 147456\n"
    "buffer_load_dwordx4 v6, s[16:19], s48 offen offset:0 lds\n"
    "buffer_load_dwordx4 v7, s[16:19], s48 offen offset:1024 lds\n"
    "s_add_u32 m0, m0, 2048\n"
    "s_add_u32 s48, s48, 147456\n"
    "buffer_load_dwordx4 v6, s[16:19], s48 offen offset:0 lds\n"
    "buffer_load_dwordx4 v7, s[16:19], s48 offen offset:1024 lds\n"
    "s_add_u32 m0, m0, 2048\n"
    "s_add_u32 s48, s48, 147456\n"
    "buffer_load_dwordx4 v6, s[16:19], s48 offen offset:0 lds\n"
    "buffer_load_dwordx4 v7, s[16:19], s48 offen offset:1024 lds\n"
    "s_lshl_b32 s40, s9, 6\n"
    "v_add_u32 v72, s40, v0\n"
    "v_lshrrev_b32 v73, 6, v72\n"
    "v_add_u32 v73, s14, v73\n"
    "v_cmp_le_u32 vcc, 6, v73\n"
    "v_subrev_u32 v75, 6, v73\n"
    "s_nop 0\n"
    "v_cndmask_b32 v73, v73, v75, vcc\n"
    "v_lshlrev_b32 v73, 6, v73\n"
    "v_and_b32 v75, 63, v72\n"
    "v_add_u32 v73, v73, v75\n"
    "s_mul_i32 s41, s11, 384\n"
    "v_add_u32 v73, s41, v73\n"
    "v_lshlrev_b32 v73, 1, v73\n"
    "buffer_load_ushort v76, v73, s[44:47], 0 offen\n"
    "v_add_u32 v72, 256, v72\n"
    "v_lshrrev_b32 v74, 6, v72\n"
    "v_add_u32 v74, s14, v74\n"
    "v_cmp_le_u32 vcc, 6, v74\n"
    "v_subrev_u32 v75, 6, v74\n"
    "s_nop 0\n"
    "v_cndmask_b32 v74, v74, v75, vcc\n"
    "v_lshlrev_b32 v74, 6, v74\n"
    "v_and_b32 v75, 63, v72\n"
    "v_add_u32 v74, v74, v75\n"
    "s_mul_i32 s41, s11, 384\n"
    "v_add_u32 v74, s41, v74\n"
    "v_lshlrev_b32 v74, 1, v74\n"
    "v_cmp_le_u32 vcc, 384, v72\n"
    "v_mov_b32 v75, 0x7fffffff\n"
    "s_nop 0\n"
    "v_cndmask_b32 v74, v74, v75, vcc\n"
    "buffer_load_ushort v77, v74, s[44:47], 0 offen\n"
    "buffer_load_ushort v78, v73, s[44:47], 0 offen\n"
    "s_load_dwordx2 s[86:87], %0, 88\n"
    "s_load_dwordx2 s[88:89], %0, 96\n"
    "s_load_dwordx2 s[90:91], %0, 104\n"
    "s_load_dwordx2 s[92:93], %0, 64\n"
    "s_lshl_b32 s40, s10, 9\n"
    "s_lshl_b32 s41, s12, 8\n"
    "s_add_u32 s40, s40, s41\n"
    "s_add_u32 s55, s40, 4096\n"
    "v_and_b32 v160, 31, v0\n"
    "v_lshlrev_b32 v160, 3, v160\n"
    "s_lshl_b32 s40, s10, 9\n"
    "s_add_u32 s55, s40, 4096\n"
    "v_lshlrev_b32 v160, 3, v0\n"
    "s_waitcnt vmcnt(0)\n"
    "buffer_load_dwordx2 v[162:163], v160, s[72:75], s55 offen sc1\n"
    "s_lshl_b32 s40, s9, 8\n"
    "s_add_u32 s40, s40, 147456\n"
    "v_lshl_add_u32 v72, v0, 2, s40\n"
    "v_lshlrev_b32 v76, 16, v76\n"
    "v_lshlrev_b32 v77, 16, v77\n"
    "ds_write_b32 v72, v76\n"
    "ds_write_b32 v72, v77 offset:1024\n"
    "v_bfe_u32 v72, v0, 1, 2\n"
    "v_bfe_u32 v73, v0, 4, 2\n"
    "v_xor_b32 v72, v72, v73\n"
    "v_lshlrev_b32 v72, 5, v72\n"
    "v_and_b32 v73, 1, v0\n"
    "v_lshl_add_u32 v72, v73, 4, v72\n"
    "v_lshrrev_b32 v73, 3, v0\n"
    "v_mul_u32_u24 v74, 1152, v73\n"
    "v_add_u32 v9, v74, v72\n"
    "v_add_u32 v10, 8192, v9\n"
    "v_add_u32 v11, 16384, v9\n"
    "v_add_u32 v12, 24576, v9\n"
    "s_mul_i32 s40, s9, 12\n"
    "s_add_u32 s40, s40, 0\n"
    "s_lshr_b32 s41, s40, 3\n"
    "s_add_u32 s41, s41, s14\n"
    "s_cmp_ge_u32 s41, 6\n"
    "s_cselect_b32 s42, 6, 0\n"
    "s_sub_u32 s41, s41, s42\n"
    "s_lshl_b32 s41, s41, 6\n"
    "s_and_b32 s40, s40, 7\n"
    "s_lshl_b32 s40, s40, 3\n"
    "s_add_u32 s41, s41, s40\n"
    "s_mul_i32 s40, s11, 384\n"
    "s_add_u32 s41, s41, s40\n"
    "s_mul_i32 s50, s41, 1152\n"
    "s_mul_i32 s40, s9, 12\n"
    "s_add_u32 s40, s40, 4\n"
    "s_lshr_b32 s41, s40, 3\n"
    "s_add_u32 s41, s41, s14\n"
    "s_cmp_ge_u32 s41, 6\n"
    "s_cselect_b32 s42, 6, 0\n"
    "s_sub_u32 s41, s41, s42\n"
    "s_lshl_b32 s41, s41, 6\n"
    "s_and_b32 s40, s40, 7\n"
    "s_lshl_b32 s40, s40, 3\n"
    "s_add_u32 s41, s41, s40\n"
    "s_mul_i32 s40, s11, 384\n"
    "s_add_u32 s41, s41, s40\n"
    "s_mul_i32 s51, s41, 1152\n"
    "s_mul_i32 s40, s9, 12\n"
    "s_add_u32 s40, s40, 8\n"
    "s_lshr_b32 s41, s40, 3\n"
    "s_add_u32 s41, s41, s14\n"
    "s_cmp_ge_u32 s41, 6\n"
    "s_cselect_b32 s42, 6, 0\n"
    "s_sub_u32 s41, s41, s42\n"
    "s_lshl_b32 s41, s41, 6\n"
    "s_and_b32 s40, s40, 7\n"
    "s_lshl_b32 s40, s40, 3\n"
    "s_add_u32 s41, s41, s40\n"
    "s_mul_i32 s40, s11, 384\n"
    "s_add_u32 s41, s41, s40\n"
    "s_mul_i32 s52, s41, 1152\n"
    "v_lshrrev_b32 v72, 4, v0\n"
    "v_bfe_u32 v73, v0, 2, 2\n"
    "v_lshl_add_u32 v74, v72, 3, v73\n"
    "v_lshlrev_b32 v74, 7, v74\n"
    "v_bfe_u32 v75, v0, 3, 1\n"
    "v_bfe_u32 v79, v0, 4, 1\n"
    "v_lshl_add_u32 v75, v79, 1, v75\n"
    "s_lshl_b32 s40, s13, 1\n"
    "v_xor_b32 v75, s40, v75\n"
    "v_lshl_add_u32 v74, v75, 5, v74\n"
    "v_and_b32 v75, 3, v0\n"
    "v_lshl_add_u32 v1, v75, 3, v74\n"
    "v_xor_b32 v2, 32, v1\n"
    "v_lshrrev_b32 v72, 2, v0\n"
    "v_lshlrev_b32 v73, 7, v72\n"
    "v_bfe_u32 v74, v0, 3, 2\n"
    "v_xor_b32 v74, s40, v74\n"
    "v_lshl_add_u32 v73, v74, 5, v73\n"
    "v_and_b32 v74, 3, v0\n"
    "v_lshl_add_u32 v3, v74, 3, v73\n"
    "v_xor_b32 v4, 32, v3\n"
    "v_lshrrev_b32 v72, 4, v0\n"
    "v_lshlrev_b32 v72, 4, v72\n"
    "s_lshl_b32 s40, s13, 7\n"
    "s_add_u32 s40, s40, 147456\n"
    "v_add_u32 v14, s40, v72\n"
    "v_mov_b32 v15, 0xbdd2d3e8\n"
    "v_mov_b32 v8, v13\n"
    "v_add_u32 v5, 17408, v13\n"
    "s_lshl_b32 s40, s12, 12\n"
    "v_add_u32 v196, s40, v13\n"
    "v_add_u32 v197, 149504, v196\n"
    "v_add_u32 v198, 139264, v196\n"
    "v_add_u32 v196, 73728, v196\n"
    "s_waitcnt lgkmcnt(0)\n"
    "s_mov_b32 s20, s86\n"
    "s_and_b32 s21, s87, 0xffff\n"
    "s_mov_b32 s22, 5308416\n"
    "s_mov_b32 s23, 0x20000\n"
    "s_mov_b32 s96, s88\n"
    "s_and_b32 s97, s89, 0xffff\n"
    "s_mov_b32 s98, 1152\n"
    "s_mov_b32 s99, 0x20000\n"
    "s_mov_b32 s80, s90\n"
    "s_and_b32 s81, s91, 0xffff\n"
    "s_mov_b32 s82, 4718592\n"
    "s_mov_b32 s83, 0x20000\n"
    "s_mov_b32 s88, s92\n"
    "s_and_b32 s89, s93, 0xffff\n"
    "s_mov_b32 s90, 1152\n"
    "s_mov_b32 s91, 0x20000\n"
    "s_cmp_lt_u32 s9, 2\n"
    "s_cselect_b32 s44, s96, s88\n"
    "s_cselect_b32 s45, s97, s89\n"
    "s_cselect_b32 s46, s98, s90\n"
    "s_cselect_b32 s47, s99, s91\n"
    "s_and_b32 s40, s9, 1\n"
    "s_lshl_b32 s40, s40, 10\n"
    "s_lshl_b32 s41, s9, 10\n"
    "s_add_u32 m0, s41, 157696\n"
    "s_nop 0\n"
    "buffer_load_dwordx4 v13, s[44:47], s40 offen lds\n"
    "s_add_u32 s24, s66, 1048576\n"
    "s_addc_u32 s25, s67, 0\n"
    "s_and_b32 s25, s25, 0xffff\n"
    "s_mov_b32 s26, 2359296\n"
    "s_mov_b32 s27, 0x20000\n"
    "s_add_u32 s28, s66, 16777216\n"
    "s_addc_u32 s29, s67, 0\n"
    "s_and_b32 s29, s29, 0xffff\n"
    "s_mov_b32 s30, 37748736\n"
    "s_mov_b32 s31, 0x20000\n"
    "s_mul_i32 s40, s10, 147456\n"
    "s_mul_i32 s41, s9, 36864\n"
    "s_add_u32 s56, s40, s41\n"
    "s_waitcnt vmcnt(1)\n"
    "v_cmp_eq_u32 vcc, s84, v162\n"
    "v_cmp_eq_u32 s[40:41], s85, v163\n"
    "s_nop 1\n"
    "s_and_b64 vcc, vcc, s[40:41]\n"
    "s_cmp_eq_u64 vcc, -1\n"
    "s_cbranch_scc1 POK_2\n"
    "s_mov_b32 s42, 0\n"
    "PLP_3:\n"
    "s_sleep 1\n"
    "buffer_load_dwordx2 v[162:163], v160, s[72:75], s55 offen sc1\n"
    "s_waitcnt vmcnt(0)\n"
    "v_cmp_eq_u32 vcc, s84, v162\n"
    "v_cmp_eq_u32 s[40:41], s85, v163\n"
    "s_nop 1\n"
    "s_and_b64 vcc, vcc, s[40:41]\n"
    "s_cmp_eq_u64 vcc, -1\n"
    "s_cbranch_scc1 POK_2\n"
    "s_add_u32 s42, s42, 1\n"
    "s_cmp_lt_u32 s42, 20000\n"
    "s_cbranch_scc1 PLP_3\n"
    "POK_2:\n"
    "s_lshl_b32 s40, s9, 11\n"
    "s_add_u32 m0, s40, 73728\n"
    "s_add_u32 s41, s56, 0\n"
    "buffer_load_dwordx4 v8, s[24:27], s41 offen sc1 lds\n"
    "buffer_load_dwordx4 v5, s[24:27], s41 offen offset:1024 sc1 lds\n"
    "s_lshl_b32 s40, s9, 11\n"
    "s_add_u32 m0, s40, 81920\n"
    "s_add_u32 s41, s56, 1024\n"
    "buffer_load_dwordx4 v8, s[24:27], s41 offen sc1 lds\n"
    "buffer_load_dwordx4 v5, s[24:27], s41 offen offset:1024 sc1 lds\n"
    "s_lshl_b32 s40, s9, 11\n"
    "s_add_u32 m0, s40, 90112\n"
    "s_add_u32 s41, s56, 2048\n"
    "buffer_load_dwordx4 v8, s[24:27], s41 offen sc1 lds\n"
    "buffer_load_dwordx4 v5, s[24:27], s41 offen offset:1024 sc1 lds\n"
    "s_lshl_b32 s40, s9, 11\n"
    "s_add_u32 m0, s40, 98304\n"
    "s_add_u32 s41, s56, 3072\n"
    "buffer_load_dwordx4 v8, s[24:27], s41 offen sc1 lds\n"
    "buffer_load_dwordx4 v5, s[24:27], s41 offen offset:1024 sc1 lds\n"
    "s_lshl_b32 s40, s9, 11\n"
    "s_add_u32 m0, s40, 106496\n"
    "s_add_u32 s41, s56, 4096\n"
    "buffer_load_dwordx4 v8, s[24:27], s41 offen sc1 lds\n"
    "buffer_load_dwordx4 v5, s[24:27], s41 offen offset:1024 sc1 lds\n"
    "s_lshl_b32 s40, s9, 11\n"
    "s_add_u32 m0, s40, 114688\n"
    "s_add_u32 s41, s56, 5120\n"
    "buffer_load_dwordx4 v8, s[24:27], s41 offen sc1 lds\n"
    "buffer_load_dwordx4 v5, s[24:27], s41 offen offset:1024 sc1 lds\n"
    "s_lshl_b32 s40, s9, 11\n"
    "s_add_u32 m0, s40, 122880\n"
    "s_add_u32 s41, s56, 6144\n"
    "buffer_load_dwordx4 v8, s[24:27], s41 offen sc1 lds\n"
    "buffer_load_dwordx4 v5, s[24:27], s41 offen offset:1024 sc1 lds\n"
    "s_lshl_b32 s40, s9, 11\n"
    "s_add_u32 m0, s40, 131072\n"
    "s_add_u32 s41, s56, 7168\n"
    "buffer_load_dwordx4 v8, s[24:27], s41 offen sc1 lds\n"
    "buffer_load_dwordx4 v5, s[24:27], s41 offen offset:1024 sc1 lds\n"
    "s_lshl_b32 s40, s9, 11\n"
    "s_add_u32 m0, s40, 139264\n"
    "s_add_u32 s41, s56, 8192\n"
    "buffer_load_dwordx4 v8, s[24:27], s41 offen sc1 lds\n"
    "buffer_load_dwordx4 v5, s[24:27], s41 offen offset:1024 sc1 lds\n"
    "s_lshl_b32 s40, s9, 11\n"
    "s_add_u32 m0, s40, 149504\n"
    "s_add_u32 s41, s56, 9216\n"
    "buffer_load_dwordx4 v8, s[24:27], s41 offen sc1 lds\n"
    "buffer_load_dwordx4 v5, s[24:27], s41 offen offset:1024 sc1 lds\n"
    "v_add_u32 v192, 65536, v1\n"
    "v_add_u32 v193, 65536, v2\n"
    "s_add_u32 s40, s14, 1\n"
    "s_cmp_ge_u32 s40, 6\n"
    "s_cselect_b32 s41, 6, 0\n"
    "s_sub_u32 s40, s40, s41\n"
    "s_lshl_b32 s40, s40, 7\n"
    "s_mul_i32 s47, s11, 768\n"
    "s_add_u32 s47, s47, s40\n"
    "s_mul_i32 s40, s9, 147456\n"
    "s_add_u32 s47, s47, s40\n"
    "s_waitcnt vmcnt(16)\n"
    "s_waitcnt lgkmcnt(0)\n"
    "s_barrier\n"
    "ds_read_b128 v[32:35], v14 offset:0\n"
    "ds_read_b128 v[36:39], v14 offset:64\n"
    "ds_read_b128 a[0:3], v196 offset:0\n"
    "ds_read_b128 a[4:7], v196 offset:1024\n"
    "ds_read_b128 a[8:11], v196 offset:2048\n"
    "ds_read_b128 a[12:15], v196 offset:3072\n"
    "ds_read_b64_tr_b16 v[16:17], v1 offset:0\n"
    "ds_read_b64_tr_b16 v[18:19], v1 offset:512\n"
    "ds_read_b64_tr_b16 v[20:21], v2 offset:0\n"
    "ds_read_b64_tr_b16 v[22:23], v2 offset:512\n"
    "ds_read_b128 a[16:19], v196 offset:8192\n"
    "ds_read_b128 a[20:23], v196 offset:9216\n"
    "ds_read_b128 a[24:27], v196 offset:10240\n"
    "ds_read_b128 a[28:31], v196 offset:11264\n"
    "s_waitcnt lgkmcnt(8)\n"
    "ds_read_b64_tr_b16 v[24:25], v1 offset:4096\n"
    "ds_read_b64_tr_b16 v[26:27], v1 offset:4608\n"
    "ds_read_b64_tr_b16 v[28:29], v2 offset:4096\n"
    "ds_read_b64_tr_b16 v[30:31], v2 offset:4608\n"
    "s_waitcnt vmcnt(12)\n"
    "s_waitcnt lgkmcnt(0)\n"
    "s_barrier\n"
    "v_mfma_f32_16x16x32_bf16 v[40:43], v[16:19], a[0:3], v[32:35]\n"
    "ds_read_b128 a[32:35], v196 offset:16384\n"
    "v_mfma_f32_16x16x32_bf16 v[44:47], v[16:19], a[4:7], v[32:35]\n"
    "ds_read_b128 a[36:39], v196 offset:17408\n"
    "v_mfma_f32_16x16x32_bf16 v[48:51], v[16:19], a[8:11], v[32:35]\n"
    "ds_read_b128 a[40:43], v196 offset:18432\n"
    "v_mfma_f32_16x16x32_bf16 v[52:55], v[16:19], a[12:15], v[32:35]\n"
    "ds_read_b128 a[44:47], v196 offset:19456\n"
    "s_lshl_b32 s40, s9, 11\n"
    "s_add_u32 m0, s40, 73728\n"
    "s_add_u32 s41, s56, 10240\n"
    "buffer_load_dwordx4 v8, s[24:27], s41 offen sc1 lds\n"
    "buffer_load_dwordx4 v5, s[24:27], s41 offen offset:1024 sc1 lds\n"
    "v_mfma_f32_16x16x32_bf16 v[56:59], v[20:23], a[0:3], v[36:39]\n"
    "ds_read_b64_tr_b16 v[176:177], v1 offset:8192\n"
    "v_mfma_f32_16x16x32_bf16 v[60:63], v[20:23], a[4:7], v[36:39]\n"
    "ds_read_b64_tr_b16 v[178:179], v1 offset:8704\n"
    "v_mfma_f32_16x16x32_bf16 v[64:67], v[20:23], a[8:11], v[36:39]\n"
    "ds_read_b64_tr_b16 v[180:181], v2 offset:8192\n"
    "v_mfma_f32_16x16x32_bf16 v[68:71], v[20:23], a[12:15], v[36:39]\n"
    "ds_read_b64_tr_b16 v[182:183], v2 offset:8704\n"
    "v_mfma_f32_16x16x32_bf16 v[40:43], v[24:27], a[16:19], v[40:43]\n"
    "ds_read_b128 a[48:51], v196 offset:24576\n"
    "v_mfma_f32_16x16x32_bf16 v[44:47], v[24:27], a[20:23], v[44:47]\n"
    "ds_read_b128 a[52:55], v196 offset:25600\n"
    "s_lshl_b32 s40, s9, 11\n"
    "s_add_u32 m0, s40, 81920\n"
    "s_add_u32 s41, s56, 11264\n"
    "buffer_load_dwordx4 v8, s[24:27], s41 offen sc1 lds\n"
    "buffer_load_dwordx4 v5, s[24:27], s41 offen offset:1024 sc1 lds\n"
    "v_mfma_f32_16x16x32_bf16 v[48:51], v[24:27], a[24:27], v[48:51]\n"
    "ds_read_b128 a[56:59], v196 offset:26624\n"
    "v_mfma_f32_16x16x32_bf16 v[52:55], v[24:27], a[28:31], v[52:55]\n"
    "ds_read_b128 a[60:63], v196 offset:27648\n"
    "v_mfma_f32_16x16x32_bf16 v[56:59], v[28:31], a[16:19], v[56:59]\n"
    "ds_read_b64_tr_b16 v[184:185], v1 offset:12288\n"
    "v_mfma_f32_16x16x32_bf16 v[60:63], v[28:31], a[20:23], v[60:63]\n"
    "ds_read_b64_tr_b16 v[186:187], v1 offset:12800\n"
    "v_mfma_f32_16x16x32_bf16 v[64:67], v[28:31], a[24:27], v[64:67]\n"
    "ds_read_b64_tr_b16 v[188:189], v2 offset:12288\n"
    "v_mfma_f32_16x16x32_bf16 v[68:71], v[28:31], a[28:31], v[68:71]\n"
    "ds_read_b64_tr_b16 v[190:191], v2 offset:12800\n"
    "s_waitcnt vmcnt(12)\n"
    "s_waitcnt lgkmcnt(0)\n"
    "s_barrier\n"
    "v_mfma_f32_16x16x32_bf16 v[40:43], v[176:179], a[32:35], v[40:43]\n"
    "ds_read_b128 a[64:67], v196 offset:32768\n"
    "v_mfma_f32_16x16x32_bf16 v[44:47], v[176:179], a[36:39], v[44:47]\n"
    "ds_read_b128 a[68:71], v196 offset:33792\n"
    "v_mfma_f32_16x16x32_bf16 v[48:51], v[176:179], a[40:43], v[48:51]\n"
    "ds_read_b128 a[72:75], v196 offset:34816\n"
    "v_mfma_f32_16x16x32_bf16 v[52:55], v[176:179], a[44:47], v[52:55]\n"
    "ds_read_b128 a[76:79], v196 offset:35840\n"
    "s_lshl_b32 s40, s9, 11\n"
    "s_add_u32 m0, s40, 90112\n"
    "s_add_u32 s41, s56, 12288\n"
    "buffer_load_dwordx4 v8, s[24:27], s41 offen sc1 lds\n"
    "buffer_load_dwordx4 v5, s[24:27], s41 offen offset:1024 sc1 lds\n"
    "v_mfma_f32_16x16x32_bf16 v[56:59], v[180:183], a[32:35], v[56:59]\n"
    "ds_read_b64_tr_b16 v[16:17], v1 offset:16384\n"
    "v_mfma_f32_16x16x32_bf16 v[60:63], v[180:183], a[36:39], v[60:63]\n"
    "ds_read_b64_tr_b16 v[18:19], v1 offset:16896\n"
    "v_mfma_f32_16x16x32_bf16 v[64:67], v[180:183], a[40:43], v[64:67]\n"
    "ds_read_b64_tr_b16 v[20:21], v2 offset:16384\n"
    "v_mfma_f32_16x16x32_bf16 v[68:71], v[180:183], a[44:47], v[68:71]\n"
    "ds_read_b64_tr_b16 v[22:23], v2 offset:16896\n"
    "v_mfma_f32_16x16x32_bf16 v[40:43], v[184:187], a[48:51], v[40:43]\n"
    "ds_read_b128 a[80:83], v196 offset:40960\n"
    "v_mfma_f32_16x16x32_bf16 v[44:47], v[184:187], a[52:55], v[44:47]\n"
    "ds_read_b128 a[84:87], v196 offset:41984\n"
    "s_lshl_b32 s40, s9, 11\n"
    "s_add_u32 m0, s40, 98304\n"
    "s_add_u32 s41, s56, 13312\n"
    "buffer_load_dwordx4 v8, s[24:27], s41 offen sc1 lds\n"
    "buffer_load_dwordx4 v5, s[24:27], s41 offen offset:1024 sc1 lds\n"
    "v_mfma_f32_16x16x32_bf16 v[48:51], v[184:187], a[56:59], v[48:51]\n"
    "ds_read_b128 a[88:91], v196 offset:43008\n"
    "v_mfma_f32_16x16x32_bf16 v[52:55], v[184:187], a[60:63], v[52:55]\n"
    "ds_read_b128 a[92:95], v196 offset:44032\n"
    "v_mfma_f32_16x16x32_bf16 v[56:59], v[188:191], a[48:51], v[56:59]\n"
    "ds_read_b64_tr_b16 v[24:25], v1 offset:20480\n"
    "v_mfma_f32_16x16x32_bf16 v[60:63], v[188:191], a[52:55], v[60:63]\n"
    "ds_read_b64_tr_b16 v[26:27], v1 offset:20992\n"
    "v_mfma_f32_16x16x32_bf16 v[64:67], v[188:191], a[56:59], v[64:67]\n"
    "ds_read_b64_tr_b16 v[28:29], v2 offset:20480\n"
    "v_mfma_f32_16x16x32_bf16 v[68:71], v[188:191], a[60:63], v[68:71]\n"
    "ds_read_b64_tr_b16 v[30:31], v2 offset:20992\n"
    "s_waitcnt vmcnt(12)\n"
    "s_waitcnt lgkmcnt(0)\n"
    "s_barrier\n"
    "v_mfma_f32_16x16x32_bf16 v[40:43], v[16:19], a[64:67], v[40:43]\n"
    "ds_read_b128 a[96:99], v196 offset:49152\n"
    "v_mfma_f32_16x16x32_bf16 v[44:47], v[16:19], a[68:71], v[44:47]\n"
    "ds_read_b128 a[100:103], v196 offset:50176\n"
    "v_mfma_f32_16x16x32_bf16 v[48:51], v[16:19], a[72:75], v[48:51]\n"
    "ds_read_b128 a[104:107], v196 offset:51200\n"
    "v_mfma_f32_16x16x32_bf16 v[52:55], v[16:19], a[76:79], v[52:55]\n"
    "ds_read_b128 a[108:111], v196 offset:52224\n"
    "s_lshl_b32 s40, s9, 11\n"
    "s_add_u32 m0, s40, 106496\n"
    "s_add_u32 s41, s56, 14336\n"
    "buffer_load_dwordx4 v8, s[24:27], s41 offen sc1 lds\n"
    "buffer_load_dwordx4 v5, s[24:27], s41 offen offset:1024 sc1 lds\n"
    "v_mfma_f32_16x16x32_bf16 v[56:59], v[20:23], a[64:67], v[56:59]\n"
    "ds_read_b64_tr_b16 v[176:177], v1 offset:24576\n"
    "v_mfma_f32_16x16x32_bf16 v[60:63], v[20:23], a[68:71], v[60:63]\n"
    "ds_read_b64_tr_b16 v[178:179], v1 offset:25088\n"
    "v_mfma_f32_16x16x32_bf16 v[64:67], v[20:23], a[72:75], v[64:67]\n"
    "ds_read_b64_tr_b16 v[180:181], v2 offset:24576\n"
    "v_mfma_f32_16x16x32_bf16 v[68:71], v[20:23], a[76:79], v[68:71]\n"
    "ds_read_b64_tr_b16 v[182:183], v2 offset:25088\n"
    "v_mfma_f32_16x16x32_bf16 v[40:43], v[24:27], a[80:83], v[40:43]\n"
    "ds_read_b128 a[112:115], v196 offset:57344\n"
    "v_mfma_f32_16x16x32_bf16 v[44:47], v[24:27], a[84:87], v[44:47]\n"
    "ds_read_b128 a[116:119], v196 offset:58368\n"
    "s_lshl_b32 s40, s9, 11\n"
    "s_add_u32 m0, s40, 114688\n"
    "s_add_u32 s41, s56, 15360\n"
    "buffer_load_dwordx4 v8, s[24:27], s41 offen sc1 lds\n"
    "buffer_load_dwordx4 v5, s[24:27], s41 offen offset:1024 sc1 lds\n"
    "v_mfma_f32_16x16x32_bf16 v[48:51], v[24:27], a[88:91], v[48:51]\n"
    "ds_read_b128 a[120:123], v196 offset:59392\n"
    "v_mfma_f32_16x16x32_bf16 v[52:55], v[24:27], a[92:95], v[52:55]\n"
    "ds_read_b128 a[124:127], v196 offset:60416\n"
    "v_mfma_f32_16x16x32_bf16 v[56:59], v[28:31], a[80:83], v[56:59]\n"
    "ds_read_b64_tr_b16 v[184:185], v1 offset:28672\n"
    "v_mfma_f32_16x16x32_bf16 v[60:63], v[28:31], a[84:87], v[60:63]\n"
    "ds_read_b64_tr_b16 v[186:187], v1 offset:29184\n"
    "v_mfma_f32_16x16x32_bf16 v[64:67], v[28:31], a[88:91], v[64:67]\n"
    "ds_read_b64_tr_b16 v[188:189], v2 offset:28672\n"
    "v_mfma_f32_16x16x32_bf16 v[68:71], v[28:31], a[92:95], v[68:71]\n"
    "ds_read_b64_tr_b16 v[190:191], v2 offset:29184\n"
    "s_waitcnt vmcnt(12)\n"
    "s_waitcnt lgkmcnt(0)\n"
    "s_barrier\n"
    "v_mfma_f32_16x16x32_bf16 v[40:43], v[176:179], a[96:99], v[40:43]\n"
    "ds_read_b128 a[128:131], v198 offset:0\n"
    "v_mfma_f32_16x16x32_bf16 v[44:47], v[176:179], a[100:103], v[44:47]\n"
    "ds_read_b128 a[132:135], v198 offset:1024\n"
    "v_mfma_f32_16x16x32_bf16 v[48:51], v[176:179], a[104:107], v[48:51]\n"
    "ds_read_b128 a[136:139], v198 offset:2048\n"
    "v_mfma_f32_16x16x32_bf16 v[52:55], v[176:179], a[108:111], v[52:55]\n"
    "ds_read_b128 a[140:143], v198 offset:3072\n"
    "s_lshl_b32 s40, s9, 11\n"
    "s_add_u32 m0, s40, 122880\n"
    "s_add_u32 s41, s56, 16384\n"
    "buffer_load_dwordx4 v8, s[24:27], s41 offen sc1 lds\n"
    "buffer_load_dwordx4 v5, s[24:27], s41 offen offset:1024 sc1 lds\n"
    "v_mfma_f32_16x16x32_bf16 v[56:59], v[180:183], a[96:99], v[56:59]\n"
    "ds_read_b64_tr_b16 v[16:17], v1 offset:32768\n"
    "v_mfma_f32_16x16x32_bf16 v[60:63], v[180:183], a[100:103], v[60:63]\n"
    "ds_read_b64_tr_b16 v[18:19], v1 offset:33280\n"
    "v_mfma_f32_16x16x32_bf16 v[64:67], v[180:183], a[104:107], v[64:67]\n"
    "ds_read_b64_tr_b16 v[20:21], v2 offset:32768\n"
    "v_mfma_f32_16x16x32_bf16 v[68:71], v[180:183], a[108:111], v[68:71]\n"
    "ds_read_b64_tr_b16 v[22:23], v2 offset:33280\n"
    "v_mfma_f32_16x16x32_bf16 v[40:43], v[184:187], a[112:115], v[40:43]\n"
    "ds_read_b128 a[144:147], v197 offset:0\n"
    "v_mfma_f32_16x16x32_bf16 v[44:47], v[184:187], a[116:119], v[44:47]\n"
    "ds_read_b128 a[148:151], v197 offset:1024\n"
    "s_lshl_b32 s40, s9, 11\n"
    "s_add_u32 m0, s40, 131072\n"
    "s_add_u32 s41, s56, 17408\n"
    "buffer_load_dwordx4 v8, s[24:27], s41 offen sc1 lds\n"
    "buffer_load_dwordx4 v5, s[24:27], s41 offen offset:1024 sc1 lds\n"
    "v_mfma_f32_16x16x32_bf16 v[48:51], v[184:187], a[120:123], v[48:51]\n"
    "ds_read_b128 a[152:155], v197 offset:2048\n"
    "v_mfma_f32_16x16x32_bf16 v[52:55], v[184:187], a[124:127], v[52:55]\n"
    "ds_read_b128 a[156:159], v197 offset:3072\n"
    "v_mfma_f32_16x16x32_bf16 v[56:59], v[188:191], a[112:115], v[56:59]\n"
    "ds_read_b64_tr_b16 v[24:25], v1 offset:36864\n"
    "v_mfma_f32_16x16x32_bf16 v[60:63], v[188:191], a[116:119], v[60:63]\n"
    "ds_read_b64_tr_b16 v[26:27], v1 offset:37376\n"
    "v_mfma_f32_16x16x32_bf16 v[64:67], v[188:191], a[120:123], v[64:67]\n"
    "ds_read_b64_tr_b16 v[28:29], v2 offset:36864\n"
    "v_mfma_f32_16x16x32_bf16 v[68:71], v[188:191], a[124:127], v[68:71]\n"
    "ds_read_b64_tr_b16 v[30:31], v2 offset:37376\n"
    "s_waitcnt vmcnt(12)\n"
    "s_waitcnt lgkmcnt(0)\n"
    "s_barrier\n"
    "v_mfma_f32_16x16x32_bf16 v[40:43], v[16:19], a[128:131], v[40:43]\n"
    "ds_read_b128 a[160:163], v196 offset:0\n"
    "v_mfma_f32_16x16x32_bf16 v[44:47], v[16:19], a[132:135], v[44:47]\n"
    "ds_read_b128 a[164:167], v196 offset:1024\n"
    "v_mfma_f32_16x16x32_bf16 v[48:51], v[16:19], a[136:139], v[48:51]\n"
    "ds_read_b128 a[168:171], v196 offset:2048\n"
    "v_mfma_f32_16x16x32_bf16 v[52:55], v[16:19], a[140:143], v[52:55]\n"
    "ds_read_b128 a[172:175], v196 offset:3072\n"
    "s_lshl_b32 s40, s9, 11\n"
    "s_add_u32 m0, s40, 139264\n"
    "s_add_u32 s41, s47, 4718592\n"
    "buffer_load_dwordx4 v6, s[16:19], s41 offen offset:0 lds\n"
    "buffer_load_dwordx4 v7, s[16:19], s41 offen offset:1024 lds\n"
    "v_mfma_f32_16x16x32_bf16 v[56:59], v[20:23], a[128:131], v[56:59]\n"
    "ds_read_b64_tr_b16 v[176:177], v1 offset:40960\n"
    "v_mfma_f32_16x16x32_bf16 v[60:63], v[20:23], a[132:135], v[60:63]\n"
    "ds_read_b64_tr_b16 v[178:179], v1 offset:41472\n"
    "v_mfma_f32_16x16x32_bf16 v[64:67], v[20:23], a[136:139], v[64:67]\n"
    "ds_read_b64_tr_b16 v[180:181], v2 offset:40960\n"
    "v_mfma_f32_16x16x32_bf16 v[68:71], v[20:23], a[140:143], v[68:71]\n"
    "ds_read_b64_tr_b16 v[182:183], v2 offset:41472\n"
    "v_mfma_f32_16x16x32_bf16 v[40:43], v[24:27], a[144:147], v[40:43]\n"
    "ds_read_b128 a[176:179], v196 offset:8192\n"
    "v_mfma_f32_16x16x32_bf16 v[44:47], v[24:27], a[148:151], v[44:47]\n"
    "ds_read_b128 a[180:183], v196 offset:9216\n"
    "v_mfma_f32_16x16x32_bf16 v[48:51], v[24:27], a[152:155], v[48:51]\n"
    "ds_read_b128 a[184:187], v196 offset:10240\n"
    "v_mfma_f32_16x16x32_bf16 v[52:55], v[24:27], a[156:159], v[52:55]\n"
    "ds_read_b128 a[188:191], v196 offset:11264\n"
    "v_mfma_f32_16x16x32_bf16 v[56:59], v[28:31], a[144:147], v[56:59]\n"
    "ds_read_b64_tr_b16 v[184:185], v1 offset:45056\n"
    "v_mfma_f32_16x16x32_bf16 v[60:63], v[28:31], a[148:151], v[60:63]\n"
    "ds_read_b64_tr_b16 v[186:187], v1 offset:45568\n"
    "v_mfma_f32_16x16x32_bf16 v[64:67], v[28:31], a[152:155], v[64:67]\n"
    "ds_read_b64_tr_b16 v[188:189], v2 offset:45056\n"
    "v_mfma_f32_16x16x32_bf16 v[68:71], v[28:31], a[156:159], v[68:71]\n"
    "ds_read_b64_tr_b16 v[190:191], v2 offset:45568\n"
    "s_waitcnt vmcnt(10)\n"
    "s_waitcnt lgkmcnt(0)\n"
    "s_barrier\n"
    "v_mfma_f32_16x16x32_bf16 v[40:43], v[176:179], a[160:163], v[40:43]\n"
    "ds_read_b128 a[192:195], v196 offset:16384\n"
    "v_mfma_f32_16x16x32_bf16 v[44:47], v[176:179], a[164:167], v[44:47]\n"
    "ds_read_b128 a[196:199], v196 offset:17408\n"
    "v_mfma_f32_16x16x32_bf16 v[48:51], v[176:179], a[168:171], v[48:51]\n"
    "ds_read_b128 a[200:203], v196 offset:18432\n"
    "v_mfma_f32_16x16x32_bf16 v[52:55], v[176:179], a[172:175], v[52:55]\n"
    "ds_read_b128 a[204:207], v196 offset:19456\n"
    "s_lshl_b32 s40, s9, 11\n"
    "s_add_u32 m0, s40, 73728\n"
    "s_add_u32 s41, s47, 0\n"
    "buffer_load_dwordx4 v6, s[16:19], s41 offen offset:0 lds\n"
    "buffer_load_dwordx4 v7, s[16:19], s41 offen offset:1024 lds\n"
    "v_mfma_f32_16x16x32_bf16 v[56:59], v[180:183], a[160:163], v[56:59]\n"
    "ds_read_b64_tr_b16 v[16:17], v1 offset:49152\n"
    "v_mfma_f32_16x16x32_bf16 v[60:63], v[180:183], a[164:167], v[60:63]\n"
    "ds_read_b64_tr_b16 v[18:19], v1 offset:49664\n"
    "v_mfma_f32_16x16x32_bf16 v[64:67], v[180:183], a[168:171], v[64:67]\n"
    "ds_read_b64_tr_b16 v[20:21], v2 offset:49152\n"
    "v_mfma_f32_16x16x32_bf16 v[68:71], v[180:183], a[172:175], v[68:71]\n"
    "ds_read_b64_tr_b16 v[22:23], v2 offset:49664\n"
    "v_mfma_f32_16x16x32_bf16 v[40:43], v[184:187], a[176:179], v[40:43]\n"
    "ds_read_b128 a[208:211], v196 offset:24576\n"
    "v_mfma_f32_16x16x32_bf16 v[44:47], v[184:187], a[180:183], v[44:47]\n"
    "ds_read_b128 a[212:215], v196 offset:25600\n"
    "s_lshl_b32 s40, s9, 11\n"
    "s_add_u32 m0, s40, 81920\n"
    "s_add_u32 s41, s47, 589824\n"
    "buffer_load_dwordx4 v6, s[16:19], s41 offen offset:0 lds\n"
    "buffer_load_dwordx4 v7, s[16:19], s41 offen offset:1024 lds\n"
    "v_mfma_f32_16x16x32_bf16 v[48:51], v[184:187], a[184:187], v[48:51]\n"
    "ds_read_b128 a[216:219], v196 offset:26624\n"
    "v_mfma_f32_16x16x32_bf16 v[52:55], v[184:187], a[188:191], v[52:55]\n"
    "ds_read_b128 a[220:223], v196 offset:27648\n"
    "v_mfma_f32_16x16x32_bf16 v[56:59], v[188:191], a[176:179], v[56:59]\n"
    "ds_read_b64_tr_b16 v[24:25], v1 offset:53248\n"
    "v_mfma_f32_16x16x32_bf16 v[60:63], v[188:191], a[180:183], v[60:63]\n"
    "ds_read_b64_tr_b16 v[26:27], v1 offset:53760\n"
    "v_mfma_f32_16x16x32_bf16 v[64:67], v[188:191], a[184:187], v[64:67]\n"
    "ds_read_b64_tr_b16 v[28:29], v2 offset:53248\n"
    "v_mfma_f32_16x16x32_bf16 v[68:71], v[188:191], a[188:191], v[68:71]\n"
    "ds_read_b64_tr_b16 v[30:31], v2 offset:53760\n"
    "s_waitcnt vmcnt(10)\n"
    "s_waitcnt lgkmcnt(0)\n"
    "s_barrier\n"
    "v_mfma_f32_16x16x32_bf16 v[40:43], v[16:19], a[192:195], v[40:43]\n"
    "ds_read_b128 a[224:227], v196 offset:32768\n"
    "v_mfma_f32_16x16x32_bf16 v[44:47], v[16:19], a[196:199], v[44:47]\n"
    "ds_read_b128 a[228:231], v196 offset:33792\n"
    "v_mfma_f32_16x16x32_bf16 v[48:51], v[16:19], a[200:203], v[48:51]\n"
    "ds_read_b128 a[232:235], v196 offset:34816\n"
    "v_mfma_f32_16x16x32_bf16 v[52:55], v[16:19], a[204:207], v[52:55]\n"
    "ds_read_b128 a[236:239], v196 offset:35840\n"
    "s_lshl_b32 s40, s9, 11\n"
    "s_add_u32 m0, s40, 90112\n"
    "s_add_u32 s41, s47, 1179648\n"
    "buffer_load_dwordx4 v6, s[16:19], s41 offen offset:0 lds\n"
    "buffer_load_dwordx4 v7, s[16:19], s41 offen offset:1024 lds\n"
    "v_mfma_f32_16x16x32_bf16 v[56:59], v[20:23], a[192:195], v[56:59]\n"
    "ds_read_b64_tr_b16 v[176:177], v1 offset:57344\n"
    "v_mfma_f32_16x16x32_bf16 v[60:63], v[20:23], a[196:199], v[60:63]\n"
    "ds_read_b64_tr_b16 v[178:179], v1 offset:57856\n"
    "v_mfma_f32_16x16x32_bf16 v[64:67], v[20:23], a[200:203], v[64:67]\n"
    "ds_read_b64_tr_b16 v[180:181], v2 offset:57344\n"
    "v_mfma_f32_16x16x32_bf16 v[68:71], v[20:23], a[204:207], v[68:71]\n"
    "ds_read_b64_tr_b16 v[182:183], v2 offset:57856\n"
    "v_mfma_f32_16x16x32_bf16 v[40:43], v[24:27], a[208:211], v[40:43]\n"
    "ds_read_b128 a[240:243], v196 offset:40960\n"
    "v_mfma_f32_16x16x32_bf16 v[44:47], v[24:27], a[212:215], v[44:47]\n"
    "ds_read_b128 a[244:247], v196 offset:41984\n"
    "s_lshl_b32 s40, s9, 11\n"
    "s_add_u32 m0, s40, 98304\n"
    "s_add_u32 s41, s47, 1769472\n"
    "buffer_load_dwordx4 v6, s[16:19], s41 offen offset:0 lds\n"
    "buffer_load_dwordx4 v7, s[16:19], s41 offen offset:1024 lds\n"
    "v_mfma_f32_16x16x32_bf16 v[48:51], v[24:27], a[216:219], v[48:51]\n"
    "ds_read_b128 a[248:251], v196 offset:43008\n"
    "v_mfma_f32_16x16x32_bf16 v[52:55], v[24:27], a[220:223], v[52:55]\n"
    "ds_read_b128 a[252:255], v196 offset:44032\n"
    "v_mfma_f32_16x16x32_bf16 v[56:59], v[28:31], a[208:211], v[56:59]\n"
    "ds_read_b64_tr_b16 v[184:185], v1 offset:61440\n"
    "v_mfma_f32_16x16x32_bf16 v[60:63], v[28:31], a[212:215], v[60:63]\n"
    "ds_read_b64_tr_b16 v[186:187], v1 offset:61952\n"
    "v_mfma_f32_16x16x32_bf16 v[64:67], v[28:31], a[216:219], v[64:67]\n"
    "ds_read_b64_tr_b16 v[188:189], v2 offset:61440\n"
    "v_mfma_f32_16x16x32_bf16 v[68:71], v[28:31], a[220:223], v[68:71]\n"
    "ds_read_b64_tr_b16 v[190:191], v2 offset:61952\n"
    "s_waitcnt vmcnt(10)\n"
    "s_waitcnt lgkmcnt(0)\n"
    "s_barrier\n"
    "v_mfma_f32_16x16x32_bf16 v[40:43], v[176:179], a[224:227], v[40:43]\n"
    "ds_read_b128 v[224:227], v196 offset:49152\n"
    "v_mfma_f32_16x16x32_bf16 v[44:47], v[176:179], a[228:231], v[44:47]\n"
    "ds_read_b128 v[228:231], v196 offset:50176\n"
    "v_mfma_f32_16x16x32_bf16 v[48:51], v[176:179], a[232:235], v[48:51]\n"
    "ds_read_b128 v[232:235], v196 offset:51200\n"
    "v_mfma_f32_16x16x32_bf16 v[52:55], v[176:179], a[236:239], v[52:55]\n"
    "ds_read_b128 v[236:239], v196 offset:52224\n"
    "s_lshl_b32 s40, s9, 11\n"
    "s_add_u32 m0, s40, 106496\n"
    "s_add_u32 s41, s47, 2359296\n"
    "buffer_load_dwordx4 v6, s[16:19], s41 offen offset:0 lds\n"
    "buffer_load_dwordx4 v7, s[16:19], s41 offen offset:1024 lds\n"
    "v_mfma_f32_16x16x32_bf16 v[56:59], v[180:183], a[224:227], v[56:59]\n"
    "ds_read_b64_tr_b16 v[16:17], v192 offset:0\n"
    "v_mfma_f32_16x16x32_bf16 v[60:63], v[180:183], a[228:231], v[60:63]\n"
    "ds_read_b64_tr_b16 v[18:19], v192 offset:512\n"
    "v_mfma_f32_16x16x32_bf16 v[64:67], v[180:183], a[232:235], v[64:67]\n"
    "ds_read_b64_tr_b16 v[20:21], v193 offset:0\n"
    "v_mfma_f32_16x16x32_bf16 v[68:71], v[180:183], a[236:239], v[68:71]\n"
    "ds_read_b64_tr_b16 v[22:23], v193 offset:512\n"
    "v_mfma_f32_16x16x32_bf16 v[40:43], v[184:187], a[240:243], v[40:43]\n"
    "ds_read_b128 v[240:243], v196 offset:57344\n"
    "v_mfma_f32_16x16x32_bf16 v[44:47], v[184:187], a[244:247], v[44:47]\n"
    "ds_read_b128 v[244:247], v196 offset:58368\n"
    "s_lshl_b32 s40, s9, 11\n"
    "s_add_u32 m0, s40, 114688\n"
    "s_add_u32 s41, s47, 2949120\n"
    "buffer_load_dwordx4 v6, s[16:19], s41 offen offset:0 lds\n"
    "buffer_load_dwordx4 v7, s[16:19], s41 offen offset:1024 lds\n"
    "v_mfma_f32_16x16x32_bf16 v[48:51], v[184:187], a[248:251], v[48:51]\n"
    "ds_read_b128 v[248:251], v196 offset:59392\n"
    "v_mfma_f32_16x16x32_bf16 v[52:55], v[184:187], a[252:255], v[52:55]\n"
    "ds_read_b128 v[252:255], v196 offset:60416\n"
    "v_mfma_f32_16x16x32_bf16 v[56:59], v[188:191], a[240:243], v[56:59]\n"
    "ds_read_b64_tr_b16 v[24:25], v192 offset:4096\n"
    "v_mfma_f32_16x16x32_bf16 v[60:63], v[188:191], a[244:247], v[60:63]\n"
    "ds_read_b64_tr_b16 v[26:27], v192 offset:4608\n"
    "v_mfma_f32_16x16x32_bf16 v[64:67], v[188:191], a[248:251], v[64:67]\n"
    "ds_read_b64_tr_b16 v[28:29], v193 offset:4096\n"
    "v_mfma_f32_16x16x32_bf16 v[68:71], v[188:191], a[252:255], v[68:71]\n"
    "ds_read_b64_tr_b16 v[30:31], v193 offset:4608\n"
    "s_waitcnt lgkmcnt(0)\n"
    "s_barrier\n"
    "v_mfma_f32_16x16x32_bf16 v[40:43], v[16:19], v[224:227], v[40:43]\n"
    "v_mfma_f32_16x16x32_bf16 v[44:47], v[16:19], v[228:231], v[44:47]\n"
    "v_mfma_f32_16x16x32_bf16 v[48:51], v[16:19], v[232:235], v[48:51]\n"
    "v_mfma_f32_16x16x32_bf16 v[52:55], v[16:19], v[236:239], v[52:55]\n"
    "s_lshl_b32 s40, s9, 11\n"
    "s_add_u32 m0, s40, 122880\n"
    "s_add_u32 s41, s47, 3538944\n"
    "buffer_load_dwordx4 v6, s[16:19], s41 offen offset:0 lds\n"
    "buffer_load_dwordx4 v7, s[16:19], s41 offen offset:1024 lds\n"
    "v_mfma_f32_16x16x32_bf16 v[56:59], v[20:23], v[224:227], v[56:59]\n"
    "v_mfma_f32_16x16x32_bf16 v[60:63], v[20:23], v[228:231], v[60:63]\n"
    "v_mfma_f32_16x16x32_bf16 v[64:67], v[20:23], v[232:235], v[64:67]\n"
    "v_mfma_f32_16x16x32_bf16 v[68:71], v[20:23], v[236:239], v[68:71]\n"
    "v_mfma_f32_16x16x32_bf16 v[40:43], v[24:27], v[240:243], v[40:43]\n"
    "v_mfma_f32_16x16x32_bf16 v[44:47], v[24:27], v[244:247], v[44:47]\n"
    "s_lshl_b32 s40, s9, 11\n"
    "s_add_u32 m0, s40, 131072\n"
    "s_add_u32 s41, s47, 4128768\n"
    "buffer_load_dwordx4 v6, s[16:19], s41 offen offset:0 lds\n"
    "buffer_load_dwordx4 v7, s[16:19], s41 offen offset:1024 lds\n"
    "v_mfma_f32_16x16x32_bf16 v[48:51], v[24:27], v[248:251], v[48:51]\n"
    "v_mfma_f32_16x16x32_bf16 v[52:55], v[24:27], v[252:255], v[52:55]\n"
    "v_mfma_f32_16x16x32_bf16 v[56:59], v[28:31], v[240:243], v[56:59]\n"
    "v_mfma_f32_16x16x32_bf16 v[60:63], v[28:31], v[244:247], v[60:63]\n"
    "v_mfma_f32_16x16x32_bf16 v[64:67], v[28:31], v[248:251], v[64:67]\n"
    "v_mfma_f32_16x16x32_bf16 v[68:71], v[28:31], v[252:255], v[68:71]\n"
    "v_mul_f32 v72, v40, v40\n"
    "v_mul_f32 v73, v41, v41\n"
    "v_mul_f32 v74, v42, v42\n"
    "v_mul_f32 v75, v43, v43\n"
    "v_fmaak_f32 v72, v72, v15, 0xc0135761\n"
    "v_fmaak_f32 v73, v73, v15, 0xc0135761\n"
    "v_fmaak_f32 v74, v74, v15, 0xc0135761\n"
    "v_fmaak_f32 v75, v75, v15, 0xc0135761\n"
    "v_mul_f32 v72, v40, v72\n"
    "v_mul_f32 v73, v41, v73\n"
    "v_mul_f32 v74, v42, v74\n"
    "v_mul_f32 v75, v43, v75\n"
    "v_exp_f32 v72, v72\n"
    "v_exp_f32 v73, v73\n"
    "v_exp_f32 v74, v74\n"
    "v_exp_f32 v75, v75\n"
    "v_add_f32 v72, 1.0, v72\n"
    "v_add_f32 v73, 1.0, v73\n"
    "v_add_f32 v74, 1.0, v74\n"
    "v_add_f32 v75, 1.0, v75\n"
    "v_rcp_f32 v72, v72\n"
    "v_rcp_f32 v73, v73\n"
    "v_rcp_f32 v74, v74\n"
    "v_rcp_f32 v75, v75\n"
    "v_mul_f32 v72, v40, v72\n"
    "v_mul_f32 v73, v41, v73\n"
    "v_mul_f32 v74, v42, v74\n"
    "v_mul_f32 v75, v43, v75\n"
    "v_cvt_pk_bf16_f32 v80, v72, v73\n"
    "v_cvt_pk_bf16_f32 v81, v74, v75\n"
    "v_mul_f32 v76, v44, v44\n"
    "v_mul_f32 v77, v45, v45\n"
    "v_mul_f32 v78, v46, v46\n"
    "v_mul_f32 v79, v47, v47\n"
    "v_fmaak_f32 v76, v76, v15, 0xc0135761\n"
    "v_fmaak_f32 v77, v77, v15, 0xc0135761\n"
    "v_fmaak_f32 v78, v78, v15, 0xc0135761\n"
    "v_fmaak_f32 v79, v79, v15, 0xc0135761\n"
    "v_mul_f32 v76, v44, v76\n"
    "v_mul_f32 v77, v45, v77\n"
    "v_mul_f32 v78, v46, v78\n"
    "v_mul_f32 v79, v47, v79\n"
    "v_exp_f32 v76, v76\n"
    "v_exp_f32 v77, v77\n"
    "v_exp_f32 v78, v78\n"
    "v_exp_f32 v79, v79\n"
    "v_add_f32 v76, 1.0, v76\n"
    "v_add_f32 v77, 1.0, v77\n"
    "v_add_f32 v78, 1.0, v78\n"
    "v_add_f32 v79, 1.0, v79\n"
    "v_rcp_f32 v76, v76\n"
    "v_rcp_f32 v77, v77\n"
    "v_rcp_f32 v78, v78\n"
    "v_rcp_f32 v79, v79\n"
    "v_mul_f32 v76, v44, v76\n"
    "v_mul_f32 v77, v45, v77\n"
    "v_mul_f32 v78, v46, v78\n"
    "v_mul_f32 v79, v47, v79\n"
    "v_cvt_pk_bf16_f32 v84, v76, v77\n"
    "v_cvt_pk_bf16_f32 v85, v78, v79\n"
    "v_mul_f32 v72, v48, v48\n"
    "v_mul_f32 v73, v49, v49\n"
    "v_mul_f32 v74, v50, v50\n"
    "v_mul_f32 v75, v51, v51\n"
    "v_fmaak_f32 v72, v72, v15, 0xc0135761\n"
    "v_fmaak_f32 v73, v73, v15, 0xc0135761\n"
    "v_fmaak_f32 v74, v74, v15, 0xc0135761\n"
    "v_fmaak_f32 v75, v75, v15, 0xc0135761\n"
    "v_mul_f32 v72, v48, v72\n"
    "v_mul_f32 v73, v49, v73\n"
    "v_mul_f32 v74, v50, v74\n"
    "v_mul_f32 v75, v51, v75\n"
    "v_exp_f32 v72, v72\n"
    "v_exp_f32 v73, v73\n"
    "v_exp_f32 v74, v74\n"
    "v_exp_f32 v75, v75\n"
    "v_add_f32 v72, 1.0, v72\n"
    "v_add_f32 v73, 1.0, v73\n"
    "v_add_f32 v74, 1.0, v74\n"
    "v_add_f32 v75, 1.0, v75\n"
    "v_rcp_f32 v72, v72\n"
    "v_rcp_f32 v73, v73\n"
    "v_rcp_f32 v74, v74\n"
    "v_rcp_f32 v75, v75\n"
    "v_mul_f32 v72, v48, v72\n"
    "v_mul_f32 v73, v49, v73\n"
    "v_mul_f32 v74, v50, v74\n"
    "v_mul_f32 v75, v51, v75\n"
    "v_cvt_pk_bf16_f32 v88, v72, v73\n"
    "v_cvt_pk_bf16_f32 v89, v74, v75\n"
    "v_mul_f32 v76, v52, v52\n"
    "v_mul_f32 v77, v53, v53\n"
    "v_mul_f32 v78, v54, v54\n"
    "v_mul_f32 v79, v55, v55\n"
    "v_fmaak_f32 v76, v76, v15, 0xc0135761\n"
    "v_fmaak_f32 v77, v77, v15, 0xc0135761\n"
    "v_fmaak_f32 v78, v78, v15, 0xc0135761\n"
    "v_fmaak_f32 v79, v79, v15, 0xc0135761\n"
    "v_mul_f32 v76, v52, v76\n"
    "v_mul_f32 v77, v53, v77\n"
    "v_mul_f32 v78, v54, v78\n"
    "v_mul_f32 v79, v55, v79\n"
    "v_exp_f32 v76, v76\n"
    "v_exp_f32 v77, v77\n"
    "v_exp_f32 v78, v78\n"
    "v_exp_f32 v79, v79\n"
    "v_add_f32 v76, 1.0, v76\n"
    "v_add_f32 v77, 1.0, v77\n"
    "v_add_f32 v78, 1.0, v78\n"
    "v_add_f32 v79, 1.0, v79\n"
    "v_rcp_f32 v76, v76\n"
    "v_rcp_f32 v77, v77\n"
    "v_rcp_f32 v78, v78\n"
    "v_rcp_f32 v79, v79\n"
    "v_mul_f32 v76, v52, v76\n"
    "v_mul_f32 v77, v53, v77\n"
    "v_mul_f32 v78, v54, v78\n"
    "v_mul_f32 v79, v55, v79\n"
    "v_cvt_pk_bf16_f32 v92, v76, v77\n"
    "v_cvt_pk_bf16_f32 v93, v78, v79\n"
    "v_add_u32 v192, 73728, v1\n"
    "v_add_u32 v193, 139264, v1\n"
    "ds_read_b128 v[32:35], v14 offset:256\n"
    "s_waitcnt vmcnt(0)\n"
    "s_barrier\n"
    "ds_read_b64_tr_b16 v[16:17], v192 offset:0\n"
    "ds_read_b64_tr_b16 v[18:19], v192 offset:512\n"
    "ds_read_b64_tr_b16 v[20:21], v192 offset:4096\n"
    "ds_read_b64_tr_b16 v[22:23], v192 offset:4608\n"
    "ds_read_b64_tr_b16 v[24:25], v192 offset:8192\n"
    "ds_read_b64_tr_b16 v[26:27], v192 offset:8704\n"
    "ds_read_b64_tr_b16 v[28:29], v192 offset:12288\n"
    "ds_read_b64_tr_b16 v[30:31], v192 offset:12800\n"
    "ds_read_b64_tr_b16 v[176:177], v192 offset:16384\n"
    "ds_read_b64_tr_b16 v[178:179], v192 offset:16896\n"
    "s_waitcnt lgkmcnt(10)\n"
    "s_waitcnt lgkmcnt(6)\n"
    "v_mfma_f32_16x16x32_bf16 v[40:43], v[16:19], a[0:3], v[32:35]\n"
    "ds_read_b64_tr_b16 v[180:181], v192 offset:20480\n"
    "v_add_u32 v194, 73728, v2\n"
    "v_mfma_f32_16x16x32_bf16 v[44:47], v[16:19], a[4:7], v[32:35]\n"
    "ds_read_b64_tr_b16 v[182:183], v192 offset:20992\n"
    "v_add_u32 v195, 139264, v2\n"
    "v_mfma_f32_16x16x32_bf16 v[48:51], v[16:19], a[8:11], v[32:35]\n"
    "v_mfma_f32_16x16x32_bf16 v[52:55], v[16:19], a[12:15], v[32:35]\n"
    "s_add_u32 s40, s14, 2\n"
    "s_cmp_ge_u32 s40, 6\n"
    "s_cselect_b32 s41, 6, 0\n"
    "s_sub_u32 s40, s40, s41\n"
    "s_lshl_b32 s40, s40, 7\n"
    "s_mul_i32 s41, s9, 18432\n"
    "s_add_u32 m0, s41, 0\n"
    "s_add_u32 s48, s49, s40\n"
    "buffer_load_dwordx4 v6, s[16:19], s48 offen offset:0 lds\n"
    "v_mfma_f32_16x16x32_bf16 v[40:43], v[20:23], a[16:19], v[40:43]\n"
    "ds_read_b64_tr_b16 v[16:17], v192 offset:24576\n"
    "v_mul_f32 v72, v56, v56\n"
    "v_mul_f32 v73, v57, v57\n"
    "v_mfma_f32_16x16x32_bf16 v[44:47], v[20:23], a[20:23], v[44:47]\n"
    "ds_read_b64_tr_b16 v[18:19], v192 offset:25088\n"
    "v_mul_f32 v74, v58, v58\n"
    "v_mul_f32 v75, v59, v59\n"
    "v_mfma_f32_16x16x32_bf16 v[48:51], v[20:23], a[24:27], v[48:51]\n"
    "v_fmaak_f32 v72, v72, v15, 0xc0135761\n"
    "v_fmaak_f32 v73, v73, v15, 0xc0135761\n"
    "v_mfma_f32_16x16x32_bf16 v[52:55], v[20:23], a[28:31], v[52:55]\n"
    "v_fmaak_f32 v74, v74, v15, 0xc0135761\n"
    "v_fmaak_f32 v75, v75, v15, 0xc0135761\n"
    "s_waitcnt lgkmcnt(6)\n"
    "v_mfma_f32_16x16x32_bf16 v[40:43], v[24:27], a[32:35], v[40:43]\n"
    "ds_read_b64_tr_b16 v[20:21], v192 offset:28672\n"
    "v_mul_f32 v72, v56, v72\n"
    "v_mul_f32 v73, v57, v73\n"
    "v_mfma_f32_16x16x32_bf16 v[44:47], v[24:27], a[36:39], v[44:47]\n"
    "buffer_load_dwordx4 v7, s[16:19], s48 offen offset:1024 lds\n"
    "ds_read_b64_tr_b16 v[22:23], v192 offset:29184\n"
    "v_mul_f32 v74, v58, v74\n"
    "v_mul_f32 v75, v59, v75\n"
    "v_mfma_f32_16x16x32_bf16 v[48:51], v[24:27], a[40:43], v[48:51]\n"
    "v_exp_f32 v72, v72\n"
    "v_exp_f32 v73, v73\n"
    "v_mfma_f32_16x16x32_bf16 v[52:55], v[24:27], a[44:47], v[52:55]\n"
    "v_exp_f32 v74, v74\n"
    "v_mfma_f32_16x16x32_bf16 v[40:43], v[28:31], a[48:51], v[40:43]\n"
    "ds_read_b64_tr_b16 v[24:25], v192 offset:32768\n"
    "v_exp_f32 v75, v75\n"
    "v_add_f32 v72, 1.0, v72\n"
    "v_mfma_f32_16x16x32_bf16 v[44:47], v[28:31], a[52:55], v[44:47]\n"
    "ds_read_b64_tr_b16 v[26:27], v192 offset:33280\n"
    "v_add_f32 v73, 1.0, v73\n"
    "v_add_f32 v74, 1.0, v74\n"
    "v_mfma_f32_16x16x32_bf16 v[48:51], v[28:31], a[56:59], v[48:51]\n"
    "v_add_f32 v75, 1.0, v75\n"
    "v_rcp_f32 v72, v72\n"
    "v_mfma_f32_16x16x32_bf16 v[52:55], v[28:31], a[60:63], v[52:55]\n"
    "s_add_u32 m0, m0, 2048\n"
    "s_add_u32 s48, s48, 147456\n"
    "buffer_load_dwordx4 v6, s[16:19], s48 offen offset:0 lds\n"
    "v_rcp_f32 v73, v73\n"
    "s_waitcnt lgkmcnt(6)\n"
    "v_mfma_f32_16x16x32_bf16 v[40:43], v[176:179], a[64:67], v[40:43]\n"
    "ds_read_b64_tr_b16 v[28:29], v192 offset:36864\n"
    "v_rcp_f32 v74, v74\n"
    "v_rcp_f32 v75, v75\n"
    "v_mfma_f32_16x16x32_bf16 v[44:47], v[176:179], a[68:71], v[44:47]\n"
    "ds_read_b64_tr_b16 v[30:31], v192 offset:37376\n"
    "v_mul_f32 v72, v56, v72\n"
    "v_mul_f32 v73, v57, v73\n"
    "v_mfma_f32_16x16x32_bf16 v[48:51], v[176:179], a[72:75], v[48:51]\n"
    "v_mul_f32 v74, v58, v74\n"
    "v_mul_f32 v75, v59, v75\n"
    "v_mfma_f32_16x16x32_bf16 v[52:55], v[176:179], a[76:79], v[52:55]\n"
    "v_cvt_pk_bf16_f32 v82, v72, v73\n"
    "v_mfma_f32_16x16x32_bf16 v[40:43], v[180:183], a[80:83], v[40:43]\n"
    "ds_read_b64_tr_b16 v[176:177], v192 offset:40960\n"
    "v_cvt_pk_bf16_f32 v83, v74, v75\n"
    "v_mul_f32 v76, v60, v60\n"
    "v_mfma_f32_16x16x32_bf16 v[44:47], v[180:183], a[84:87], v[44:47]\n"
    "buffer_load_dwordx4 v7, s[16:19], s48 offen offset:1024 lds\n"
    "ds_read_b64_tr_b16 v[178:179], v192 offset:41472\n"
    "v_mul_f32 v77, v61, v61\n"
    "v_mul_f32 v78, v62, v62\n"
    "v_mfma_f32_16x16x32_bf16 v[48:51], v[180:183], a[88:91], v[48:51]\n"
    "v_mul_f32 v79, v63, v63\n"
    "v_fmaak_f32 v76, v76, v15, 0xc0135761\n"
    "v_mfma_f32_16x16x32_bf16 v[52:55], v[180:183], a[92:95], v[52:55]\n"
    "v_fmaak_f32 v77, v77, v15, 0xc0135761\n"
    "s_waitcnt lgkmcnt(6)\n"
    "v_mfma_f32_16x16x32_bf16 v[40:43], v[16:19], a[96:99], v[40:43]\n"
    "ds_read_b64_tr_b16 v[180:181], v192 offset:45056\n"
    "v_fmaak_f32 v78, v78, v15, 0xc0135761\n"
    "v_fmaak_f32 v79, v79, v15, 0xc0135761\n"
    "v_mfma_f32_16x16x32_bf16 v[44:47], v[16:19], a[100:103], v[44:47]\n"
    "ds_read_b64_tr_b16 v[182:183], v192 offset:45568\n"
    "v_mul_f32 v76, v60, v76\n"
    "v_mul_f32 v77, v61, v77\n"
    "v_mfma_f32_16x16x32_bf16 v[48:51], v[16:19], a[104:107], v[48:51]\n"
    "v_mul_f32 v78, v62, v78\n"
    "v_mul_f32 v79, v63, v79\n"
    "v_mfma_f32_16x16x32_bf16 v[52:55], v[16:19], a[108:111], v[52:55]\n"
    "s_add_u32 m0, m0, 2048\n"
    "s_add_u32 s48, s48, 147456\n"
    "buffer_load_dwordx4 v6, s[16:19], s48 offen offset:0 lds\n"
    "v_exp_f32 v76, v76\n"
    "v_mfma_f32_16x16x32_bf16 v[40:43], v[20:23], a[112:115], v[40:43]\n"
    "ds_read_b64_tr_b16 v[16:17], v192 offset:49152\n"
    "v_exp_f32 v77, v77\n"
    "v_exp_f32 v78, v78\n"
    "v_mfma_f32_16x16x32_bf16 v[44:47], v[20:23], a[116:119], v[44:47]\n"
    "ds_read_b64_tr_b16 v[18:19], v192 offset:49664\n"
    "v_exp_f32 v79, v79\n"
    "v_add_f32 v76, 1.0, v76\n"
    "v_mfma_f32_16x16x32_bf16 v[48:51], v[20:23], a[120:123], v[48:51]\n"
    "v_add_f32 v77, 1.0, v77\n"
    "v_add_f32 v78, 1.0, v78\n"
    "v_mfma_f32_16x16x32_bf16 v[52:55], v[20:23], a[124:127], v[52:55]\n"
    "v_add_f32 v79, 1.0, v79\n"
    "s_waitcnt lgkmcnt(6)\n"
    "v_mfma_f32_16x16x32_bf16 v[40:43], v[24:27], a[128:131], v[40:43]\n"
    "ds_read_b64_tr_b16 v[20:21], v192 offset:53248\n"
    "ds_read_b128 v[36:39], v14 offset:320\n"
    "v_rcp_f32 v76, v76\n"
    "v_mfma_f32_16x16x32_bf16 v[44:47], v[24:27], a[132:135], v[44:47]\n"
    "buffer_load_dwordx4 v7, s[16:19], s48 offen offset:1024 lds\n"
    "ds_read_b64_tr_b16 v[22:23], v192 offset:53760\n"
    "v_rcp_f32 v77, v77\n"
    "v_rcp_f32 v78, v78\n"
    "v_mfma_f32_16x16x32_bf16 v[48:51], v[24:27], a[136:139], v[48:51]\n"
    "v_rcp_f32 v79, v79\n"
    "v_mul_f32 v76, v60, v76\n"
    "v_mfma_f32_16x16x32_bf16 v[52:55], v[24:27], a[140:143], v[52:55]\n"
    "v_mul_f32 v77, v61, v77\n"
    "v_mul_f32 v78, v62, v78\n"
    "v_mfma_f32_16x16x32_bf16 v[40:43], v[28:31], a[144:147], v[40:43]\n"
    "ds_read_b64_tr_b16 v[24:25], v192 offset:57344\n"
    "v_mul_f32 v79, v63, v79\n"
    "v_cvt_pk_bf16_f32 v86, v76, v77\n"
    "v_mfma_f32_16x16x32_bf16 v[44:47], v[28:31], a[148:151], v[44:47]\n"
    "ds_read_b64_tr_b16 v[26:27], v192 offset:57856\n"
    "v_cvt_pk_bf16_f32 v87, v78, v79\n"
    "v_mul_f32 v72, v64, v64\n"
    "v_mfma_f32_16x16x32_bf16 v[48:51], v[28:31], a[152:155], v[48:51]\n"
    "v_mul_f32 v73, v65, v65\n"
    "v_mul_f32 v74, v66, v66\n"
    "v_mfma_f32_16x16x32_bf16 v[52:55], v[28:31], a[156:159], v[52:55]\n"
    "s_add_u32 m0, m0, 2048\n"
    "s_add_u32 s48, s48, 147456\n"
    "buffer_load_dwordx4 v6, s[16:19], s48 offen offset:0 lds\n"
    "v_mul_f32 v75, v67, v67\n"
    "s_waitcnt lgkmcnt(7)\n"
    "v_mfma_f32_16x16x32_bf16 v[40:43], v[176:179], a[160:163], v[40:43]\n"
    "ds_read_b64_tr_b16 v[28:29], v192 offset:61440\n"
    "v_fmaak_f32 v72, v72, v15, 0xc0135761\n"
    "v_fmaak_f32 v73, v73, v15, 0xc0135761\n"
    "v_mfma_f32_16x16x32_bf16 v[44:47], v[176:179], a[164:167], v[44:47]\n"
    "ds_read_b64_tr_b16 v[30:31], v192 offset:61952\n"
    "v_fmaak_f32 v74, v74, v15, 0xc0135761\n"
    "v_fmaak_f32 v75, v75, v15, 0xc0135761\n"
    "v_mfma_f32_16x16x32_bf16 v[48:51], v[176:179], a[168:171], v[48:51]\n"
    "v_mul_f32 v72, v64, v72\n"
    "v_mul_f32 v73, v65, v73\n"
    "v_mfma_f32_16x16x32_bf16 v[52:55], v[176:179], a[172:175], v[52:55]\n"
    "v_mul_f32 v74, v66, v74\n"
    "v_mfma_f32_16x16x32_bf16 v[40:43], v[180:183], a[176:179], v[40:43]\n"
    "ds_read_b64_tr_b16 v[176:177], v193 offset:0\n"
    "v_mul_f32 v75, v67, v75\n"
    "v_exp_f32 v72, v72\n"
    "v_mfma_f32_16x16x32_bf16 v[44:47], v[180:183], a[180:183], v[44:47]\n"
    "buffer_load_dwordx4 v7, s[16:19], s48 offen offset:1024 lds\n"
    "ds_read_b64_tr_b16 v[178:179], v193 offset:512\n"
    "v_exp_f32 v73, v73\n"
    "v_exp_f32 v74, v74\n"
    "v_mfma_f32_16x16x32_bf16 v[48:51], v[180:183], a[184:187], v[48:51]\n"
    "v_exp_f32 v75, v75\n"
    "v_add_f32 v72, 1.0, v72\n"
    "v_mfma_f32_16x16x32_bf16 v[52:55], v[180:183], a[188:191], v[52:55]\n"
    "v_add_f32 v73, 1.0, v73\n"
    "s_waitcnt lgkmcnt(6)\n"
    "v_mfma_f32_16x16x32_bf16 v[40:43], v[16:19], a[192:195], v[40:43]\n"
    "ds_read_b64_tr_b16 v[180:181], v193 offset:4096\n"
    "v_add_f32 v74, 1.0, v74\n"
    "v_add_f32 v75, 1.0, v75\n"
    "v_mfma_f32_16x16x32_bf16 v[44:47], v[16:19], a[196:199], v[44:47]\n"
    "ds_read_b64_tr_b16 v[182:183], v193 offset:4608\n"
    "v_rcp_f32 v72, v72\n"
    "v_rcp_f32 v73, v73\n"
    "v_mfma_f32_16x16x32_bf16 v[48:51], v[16:19], a[200:203], v[48:51]\n"
    "v_rcp_f32 v74, v74\n"
    "v_rcp_f32 v75, v75\n"
    "v_mfma_f32_16x16x32_bf16 v[52:55], v[16:19], a[204:207], v[52:55]\n"
    "s_add_u32 m0, m0, 2048\n"
    "s_add_u32 s48, s48, 147456\n"
    "buffer_load_dwordx4 v6, s[16:19], s48 offen offset:0 lds\n"
    "v_mul_f32 v72, v64, v72\n"
    "v_mfma_f32_16x16x32_bf16 v[40:43], v[20:23], a[208:211], v[40:43]\n"
    "ds_read_b64_tr_b16 v[16:17], v194 offset:0\n"
    "v_mul_f32 v73, v65, v73\n"
    "v_mul_f32 v74, v66, v74\n"
    "v_mfma_f32_16x16x32_bf16 v[44:47], v[20:23], a[212:215], v[44:47]\n"
    "ds_read_b64_tr_b16 v[18:19], v194 offset:512\n"
    "v_mul_f32 v75, v67, v75\n"
    "v_cvt_pk_bf16_f32 v90, v72, v73\n"
    "v_mfma_f32_16x16x32_bf16 v[48:51], v[20:23], a[216:219], v[48:51]\n"
    "v_cvt_pk_bf16_f32 v91, v74, v75\n"
    "v_mul_f32 v76, v68, v68\n"
    "v_mfma_f32_16x16x32_bf16 v[52:55], v[20:23], a[220:223], v[52:55]\n"
    "v_mul_f32 v77, v69, v69\n"
    "s_waitcnt lgkmcnt(6)\n"
    "v_mfma_f32_16x16x32_bf16 v[40:43], v[24:27], a[224:227], v[40:43]\n"
    "ds_read_b64_tr_b16 v[20:21], v194 offset:4096\n"
    "v_mul_f32 v78, v70, v70\n"
    "v_mul_f32 v79, v71, v71\n"
    "v_mfma_f32_16x16x32_bf16 v[44:47], v[24:27], a[228:231], v[44:47]\n"
    "buffer_load_dwordx4 v7, s[16:19], s48 offen offset:1024 lds\n"
    "ds_read_b64_tr_b16 v[22:23], v194 offset:4608\n"
    "v_fmaak_f32 v76, v76, v15, 0xc0135761\n"
    "v_fmaak_f32 v77, v77, v15, 0xc0135761\n"
    "v_mfma_f32_16x16x32_bf16 v[48:51], v[24:27], a[232:235], v[48:51]\n"
    "v_fmaak_f32 v78, v78, v15, 0xc0135761\n"
    "v_fmaak_f32 v79, v79, v15, 0xc0135761\n"
    "v_mfma_f32_16x16x32_bf16 v[52:55], v[24:27], a[236:239], v[52:55]\n"
    "v_mul_f32 v76, v68, v76\n"
    "v_mfma_f32_16x16x32_bf16 v[40:43], v[28:31], a[240:243], v[40:43]\n"
    "ds_read_b64_tr_b16 v[24:25], v194 offset:8192\n"
    "v_mul_f32 v77, v69, v77\n"
    "v_mul_f32 v78, v70, v78\n"
    "v_mfma_f32_16x16x32_bf16 v[44:47], v[28:31], a[244:247], v[44:47]\n"
    "ds_read_b64_tr_b16 v[26:27], v194 offset:8704\n"
    "v_mul_f32 v79, v71, v79\n"
    "v_exp_f32 v76, v76\n"
    "v_mfma_f32_16x16x32_bf16 v[48:51], v[28:31], a[248:251], v[48:51]\n"
    "v_exp_f32 v77, v77\n"
    "v_exp_f32 v78, v78\n"
    "v_mfma_f32_16x16x32_bf16 v[52:55], v[28:31], a[252:255], v[52:55]\n"
    "s_add_u32 m0, m0, 2048\n"
    "s_add_u32 s48, s48, 147456\n"
    "buffer_load_dwordx4 v6, s[16:19], s48 offen offset:0 lds\n"
    "v_exp_f32 v79, v79\n"
    "s_waitcnt lgkmcnt(6)\n"
    "v_mfma_f32_16x16x32_bf16 v[40:43], v[176:179], v[224:227], v[40:43]\n"
    "ds_read_b64_tr_b16 v[28:29], v194 offset:12288\n"
    "v_add_f32 v76, 1.0, v76\n"
    "v_add_f32 v77, 1.0, v77\n"
    "v_mfma_f32_16x16x32_bf16 v[44:47], v[176:179], v[228:231], v[44:47]\n"
    "ds_read_b64_tr_b16 v[30:31], v194 offset:12800\n"
    "v_add_f32 v78, 1.0, v78\n"
    "v_add_f32 v79, 1.0, v79\n"
    "v_mfma_f32_16x16x32_bf16 v[48:51], v[176:179], v[232:235], v[48:51]\n"
    "v_rcp_f32 v76, v76\n"
    "v_rcp_f32 v77, v77\n"
    "v_mfma_f32_16x16x32_bf16 v[52:55], v[176:179], v[236:239], v[52:55]\n"
    "v_rcp_f32 v78, v78\n"
    "v_mfma_f32_16x16x32_bf16 v[40:43], v[180:183], v[240:243], v[40:43]\n"
    "ds_read_b64_tr_b16 v[176:177], v194 offset:16384\n"
    "v_rcp_f32 v79, v79\n"
    "v_mul_f32 v76, v68, v76\n"
    "v_mfma_f32_16x16x32_bf16 v[44:47], v[180:183], v[244:247], v[44:47]\n"
    "ds_read_b64_tr_b16 v[178:179], v194 offset:16896\n"
    "v_mul_f32 v77, v69, v77\n"
    "v_mul_f32 v78, v70, v78\n"
    "v_mfma_f32_16x16x32_bf16 v[48:51], v[180:183], v[248:251], v[48:51]\n"
    "buffer_load_dwordx4 v7, s[16:19], s48 offen offset:1024 lds\n"
    "v_mul_f32 v79, v71, v79\n"
    "v_cvt_pk_bf16_f32 v94, v76, v77\n"
    "v_mfma_f32_16x16x32_bf16 v[52:55], v[180:183], v[252:255], v[52:55]\n"
    "v_cvt_pk_bf16_f32 v95, v78, v79\n"
    "s_waitcnt lgkmcnt(6)\n"
    "v_mfma_f32_16x16x32_bf16 v[56:59], v[16:19], a[0:3], v[36:39]\n"
    "ds_read_b64_tr_b16 v[180:181], v194 offset:20480\n"
    "v_add_u32 v192, 0, v1\n"
    "v_mfma_f32_16x16x32_bf16 v[60:63], v[16:19], a[4:7], v[36:39]\n"
    "s_add_u32 m0, m0, 2048\n"
    "s_add_u32 s48, s48, 147456\n"
    "buffer_load_dwordx4 v6, s[16:19], s48 offen offset:0 lds\n"
    "ds_read_b64_tr_b16 v[182:183], v194 offset:20992\n"
    "v_add_u32 v193, 65536, v1\n"
    "v_mfma_f32_16x16x32_bf16 v[64:67], v[16:19], a[8:11], v[36:39]\n"
    "v_mfma_f32_16x16x32_bf16 v[68:71], v[16:19], a[12:15], v[36:39]\n"
    "v_mfma_f32_16x16x32_bf16 v[56:59], v[20:23], a[16:19], v[56:59]\n"
    "ds_read_b64_tr_b16 v[16:17], v194 offset:24576\n"
    "v_mul_f32 v72, v40, v40\n"
    "v_mul_f32 v73, v41, v41\n"
    "v_mfma_f32_16x16x32_bf16 v[60:63], v[20:23], a[20:23], v[60:63]\n"
    "ds_read_b64_tr_b16 v[18:19], v194 offset:25088\n"
    "v_mul_f32 v74, v42, v42\n"
    "v_mul_f32 v75, v43, v43\n"
    "v_mfma_f32_16x16x32_bf16 v[64:67], v[20:23], a[24:27], v[64:67]\n"
    "buffer_load_dwordx4 v7, s[16:19], s48 offen offset:1024 lds\n"
    "v_fmaak_f32 v72, v72, v15, 0xc0135761\n"
    "v_fmaak_f32 v73, v73, v15, 0xc0135761\n"
    "v_mfma_f32_16x16x32_bf16 v[68:71], v[20:23], a[28:31], v[68:71]\n"
    "v_fmaak_f32 v74, v74, v15, 0xc0135761\n"
    "v_fmaak_f32 v75, v75, v15, 0xc0135761\n"
    "s_waitcnt lgkmcnt(6)\n"
    "v_mfma_f32_16x16x32_bf16 v[56:59], v[24:27], a[32:35], v[56:59]\n"
    "ds_read_b64_tr_b16 v[20:21], v194 offset:28672\n"
    "v_mul_f32 v72, v40, v72\n"
    "v_mul_f32 v73, v41, v73\n"
    "v_mfma_f32_16x16x32_bf16 v[60:63], v[24:27], a[36:39], v[60:63]\n"
    "ds_read_b64_tr_b16 v[22:23], v194 offset:29184\n"
    "v_mul_f32 v74, v42, v74\n"
    "v_mul_f32 v75, v43, v75\n"
    "v_mfma_f32_16x16x32_bf16 v[64:67], v[24:27], a[40:43], v[64:67]\n"
    "v_exp_f32 v72, v72\n"
    "v_exp_f32 v73, v73\n"
    "v_mfma_f32_16x16x32_bf16 v[68:71], v[24:27], a[44:47], v[68:71]\n"
    "v_exp_f32 v74, v74\n"
    "v_mfma_f32_16x16x32_bf16 v[56:59], v[28:31], a[48:51], v[56:59]\n"
    "s_add_u32 m0, m0, 2048\n"
    "s_add_u32 s48, s48, 147456\n"
    "buffer_load_dwordx4 v6, s[16:19], s48 offen offset:0 lds\n"
    "ds_read_b64_tr_b16 v[24:25], v194 offset:32768\n"
    "v_exp_f32 v75, v75\n"
    "v_add_f32 v72, 1.0, v72\n"
    "v_mfma_f32_16x16x32_bf16 v[60:63], v[28:31], a[52:55], v[60:63]\n"
    "ds_read_b64_tr_b16 v[26:27], v194 offset:33280\n"
    "v_add_f32 v73, 1.0, v73\n"
    "v_add_f32 v74, 1.0, v74\n"
    "v_mfma_f32_16x16x32_bf16 v[64:67], v[28:31], a[56:59], v[64:67]\n"
    "v_add_f32 v75, 1.0, v75\n"
    "v_rcp_f32 v72, v72\n"
    "v_mfma_f32_16x16x32_bf16 v[68:71], v[28:31], a[60:63], v[68:71]\n"
    "v_rcp_f32 v73, v73\n"
    "s_waitcnt lgkmcnt(6)\n"
    "v_mfma_f32_16x16x32_bf16 v[56:59], v[176:179], a[64:67], v[56:59]\n"
    "ds_read_b64_tr_b16 v[28:29], v194 offset:36864\n"
    "v_rcp_f32 v74, v74\n"
    "v_rcp_f32 v75, v75\n"
    "v_mfma_f32_16x16x32_bf16 v[60:63], v[176:179], a[68:71], v[60:63]\n"
    "ds_read_b64_tr_b16 v[30:31], v194 offset:37376\n"
    "v_mul_f32 v72, v40, v72\n"
    "v_mul_f32 v73, v41, v73\n"
    "v_mfma_f32_16x16x32_bf16 v[64:67], v[176:179], a[72:75], v[64:67]\n"
    "buffer_load_dwordx4 v7, s[16:19], s48 offen offset:1024 lds\n"
    "v_mul_f32 v74, v42, v74\n"
    "v_mul_f32 v75, v43, v75\n"
    "v_mfma_f32_16x16x32_bf16 v[68:71], v[176:179], a[76:79], v[68:71]\n"
    "v_cvt_pk_bf16_f32 v96, v72, v73\n"
    "v_mfma_f32_16x16x32_bf16 v[56:59], v[180:183], a[80:83], v[56:59]\n"
    "ds_read_b64_tr_b16 v[176:177], v194 offset:40960\n"
    "v_cvt_pk_bf16_f32 v97, v74, v75\n"
    "v_mul_f32 v76, v44, v44\n"
    "v_mfma_f32_16x16x32_bf16 v[60:63], v[180:183], a[84:87], v[60:63]\n"
    "ds_read_b64_tr_b16 v[178:179], v194 offset:41472\n"
    "v_mul_f32 v77, v45, v45\n"
    "v_mul_f32 v78, v46, v46\n"
    "v_mfma_f32_16x16x32_bf16 v[64:67], v[180:183], a[88:91], v[64:67]\n"
    "v_mul_f32 v79, v47, v47\n"
    "v_fmaak_f32 v76, v76, v15, 0xc0135761\n"
    "v_mfma_f32_16x16x32_bf16 v[68:71], v[180:183], a[92:95], v[68:71]\n"
    "v_fmaak_f32 v77, v77, v15, 0xc0135761\n"
    "s_waitcnt lgkmcnt(6)\n"
    "v_mfma_f32_16x16x32_bf16 v[56:59], v[16:19], a[96:99], v[56:59]\n"
    "s_add_u32 m0, m0, 2048\n"
    "s_add_u32 s48, s48, 147456\n"
    "buffer_load_dwordx4 v6, s[16:19], s48 offen offset:0 lds\n"
    "ds_read_b64_tr_b16 v[180:181], v194 offset:45056\n"
    "v_fmaak_f32 v78, v78, v15, 0xc0135761\n"
    "v_fmaak_f32 v79, v79, v15, 0xc0135761\n"
    "v_mfma_f32_16x16x32_bf16 v[60:63], v[16:19], a[100:103], v[60:63]\n"
    "ds_read_b64_tr_b16 v[182:183], v194 offset:45568\n"
    "v_mul_f32 v76, v44, v76\n"
    "v_mul_f32 v77, v45, v77\n"
    "v_mfma_f32_16x16x32_bf16 v[64:67], v[16:19], a[104:107], v[64:67]\n"
    "v_mul_f32 v78, v46, v78\n"
    "v_mul_f32 v79, v47, v79\n"
    "v_mfma_f32_16x16x32_bf16 v[68:71], v[16:19], a[108:111], v[68:71]\n"
    "v_exp_f32 v76, v76\n"
    "v_mfma_f32_16x16x32_bf16 v[56:59], v[20:23], a[112:115], v[56:59]\n"
    "ds_read_b64_tr_b16 v[16:17], v194 offset:49152\n"
    "v_exp_f32 v77, v77\n"
    "v_exp_f32 v78, v78\n"
    "v_mfma_f32_16x16x32_bf16 v[60:63], v[20:23], a[116:119], v[60:63]\n"
    "ds_read_b64_tr_b16 v[18:19], v194 offset:49664\n"
    "v_exp_f32 v79, v79\n"
    "v_add_f32 v76, 1.0, v76\n"
    "v_mfma_f32_16x16x32_bf16 v[64:67], v[20:23], a[120:123], v[64:67]\n"
    "buffer_load_dwordx4 v7, s[16:19], s48 offen offset:1024 lds\n"
    "v_add_f32 v77, 1.0, v77\n"
    "v_add_f32 v78, 1.0, v78\n"
    "v_mfma_f32_16x16x32_bf16 v[68:71], v[20:23], a[124:127], v[68:71]\n"
    "v_add_f32 v79, 1.0, v79\n"
    "s_waitcnt lgkmcnt(6)\n"
    "v_mfma_f32_16x16x32_bf16 v[56:59], v[24:27], a[128:131], v[56:59]\n"
    "ds_read_b64_tr_b16 v[20:21], v194 offset:53248\n"
    "ds_read_b128 v[32:35], v14 offset:512\n"
    "v_rcp_f32 v76, v76\n"
    "v_mfma_f32_16x16x32_bf16 v[60:63], v[24:27], a[132:135], v[60:63]\n"
    "ds_read_b64_tr_b16 v[22:23], v194 offset:53760\n"
    "v_rcp_f32 v77, v77\n"
    "v_rcp_f32 v78, v78\n"
    "v_mfma_f32_16x16x32_bf16 v[64:67], v[24:27], a[136:139], v[64:67]\n"
    "v_rcp_f32 v79, v79\n"
    "v_mul_f32 v76, v44, v76\n"
    "v_mfma_f32_16x16x32_bf16 v[68:71], v[24:27], a[140:143], v[68:71]\n"
    "v_mul_f32 v77, v45, v77\n"
    "v_mul_f32 v78, v46, v78\n"
    "v_mfma_f32_16x16x32_bf16 v[56:59], v[28:31], a[144:147], v[56:59]\n"
    "ds_read_b64_tr_b16 v[24:25], v194 offset:57344\n"
    "v_mul_f32 v79, v47, v79\n"
    "v_cvt_pk_bf16_f32 v100, v76, v77\n"
    "v_mfma_f32_16x16x32_bf16 v[60:63], v[28:31], a[148:151], v[60:63]\n"
    "ds_read_b64_tr_b16 v[26:27], v194 offset:57856\n"
    "v_cvt_pk_bf16_f32 v101, v78, v79\n"
    "v_mul_f32 v72, v48, v48\n"
    "v_mfma_f32_16x16x32_bf16 v[64:67], v[28:31], a[152:155], v[64:67]\n"
    "v_mul_f32 v73, v49, v49\n"
    "v_mul_f32 v74, v50, v50\n"
    "v_mfma_f32_16x16x32_bf16 v[68:71], v[28:31], a[156:159], v[68:71]\n"
    "v_mul_f32 v75, v51, v51\n"
    "s_waitcnt lgkmcnt(7)\n"
    "v_mfma_f32_16x16x32_bf16 v[56:59], v[176:179], a[160:163], v[56:59]\n"
    "ds_read_b64_tr_b16 v[28:29], v194 offset:61440\n"
    "v_fmaak_f32 v72, v72, v15, 0xc0135761\n"
    "v_fmaak_f32 v73, v73, v15, 0xc0135761\n"
    "v_mfma_f32_16x16x32_bf16 v[60:63], v[176:179], a[164:167], v[60:63]\n"
    "ds_read_b64_tr_b16 v[30:31], v194 offset:61952\n"
    "v_fmaak_f32 v74, v74, v15, 0xc0135761\n"
    "v_fmaak_f32 v75, v75, v15, 0xc0135761\n"
    "v_mfma_f32_16x16x32_bf16 v[64:67], v[176:179], a[168:171], v[64:67]\n"
    "v_mul_f32 v72, v48, v72\n"
    "v_mul_f32 v73, v49, v73\n"
    "v_mfma_f32_16x16x32_bf16 v[68:71], v[176:179], a[172:175], v[68:71]\n"
    "v_mul_f32 v74, v50, v74\n"
    "v_mfma_f32_16x16x32_bf16 v[56:59], v[180:183], a[176:179], v[56:59]\n"
    "ds_read_b64_tr_b16 v[176:177], v195 offset:0\n"
    "v_mul_f32 v75, v51, v75\n"
    "v_exp_f32 v72, v72\n"
    "v_mfma_f32_16x16x32_bf16 v[60:63], v[180:183], a[180:183], v[60:63]\n"
    "ds_read_b64_tr_b16 v[178:179], v195 offset:512\n"
    "v_exp_f32 v73, v73\n"
    "v_exp_f32 v74, v74\n"
    "v_mfma_f32_16x16x32_bf16 v[64:67], v[180:183], a[184:187], v[64:67]\n"
    "v_exp_f32 v75, v75\n"
    "v_add_f32 v72, 1.0, v72\n"
    "v_mfma_f32_16x16x32_bf16 v[68:71], v[180:183], a[188:191], v[68:71]\n"
    "v_add_f32 v73, 1.0, v73\n"
    "s_waitcnt lgkmcnt(6)\n"
    "v_mfma_f32_16x16x32_bf16 v[56:59], v[16:19], a[192:195], v[56:59]\n"
    "ds_read_b64_tr_b16 v[180:181], v195 offset:4096\n"
    "v_add_f32 v74, 1.0, v74\n"
    "v_add_f32 v75, 1.0, v75\n"
    "v_mfma_f32_16x16x32_bf16 v[60:63], v[16:19], a[196:199], v[60:63]\n"
    "ds_read_b64_tr_b16 v[182:183], v195 offset:4608\n"
    "v_rcp_f32 v72, v72\n"
    "v_rcp_f32 v73, v73\n"
    "v_mfma_f32_16x16x32_bf16 v[64:67], v[16:19], a[200:203], v[64:67]\n"
    "v_rcp_f32 v74, v74\n"
    "v_rcp_f32 v75, v75\n"
    "v_mfma_f32_16x16x32_bf16 v[68:71], v[16:19], a[204:207], v[68:71]\n"
    "v_mul_f32 v72, v48, v72\n"
    "v_mfma_f32_16x16x32_bf16 v[56:59], v[20:23], a[208:211], v[56:59]\n"
    "v_mul_f32 v73, v49, v73\n"
    "v_mul_f32 v74, v50, v74\n"
    "v_mfma_f32_16x16x32_bf16 v[60:63], v[20:23], a[212:215], v[60:63]\n"
    "v_mul_f32 v75, v51, v75\n"
    "v_cvt_pk_bf16_f32 v104, v72, v73\n"
    "v_mfma_f32_16x16x32_bf16 v[64:67], v[20:23], a[216:219], v[64:67]\n"
    "v_cvt_pk_bf16_f32 v105, v74, v75\n"
    "v_mul_f32 v76, v52, v52\n"
    "v_mfma_f32_16x16x32_bf16 v[68:71], v[20:23], a[220:223], v[68:71]\n"
    "v_mul_f32 v77, v53, v53\n"
    "s_waitcnt lgkmcnt(4)\n"
    "v_mfma_f32_16x16x32_bf16 v[56:59], v[24:27], a[224:227], v[56:59]\n"
    "v_mul_f32 v78, v54, v54\n"
    "v_mul_f32 v79, v55, v55\n"
    "v_mfma_f32_16x16x32_bf16 v[60:63], v[24:27], a[228:231], v[60:63]\n"
    "v_fmaak_f32 v76, v76, v15, 0xc0135761\n"
    "v_fmaak_f32 v77, v77, v15, 0xc0135761\n"
    "v_mfma_f32_16x16x32_bf16 v[64:67], v[24:27], a[232:235], v[64:67]\n"
    "v_fmaak_f32 v78, v78, v15, 0xc0135761\n"
    "v_fmaak_f32 v79, v79, v15, 0xc0135761\n"
    "v_mfma_f32_16x16x32_bf16 v[68:71], v[24:27], a[236:239], v[68:71]\n"
    "v_mul_f32 v76, v52, v76\n"
    "s_waitcnt lgkmcnt(0)\n"
    "s_waitcnt vmcnt(0)\n"
    "s_barrier\n"
    "v_mfma_f32_16x16x32_bf16 v[56:59], v[28:31], a[240:243], v[56:59]\n"
    "ds_read_b64_tr_b16 v[16:17], v192 offset:0\n"
    "v_mul_f32 v77, v53, v77\n"
    "v_mul_f32 v78, v54, v78\n"
    "v_mfma_f32_16x16x32_bf16 v[60:63], v[28:31], a[244:247], v[60:63]\n"
    "ds_read_b64_tr_b16 v[18:19], v192 offset:512\n"
    "v_mul_f32 v79, v55, v79\n"
    "v_exp_f32 v76, v76\n"
    "v_mfma_f32_16x16x32_bf16 v[64:67], v[28:31], a[248:251], v[64:67]\n"
    "ds_read_b64_tr_b16 v[20:21], v192 offset:4096\n"
    "v_exp_f32 v77, v77\n"
    "v_exp_f32 v78, v78\n"
    "v_mfma_f32_16x16x32_bf16 v[68:71], v[28:31], a[252:255], v[68:71]\n"
    "ds_read_b64_tr_b16 v[22:23], v192 offset:4608\n"
    "v_exp_f32 v79, v79\n"
    "v_mfma_f32_16x16x32_bf16 v[56:59], v[176:179], v[224:227], v[56:59]\n"
    "ds_read_b64_tr_b16 v[24:25], v192 offset:8192\n"
    "v_add_f32 v76, 1.0, v76\n"
    "v_add_f32 v77, 1.0, v77\n"
    "v_mfma_f32_16x16x32_bf16 v[60:63], v[176:179], v[228:231], v[60:63]\n"
    "ds_read_b64_tr_b16 v[26:27], v192 offset:8704\n"
    "v_add_f32 v78, 1.0, v78\n"
    "v_add_f32 v79, 1.0, v79\n"
    "v_mfma_f32_16x16x32_bf16 v[64:67], v[176:179], v[232:235], v[64:67]\n"
    "ds_read_b64_tr_b16 v[28:29], v192 offset:12288\n"
    "v_rcp_f32 v76, v76\n"
    "v_rcp_f32 v77, v77\n"
    "v_mfma_f32_16x16x32_bf16 v[68:71], v[176:179], v[236:239], v[68:71]\n"
    "ds_read_b64_tr_b16 v[30:31], v192 offset:12800\n"
    "v_rcp_f32 v78, v78\n"
    "v_mfma_f32_16x16x32_bf16 v[56:59], v[180:183], v[240:243], v[56:59]\n"
    "ds_read_b64_tr_b16 v[176:177], v192 offset:16384\n"
    "v_rcp_f32 v79, v79\n"
    "v_mul_f32 v76, v52, v76\n"
    "v_mfma_f32_16x16x32_bf16 v[60:63], v[180:183], v[244:247], v[60:63]\n"
    "ds_read_b64_tr_b16 v[178:179], v192 offset:16896\n"
    "v_mul_f32 v77, v53, v77\n"
    "v_mul_f32 v78, v54, v78\n"
    "v_mfma_f32_16x16x32_bf16 v[64:67], v[180:183], v[248:251], v[64:67]\n"
    "v_mul_f32 v79, v55, v79\n"
    "v_cvt_pk_bf16_f32 v108, v76, v77\n"
    "v_mfma_f32_16x16x32_bf16 v[68:71], v[180:183], v[252:255], v[68:71]\n"
    "v_cvt_pk_bf16_f32 v109, v78, v79\n"
    "s_waitcnt lgkmcnt(6)\n"
    "v_mfma_f32_16x16x32_bf16 v[40:43], v[16:19], a[0:3], v[32:35]\n"
    "ds_read_b64_tr_b16 v[180:181], v192 offset:20480\n"
    "v_add_u32 v194, 0, v2\n"
    "v_mfma_f32_16x16x32_bf16 v[44:47], v[16:19], a[4:7], v[32:35]\n"
    "ds_read_b64_tr_b16 v[182:183], v192 offset:20992\n"
    "v_add_u32 v195, 65536, v2\n"
    "v_mfma_f32_16x16x32_bf16 v[48:51], v[16:19], a[8:11], v[32:35]\n"
    "v_mfma_f32_16x16x32_bf16 v[52:55], v[16:19], a[12:15], v[32:35]\n"
    "s_add_u32 s40, s14, 3\n"
    "s_cmp_ge_u32 s40, 6\n"
    "s_cselect_b32 s41, 6, 0\n"
    "s_sub_u32 s40, s40, s41\n"
    "s_lshl_b32 s40, s40, 7\n"
    "s_mul_i32 s41, s9, 18432\n"
    "s_add_u32 m0, s41, 73728\n"
    "s_add_u32 s48, s49, s40\n"
    "buffer_load_dwordx4 v6, s[16:19], s48 offen offset:0 lds\n"
    "v_mfma_f32_16x16x32_bf16 v[40:43], v[20:23], a[16:19], v[40:43]\n"
    "ds_read_b64_tr_b16 v[16:17], v192 offset:24576\n"
    "v_mul_f32 v72, v56, v56\n"
    "v_mul_f32 v73, v57, v57\n"
    "v_mfma_f32_16x16x32_bf16 v[44:47], v[20:23], a[20:23], v[44:47]\n"
    "ds_read_b64_tr_b16 v[18:19], v192 offset:25088\n"
    "v_mul_f32 v74, v58, v58\n"
    "v_mul_f32 v75, v59, v59\n"
    "v_mfma_f32_16x16x32_bf16 v[48:51], v[20:23], a[24:27], v[48:51]\n"
    "v_fmaak_f32 v72, v72, v15, 0xc0135761\n"
    "v_fmaak_f32 v73, v73, v15, 0xc0135761\n"
    "v_mfma_f32_16x16x32_bf16 v[52:55], v[20:23], a[28:31], v[52:55]\n"
    "v_fmaak_f32 v74, v74, v15, 0xc0135761\n"
    "v_fmaak_f32 v75, v75, v15, 0xc0135761\n"
    "s_waitcnt lgkmcnt(6)\n"
    "v_mfma_f32_16x16x32_bf16 v[40:43], v[24:27], a[32:35], v[40:43]\n"
    "ds_read_b64_tr_b16 v[20:21], v192 offset:28672\n"
    "v_mul_f32 v72, v56, v72\n"
    "v_mul_f32 v73, v57, v73\n"
    "v_mfma_f32_16x16x32_bf16 v[44:47], v[24:27], a[36:39], v[44:47]\n"
    "buffer_load_dwordx4 v7, s[16:19], s48 offen offset:1024 lds\n"
    "ds_read_b64_tr_b16 v[22:23], v192 offset:29184\n"
    "v_mul_f32 v74, v58, v74\n"
    "v_mul_f32 v75, v59, v75\n"
    "v_mfma_f32_16x16x32_bf16 v[48:51], v[24:27], a[40:43], v[48:51]\n"
    "v_exp_f32 v72, v72\n"
    "v_exp_f32 v73, v73\n"
    "v_mfma_f32_16x16x32_bf16 v[52:55], v[24:27], a[44:47], v[52:55]\n"
    "v_exp_f32 v74, v74\n"
    "v_mfma_f32_16x16x32_bf16 v[40:43], v[28:31], a[48:51], v[40:43]\n"
    "ds_read_b64_tr_b16 v[24:25], v192 offset:32768\n"
    "v_exp_f32 v75, v75\n"
    "v_add_f32 v72, 1.0, v72\n"
    "v_mfma_f32_16x16x32_bf16 v[44:47], v[28:31], a[52:55], v[44:47]\n"
    "ds_read_b64_tr_b16 v[26:27], v192 offset:33280\n"
    "v_add_f32 v73, 1.0, v73\n"
    "v_add_f32 v74, 1.0, v74\n"
    "v_mfma_f32_16x16x32_bf16 v[48:51], v[28:31], a[56:59], v[48:51]\n"
    "v_add_f32 v75, 1.0, v75\n"
    "v_rcp_f32 v72, v72\n"
    "v_mfma_f32_16x16x32_bf16 v[52:55], v[28:31], a[60:63], v[52:55]\n"
    "s_add_u32 m0, m0, 2048\n"
    "s_add_u32 s48, s48, 147456\n"
    "buffer_load_dwordx4 v6, s[16:19], s48 offen offset:0 lds\n"
    "v_rcp_f32 v73, v73\n"
    "s_waitcnt lgkmcnt(6)\n"
    "v_mfma_f32_16x16x32_bf16 v[40:43], v[176:179], a[64:67], v[40:43]\n"
    "ds_read_b64_tr_b16 v[28:29], v192 offset:36864\n"
    "v_rcp_f32 v74, v74\n"
    "v_rcp_f32 v75, v75\n"
    "v_mfma_f32_16x16x32_bf16 v[44:47], v[176:179], a[68:71], v[44:47]\n"
    "ds_read_b64_tr_b16 v[30:31], v192 offset:37376\n"
    "v_mul_f32 v72, v56, v72\n"
    "v_mul_f32 v73, v57, v73\n"
    "v_mfma_f32_16x16x32_bf16 v[48:51], v[176:179], a[72:75], v[48:51]\n"
    "v_mul_f32 v74, v58, v74\n"
    "v_mul_f32 v75, v59, v75\n"
    "v_mfma_f32_16x16x32_bf16 v[52:55], v[176:179], a[76:79], v[52:55]\n"
    "v_cvt_pk_bf16_f32 v98, v72, v73\n"
    "v_mfma_f32_16x16x32_bf16 v[40:43], v[180:183], a[80:83], v[40:43]\n"
    "ds_read_b64_tr_b16 v[176:177], v192 offset:40960\n"
    "v_cvt_pk_bf16_f32 v99, v74, v75\n"
    "v_mul_f32 v76, v60, v60\n"
    "v_mfma_f32_16x16x32_bf16 v[44:47], v[180:183], a[84:87], v[44:47]\n"
    "buffer_load_dwordx4 v7, s[16:19], s48 offen offset:1024 lds\n"
    "ds_read_b64_tr_b16 v[178:179], v192 offset:41472\n"
    "v_mul_f32 v77, v61, v61\n"
    "v_mul_f32 v78, v62, v62\n"
    "v_mfma_f32_16x16x32_bf16 v[48:51], v[180:183], a[88:91], v[48:51]\n"
    "v_mul_f32 v79, v63, v63\n"
    "v_fmaak_f32 v76, v76, v15, 0xc0135761\n"
    "v_mfma_f32_16x16x32_bf16 v[52:55], v[180:183], a[92:95], v[52:55]\n"
    "v_fmaak_f32 v77, v77, v15, 0xc0135761\n"
    "s_waitcnt lgkmcnt(6)\n"
    "v_mfma_f32_16x16x32_bf16 v[40:43], v[16:19], a[96:99], v[40:43]\n"
    "ds_read_b64_tr_b16 v[180:181], v192 offset:45056\n"
    "v_fmaak_f32 v78, v78, v15, 0xc0135761\n"
    "v_fmaak_f32 v79, v79, v15, 0xc0135761\n"
    "v_mfma_f32_16x16x32_bf16 v[44:47], v[16:19], a[100:103], v[44:47]\n"
    "ds_read_b64_tr_b16 v[182:183], v192 offset:45568\n"
    "v_mul_f32 v76, v60, v76\n"
    "v_mul_f32 v77, v61, v77\n"
    "v_mfma_f32_16x16x32_bf16 v[48:51], v[16:19], a[104:107], v[48:51]\n"
    "v_mul_f32 v78, v62, v78\n"
    "v_mul_f32 v79, v63, v79\n"
    "v_mfma_f32_16x16x32_bf16 v[52:55], v[16:19], a[108:111], v[52:55]\n"
    "s_add_u32 m0, m0, 2048\n"
    "s_add_u32 s48, s48, 147456\n"
    "buffer_load_dwordx4 v6, s[16:19], s48 offen offset:0 lds\n"
    "v_exp_f32 v76, v76\n"
    "v_mfma_f32_16x16x32_bf16 v[40:43], v[20:23], a[112:115], v[40:43]\n"
    "ds_read_b64_tr_b16 v[16:17], v192 offset:49152\n"
    "v_exp_f32 v77, v77\n"
    "v_exp_f32 v78, v78\n"
    "v_mfma_f32_16x16x32_bf16 v[44:47], v[20:23], a[116:119], v[44:47]\n"
    "ds_read_b64_tr_b16 v[18:19], v192 offset:49664\n"
    "v_exp_f32 v79, v79\n"
    "v_add_f32 v76, 1.0, v76\n"
    "v_mfma_f32_16x16x32_bf16 v[48:51], v[20:23], a[120:123], v[48:51]\n"
    "v_add_f32 v77, 1.0, v77\n"
    "v_add_f32 v78, 1.0, v78\n"
    "v_mfma_f32_16x16x32_bf16 v[52:55], v[20:23], a[124:127], v[52:55]\n"
    "v_add_f32 v79, 1.0, v79\n"
    "s_waitcnt lgkmcnt(6)\n"
    "v_mfma_f32_16x16x32_bf16 v[40:43], v[24:27], a[128:131], v[40:43]\n"
    "ds_read_b64_tr_b16 v[20:21], v192 offset:53248\n"
    "ds_read_b128 v[36:39], v14 offset:576\n"
    "v_rcp_f32 v76, v76\n"
    "v_mfma_f32_16x16x32_bf16 v[44:47], v[24:27], a[132:135], v[44:47]\n"
    "buffer_load_dwordx4 v7, s[16:19], s48 offen offset:1024 lds\n"
    "ds_read_b64_tr_b16 v[22:23], v192 offset:53760\n"
    "v_rcp_f32 v77, v77\n"
    "v_rcp_f32 v78, v78\n"
    "v_mfma_f32_16x16x32_bf16 v[48:51], v[24:27], a[136:139], v[48:51]\n"
    "v_rcp_f32 v79, v79\n"
    "v_mul_f32 v76, v60, v76\n"
    "v_mfma_f32_16x16x32_bf16 v[52:55], v[24:27], a[140:143], v[52:55]\n"
    "v_mul_f32 v77, v61, v77\n"
    "v_mul_f32 v78, v62, v78\n"
    "v_mfma_f32_16x16x32_bf16 v[40:43], v[28:31], a[144:147], v[40:43]\n"
    "ds_read_b64_tr_b16 v[24:25], v192 offset:57344\n"
    "v_mul_f32 v79, v63, v79\n"
    "v_cvt_pk_bf16_f32 v102, v76, v77\n"
    "v_mfma_f32_16x16x32_bf16 v[44:47], v[28:31], a[148:151], v[44:47]\n"
    "ds_read_b64_tr_b16 v[26:27], v192 offset:57856\n"
    "v_cvt_pk_bf16_f32 v103, v78, v79\n"
    "v_mul_f32 v72, v64, v64\n"
    "v_mfma_f32_16x16x32_bf16 v[48:51], v[28:31], a[152:155], v[48:51]\n"
    "v_mul_f32 v73, v65, v65\n"
    "v_mul_f32 v74, v66, v66\n"
    "v_mfma_f32_16x16x32_bf16 v[52:55], v[28:31], a[156:159], v[52:55]\n"
    "s_add_u32 m0, m0, 2048\n"
    "s_add_u32 s48, s48, 147456\n"
    "buffer_load_dwordx4 v6, s[16:19], s48 offen offset:0 lds\n"
    "v_mul_f32 v75, v67, v67\n"
    "s_waitcnt lgkmcnt(7)\n"
    "v_mfma_f32_16x16x32_bf16 v[40:43], v[176:179], a[160:163], v[40:43]\n"
    "ds_read_b64_tr_b16 v[28:29], v192 offset:61440\n"
    "v_fmaak_f32 v72, v72, v15, 0xc0135761\n"
    "v_fmaak_f32 v73, v73, v15, 0xc0135761\n"
    "v_mfma_f32_16x16x32_bf16 v[44:47], v[176:179], a[164:167], v[44:47]\n"
    "ds_read_b64_tr_b16 v[30:31], v192 offset:61952\n"
    "v_fmaak_f32 v74, v74, v15, 0xc0135761\n"
    "v_fmaak_f32 v75, v75, v15, 0xc0135761\n"
    "v_mfma_f32_16x16x32_bf16 v[48:51], v[176:179], a[168:171], v[48:51]\n"
    "v_mul_f32 v72, v64, v72\n"
    "v_mul_f32 v73, v65, v73\n"
    "v_mfma_f32_16x16x32_bf16 v[52:55], v[176:179], a[172:175], v[52:55]\n"
    "v_mul_f32 v74, v66, v74\n"
    "v_mfma_f32_16x16x32_bf16 v[40:43], v[180:183], a[176:179], v[40:43]\n"
    "ds_read_b64_tr_b16 v[176:177], v193 offset:0\n"
    "v_mul_f32 v75, v67, v75\n"
    "v_exp_f32 v72, v72\n"
    "v_mfma_f32_16x16x32_bf16 v[44:47], v[180:183], a[180:183], v[44:47]\n"
    "buffer_load_dwordx4 v7, s[16:19], s48 offen offset:1024 lds\n"
    "ds_read_b64_tr_b16 v[178:179], v193 offset:512\n"
    "v_exp_f32 v73, v73\n"
    "v_exp_f32 v74, v74\n"
    "v_mfma_f32_16x16x32_bf16 v[48:51], v[180:183], a[184:187], v[48:51]\n"
    "v_exp_f32 v75, v75\n"
    "v_add_f32 v72, 1.0, v72\n"
    "v_mfma_f32_16x16x32_bf16 v[52:55], v[180:183], a[188:191], v[52:55]\n"
    "v_add_f32 v73, 1.0, v73\n"
    "s_waitcnt lgkmcnt(6)\n"
    "v_mfma_f32_16x16x32_bf16 v[40:43], v[16:19], a[192:195], v[40:43]\n"
    "ds_read_b64_tr_b16 v[180:181], v193 offset:4096\n"
    "v_add_f32 v74, 1.0, v74\n"
    "v_add_f32 v75, 1.0, v75\n"
    "v_mfma_f32_16x16x32_bf16 v[44:47], v[16:19], a[196:199], v[44:47]\n"
    "ds_read_b64_tr_b16 v[182:183], v193 offset:4608\n"
    "v_rcp_f32 v72, v72\n"
    "v_rcp_f32 v73, v73\n"
    "v_mfma_f32_16x16x32_bf16 v[48:51], v[16:19], a[200:203], v[48:51]\n"
    "v_rcp_f32 v74, v74\n"
    "v_rcp_f32 v75, v75\n"
    "v_mfma_f32_16x16x32_bf16 v[52:55], v[16:19], a[204:207], v[52:55]\n"
    "s_add_u32 m0, m0, 2048\n"
    "s_add_u32 s48, s48, 147456\n"
    "buffer_load_dwordx4 v6, s[16:19], s48 offen offset:0 lds\n"
    "v_mul_f32 v72, v64, v72\n"
    "v_mfma_f32_16x16x32_bf16 v[40:43], v[20:23], a[208:211], v[40:43]\n"
    "ds_read_b64_tr_b16 v[16:17], v194 offset:0\n"
    "v_mul_f32 v73, v65, v73\n"
    "v_mul_f32 v74, v66, v74\n"
    "v_mfma_f32_16x16x32_bf16 v[44:47], v[20:23], a[212:215], v[44:47]\n"
    "ds_read_b64_tr_b16 v[18:19], v194 offset:512\n"
    "v_mul_f32 v75, v67, v75\n"
    "v_cvt_pk_bf16_f32 v106, v72, v73\n"
    "v_mfma_f32_16x16x32_bf16 v[48:51], v[20:23], a[216:219], v[48:51]\n"
    "v_cvt_pk_bf16_f32 v107, v74, v75\n"
    "v_mul_f32 v76, v68, v68\n"
    "v_mfma_f32_16x16x32_bf16 v[52:55], v[20:23], a[220:223], v[52:55]\n"
    "v_mul_f32 v77, v69, v69\n"
    "s_waitcnt lgkmcnt(6)\n"
    "v_mfma_f32_16x16x32_bf16 v[40:43], v[24:27], a[224:227], v[40:43]\n"
    "ds_read_b64_tr_b16 v[20:21], v194 offset:4096\n"
    "v_mul_f32 v78, v70, v70\n"
    "v_mul_f32 v79, v71, v71\n"
    "v_mfma_f32_16x16x32_bf16 v[44:47], v[24:27], a[228:231], v[44:47]\n"
    "buffer_load_dwordx4 v7, s[16:19], s48 offen offset:1024 lds\n"
    "ds_read_b64_tr_b16 v[22:23], v194 offset:4608\n"
    "v_fmaak_f32 v76, v76, v15, 0xc0135761\n"
    "v_fmaak_f32 v77, v77, v15, 0xc0135761\n"
    "v_mfma_f32_16x16x32_bf16 v[48:51], v[24:27], a[232:235], v[48:51]\n"
    "v_fmaak_f32 v78, v78, v15, 0xc0135761\n"
    "v_fmaak_f32 v79, v79, v15, 0xc0135761\n"
    "v_mfma_f32_16x16x32_bf16 v[52:55], v[24:27], a[236:239], v[52:55]\n"
    "v_mul_f32 v76, v68, v76\n"
    "v_mfma_f32_16x16x32_bf16 v[40:43], v[28:31], a[240:243], v[40:43]\n"
    "ds_read_b64_tr_b16 v[24:25], v194 offset:8192\n"
    "v_mul_f32 v77, v69, v77\n"
    "v_mul_f32 v78, v70, v78\n"
    "v_mfma_f32_16x16x32_bf16 v[44:47], v[28:31], a[244:247], v[44:47]\n"
    "ds_read_b64_tr_b16 v[26:27], v194 offset:8704\n"
    "v_mul_f32 v79, v71, v79\n"
    "v_exp_f32 v76, v76\n"
    "v_mfma_f32_16x16x32_bf16 v[48:51], v[28:31], a[248:251], v[48:51]\n"
    "v_exp_f32 v77, v77\n"
    "v_exp_f32 v78, v78\n"
    "v_mfma_f32_16x16x32_bf16 v[52:55], v[28:31], a[252:255], v[52:55]\n"
    "s_add_u32 m0, m0, 2048\n"
    "s_add_u32 s48, s48, 147456\n"
    "buffer_load_dwordx4 v6, s[16:19], s48 offen offset:0 lds\n"
    "v_exp_f32 v79, v79\n"
    "s_waitcnt lgkmcnt(6)\n"
    "v_mfma_f32_16x16x32_bf16 v[40:43], v[176:179], v[224:227], v[40:43]\n"
    "ds_read_b64_tr_b16 v[28:29], v194 offset:12288\n"
    "v_add_f32 v76, 1.0, v76\n"
    "v_add_f32 v77, 1.0, v77\n"
    "v_mfma_f32_16x16x32_bf16 v[44:47], v[176:179], v[228:231], v[44:47]\n"
    "ds_read_b64_tr_b16 v[30:31], v194 offset:12800\n"
    "v_add_f32 v78, 1.0, v78\n"
    "v_add_f32 v79, 1.0, v79\n"
    "v_mfma_f32_16x16x32_bf16 v[48:51], v[176:179], v[232:235], v[48:51]\n"
    "v_rcp_f32 v76, v76\n"
    "v_rcp_f32 v77, v77\n"
    "v_mfma_f32_16x16x32_bf16 v[52:55], v[176:179], v[236:239], v[52:55]\n"
    "v_rcp_f32 v78, v78\n"
    "v_mfma_f32_16x16x32_bf16 v[40:43], v[180:183], v[240:243], v[40:43]\n"
    "ds_read_b64_tr_b16 v[176:177], v194 offset:16384\n"
    "v_rcp_f32 v79, v79\n"
    "v_mul_f32 v76, v68, v76\n"
    "v_mfma_f32_16x16x32_bf16 v[44:47], v[180:183], v[244:247], v[44:47]\n"
    "ds_read_b64_tr_b16 v[178:179], v194 offset:16896\n"
    "v_mul_f32 v77, v69, v77\n"
    "v_mul_f32 v78, v70, v78\n"
    "v_mfma_f32_16x16x32_bf16 v[48:51], v[180:183], v[248:251], v[48:51]\n"
    "buffer_load_dwordx4 v7, s[16:19], s48 offen offset:1024 lds\n"
    "v_mul_f32 v79, v71, v79\n"
    "v_cvt_pk_bf16_f32 v110, v76, v77\n"
    "v_mfma_f32_16x16x32_bf16 v[52:55], v[180:183], v[252:255], v[52:55]\n"
    "v_cvt_pk_bf16_f32 v111, v78, v79\n"
    "s_waitcnt lgkmcnt(6)\n"
    "v_mfma_f32_16x16x32_bf16 v[56:59], v[16:19], a[0:3], v[36:39]\n"
    "ds_read_b64_tr_b16 v[180:181], v194 offset:20480\n"
    "v_add_u32 v192, 73728, v1\n"
    "v_mfma_f32_16x16x32_bf16 v[60:63], v[16:19], a[4:7], v[36:39]\n"
    "s_add_u32 m0, m0, 2048\n"
    "s_add_u32 s48, s48, 147456\n"
    "buffer_load_dwordx4 v6, s[16:19], s48 offen offset:0 lds\n"
    "ds_read_b64_tr_b16 v[182:183], v194 offset:20992\n"
    "v_add_u32 v193, 139264, v1\n"
    "v_mfma_f32_16x16x32_bf16 v[64:67], v[16:19], a[8:11], v[36:39]\n"
    "v_mfma_f32_16x16x32_bf16 v[68:71], v[16:19], a[12:15], v[36:39]\n"
    "v_mfma_f32_16x16x32_bf16 v[56:59], v[20:23], a[16:19], v[56:59]\n"
    "ds_read_b64_tr_b16 v[16:17], v194 offset:24576\n"
    "v_mul_f32 v72, v40, v40\n"
    "v_mul_f32 v73, v41, v41\n"
    "v_mfma_f32_16x16x32_bf16 v[60:63], v[20:23], a[20:23], v[60:63]\n"
    "ds_read_b64_tr_b16 v[18:19], v194 offset:25088\n"
    "v_mul_f32 v74, v42, v42\n"
    "v_mul_f32 v75, v43, v43\n"
    "v_mfma_f32_16x16x32_bf16 v[64:67], v[20:23], a[24:27], v[64:67]\n"
    "buffer_load_dwordx4 v7, s[16:19], s48 offen offset:1024 lds\n"
    "v_fmaak_f32 v72, v72, v15, 0xc0135761\n"
    "v_fmaak_f32 v73, v73, v15, 0xc0135761\n"
    "v_mfma_f32_16x16x32_bf16 v[68:71], v[20:23], a[28:31], v[68:71]\n"
    "v_fmaak_f32 v74, v74, v15, 0xc0135761\n"
    "v_fmaak_f32 v75, v75, v15, 0xc0135761\n"
    "s_waitcnt lgkmcnt(6)\n"
    "v_mfma_f32_16x16x32_bf16 v[56:59], v[24:27], a[32:35], v[56:59]\n"
    "ds_read_b64_tr_b16 v[20:21], v194 offset:28672\n"
    "v_mul_f32 v72, v40, v72\n"
    "v_mul_f32 v73, v41, v73\n"
    "v_mfma_f32_16x16x32_bf16 v[60:63], v[24:27], a[36:39], v[60:63]\n"
    "ds_read_b64_tr_b16 v[22:23], v194 offset:29184\n"
    "v_mul_f32 v74, v42, v74\n"
    "v_mul_f32 v75, v43, v75\n"
    "v_mfma_f32_16x16x32_bf16 v[64:67], v[24:27], a[40:43], v[64:67]\n"
    "v_exp_f32 v72, v72\n"
    "v_exp_f32 v73, v73\n"
    "v_mfma_f32_16x16x32_bf16 v[68:71], v[24:27], a[44:47], v[68:71]\n"
    "v_exp_f32 v74, v74\n"
    "v_mfma_f32_16x16x32_bf16 v[56:59], v[28:31], a[48:51], v[56:59]\n"
    "s_add_u32 m0, m0, 2048\n"
    "s_add_u32 s48, s48, 147456\n"
    "buffer_load_dwordx4 v6, s[16:19], s48 offen offset:0 lds\n"
    "ds_read_b64_tr_b16 v[24:25], v194 offset:32768\n"
    "v_exp_f32 v75, v75\n"
    "v_add_f32 v72, 1.0, v72\n"
    "v_mfma_f32_16x16x32_bf16 v[60:63], v[28:31], a[52:55], v[60:63]\n"
    "ds_read_b64_tr_b16 v[26:27], v194 offset:33280\n"
    "v_add_f32 v73, 1.0, v73\n"
    "v_add_f32 v74, 1.0, v74\n"
    "v_mfma_f32_16x16x32_bf16 v[64:67], v[28:31], a[56:59], v[64:67]\n"
    "v_add_f32 v75, 1.0, v75\n"
    "v_rcp_f32 v72, v72\n"
    "v_mfma_f32_16x16x32_bf16 v[68:71], v[28:31], a[60:63], v[68:71]\n"
    "v_rcp_f32 v73, v73\n"
    "s_waitcnt lgkmcnt(6)\n"
    "v_mfma_f32_16x16x32_bf16 v[56:59], v[176:179], a[64:67], v[56:59]\n"
    "ds_read_b64_tr_b16 v[28:29], v194 offset:36864\n"
    "v_rcp_f32 v74, v74\n"
    "v_rcp_f32 v75, v75\n"
    "v_mfma_f32_16x16x32_bf16 v[60:63], v[176:179], a[68:71], v[60:63]\n"
    "ds_read_b64_tr_b16 v[30:31], v194 offset:37376\n"
    "v_mul_f32 v72, v40, v72\n"
    "v_mul_f32 v73, v41, v73\n"
    "v_mfma_f32_16x16x32_bf16 v[64:67], v[176:179], a[72:75], v[64:67]\n"
    "buffer_load_dwordx4 v7, s[16:19], s48 offen offset:1024 lds\n"
    "v_mul_f32 v74, v42, v74\n"
    "v_mul_f32 v75, v43, v75\n"
    "v_mfma_f32_16x16x32_bf16 v[68:71], v[176:179], a[76:79], v[68:71]\n"
    "v_cvt_pk_bf16_f32 v112, v72, v73\n"
    "v_mfma_f32_16x16x32_bf16 v[56:59], v[180:183], a[80:83], v[56:59]\n"
    "ds_read_b64_tr_b16 v[176:177], v194 offset:40960\n"
    "v_cvt_pk_bf16_f32 v113, v74, v75\n"
    "v_mul_f32 v76, v44, v44\n"
    "v_mfma_f32_16x16x32_bf16 v[60:63], v[180:183], a[84:87], v[60:63]\n"
    "ds_read_b64_tr_b16 v[178:179], v194 offset:41472\n"
    "v_mul_f32 v77, v45, v45\n"
    "v_mul_f32 v78, v46, v46\n"
    "v_mfma_f32_16x16x32_bf16 v[64:67], v[180:183], a[88:91], v[64:67]\n"
    "v_mul_f32 v79, v47, v47\n"
    "v_fmaak_f32 v76, v76, v15, 0xc0135761\n"
    "v_mfma_f32_16x16x32_bf16 v[68:71], v[180:183], a[92:95], v[68:71]\n"
    "v_fmaak_f32 v77, v77, v15, 0xc0135761\n"
    "s_waitcnt lgkmcnt(6)\n"
    "v_mfma_f32_16x16x32_bf16 v[56:59], v[16:19], a[96:99], v[56:59]\n"
    "s_add_u32 m0, m0, 2048\n"
    "s_add_u32 s48, s48, 147456\n"
    "buffer_load_dwordx4 v6, s[16:19], s48 offen offset:0 lds\n"
    "ds_read_b64_tr_b16 v[180:181], v194 offset:45056\n"
    "v_fmaak_f32 v78, v78, v15, 0xc0135761\n"
    "v_fmaak_f32 v79, v79, v15, 0xc0135761\n"
    "v_mfma_f32_16x16x32_bf16 v[60:63], v[16:19], a[100:103], v[60:63]\n"
    "ds_read_b64_tr_b16 v[182:183], v194 offset:45568\n"
    "v_mul_f32 v76, v44, v76\n"
    "v_mul_f32 v77, v45, v77\n"
    "v_mfma_f32_16x16x32_bf16 v[64:67], v[16:19], a[104:107], v[64:67]\n"
    "v_mul_f32 v78, v46, v78\n"
    "v_mul_f32 v79, v47, v79\n"
    "v_mfma_f32_16x16x32_bf16 v[68:71], v[16:19], a[108:111], v[68:71]\n"
    "v_exp_f32 v76, v76\n"
    "v_mfma_f32_16x16x32_bf16 v[56:59], v[20:23], a[112:115], v[56:59]\n"
    "ds_read_b64_tr_b16 v[16:17], v194 offset:49152\n"
    "v_exp_f32 v77, v77\n"
    "v_exp_f32 v78, v78\n"
    "v_mfma_f32_16x16x32_bf16 v[60:63], v[20:23], a[116:119], v[60:63]\n"
    "ds_read_b64_tr_b16 v[18:19], v194 offset:49664\n"
    "v_exp_f32 v79, v79\n"
    "v_add_f32 v76, 1.0, v76\n"
    "v_mfma_f32_16x16x32_bf16 v[64:67], v[20:23], a[120:123], v[64:67]\n"
    "buffer_load_dwordx4 v7, s[16:19], s48 offen offset:1024 lds\n"
    "v_add_f32 v77, 1.0, v77\n"
    "v_add_f32 v78, 1.0, v78\n"
    "v_mfma_f32_16x16x32_bf16 v[68:71], v[20:23], a[124:127], v[68:71]\n"
    "v_add_f32 v79, 1.0, v79\n"
    "s_waitcnt lgkmcnt(6)\n"
    "v_mfma_f32_16x16x32_bf16 v[56:59], v[24:27], a[128:131], v[56:59]\n"
    "ds_read_b64_tr_b16 v[20:21], v194 offset:53248\n"
    "ds_read_b128 v[32:35], v14 offset:768\n"
    "v_rcp_f32 v76, v76\n"
    "v_mfma_f32_16x16x32_bf16 v[60:63], v[24:27], a[132:135], v[60:63]\n"
    "ds_read_b64_tr_b16 v[22:23], v194 offset:53760\n"
    "v_rcp_f32 v77, v77\n"
    "v_rcp_f32 v78, v78\n"
    "v_mfma_f32_16x16x32_bf16 v[64:67], v[24:27], a[136:139], v[64:67]\n"
    "v_rcp_f32 v79, v79\n"
    "v_mul_f32 v76, v44, v76\n"
    "v_mfma_f32_16x16x32_bf16 v[68:71], v[24:27], a[140:143], v[68:71]\n"
    "v_mul_f32 v77, v45, v77\n"
    "v_mul_f32 v78, v46, v78\n"
    "v_mfma_f32_16x16x32_bf16 v[56:59], v[28:31], a[144:147], v[56:59]\n"
    "ds_read_b64_tr_b16 v[24:25], v194 offset:57344\n"
    "v_mul_f32 v79, v47, v79\n"
    "v_cvt_pk_bf16_f32 v116, v76, v77\n"
    "v_mfma_f32_16x16x32_bf16 v[60:63], v[28:31], a[148:151], v[60:63]\n"
    "ds_read_b64_tr_b16 v[26:27], v194 offset:57856\n"
    "v_cvt_pk_bf16_f32 v117, v78, v79\n"
    "v_mul_f32 v72, v48, v48\n"
    "v_mfma_f32_16x16x32_bf16 v[64:67], v[28:31], a[152:155], v[64:67]\n"
    "v_mul_f32 v73, v49, v49\n"
    "v_mul_f32 v74, v50, v50\n"
    "v_mfma_f32_16x16x32_bf16 v[68:71], v[28:31], a[156:159], v[68:71]\n"
    "v_mul_f32 v75, v51, v51\n"
    "s_waitcnt lgkmcnt(7)\n"
    "v_mfma_f32_16x16x32_bf16 v[56:59], v[176:179], a[160:163], v[56:59]\n"
    "ds_read_b64_tr_b16 v[28:29], v194 offset:61440\n"
    "v_fmaak_f32 v72, v72, v15, 0xc0135761\n"
    "v_fmaak_f32 v73, v73, v15, 0xc0135761\n"
    "v_mfma_f32_16x16x32_bf16 v[60:63], v[176:179], a[164:167], v[60:63]\n"
    "ds_read_b64_tr_b16 v[30:31], v194 offset:61952\n"
    "v_fmaak_f32 v74, v74, v15, 0xc0135761\n"
    "v_fmaak_f32 v75, v75, v15, 0xc0135761\n"
    "v_mfma_f32_16x16x32_bf16 v[64:67], v[176:179], a[168:171], v[64:67]\n"
    "v_mul_f32 v72, v48, v72\n"
    "v_mul_f32 v73, v49, v73\n"
    "v_mfma_f32_16x16x32_bf16 v[68:71], v[176:179], a[172:175], v[68:71]\n"
    "v_mul_f32 v74, v50, v74\n"
    "v_mfma_f32_16x16x32_bf16 v[56:59], v[180:183], a[176:179], v[56:59]\n"
    "ds_read_b64_tr_b16 v[176:177], v195 offset:0\n"
    "v_mul_f32 v75, v51, v75\n"
    "v_exp_f32 v72, v72\n"
    "v_mfma_f32_16x16x32_bf16 v[60:63], v[180:183], a[180:183], v[60:63]\n"
    "ds_read_b64_tr_b16 v[178:179], v195 offset:512\n"
    "v_exp_f32 v73, v73\n"
    "v_exp_f32 v74, v74\n"
    "v_mfma_f32_16x16x32_bf16 v[64:67], v[180:183], a[184:187], v[64:67]\n"
    "v_exp_f32 v75, v75\n"
    "v_add_f32 v72, 1.0, v72\n"
    "v_mfma_f32_16x16x32_bf16 v[68:71], v[180:183], a[188:191], v[68:71]\n"
    "v_add_f32 v73, 1.0, v73\n"
    "s_waitcnt lgkmcnt(6)\n"
    "v_mfma_f32_16x16x32_bf16 v[56:59], v[16:19], a[192:195], v[56:59]\n"
    "ds_read_b64_tr_b16 v[180:181], v195 offset:4096\n"
    "v_add_f32 v74, 1.0, v74\n"
    "v_add_f32 v75, 1.0, v75\n"
    "v_mfma_f32_16x16x32_bf16 v[60:63], v[16:19], a[196:199], v[60:63]\n"
    "ds_read_b64_tr_b16 v[182:183], v195 offset:4608\n"
    "v_rcp_f32 v72, v72\n"
    "v_rcp_f32 v73, v73\n"
    "v_mfma_f32_16x16x32_bf16 v[64:67], v[16:19], a[200:203], v[64:67]\n"
    "v_rcp_f32 v74, v74\n"
    "v_rcp_f32 v75, v75\n"
    "v_mfma_f32_16x16x32_bf16 v[68:71], v[16:19], a[204:207], v[68:71]\n"
    "v_mul_f32 v72, v48, v72\n"
    "v_mfma_f32_16x16x32_bf16 v[56:59], v[20:23], a[208:211], v[56:59]\n"
    "v_mul_f32 v73, v49, v73\n"
    "v_mul_f32 v74, v50, v74\n"
    "v_mfma_f32_16x16x32_bf16 v[60:63], v[20:23], a[212:215], v[60:63]\n"
    "v_mul_f32 v75, v51, v75\n"
    "v_cvt_pk_bf16_f32 v120, v72, v73\n"
    "v_mfma_f32_16x16x32_bf16 v[64:67], v[20:23], a[216:219], v[64:67]\n"
    "v_cvt_pk_bf16_f32 v121, v74, v75\n"
    "v_mul_f32 v76, v52, v52\n"
    "v_mfma_f32_16x16x32_bf16 v[68:71], v[20:23], a[220:223], v[68:71]\n"
    "v_mul_f32 v77, v53, v53\n"
    "s_waitcnt lgkmcnt(4)\n"
    "v_mfma_f32_16x16x32_bf16 v[56:59], v[24:27], a[224:227], v[56:59]\n"
    "v_mul_f32 v78, v54, v54\n"
    "v_mul_f32 v79, v55, v55\n"
    "v_mfma_f32_16x16x32_bf16 v[60:63], v[24:27], a[228:231], v[60:63]\n"
    "v_fmaak_f32 v76, v76, v15, 0xc0135761\n"
    "v_fmaak_f32 v77, v77, v15, 0xc0135761\n"
    "v_mfma_f32_16x16x32_bf16 v[64:67], v[24:27], a[232:235], v[64:67]\n"
    "v_fmaak_f32 v78, v78, v15, 0xc0135761\n"
    "v_fmaak_f32 v79, v79, v15, 0xc0135761\n"
    "v_mfma_f32_16x16x32_bf16 v[68:71], v[24:27], a[236:239], v[68:71]\n"
    "v_mul_f32 v76, v52, v76\n"
    "s_waitcnt lgkmcnt(0)\n"
    "s_waitcnt vmcnt(0)\n"
    "s_barrier\n"
    "v_mfma_f32_16x16x32_bf16 v[56:59], v[28:31], a[240:243], v[56:59]\n"
    "ds_read_b64_tr_b16 v[16:17], v192 offset:0\n"
    "v_mul_f32 v77, v53, v77\n"
    "v_mul_f32 v78, v54, v78\n"
    "v_mfma_f32_16x16x32_bf16 v[60:63], v[28:31], a[244:247], v[60:63]\n"
    "ds_read_b64_tr_b16 v[18:19], v192 offset:512\n"
    "v_mul_f32 v79, v55, v79\n"
    "v_exp_f32 v76, v76\n"
    "v_mfma_f32_16x16x32_bf16 v[64:67], v[28:31], a[248:251], v[64:67]\n"
    "ds_read_b64_tr_b16 v[20:21], v192 offset:4096\n"
    "v_exp_f32 v77, v77\n"
    "v_exp_f32 v78, v78\n"
    "v_mfma_f32_16x16x32_bf16 v[68:71], v[28:31], a[252:255], v[68:71]\n"
    "ds_read_b64_tr_b16 v[22:23], v192 offset:4608\n"
    "v_exp_f32 v79, v79\n"
    "v_mfma_f32_16x16x32_bf16 v[56:59], v[176:179], v[224:227], v[56:59]\n"
    "ds_read_b64_tr_b16 v[24:25], v192 offset:8192\n"
    "v_add_f32 v76, 1.0, v76\n"
    "v_add_f32 v77, 1.0, v77\n"
    "v_mfma_f32_16x16x32_bf16 v[60:63], v[176:179], v[228:231], v[60:63]\n"
    "ds_read_b64_tr_b16 v[26:27], v192 offset:8704\n"
    "v_add_f32 v78, 1.0, v78\n"
    "v_add_f32 v79, 1.0, v79\n"
    "v_mfma_f32_16x16x32_bf16 v[64:67], v[176:179], v[232:235], v[64:67]\n"
    "ds_read_b64_tr_b16 v[28:29], v192 offset:12288\n"
    "v_rcp_f32 v76, v76\n"
    "v_rcp_f32 v77, v77\n"
    "v_mfma_f32_16x16x32_bf16 v[68:71], v[176:179], v[236:239], v[68:71]\n"
    "ds_read_b64_tr_b16 v[30:31], v192 offset:12800\n"
    "v_rcp_f32 v78, v78\n"
    "v_mfma_f32_16x16x32_bf16 v[56:59], v[180:183], v[240:243], v[56:59]\n"
    "ds_read_b64_tr_b16 v[176:177], v192 offset:16384\n"
    "v_rcp_f32 v79, v79\n"
    "v_mul_f32 v76, v52, v76\n"
    "v_mfma_f32_16x16x32_bf16 v[60:63], v[180:183], v[244:247], v[60:63]\n"
    "ds_read_b64_tr_b16 v[178:179], v192 offset:16896\n"
    "v_mul_f32 v77, v53, v77\n"
    "v_mul_f32 v78, v54, v78\n"
    "v_mfma_f32_16x16x32_bf16 v[64:67], v[180:183], v[248:251], v[64:67]\n"
    "v_mul_f32 v79, v55, v79\n"
    "v_cvt_pk_bf16_f32 v124, v76, v77\n"
    "v_mfma_f32_16x16x32_bf16 v[68:71], v[180:183], v[252:255], v[68:71]\n"
    "v_cvt_pk_bf16_f32 v125, v78, v79\n"
    "s_waitcnt lgkmcnt(6)\n"
    "v_mfma_f32_16x16x32_bf16 v[40:43], v[16:19], a[0:3], v[32:35]\n"
    "ds_read_b64_tr_b16 v[180:181], v192 offset:20480\n"
    "v_add_u32 v194, 73728, v2\n"
    "v_mfma_f32_16x16x32_bf16 v[44:47], v[16:19], a[4:7], v[32:35]\n"
    "ds_read_b64_tr_b16 v[182:183], v192 offset:20992\n"
    "v_add_u32 v195, 139264, v2\n"
    "v_mfma_f32_16x16x32_bf16 v[48:51], v[16:19], a[8:11], v[32:35]\n"
    "v_mfma_f32_16x16x32_bf16 v[52:55], v[16:19], a[12:15], v[32:35]\n"
    "s_add_u32 s40, s14, 4\n"
    "s_cmp_ge_u32 s40, 6\n"
    "s_cselect_b32 s41, 6, 0\n"
    "s_sub_u32 s40, s40, s41\n"
    "s_lshl_b32 s40, s40, 7\n"
    "s_mul_i32 s41, s9, 18432\n"
    "s_add_u32 m0, s41, 0\n"
    "s_add_u32 s48, s49, s40\n"
    "buffer_load_dwordx4 v6, s[16:19], s48 offen offset:0 lds\n"
    "v_mfma_f32_16x16x32_bf16 v[40:43], v[20:23], a[16:19], v[40:43]\n"
    "ds_read_b64_tr_b16 v[16:17], v192 offset:24576\n"
    "v_mul_f32 v72, v56, v56\n"
    "v_mul_f32 v73, v57, v57\n"
    "v_mfma_f32_16x16x32_bf16 v[44:47], v[20:23], a[20:23], v[44:47]\n"
    "ds_read_b64_tr_b16 v[18:19], v192 offset:25088\n"
    "v_mul_f32 v74, v58, v58\n"
    "v_mul_f32 v75, v59, v59\n"
    "v_mfma_f32_16x16x32_bf16 v[48:51], v[20:23], a[24:27], v[48:51]\n"
    "v_fmaak_f32 v72, v72, v15, 0xc0135761\n"
    "v_fmaak_f32 v73, v73, v15, 0xc0135761\n"
    "v_mfma_f32_16x16x32_bf16 v[52:55], v[20:23], a[28:31], v[52:55]\n"
    "v_fmaak_f32 v74, v74, v15, 0xc0135761\n"
    "v_fmaak_f32 v75, v75, v15, 0xc0135761\n"
    "s_waitcnt lgkmcnt(6)\n"
    "v_mfma_f32_16x16x32_bf16 v[40:43], v[24:27], a[32:35], v[40:43]\n"
    "ds_read_b64_tr_b16 v[20:21], v192 offset:28672\n"
    "v_mul_f32 v72, v56, v72\n"
    "v_mul_f32 v73, v57, v73\n"
    "v_mfma_f32_16x16x32_bf16 v[44:47], v[24:27], a[36:39], v[44:47]\n"
    "buffer_load_dwordx4 v7, s[16:19], s48 offen offset:1024 lds\n"
    "ds_read_b64_tr_b16 v[22:23], v192 offset:29184\n"
    "v_mul_f32 v74, v58, v74\n"
    "v_mul_f32 v75, v59, v75\n"
    "v_mfma_f32_16x16x32_bf16 v[48:51], v[24:27], a[40:43], v[48:51]\n"
    "v_exp_f32 v72, v72\n"
    "v_exp_f32 v73, v73\n"
    "v_mfma_f32_16x16x32_bf16 v[52:55], v[24:27], a[44:47], v[52:55]\n"
    "v_exp_f32 v74, v74\n"
    "v_mfma_f32_16x16x32_bf16 v[40:43], v[28:31], a[48:51], v[40:43]\n"
    "ds_read_b64_tr_b16 v[24:25], v192 offset:32768\n"
    "v_exp_f32 v75, v75\n"
    "v_add_f32 v72, 1.0, v72\n"
    "v_mfma_f32_16x16x32_bf16 v[44:47], v[28:31], a[52:55], v[44:47]\n"
    "ds_read_b64_tr_b16 v[26:27], v192 offset:33280\n"
    "v_add_f32 v73, 1.0, v73\n"
    "v_add_f32 v74, 1.0, v74\n"
    "v_mfma_f32_16x16x32_bf16 v[48:51], v[28:31], a[56:59], v[48:51]\n"
    "v_add_f32 v75, 1.0, v75\n"
    "v_rcp_f32 v72, v72\n"
    "v_mfma_f32_16x16x32_bf16 v[52:55], v[28:31], a[60:63], v[52:55]\n"
    "s_add_u32 m0, m0, 2048\n"
    "s_add_u32 s48, s48, 147456\n"
    "buffer_load_dwordx4 v6, s[16:19], s48 offen offset:0 lds\n"
    "v_rcp_f32 v73, v73\n"
    "s_waitcnt lgkmcnt(6)\n"
    "v_mfma_f32_16x16x32_bf16 v[40:43], v[176:179], a[64:67], v[40:43]\n"
    "ds_read_b64_tr_b16 v[28:29], v192 offset:36864\n"
    "v_rcp_f32 v74, v74\n"
    "v_rcp_f32 v75, v75\n"
    "v_mfma_f32_16x16x32_bf16 v[44:47], v[176:179], a[68:71], v[44:47]\n"
    "ds_read_b64_tr_b16 v[30:31], v192 offset:37376\n"
    "v_mul_f32 v72, v56, v72\n"
    "v_mul_f32 v73, v57, v73\n"
    "v_mfma_f32_16x16x32_bf16 v[48:51], v[176:179], a[72:75], v[48:51]\n"
    "v_mul_f32 v74, v58, v74\n"
    "v_mul_f32 v75, v59, v75\n"
    "v_mfma_f32_16x16x32_bf16 v[52:55], v[176:179], a[76:79], v[52:55]\n"
    "v_cvt_pk_bf16_f32 v114, v72, v73\n"
    "v_mfma_f32_16x16x32_bf16 v[40:43], v[180:183], a[80:83], v[40:43]\n"
    "ds_read_b64_tr_b16 v[176:177], v192 offset:40960\n"
    "v_cvt_pk_bf16_f32 v115, v74, v75\n"
    "v_mul_f32 v76, v60, v60\n"
    "v_mfma_f32_16x16x32_bf16 v[44:47], v[180:183], a[84:87], v[44:47]\n"
    "buffer_load_dwordx4 v7, s[16:19], s48 offen offset:1024 lds\n"
    "ds_read_b64_tr_b16 v[178:179], v192 offset:41472\n"
    "v_mul_f32 v77, v61, v61\n"
    "v_mul_f32 v78, v62, v62\n"
    "v_mfma_f32_16x16x32_bf16 v[48:51], v[180:183], a[88:91], v[48:51]\n"
    "v_mul_f32 v79, v63, v63\n"
    "v_fmaak_f32 v76, v76, v15, 0xc0135761\n"
    "v_mfma_f32_16x16x32_bf16 v[52:55], v[180:183], a[92:95], v[52:55]\n"
    "v_fmaak_f32 v77, v77, v15, 0xc0135761\n"
    "s_waitcnt lgkmcnt(6)\n"
    "v_mfma_f32_16x16x32_bf16 v[40:43], v[16:19], a[96:99], v[40:43]\n"
    "ds_read_b64_tr_b16 v[180:181], v192 offset:45056\n"
    "v_fmaak_f32 v78, v78, v15, 0xc0135761\n"
    "v_fmaak_f32 v79, v79, v15, 0xc0135761\n"
    "v_mfma_f32_16x16x32_bf16 v[44:47], v[16:19], a[100:103], v[44:47]\n"
    "ds_read_b64_tr_b16 v[182:183], v192 offset:45568\n"
    "v_mul_f32 v76, v60, v76\n"
    "v_mul_f32 v77, v61, v77\n"
    "v_mfma_f32_16x16x32_bf16 v[48:51], v[16:19], a[104:107], v[48:51]\n"
    "v_mul_f32 v78, v62, v78\n"
    "v_mul_f32 v79, v63, v79\n"
    "v_mfma_f32_16x16x32_bf16 v[52:55], v[16:19], a[108:111], v[52:55]\n"
    "s_add_u32 m0, m0, 2048\n"
    "s_add_u32 s48, s48, 147456\n"
    "buffer_load_dwordx4 v6, s[16:19], s48 offen offset:0 lds\n"
    "v_exp_f32 v76, v76\n"
    "v_mfma_f32_16x16x32_bf16 v[40:43], v[20:23], a[112:115], v[40:43]\n"
    "ds_read_b64_tr_b16 v[16:17], v192 offset:49152\n"
    "v_exp_f32 v77, v77\n"
    "v_exp_f32 v78, v78\n"
    "v_mfma_f32_16x16x32_bf16 v[44:47], v[20:23], a[116:119], v[44:47]\n"
    "ds_read_b64_tr_b16 v[18:19], v192 offset:49664\n"
    "v_exp_f32 v79, v79\n"
    "v_add_f32 v76, 1.0, v76\n"
    "v_mfma_f32_16x16x32_bf16 v[48:51], v[20:23], a[120:123], v[48:51]\n"
    "v_add_f32 v77, 1.0, v77\n"
    "v_add_f32 v78, 1.0, v78\n"
    "v_mfma_f32_16x16x32_bf16 v[52:55], v[20:23], a[124:127], v[52:55]\n"
    "v_add_f32 v79, 1.0, v79\n"
    "s_waitcnt lgkmcnt(6)\n"
    "v_mfma_f32_16x16x32_bf16 v[40:43], v[24:27], a[128:131], v[40:43]\n"
    "ds_read_b64_tr_b16 v[20:21], v192 offset:53248\n"
    "ds_read_b128 v[36:39], v14 offset:832\n"
    "v_rcp_f32 v76, v76\n"
    "v_mfma_f32_16x16x32_bf16 v[44:47], v[24:27], a[132:135], v[44:47]\n"
    "buffer_load_dwordx4 v7, s[16:19], s48 offen offset:1024 lds\n"
    "ds_read_b64_tr_b16 v[22:23], v192 offset:53760\n"
    "v_rcp_f32 v77, v77\n"
    "v_rcp_f32 v78, v78\n"
    "v_mfma_f32_16x16x32_bf16 v[48:51], v[24:27], a[136:139], v[48:51]\n"
    "v_rcp_f32 v79, v79\n"
    "v_mul_f32 v76, v60, v76\n"
    "v_mfma_f32_16x16x32_bf16 v[52:55], v[24:27], a[140:143], v[52:55]\n"
    "v_mul_f32 v77, v61, v77\n"
    "v_mul_f32 v78, v62, v78\n"
    "v_mfma_f32_16x16x32_bf16 v[40:43], v[28:31], a[144:147], v[40:43]\n"
    "ds_read_b64_tr_b16 v[24:25], v192 offset:57344\n"
    "v_mul_f32 v79, v63, v79\n"
    "v_cvt_pk_bf16_f32 v118, v76, v77\n"
    "v_mfma_f32_16x16x32_bf16 v[44:47], v[28:31], a[148:151], v[44:47]\n"
    "ds_read_b64_tr_b16 v[26:27], v192 offset:57856\n"
    "v_cvt_pk_bf16_f32 v119, v78, v79\n"
    "v_mul_f32 v72, v64, v64\n"
    "v_mfma_f32_16x16x32_bf16 v[48:51], v[28:31], a[152:155], v[48:51]\n"
    "v_mul_f32 v73, v65, v65\n"
    "v_mul_f32 v74, v66, v66\n"
    "v_mfma_f32_16x16x32_bf16 v[52:55], v[28:31], a[156:159], v[52:55]\n"
    "s_add_u32 m0, m0, 2048\n"
    "s_add_u32 s48, s48, 147456\n"
    "buffer_load_dwordx4 v6, s[16:19], s48 offen offset:0 lds\n"
    "v_mul_f32 v75, v67, v67\n"
    "s_waitcnt lgkmcnt(7)\n"
    "v_mfma_f32_16x16x32_bf16 v[40:43], v[176:179], a[160:163], v[40:43]\n"
    "ds_read_b64_tr_b16 v[28:29], v192 offset:61440\n"
    "v_fmaak_f32 v72, v72, v15, 0xc0135761\n"
    "v_fmaak_f32 v73, v73, v15, 0xc0135761\n"
    "v_mfma_f32_16x16x32_bf16 v[44:47], v[176:179], a[164:167], v[44:47]\n"
    "ds_read_b64_tr_b16 v[30:31], v192 offset:61952\n"
    "v_fmaak_f32 v74, v74, v15, 0xc0135761\n"
    "v_fmaak_f32 v75, v75, v15, 0xc0135761\n"
    "v_mfma_f32_16x16x32_bf16 v[48:51], v[176:179], a[168:171], v[48:51]\n"
    "v_mul_f32 v72, v64, v72\n"
    "v_mul_f32 v73, v65, v73\n"
    "v_mfma_f32_16x16x32_bf16 v[52:55], v[176:179], a[172:175], v[52:55]\n"
    "v_mul_f32 v74, v66, v74\n"
    "v_mfma_f32_16x16x32_bf16 v[40:43], v[180:183], a[176:179], v[40:43]\n"
    "ds_read_b64_tr_b16 v[176:177], v193 offset:0\n"
    "v_mul_f32 v75, v67, v75\n"
    "v_exp_f32 v72, v72\n"
    "v_mfma_f32_16x16x32_bf16 v[44:47], v[180:183], a[180:183], v[44:47]\n"
    "buffer_load_dwordx4 v7, s[16:19], s48 offen offset:1024 lds\n"
    "ds_read_b64_tr_b16 v[178:179], v193 offset:512\n"
    "v_exp_f32 v73, v73\n"
    "v_exp_f32 v74, v74\n"
    "v_mfma_f32_16x16x32_bf16 v[48:51], v[180:183], a[184:187], v[48:51]\n"
    "v_exp_f32 v75, v75\n"
    "v_add_f32 v72, 1.0, v72\n"
    "v_mfma_f32_16x16x32_bf16 v[52:55], v[180:183], a[188:191], v[52:55]\n"
    "v_add_f32 v73, 1.0, v73\n"
    "s_waitcnt lgkmcnt(6)\n"
    "v_mfma_f32_16x16x32_bf16 v[40:43], v[16:19], a[192:195], v[40:43]\n"
    "ds_read_b64_tr_b16 v[180:181], v193 offset:4096\n"
    "v_add_f32 v74, 1.0, v74\n"
    "v_add_f32 v75, 1.0, v75\n"
    "v_mfma_f32_16x16x32_bf16 v[44:47], v[16:19], a[196:199], v[44:47]\n"
    "ds_read_b64_tr_b16 v[182:183], v193 offset:4608\n"
    "v_rcp_f32 v72, v72\n"
    "v_rcp_f32 v73, v73\n"
    "v_mfma_f32_16x16x32_bf16 v[48:51], v[16:19], a[200:203], v[48:51]\n"
    "v_rcp_f32 v74, v74\n"
    "v_rcp_f32 v75, v75\n"
    "v_mfma_f32_16x16x32_bf16 v[52:55], v[16:19], a[204:207], v[52:55]\n"
    "s_add_u32 m0, m0, 2048\n"
    "s_add_u32 s48, s48, 147456\n"
    "buffer_load_dwordx4 v6, s[16:19], s48 offen offset:0 lds\n"
    "v_mul_f32 v72, v64, v72\n"
    "v_mfma_f32_16x16x32_bf16 v[40:43], v[20:23], a[208:211], v[40:43]\n"
    "ds_read_b64_tr_b16 v[16:17], v194 offset:0\n"
    "v_mul_f32 v73, v65, v73\n"
    "v_mul_f32 v74, v66, v74\n"
    "v_mfma_f32_16x16x32_bf16 v[44:47], v[20:23], a[212:215], v[44:47]\n"
    "ds_read_b64_tr_b16 v[18:19], v194 offset:512\n"
    "v_mul_f32 v75, v67, v75\n"
    "v_cvt_pk_bf16_f32 v122, v72, v73\n"
    "v_mfma_f32_16x16x32_bf16 v[48:51], v[20:23], a[216:219], v[48:51]\n"
    "v_cvt_pk_bf16_f32 v123, v74, v75\n"
    "v_mul_f32 v76, v68, v68\n"
    "v_mfma_f32_16x16x32_bf16 v[52:55], v[20:23], a[220:223], v[52:55]\n"
    "v_mul_f32 v77, v69, v69\n"
    "s_waitcnt lgkmcnt(6)\n"
    "v_mfma_f32_16x16x32_bf16 v[40:43], v[24:27], a[224:227], v[40:43]\n"
    "ds_read_b64_tr_b16 v[20:21], v194 offset:4096\n"
    "v_mul_f32 v78, v70, v70\n"
    "v_mul_f32 v79, v71, v71\n"
    "v_mfma_f32_16x16x32_bf16 v[44:47], v[24:27], a[228:231], v[44:47]\n"
    "buffer_load_dwordx4 v7, s[16:19], s48 offen offset:1024 lds\n"
    "ds_read_b64_tr_b16 v[22:23], v194 offset:4608\n"
    "v_fmaak_f32 v76, v76, v15, 0xc0135761\n"
    "v_fmaak_f32 v77, v77, v15, 0xc0135761\n"
    "v_mfma_f32_16x16x32_bf16 v[48:51], v[24:27], a[232:235], v[48:51]\n"
    "v_fmaak_f32 v78, v78, v15, 0xc0135761\n"
    "v_fmaak_f32 v79, v79, v15, 0xc0135761\n"
    "v_mfma_f32_16x16x32_bf16 v[52:55], v[24:27], a[236:239], v[52:55]\n"
    "v_mul_f32 v76, v68, v76\n"
    "v_mfma_f32_16x16x32_bf16 v[40:43], v[28:31], a[240:243], v[40:43]\n"
    "ds_read_b64_tr_b16 v[24:25], v194 offset:8192\n"
    "v_mul_f32 v77, v69, v77\n"
    "v_mul_f32 v78, v70, v78\n"
    "v_mfma_f32_16x16x32_bf16 v[44:47], v[28:31], a[244:247], v[44:47]\n"
    "ds_read_b64_tr_b16 v[26:27], v194 offset:8704\n"
    "v_mul_f32 v79, v71, v79\n"
    "v_exp_f32 v76, v76\n"
    "v_mfma_f32_16x16x32_bf16 v[48:51], v[28:31], a[248:251], v[48:51]\n"
    "v_exp_f32 v77, v77\n"
    "v_exp_f32 v78, v78\n"
    "v_mfma_f32_16x16x32_bf16 v[52:55], v[28:31], a[252:255], v[52:55]\n"
    "s_add_u32 m0, m0, 2048\n"
    "s_add_u32 s48, s48, 147456\n"
    "buffer_load_dwordx4 v6, s[16:19], s48 offen offset:0 lds\n"
    "v_exp_f32 v79, v79\n"
    "s_waitcnt lgkmcnt(6)\n"
    "v_mfma_f32_16x16x32_bf16 v[40:43], v[176:179], v[224:227], v[40:43]\n"
    "ds_read_b64_tr_b16 v[28:29], v194 offset:12288\n"
    "v_add_f32 v76, 1.0, v76\n"
    "v_add_f32 v77, 1.0, v77\n"
    "v_mfma_f32_16x16x32_bf16 v[44:47], v[176:179], v[228:231], v[44:47]\n"
    "ds_read_b64_tr_b16 v[30:31], v194 offset:12800\n"
    "v_add_f32 v78, 1.0, v78\n"
    "v_add_f32 v79, 1.0, v79\n"
    "v_mfma_f32_16x16x32_bf16 v[48:51], v[176:179], v[232:235], v[48:51]\n"
    "v_rcp_f32 v76, v76\n"
    "v_rcp_f32 v77, v77\n"
    "v_mfma_f32_16x16x32_bf16 v[52:55], v[176:179], v[236:239], v[52:55]\n"
    "v_rcp_f32 v78, v78\n"
    "v_mfma_f32_16x16x32_bf16 v[40:43], v[180:183], v[240:243], v[40:43]\n"
    "ds_read_b64_tr_b16 v[176:177], v194 offset:16384\n"
    "v_rcp_f32 v79, v79\n"
    "v_mul_f32 v76, v68, v76\n"
    "v_mfma_f32_16x16x32_bf16 v[44:47], v[180:183], v[244:247], v[44:47]\n"
    "ds_read_b64_tr_b16 v[178:179], v194 offset:16896\n"
    "v_mul_f32 v77, v69, v77\n"
    "v_mul_f32 v78, v70, v78\n"
    "v_mfma_f32_16x16x32_bf16 v[48:51], v[180:183], v[248:251], v[48:51]\n"
    "buffer_load_dwordx4 v7, s[16:19], s48 offen offset:1024 lds\n"
    "v_mul_f32 v79, v71, v79\n"
    "v_cvt_pk_bf16_f32 v126, v76, v77\n"
    "v_mfma_f32_16x16x32_bf16 v[52:55], v[180:183], v[252:255], v[52:55]\n"
    "v_cvt_pk_bf16_f32 v127, v78, v79\n"
    "s_waitcnt lgkmcnt(6)\n"
    "v_mfma_f32_16x16x32_bf16 v[56:59], v[16:19], a[0:3], v[36:39]\n"
    "ds_read_b64_tr_b16 v[180:181], v194 offset:20480\n"
    "v_add_u32 v192, 0, v1\n"
    "v_mfma_f32_16x16x32_bf16 v[60:63], v[16:19], a[4:7], v[36:39]\n"
    "s_add_u32 m0, m0, 2048\n"
    "s_add_u32 s48, s48, 147456\n"
    "buffer_load_dwordx4 v6, s[16:19], s48 offen offset:0 lds\n"
    "ds_read_b64_tr_b16 v[182:183], v194 offset:20992\n"
    "v_add_u32 v193, 65536, v1\n"
    "v_mfma_f32_16x16x32_bf16 v[64:67], v[16:19], a[8:11], v[36:39]\n"
    "v_mfma_f32_16x16x32_bf16 v[68:71], v[16:19], a[12:15], v[36:39]\n"
    "v_mfma_f32_16x16x32_bf16 v[56:59], v[20:23], a[16:19], v[56:59]\n"
    "ds_read_b64_tr_b16 v[16:17], v194 offset:24576\n"
    "v_mul_f32 v72, v40, v40\n"
    "v_mul_f32 v73, v41, v41\n"
    "v_mfma_f32_16x16x32_bf16 v[60:63], v[20:23], a[20:23], v[60:63]\n"
    "ds_read_b64_tr_b16 v[18:19], v194 offset:25088\n"
    "v_mul_f32 v74, v42, v42\n"
    "v_mul_f32 v75, v43, v43\n"
    "v_mfma_f32_16x16x32_bf16 v[64:67], v[20:23], a[24:27], v[64:67]\n"
    "buffer_load_dwordx4 v7, s[16:19], s48 offen offset:1024 lds\n"
    "v_fmaak_f32 v72, v72, v15, 0xc0135761\n"
    "v_fmaak_f32 v73, v73, v15, 0xc0135761\n"
    "v_mfma_f32_16x16x32_bf16 v[68:71], v[20:23], a[28:31], v[68:71]\n"
    "v_fmaak_f32 v74, v74, v15, 0xc0135761\n"
    "v_fmaak_f32 v75, v75, v15, 0xc0135761\n"
    "s_waitcnt lgkmcnt(6)\n"
    "v_mfma_f32_16x16x32_bf16 v[56:59], v[24:27], a[32:35], v[56:59]\n"
    "ds_read_b64_tr_b16 v[20:21], v194 offset:28672\n"
    "v_mul_f32 v72, v40, v72\n"
    "v_mul_f32 v73, v41, v73\n"
    "v_mfma_f32_16x16x32_bf16 v[60:63], v[24:27], a[36:39], v[60:63]\n"
    "ds_read_b64_tr_b16 v[22:23], v194 offset:29184\n"
    "v_mul_f32 v74, v42, v74\n"
    "v_mul_f32 v75, v43, v75\n"
    "v_mfma_f32_16x16x32_bf16 v[64:67], v[24:27], a[40:43], v[64:67]\n"
    "v_exp_f32 v72, v72\n"
    "v_exp_f32 v73, v73\n"
    "v_mfma_f32_16x16x32_bf16 v[68:71], v[24:27], a[44:47], v[68:71]\n"
    "v_exp_f32 v74, v74\n"
    "v_mfma_f32_16x16x32_bf16 v[56:59], v[28:31], a[48:51], v[56:59]\n"
    "s_add_u32 m0, m0, 2048\n"
    "s_add_u32 s48, s48, 147456\n"
    "buffer_load_dwordx4 v6, s[16:19], s48 offen offset:0 lds\n"
    "ds_read_b64_tr_b16 v[24:25], v194 offset:32768\n"
    "v_exp_f32 v75, v75\n"
    "v_add_f32 v72, 1.0, v72\n"
    "v_mfma_f32_16x16x32_bf16 v[60:63], v[28:31], a[52:55], v[60:63]\n"
    "ds_read_b64_tr_b16 v[26:27], v194 offset:33280\n"
    "v_add_f32 v73, 1.0, v73\n"
    "v_add_f32 v74, 1.0, v74\n"
    "v_mfma_f32_16x16x32_bf16 v[64:67], v[28:31], a[56:59], v[64:67]\n"
    "v_add_f32 v75, 1.0, v75\n"
    "v_rcp_f32 v72, v72\n"
    "v_mfma_f32_16x16x32_bf16 v[68:71], v[28:31], a[60:63], v[68:71]\n"
    "v_rcp_f32 v73, v73\n"
    "s_waitcnt lgkmcnt(6)\n"
    "v_mfma_f32_16x16x32_bf16 v[56:59], v[176:179], a[64:67], v[56:59]\n"
    "ds_read_b64_tr_b16 v[28:29], v194 offset:36864\n"
    "v_rcp_f32 v74, v74\n"
    "v_rcp_f32 v75, v75\n"
    "v_mfma_f32_16x16x32_bf16 v[60:63], v[176:179], a[68:71], v[60:63]\n"
    "ds_read_b64_tr_b16 v[30:31], v194 offset:37376\n"
    "v_mul_f32 v72, v40, v72\n"
    "v_mul_f32 v73, v41, v73\n"
    "v_mfma_f32_16x16x32_bf16 v[64:67], v[176:179], a[72:75], v[64:67]\n"
    "buffer_load_dwordx4 v7, s[16:19], s48 offen offset:1024 lds\n"
    "v_mul_f32 v74, v42, v74\n"
    "v_mul_f32 v75, v43, v75\n"
    "v_mfma_f32_16x16x32_bf16 v[68:71], v[176:179], a[76:79], v[68:71]\n"
    "v_cvt_pk_bf16_f32 v128, v72, v73\n"
    "v_mfma_f32_16x16x32_bf16 v[56:59], v[180:183], a[80:83], v[56:59]\n"
    "ds_read_b64_tr_b16 v[176:177], v194 offset:40960\n"
    "v_cvt_pk_bf16_f32 v129, v74, v75\n"
    "v_mul_f32 v76, v44, v44\n"
    "v_mfma_f32_16x16x32_bf16 v[60:63], v[180:183], a[84:87], v[60:63]\n"
    "ds_read_b64_tr_b16 v[178:179], v194 offset:41472\n"
    "v_mul_f32 v77, v45, v45\n"
    "v_mul_f32 v78, v46, v46\n"
    "v_mfma_f32_16x16x32_bf16 v[64:67], v[180:183], a[88:91], v[64:67]\n"
    "v_mul_f32 v79, v47, v47\n"
    "v_fmaak_f32 v76, v76, v15, 0xc0135761\n"
    "v_mfma_f32_16x16x32_bf16 v[68:71], v[180:183], a[92:95], v[68:71]\n"
    "v_fmaak_f32 v77, v77, v15, 0xc0135761\n"
    "s_waitcnt lgkmcnt(6)\n"
    "v_mfma_f32_16x16x32_bf16 v[56:59], v[16:19], a[96:99], v[56:59]\n"
    "s_add_u32 m0, m0, 2048\n"
    "s_add_u32 s48, s48, 147456\n"
    "buffer_load_dwordx4 v6, s[16:19], s48 offen offset:0 lds\n"
    "ds_read_b64_tr_b16 v[180:181], v194 offset:45056\n"
    "v_fmaak_f32 v78, v78, v15, 0xc0135761\n"
    "v_fmaak_f32 v79, v79, v15, 0xc0135761\n"
    "v_mfma_f32_16x16x32_bf16 v[60:63], v[16:19], a[100:103], v[60:63]\n"
    "ds_read_b64_tr_b16 v[182:183], v194 offset:45568\n"
    "v_mul_f32 v76, v44, v76\n"
    "v_mul_f32 v77, v45, v77\n"
    "v_mfma_f32_16x16x32_bf16 v[64:67], v[16:19], a[104:107], v[64:67]\n"
    "v_mul_f32 v78, v46, v78\n"
    "v_mul_f32 v79, v47, v79\n"
    "v_mfma_f32_16x16x32_bf16 v[68:71], v[16:19], a[108:111], v[68:71]\n"
    "v_exp_f32 v76, v76\n"
    "v_mfma_f32_16x16x32_bf16 v[56:59], v[20:23], a[112:115], v[56:59]\n"
    "ds_read_b64_tr_b16 v[16:17], v194 offset:49152\n"
    "v_exp_f32 v77, v77\n"
    "v_exp_f32 v78, v78\n"
    "v_mfma_f32_16x16x32_bf16 v[60:63], v[20:23], a[116:119], v[60:63]\n"
    "ds_read_b64_tr_b16 v[18:19], v194 offset:49664\n"
    "v_exp_f32 v79, v79\n"
    "v_add_f32 v76, 1.0, v76\n"
    "v_mfma_f32_16x16x32_bf16 v[64:67], v[20:23], a[120:123], v[64:67]\n"
    "buffer_load_dwordx4 v7, s[16:19], s48 offen offset:1024 lds\n"
    "v_add_f32 v77, 1.0, v77\n"
    "v_add_f32 v78, 1.0, v78\n"
    "v_mfma_f32_16x16x32_bf16 v[68:71], v[20:23], a[124:127], v[68:71]\n"
    "v_add_f32 v79, 1.0, v79\n"
    "s_waitcnt lgkmcnt(6)\n"
    "v_mfma_f32_16x16x32_bf16 v[56:59], v[24:27], a[128:131], v[56:59]\n"
    "ds_read_b64_tr_b16 v[20:21], v194 offset:53248\n"
    "ds_read_b128 v[32:35], v14 offset:1024\n"
    "v_rcp_f32 v76, v76\n"
    "v_mfma_f32_16x16x32_bf16 v[60:63], v[24:27], a[132:135], v[60:63]\n"
    "ds_read_b64_tr_b16 v[22:23], v194 offset:53760\n"
    "v_rcp_f32 v77, v77\n"
    "v_rcp_f32 v78, v78\n"
    "v_mfma_f32_16x16x32_bf16 v[64:67], v[24:27], a[136:139], v[64:67]\n"
    "v_rcp_f32 v79, v79\n"
    "v_mul_f32 v76, v44, v76\n"
    "v_mfma_f32_16x16x32_bf16 v[68:71], v[24:27], a[140:143], v[68:71]\n"
    "v_mul_f32 v77, v45, v77\n"
    "v_mul_f32 v78, v46, v78\n"
    "v_mfma_f32_16x16x32_bf16 v[56:59], v[28:31], a[144:147], v[56:59]\n"
    "ds_read_b64_tr_b16 v[24:25], v194 offset:57344\n"
    "v_mul_f32 v79, v47, v79\n"
    "v_cvt_pk_bf16_f32 v132, v76, v77\n"
    "v_mfma_f32_16x16x32_bf16 v[60:63], v[28:31], a[148:151], v[60:63]\n"
    "ds_read_b64_tr_b16 v[26:27], v194 offset:57856\n"
    "v_cvt_pk_bf16_f32 v133, v78, v79\n"
    "v_mul_f32 v72, v48, v48\n"
    "v_mfma_f32_16x16x32_bf16 v[64:67], v[28:31], a[152:155], v[64:67]\n"
    "v_mul_f32 v73, v49, v49\n"
    "v_mul_f32 v74, v50, v50\n"
    "v_mfma_f32_16x16x32_bf16 v[68:71], v[28:31], a[156:159], v[68:71]\n"
    "v_mul_f32 v75, v51, v51\n"
    "s_waitcnt lgkmcnt(7)\n"
    "v_mfma_f32_16x16x32_bf16 v[56:59], v[176:179], a[160:163], v[56:59]\n"
    "ds_read_b64_tr_b16 v[28:29], v194 offset:61440\n"
    "v_fmaak_f32 v72, v72, v15, 0xc0135761\n"
    "v_fmaak_f32 v73, v73, v15, 0xc0135761\n"
    "v_mfma_f32_16x16x32_bf16 v[60:63], v[176:179], a[164:167], v[60:63]\n"
    "ds_read_b64_tr_b16 v[30:31], v194 offset:61952\n"
    "v_fmaak_f32 v74, v74, v15, 0xc0135761\n"
    "v_fmaak_f32 v75, v75, v15, 0xc0135761\n"
    "v_mfma_f32_16x16x32_bf16 v[64:67], v[176:179], a[168:171], v[64:67]\n"
    "v_mul_f32 v72, v48, v72\n"
    "v_mul_f32 v73, v49, v73\n"
    "v_mfma_f32_16x16x32_bf16 v[68:71], v[176:179], a[172:175], v[68:71]\n"
    "v_mul_f32 v74, v50, v74\n"
    "v_mfma_f32_16x16x32_bf16 v[56:59], v[180:183], a[176:179], v[56:59]\n"
    "ds_read_b64_tr_b16 v[176:177], v195 offset:0\n"
    "v_mul_f32 v75, v51, v75\n"
    "v_exp_f32 v72, v72\n"
    "v_mfma_f32_16x16x32_bf16 v[60:63], v[180:183], a[180:183], v[60:63]\n"
    "ds_read_b64_tr_b16 v[178:179], v195 offset:512\n"
    "v_exp_f32 v73, v73\n"
    "v_exp_f32 v74, v74\n"
    "v_mfma_f32_16x16x32_bf16 v[64:67], v[180:183], a[184:187], v[64:67]\n"
    "v_exp_f32 v75, v75\n"
    "v_add_f32 v72, 1.0, v72\n"
    "v_mfma_f32_16x16x32_bf16 v[68:71], v[180:183], a[188:191], v[68:71]\n"
    "v_add_f32 v73, 1.0, v73\n"
    "s_waitcnt lgkmcnt(6)\n"
    "v_mfma_f32_16x16x32_bf16 v[56:59], v[16:19], a[192:195], v[56:59]\n"
    "ds_read_b64_tr_b16 v[180:181], v195 offset:4096\n"
    "v_add_f32 v74, 1.0, v74\n"
    "v_add_f32 v75, 1.0, v75\n"
    "v_mfma_f32_16x16x32_bf16 v[60:63], v[16:19], a[196:199], v[60:63]\n"
    "ds_read_b64_tr_b16 v[182:183], v195 offset:4608\n"
    "v_rcp_f32 v72, v72\n"
    "v_rcp_f32 v73, v73\n"
    "v_mfma_f32_16x16x32_bf16 v[64:67], v[16:19], a[200:203], v[64:67]\n"
    "v_rcp_f32 v74, v74\n"
    "v_rcp_f32 v75, v75\n"
    "v_mfma_f32_16x16x32_bf16 v[68:71], v[16:19], a[204:207], v[68:71]\n"
    "v_mul_f32 v72, v48, v72\n"
    "v_mfma_f32_16x16x32_bf16 v[56:59], v[20:23], a[208:211], v[56:59]\n"
    "v_mul_f32 v73, v49, v73\n"
    "v_mul_f32 v74, v50, v74\n"
    "v_mfma_f32_16x16x32_bf16 v[60:63], v[20:23], a[212:215], v[60:63]\n"
    "v_mul_f32 v75, v51, v75\n"
    "v_cvt_pk_bf16_f32 v136, v72, v73\n"
    "v_mfma_f32_16x16x32_bf16 v[64:67], v[20:23], a[216:219], v[64:67]\n"
    "v_cvt_pk_bf16_f32 v137, v74, v75\n"
    "v_mul_f32 v76, v52, v52\n"
    "v_mfma_f32_16x16x32_bf16 v[68:71], v[20:23], a[220:223], v[68:71]\n"
    "v_mul_f32 v77, v53, v53\n"
    "s_waitcnt lgkmcnt(4)\n"
    "v_mfma_f32_16x16x32_bf16 v[56:59], v[24:27], a[224:227], v[56:59]\n"
    "v_mul_f32 v78, v54, v54\n"
    "v_mul_f32 v79, v55, v55\n"
    "v_mfma_f32_16x16x32_bf16 v[60:63], v[24:27], a[228:231], v[60:63]\n"
    "v_fmaak_f32 v76, v76, v15, 0xc0135761\n"
    "v_fmaak_f32 v77, v77, v15, 0xc0135761\n"
    "v_mfma_f32_16x16x32_bf16 v[64:67], v[24:27], a[232:235], v[64:67]\n"
    "v_fmaak_f32 v78, v78, v15, 0xc0135761\n"
    "v_fmaak_f32 v79, v79, v15, 0xc0135761\n"
    "v_mfma_f32_16x16x32_bf16 v[68:71], v[24:27], a[236:239], v[68:71]\n"
    "v_mul_f32 v76, v52, v76\n"
    "s_waitcnt lgkmcnt(0)\n"
    "s_waitcnt vmcnt(0)\n"
    "s_barrier\n"
    "v_mfma_f32_16x16x32_bf16 v[56:59], v[28:31], a[240:243], v[56:59]\n"
    "ds_read_b64_tr_b16 v[16:17], v192 offset:0\n"
    "v_mul_f32 v77, v53, v77\n"
    "v_mul_f32 v78, v54, v78\n"
    "v_mfma_f32_16x16x32_bf16 v[60:63], v[28:31], a[244:247], v[60:63]\n"
    "ds_read_b64_tr_b16 v[18:19], v192 offset:512\n"
    "v_mul_f32 v79, v55, v79\n"
    "v_exp_f32 v76, v76\n"
    "v_mfma_f32_16x16x32_bf16 v[64:67], v[28:31], a[248:251], v[64:67]\n"
    "ds_read_b64_tr_b16 v[20:21], v192 offset:4096\n"
    "v_exp_f32 v77, v77\n"
    "v_exp_f32 v78, v78\n"
    "v_mfma_f32_16x16x32_bf16 v[68:71], v[28:31], a[252:255], v[68:71]\n"
    "ds_read_b64_tr_b16 v[22:23], v192 offset:4608\n"
    "v_exp_f32 v79, v79\n"
    "v_mfma_f32_16x16x32_bf16 v[56:59], v[176:179], v[224:227], v[56:59]\n"
    "ds_read_b64_tr_b16 v[24:25], v192 offset:8192\n"
    "v_add_f32 v76, 1.0, v76\n"
    "v_add_f32 v77, 1.0, v77\n"
    "v_mfma_f32_16x16x32_bf16 v[60:63], v[176:179], v[228:231], v[60:63]\n"
    "ds_read_b64_tr_b16 v[26:27], v192 offset:8704\n"
    "v_add_f32 v78, 1.0, v78\n"
    "v_add_f32 v79, 1.0, v79\n"
    "v_mfma_f32_16x16x32_bf16 v[64:67], v[176:179], v[232:235], v[64:67]\n"
    "ds_read_b64_tr_b16 v[28:29], v192 offset:12288\n"
    "v_rcp_f32 v76, v76\n"
    "v_rcp_f32 v77, v77\n"
    "v_mfma_f32_16x16x32_bf16 v[68:71], v[176:179], v[236:239], v[68:71]\n"
    "ds_read_b64_tr_b16 v[30:31], v192 offset:12800\n"
    "v_rcp_f32 v78, v78\n"
    "v_mfma_f32_16x16x32_bf16 v[56:59], v[180:183], v[240:243], v[56:59]\n"
    "ds_read_b64_tr_b16 v[176:177], v192 offset:16384\n"
    "v_rcp_f32 v79, v79\n"
    "v_mul_f32 v76, v52, v76\n"
    "v_mfma_f32_16x16x32_bf16 v[60:63], v[180:183], v[244:247], v[60:63]\n"
    "ds_read_b64_tr_b16 v[178:179], v192 offset:16896\n"
    "v_mul_f32 v77, v53, v77\n"
    "v_mul_f32 v78, v54, v78\n"
    "v_mfma_f32_16x16x32_bf16 v[64:67], v[180:183], v[248:251], v[64:67]\n"
    "v_mul_f32 v79, v55, v79\n"
    "v_cvt_pk_bf16_f32 v140, v76, v77\n"
    "v_mfma_f32_16x16x32_bf16 v[68:71], v[180:183], v[252:255], v[68:71]\n"
    "v_cvt_pk_bf16_f32 v141, v78, v79\n"
    "s_waitcnt lgkmcnt(6)\n"
    "v_mfma_f32_16x16x32_bf16 v[40:43], v[16:19], a[0:3], v[32:35]\n"
    "ds_read_b64_tr_b16 v[180:181], v192 offset:20480\n"
    "v_add_u32 v194, 0, v2\n"
    "v_mfma_f32_16x16x32_bf16 v[44:47], v[16:19], a[4:7], v[32:35]\n"
    "ds_read_b64_tr_b16 v[182:183], v192 offset:20992\n"
    "v_add_u32 v195, 65536, v2\n"
    "v_mfma_f32_16x16x32_bf16 v[48:51], v[16:19], a[8:11], v[32:35]\n"
    "v_mfma_f32_16x16x32_bf16 v[52:55], v[16:19], a[12:15], v[32:35]\n"
    "s_add_u32 s40, s14, 5\n"
    "s_cmp_ge_u32 s40, 6\n"
    "s_cselect_b32 s41, 6, 0\n"
    "s_sub_u32 s40, s40, s41\n"
    "s_lshl_b32 s40, s40, 7\n"
    "s_mul_i32 s41, s9, 18432\n"
    "s_add_u32 m0, s41, 73728\n"
    "s_add_u32 s48, s49, s40\n"
    "buffer_load_dwordx4 v6, s[16:19], s48 offen offset:0 lds\n"
    "v_mfma_f32_16x16x32_bf16 v[40:43], v[20:23], a[16:19], v[40:43]\n"
    "ds_read_b64_tr_b16 v[16:17], v192 offset:24576\n"
    "v_mul_f32 v72, v56, v56\n"
    "v_mul_f32 v73, v57, v57\n"
    "v_mfma_f32_16x16x32_bf16 v[44:47], v[20:23], a[20:23], v[44:47]\n"
    "ds_read_b64_tr_b16 v[18:19], v192 offset:25088\n"
    "v_mul_f32 v74, v58, v58\n"
    "v_mul_f32 v75, v59, v59\n"
    "v_mfma_f32_16x16x32_bf16 v[48:51], v[20:23], a[24:27], v[48:51]\n"
    "v_fmaak_f32 v72, v72, v15, 0xc0135761\n"
    "v_fmaak_f32 v73, v73, v15, 0xc0135761\n"
    "v_mfma_f32_16x16x32_bf16 v[52:55], v[20:23], a[28:31], v[52:55]\n"
    "v_fmaak_f32 v74, v74, v15, 0xc0135761\n"
    "v_fmaak_f32 v75, v75, v15, 0xc0135761\n"
    "s_waitcnt lgkmcnt(6)\n"
    "v_mfma_f32_16x16x32_bf16 v[40:43], v[24:27], a[32:35], v[40:43]\n"
    "ds_read_b64_tr_b16 v[20:21], v192 offset:28672\n"
    "v_mul_f32 v72, v56, v72\n"
    "v_mul_f32 v73, v57, v73\n"
    "v_mfma_f32_16x16x32_bf16 v[44:47], v[24:27], a[36:39], v[44:47]\n"
    "buffer_load_dwordx4 v7, s[16:19], s48 offen offset:1024 lds\n"
    "ds_read_b64_tr_b16 v[22:23], v192 offset:29184\n"
    "v_mul_f32 v74, v58, v74\n"
    "v_mul_f32 v75, v59, v75\n"
    "v_mfma_f32_16x16x32_bf16 v[48:51], v[24:27], a[40:43], v[48:51]\n"
    "v_exp_f32 v72, v72\n"
    "v_exp_f32 v73, v73\n"
    "v_mfma_f32_16x16x32_bf16 v[52:55], v[24:27], a[44:47], v[52:55]\n"
    "v_exp_f32 v74, v74\n"
    "v_mfma_f32_16x16x32_bf16 v[40:43], v[28:31], a[48:51], v[40:43]\n"
    "ds_read_b64_tr_b16 v[24:25], v192 offset:32768\n"
    "v_exp_f32 v75, v75\n"
    "v_add_f32 v72, 1.0, v72\n"
    "v_mfma_f32_16x16x32_bf16 v[44:47], v[28:31], a[52:55], v[44:47]\n"
    "ds_read_b64_tr_b16 v[26:27], v192 offset:33280\n"
    "v_add_f32 v73, 1.0, v73\n"
    "v_add_f32 v74, 1.0, v74\n"
    "v_mfma_f32_16x16x32_bf16 v[48:51], v[28:31], a[56:59], v[48:51]\n"
    "v_add_f32 v75, 1.0, v75\n"
    "v_rcp_f32 v72, v72\n"
    "v_mfma_f32_16x16x32_bf16 v[52:55], v[28:31], a[60:63], v[52:55]\n"
    "s_add_u32 m0, m0, 2048\n"
    "s_add_u32 s48, s48, 147456\n"
    "buffer_load_dwordx4 v6, s[16:19], s48 offen offset:0 lds\n"
    "v_rcp_f32 v73, v73\n"
    "s_waitcnt lgkmcnt(6)\n"
    "v_mfma_f32_16x16x32_bf16 v[40:43], v[176:179], a[64:67], v[40:43]\n"
    "ds_read_b64_tr_b16 v[28:29], v192 offset:36864\n"
    "v_rcp_f32 v74, v74\n"
    "v_rcp_f32 v75, v75\n"
    "v_mfma_f32_16x16x32_bf16 v[44:47], v[176:179], a[68:71], v[44:47]\n"
    "ds_read_b64_tr_b16 v[30:31], v192 offset:37376\n"
    "v_mul_f32 v72, v56, v72\n"
    "v_mul_f32 v73, v57, v73\n"
    "v_mfma_f32_16x16x32_bf16 v[48:51], v[176:179], a[72:75], v[48:51]\n"
    "v_mul_f32 v74, v58, v74\n"
    "v_mul_f32 v75, v59, v75\n"
    "v_mfma_f32_16x16x32_bf16 v[52:55], v[176:179], a[76:79], v[52:55]\n"
    "v_cvt_pk_bf16_f32 v130, v72, v73\n"
    "v_mfma_f32_16x16x32_bf16 v[40:43], v[180:183], a[80:83], v[40:43]\n"
    "ds_read_b64_tr_b16 v[176:177], v192 offset:40960\n"
    "v_cvt_pk_bf16_f32 v131, v74, v75\n"
    "v_mul_f32 v76, v60, v60\n"
    "v_mfma_f32_16x16x32_bf16 v[44:47], v[180:183], a[84:87], v[44:47]\n"
    "buffer_load_dwordx4 v7, s[16:19], s48 offen offset:1024 lds\n"
    "ds_read_b64_tr_b16 v[178:179], v192 offset:41472\n"
    "v_mul_f32 v77, v61, v61\n"
    "v_mul_f32 v78, v62, v62\n"
    "v_mfma_f32_16x16x32_bf16 v[48:51], v[180:183], a[88:91], v[48:51]\n"
    "v_mul_f32 v79, v63, v63\n"
    "v_fmaak_f32 v76, v76, v15, 0xc0135761\n"
    "v_mfma_f32_16x16x32_bf16 v[52:55], v[180:183], a[92:95], v[52:55]\n"
    "v_fmaak_f32 v77, v77, v15, 0xc0135761\n"
    "s_waitcnt lgkmcnt(6)\n"
    "v_mfma_f32_16x16x32_bf16 v[40:43], v[16:19], a[96:99], v[40:43]\n"
    "ds_read_b64_tr_b16 v[180:181], v192 offset:45056\n"
    "v_fmaak_f32 v78, v78, v15, 0xc0135761\n"
    "v_fmaak_f32 v79, v79, v15, 0xc0135761\n"
    "v_mfma_f32_16x16x32_bf16 v[44:47], v[16:19], a[100:103], v[44:47]\n"
    "ds_read_b64_tr_b16 v[182:183], v192 offset:45568\n"
    "v_mul_f32 v76, v60, v76\n"
    "v_mul_f32 v77, v61, v77\n"
    "v_mfma_f32_16x16x32_bf16 v[48:51], v[16:19], a[104:107], v[48:51]\n"
    "v_mul_f32 v78, v62, v78\n"
    "v_mul_f32 v79, v63, v79\n"
    "v_mfma_f32_16x16x32_bf16 v[52:55], v[16:19], a[108:111], v[52:55]\n"
    "s_add_u32 m0, m0, 2048\n"
    "s_add_u32 s48, s48, 147456\n"
    "buffer_load_dwordx4 v6, s[16:19], s48 offen offset:0 lds\n"
    "v_exp_f32 v76, v76\n"
    "v_mfma_f32_16x16x32_bf16 v[40:43], v[20:23], a[112:115], v[40:43]\n"
    "ds_read_b64_tr_b16 v[16:17], v192 offset:49152\n"
    "v_exp_f32 v77, v77\n"
    "v_exp_f32 v78, v78\n"
    "v_mfma_f32_16x16x32_bf16 v[44:47], v[20:23], a[116:119], v[44:47]\n"
    "ds_read_b64_tr_b16 v[18:19], v192 offset:49664\n"
    "v_exp_f32 v79, v79\n"
    "v_add_f32 v76, 1.0, v76\n"
    "v_mfma_f32_16x16x32_bf16 v[48:51], v[20:23], a[120:123], v[48:51]\n"
    "v_add_f32 v77, 1.0, v77\n"
    "v_add_f32 v78, 1.0, v78\n"
    "v_mfma_f32_16x16x32_bf16 v[52:55], v[20:23], a[124:127], v[52:55]\n"
    "v_add_f32 v79, 1.0, v79\n"
    "s_waitcnt lgkmcnt(6)\n"
    "v_mfma_f32_16x16x32_bf16 v[40:43], v[24:27], a[128:131], v[40:43]\n"
    "ds_read_b64_tr_b16 v[20:21], v192 offset:53248\n"
    "ds_read_b128 v[36:39], v14 offset:1088\n"
    "v_rcp_f32 v76, v76\n"
    "v_mfma_f32_16x16x32_bf16 v[44:47], v[24:27], a[132:135], v[44:47]\n"
    "buffer_load_dwordx4 v7, s[16:19], s48 offen offset:1024 lds\n"
    "ds_read_b64_tr_b16 v[22:23], v192 offset:53760\n"
    "v_rcp_f32 v77, v77\n"
    "v_rcp_f32 v78, v78\n"
    "v_mfma_f32_16x16x32_bf16 v[48:51], v[24:27], a[136:139], v[48:51]\n"
    "v_rcp_f32 v79, v79\n"
    "v_mul_f32 v76, v60, v76\n"
    "v_mfma_f32_16x16x32_bf16 v[52:55], v[24:27], a[140:143], v[52:55]\n"
    "v_mul_f32 v77, v61, v77\n"
    "v_mul_f32 v78, v62, v78\n"
    "v_mfma_f32_16x16x32_bf16 v[40:43], v[28:31], a[144:147], v[40:43]\n"
    "ds_read_b64_tr_b16 v[24:25], v192 offset:57344\n"
    "v_mul_f32 v79, v63, v79\n"
    "v_cvt_pk_bf16_f32 v134, v76, v77\n"
    "v_mfma_f32_16x16x32_bf16 v[44:47], v[28:31], a[148:151], v[44:47]\n"
    "ds_read_b64_tr_b16 v[26:27], v192 offset:57856\n"
    "v_cvt_pk_bf16_f32 v135, v78, v79\n"
    "v_mul_f32 v72, v64, v64\n"
    "v_mfma_f32_16x16x32_bf16 v[48:51], v[28:31], a[152:155], v[48:51]\n"
    "v_mul_f32 v73, v65, v65\n"
    "v_mul_f32 v74, v66, v66\n"
    "v_mfma_f32_16x16x32_bf16 v[52:55], v[28:31], a[156:159], v[52:55]\n"
    "s_add_u32 m0, m0, 2048\n"
    "s_add_u32 s48, s48, 147456\n"
    "buffer_load_dwordx4 v6, s[16:19], s48 offen offset:0 lds\n"
    "v_mul_f32 v75, v67, v67\n"
    "s_waitcnt lgkmcnt(7)\n"
    "v_mfma_f32_16x16x32_bf16 v[40:43], v[176:179], a[160:163], v[40:43]\n"
    "ds_read_b64_tr_b16 v[28:29], v192 offset:61440\n"
    "v_fmaak_f32 v72, v72, v15, 0xc0135761\n"
    "v_fmaak_f32 v73, v73, v15, 0xc0135761\n"
    "v_mfma_f32_16x16x32_bf16 v[44:47], v[176:179], a[164:167], v[44:47]\n"
    "ds_read_b64_tr_b16 v[30:31], v192 offset:61952\n"
    "v_fmaak_f32 v74, v74, v15, 0xc0135761\n"
    "v_fmaak_f32 v75, v75, v15, 0xc0135761\n"
    "v_mfma_f32_16x16x32_bf16 v[48:51], v[176:179], a[168:171], v[48:51]\n"
    "v_mul_f32 v72, v64, v72\n"
    "v_mul_f32 v73, v65, v73\n"
    "v_mfma_f32_16x16x32_bf16 v[52:55], v[176:179], a[172:175], v[52:55]\n"
    "v_mul_f32 v74, v66, v74\n"
    "v_mfma_f32_16x16x32_bf16 v[40:43], v[180:183], a[176:179], v[40:43]\n"
    "ds_read_b64_tr_b16 v[176:177], v193 offset:0\n"
    "v_mul_f32 v75, v67, v75\n"
    "v_exp_f32 v72, v72\n"
    "v_mfma_f32_16x16x32_bf16 v[44:47], v[180:183], a[180:183], v[44:47]\n"
    "buffer_load_dwordx4 v7, s[16:19], s48 offen offset:1024 lds\n"
    "ds_read_b64_tr_b16 v[178:179], v193 offset:512\n"
    "v_exp_f32 v73, v73\n"
    "v_exp_f32 v74, v74\n"
    "v_mfma_f32_16x16x32_bf16 v[48:51], v[180:183], a[184:187], v[48:51]\n"
    "v_exp_f32 v75, v75\n"
    "v_add_f32 v72, 1.0, v72\n"
    "v_mfma_f32_16x16x32_bf16 v[52:55], v[180:183], a[188:191], v[52:55]\n"
    "v_add_f32 v73, 1.0, v73\n"
    "s_waitcnt lgkmcnt(6)\n"
    "v_mfma_f32_16x16x32_bf16 v[40:43], v[16:19], a[192:195], v[40:43]\n"
    "ds_read_b64_tr_b16 v[180:181], v193 offset:4096\n"
    "v_add_f32 v74, 1.0, v74\n"
    "v_add_f32 v75, 1.0, v75\n"
    "v_mfma_f32_16x16x32_bf16 v[44:47], v[16:19], a[196:199], v[44:47]\n"
    "ds_read_b64_tr_b16 v[182:183], v193 offset:4608\n"
    "v_rcp_f32 v72, v72\n"
    "v_rcp_f32 v73, v73\n"
    "v_mfma_f32_16x16x32_bf16 v[48:51], v[16:19], a[200:203], v[48:51]\n"
    "v_rcp_f32 v74, v74\n"
    "v_rcp_f32 v75, v75\n"
    "v_mfma_f32_16x16x32_bf16 v[52:55], v[16:19], a[204:207], v[52:55]\n"
    "s_add_u32 m0, m0, 2048\n"
    "s_add_u32 s48, s48, 147456\n"
    "buffer_load_dwordx4 v6, s[16:19], s48 offen offset:0 lds\n"
    "v_mul_f32 v72, v64, v72\n"
    "v_mfma_f32_16x16x32_bf16 v[40:43], v[20:23], a[208:211], v[40:43]\n"
    "ds_read_b64_tr_b16 v[16:17], v194 offset:0\n"
    "v_mul_f32 v73, v65, v73\n"
    "v_mul_f32 v74, v66, v74\n"
    "v_mfma_f32_16x16x32_bf16 v[44:47], v[20:23], a[212:215], v[44:47]\n"
    "ds_read_b64_tr_b16 v[18:19], v194 offset:512\n"
    "v_mul_f32 v75, v67, v75\n"
    "v_cvt_pk_bf16_f32 v138, v72, v73\n"
    "v_mfma_f32_16x16x32_bf16 v[48:51], v[20:23], a[216:219], v[48:51]\n"
    "v_cvt_pk_bf16_f32 v139, v74, v75\n"
    "v_mul_f32 v76, v68, v68\n"
    "v_mfma_f32_16x16x32_bf16 v[52:55], v[20:23], a[220:223], v[52:55]\n"
    "v_mul_f32 v77, v69, v69\n"
    "s_waitcnt lgkmcnt(6)\n"
    "v_mfma_f32_16x16x32_bf16 v[40:43], v[24:27], a[224:227], v[40:43]\n"
    "ds_read_b64_tr_b16 v[20:21], v194 offset:4096\n"
    "v_mul_f32 v78, v70, v70\n"
    "v_mul_f32 v79, v71, v71\n"
    "v_mfma_f32_16x16x32_bf16 v[44:47], v[24:27], a[228:231], v[44:47]\n"
    "buffer_load_dwordx4 v7, s[16:19], s48 offen offset:1024 lds\n"
    "ds_read_b64_tr_b16 v[22:23], v194 offset:4608\n"
    "v_fmaak_f32 v76, v76, v15, 0xc0135761\n"
    "v_fmaak_f32 v77, v77, v15, 0xc0135761\n"
    "v_mfma_f32_16x16x32_bf16 v[48:51], v[24:27], a[232:235], v[48:51]\n"
    "v_fmaak_f32 v78, v78, v15, 0xc0135761\n"
    "v_fmaak_f32 v79, v79, v15, 0xc0135761\n"
    "v_mfma_f32_16x16x32_bf16 v[52:55], v[24:27], a[236:239], v[52:55]\n"
    "v_mul_f32 v76, v68, v76\n"
    "v_mfma_f32_16x16x32_bf16 v[40:43], v[28:31], a[240:243], v[40:43]\n"
    "ds_read_b64_tr_b16 v[24:25], v194 offset:8192\n"
    "v_mul_f32 v77, v69, v77\n"
    "v_mul_f32 v78, v70, v78\n"
    "v_mfma_f32_16x16x32_bf16 v[44:47], v[28:31], a[244:247], v[44:47]\n"
    "ds_read_b64_tr_b16 v[26:27], v194 offset:8704\n"
    "v_mul_f32 v79, v71, v79\n"
    "v_exp_f32 v76, v76\n"
    "v_mfma_f32_16x16x32_bf16 v[48:51], v[28:31], a[248:251], v[48:51]\n"
    "v_exp_f32 v77, v77\n"
    "v_exp_f32 v78, v78\n"
    "v_mfma_f32_16x16x32_bf16 v[52:55], v[28:31], a[252:255], v[52:55]\n"
    "s_add_u32 m0, m0, 2048\n"
    "s_add_u32 s48, s48, 147456\n"
    "buffer_load_dwordx4 v6, s[16:19], s48 offen offset:0 lds\n"
    "v_exp_f32 v79, v79\n"
    "s_waitcnt lgkmcnt(6)\n"
    "v_mfma_f32_16x16x32_bf16 v[40:43], v[176:179], v[224:227], v[40:43]\n"
    "ds_read_b64_tr_b16 v[28:29], v194 offset:12288\n"
    "v_add_f32 v76, 1.0, v76\n"
    "v_add_f32 v77, 1.0, v77\n"
    "v_mfma_f32_16x16x32_bf16 v[44:47], v[176:179], v[228:231], v[44:47]\n"
    "ds_read_b64_tr_b16 v[30:31], v194 offset:12800\n"
    "v_add_f32 v78, 1.0, v78\n"
    "v_add_f32 v79, 1.0, v79\n"
    "v_mfma_f32_16x16x32_bf16 v[48:51], v[176:179], v[232:235], v[48:51]\n"
    "v_rcp_f32 v76, v76\n"
    "v_rcp_f32 v77, v77\n"
    "v_mfma_f32_16x16x32_bf16 v[52:55], v[176:179], v[236:239], v[52:55]\n"
    "v_rcp_f32 v78, v78\n"
    "v_mfma_f32_16x16x32_bf16 v[40:43], v[180:183], v[240:243], v[40:43]\n"
    "ds_read_b64_tr_b16 v[176:177], v194 offset:16384\n"
    "v_rcp_f32 v79, v79\n"
    "v_mul_f32 v76, v68, v76\n"
    "v_mfma_f32_16x16x32_bf16 v[44:47], v[180:183], v[244:247], v[44:47]\n"
    "ds_read_b64_tr_b16 v[178:179], v194 offset:16896\n"
    "v_mul_f32 v77, v69, v77\n"
    "v_mul_f32 v78, v70, v78\n"
    "v_mfma_f32_16x16x32_bf16 v[48:51], v[180:183], v[248:251], v[48:51]\n"
    "buffer_load_dwordx4 v7, s[16:19], s48 offen offset:1024 lds\n"
    "v_mul_f32 v79, v71, v79\n"
    "v_cvt_pk_bf16_f32 v142, v76, v77\n"
    "v_mfma_f32_16x16x32_bf16 v[52:55], v[180:183], v[252:255], v[52:55]\n"
    "v_cvt_pk_bf16_f32 v143, v78, v79\n"
    "s_waitcnt lgkmcnt(6)\n"
    "v_mfma_f32_16x16x32_bf16 v[56:59], v[16:19], a[0:3], v[36:39]\n"
    "ds_read_b64_tr_b16 v[180:181], v194 offset:20480\n"
    "v_add_u32 v192, 73728, v1\n"
    "v_mfma_f32_16x16x32_bf16 v[60:63], v[16:19], a[4:7], v[36:39]\n"
    "s_add_u32 m0, m0, 2048\n"
    "s_add_u32 s48, s48, 147456\n"
    "buffer_load_dwordx4 v6, s[16:19], s48 offen offset:0 lds\n"
    "ds_read_b64_tr_b16 v[182:183], v194 offset:20992\n"
    "v_add_u32 v193, 139264, v1\n"
    "v_mfma_f32_16x16x32_bf16 v[64:67], v[16:19], a[8:11], v[36:39]\n"
    "v_mfma_f32_16x16x32_bf16 v[68:71], v[16:19], a[12:15], v[36:39]\n"
    "v_mfma_f32_16x16x32_bf16 v[56:59], v[20:23], a[16:19], v[56:59]\n"
    "ds_read_b64_tr_b16 v[16:17], v194 offset:24576\n"
    "v_mul_f32 v72, v40, v40\n"
    "v_mul_f32 v73, v41, v41\n"
    "v_mfma_f32_16x16x32_bf16 v[60:63], v[20:23], a[20:23], v[60:63]\n"
    "ds_read_b64_tr_b16 v[18:19], v194 offset:25088\n"
    "v_mul_f32 v74, v42, v42\n"
    "v_mul_f32 v75, v43, v43\n"
    "v_mfma_f32_16x16x32_bf16 v[64:67], v[20:23], a[24:27], v[64:67]\n"
    "buffer_load_dwordx4 v7, s[16:19], s48 offen offset:1024 lds\n"
    "v_fmaak_f32 v72, v72, v15, 0xc0135761\n"
    "v_fmaak_f32 v73, v73, v15, 0xc0135761\n"
    "v_mfma_f32_16x16x32_bf16 v[68:71], v[20:23], a[28:31], v[68:71]\n"
    "v_fmaak_f32 v74, v74, v15, 0xc0135761\n"
    "v_fmaak_f32 v75, v75, v15, 0xc0135761\n"
    "s_waitcnt lgkmcnt(6)\n"
    "v_mfma_f32_16x16x32_bf16 v[56:59], v[24:27], a[32:35], v[56:59]\n"
    "ds_read_b64_tr_b16 v[20:21], v194 offset:28672\n"
    "v_mul_f32 v72, v40, v72\n"
    "v_mul_f32 v73, v41, v73\n"
    "v_mfma_f32_16x16x32_bf16 v[60:63], v[24:27], a[36:39], v[60:63]\n"
    "ds_read_b64_tr_b16 v[22:23], v194 offset:29184\n"
    "v_mul_f32 v74, v42, v74\n"
    "v_mul_f32 v75, v43, v75\n"
    "v_mfma_f32_16x16x32_bf16 v[64:67], v[24:27], a[40:43], v[64:67]\n"
    "v_exp_f32 v72, v72\n"
    "v_exp_f32 v73, v73\n"
    "v_mfma_f32_16x16x32_bf16 v[68:71], v[24:27], a[44:47], v[68:71]\n"
    "v_exp_f32 v74, v74\n"
    "v_mfma_f32_16x16x32_bf16 v[56:59], v[28:31], a[48:51], v[56:59]\n"
    "s_add_u32 m0, m0, 2048\n"
    "s_add_u32 s48, s48, 147456\n"
    "buffer_load_dwordx4 v6, s[16:19], s48 offen offset:0 lds\n"
    "ds_read_b64_tr_b16 v[24:25], v194 offset:32768\n"
    "v_exp_f32 v75, v75\n"
    "v_add_f32 v72, 1.0, v72\n"
    "v_mfma_f32_16x16x32_bf16 v[60:63], v[28:31], a[52:55], v[60:63]\n"
    "ds_read_b64_tr_b16 v[26:27], v194 offset:33280\n"
    "v_add_f32 v73, 1.0, v73\n"
    "v_add_f32 v74, 1.0, v74\n"
    "v_mfma_f32_16x16x32_bf16 v[64:67], v[28:31], a[56:59], v[64:67]\n"
    "v_add_f32 v75, 1.0, v75\n"
    "v_rcp_f32 v72, v72\n"
    "v_mfma_f32_16x16x32_bf16 v[68:71], v[28:31], a[60:63], v[68:71]\n"
    "v_rcp_f32 v73, v73\n"
    "s_waitcnt lgkmcnt(6)\n"
    "v_mfma_f32_16x16x32_bf16 v[56:59], v[176:179], a[64:67], v[56:59]\n"
    "ds_read_b64_tr_b16 v[28:29], v194 offset:36864\n"
    "v_rcp_f32 v74, v74\n"
    "v_rcp_f32 v75, v75\n"
    "v_mfma_f32_16x16x32_bf16 v[60:63], v[176:179], a[68:71], v[60:63]\n"
    "ds_read_b64_tr_b16 v[30:31], v194 offset:37376\n"
    "v_mul_f32 v72, v40, v72\n"
    "v_mul_f32 v73, v41, v73\n"
    "v_mfma_f32_16x16x32_bf16 v[64:67], v[176:179], a[72:75], v[64:67]\n"
    "buffer_load_dwordx4 v7, s[16:19], s48 offen offset:1024 lds\n"
    "v_mul_f32 v74, v42, v74\n"
    "v_mul_f32 v75, v43, v75\n"
    "v_mfma_f32_16x16x32_bf16 v[68:71], v[176:179], a[76:79], v[68:71]\n"
    "v_cvt_pk_bf16_f32 v144, v72, v73\n"
    "v_mfma_f32_16x16x32_bf16 v[56:59], v[180:183], a[80:83], v[56:59]\n"
    "ds_read_b64_tr_b16 v[176:177], v194 offset:40960\n"
    "v_cvt_pk_bf16_f32 v145, v74, v75\n"
    "v_mul_f32 v76, v44, v44\n"
    "v_mfma_f32_16x16x32_bf16 v[60:63], v[180:183], a[84:87], v[60:63]\n"
    "ds_read_b64_tr_b16 v[178:179], v194 offset:41472\n"
    "v_mul_f32 v77, v45, v45\n"
    "v_mul_f32 v78, v46, v46\n"
    "v_mfma_f32_16x16x32_bf16 v[64:67], v[180:183], a[88:91], v[64:67]\n"
    "v_mul_f32 v79, v47, v47\n"
    "v_fmaak_f32 v76, v76, v15, 0xc0135761\n"
    "v_mfma_f32_16x16x32_bf16 v[68:71], v[180:183], a[92:95], v[68:71]\n"
    "v_fmaak_f32 v77, v77, v15, 0xc0135761\n"
    "s_waitcnt lgkmcnt(6)\n"
    "v_mfma_f32_16x16x32_bf16 v[56:59], v[16:19], a[96:99], v[56:59]\n"
    "s_add_u32 m0, m0, 2048\n"
    "s_add_u32 s48, s48, 147456\n"
    "buffer_load_dwordx4 v6, s[16:19], s48 offen offset:0 lds\n"
    "ds_read_b64_tr_b16 v[180:181], v194 offset:45056\n"
    "v_fmaak_f32 v78, v78, v15, 0xc0135761\n"
    "v_fmaak_f32 v79, v79, v15, 0xc0135761\n"
    "v_mfma_f32_16x16x32_bf16 v[60:63], v[16:19], a[100:103], v[60:63]\n"
    "ds_read_b64_tr_b16 v[182:183], v194 offset:45568\n"
    "v_mul_f32 v76, v44, v76\n"
    "v_mul_f32 v77, v45, v77\n"
    "v_mfma_f32_16x16x32_bf16 v[64:67], v[16:19], a[104:107], v[64:67]\n"
    "v_mul_f32 v78, v46, v78\n"
    "v_mul_f32 v79, v47, v79\n"
    "v_mfma_f32_16x16x32_bf16 v[68:71], v[16:19], a[108:111], v[68:71]\n"
    "v_exp_f32 v76, v76\n"
    "v_mfma_f32_16x16x32_bf16 v[56:59], v[20:23], a[112:115], v[56:59]\n"
    "ds_read_b64_tr_b16 v[16:17], v194 offset:49152\n"
    "v_exp_f32 v77, v77\n"
    "v_exp_f32 v78, v78\n"
    "v_mfma_f32_16x16x32_bf16 v[60:63], v[20:23], a[116:119], v[60:63]\n"
    "ds_read_b64_tr_b16 v[18:19], v194 offset:49664\n"
    "v_exp_f32 v79, v79\n"
    "v_add_f32 v76, 1.0, v76\n"
    "v_mfma_f32_16x16x32_bf16 v[64:67], v[20:23], a[120:123], v[64:67]\n"
    "buffer_load_dwordx4 v7, s[16:19], s48 offen offset:1024 lds\n"
    "v_add_f32 v77, 1.0, v77\n"
    "v_add_f32 v78, 1.0, v78\n"
    "v_mfma_f32_16x16x32_bf16 v[68:71], v[20:23], a[124:127], v[68:71]\n"
    "v_add_f32 v79, 1.0, v79\n"
    "s_waitcnt lgkmcnt(6)\n"
    "v_mfma_f32_16x16x32_bf16 v[56:59], v[24:27], a[128:131], v[56:59]\n"
    "ds_read_b64_tr_b16 v[20:21], v194 offset:53248\n"
    "ds_read_b128 v[32:35], v14 offset:1280\n"
    "v_rcp_f32 v76, v76\n"
    "v_mfma_f32_16x16x32_bf16 v[60:63], v[24:27], a[132:135], v[60:63]\n"
    "ds_read_b64_tr_b16 v[22:23], v194 offset:53760\n"
    "v_rcp_f32 v77, v77\n"
    "v_rcp_f32 v78, v78\n"
    "v_mfma_f32_16x16x32_bf16 v[64:67], v[24:27], a[136:139], v[64:67]\n"
    "v_rcp_f32 v79, v79\n"
    "v_mul_f32 v76, v44, v76\n"
    "v_mfma_f32_16x16x32_bf16 v[68:71], v[24:27], a[140:143], v[68:71]\n"
    "v_mul_f32 v77, v45, v77\n"
    "v_mul_f32 v78, v46, v78\n"
    "v_mfma_f32_16x16x32_bf16 v[56:59], v[28:31], a[144:147], v[56:59]\n"
    "ds_read_b64_tr_b16 v[24:25], v194 offset:57344\n"
    "v_mul_f32 v79, v47, v79\n"
    "v_cvt_pk_bf16_f32 v148, v76, v77\n"
    "v_mfma_f32_16x16x32_bf16 v[60:63], v[28:31], a[148:151], v[60:63]\n"
    "ds_read_b64_tr_b16 v[26:27], v194 offset:57856\n"
    "v_cvt_pk_bf16_f32 v149, v78, v79\n"
    "v_mul_f32 v72, v48, v48\n"
    "v_mfma_f32_16x16x32_bf16 v[64:67], v[28:31], a[152:155], v[64:67]\n"
    "v_mul_f32 v73, v49, v49\n"
    "v_mul_f32 v74, v50, v50\n"
    "v_mfma_f32_16x16x32_bf16 v[68:71], v[28:31], a[156:159], v[68:71]\n"
    "v_mul_f32 v75, v51, v51\n"
    "s_waitcnt lgkmcnt(7)\n"
    "v_mfma_f32_16x16x32_bf16 v[56:59], v[176:179], a[160:163], v[56:59]\n"
    "ds_read_b64_tr_b16 v[28:29], v194 offset:61440\n"
    "v_fmaak_f32 v72, v72, v15, 0xc0135761\n"
    "v_fmaak_f32 v73, v73, v15, 0xc0135761\n"
    "v_mfma_f32_16x16x32_bf16 v[60:63], v[176:179], a[164:167], v[60:63]\n"
    "ds_read_b64_tr_b16 v[30:31], v194 offset:61952\n"
    "v_fmaak_f32 v74, v74, v15, 0xc0135761\n"
    "v_fmaak_f32 v75, v75, v15, 0xc0135761\n"
    "v_mfma_f32_16x16x32_bf16 v[64:67], v[176:179], a[168:171], v[64:67]\n"
    "v_mul_f32 v72, v48, v72\n"
    "v_mul_f32 v73, v49, v73\n"
    "v_mfma_f32_16x16x32_bf16 v[68:71], v[176:179], a[172:175], v[68:71]\n"
    "v_mul_f32 v74, v50, v74\n"
    "v_mfma_f32_16x16x32_bf16 v[56:59], v[180:183], a[176:179], v[56:59]\n"
    "ds_read_b64_tr_b16 v[176:177], v195 offset:0\n"
    "v_mul_f32 v75, v51, v75\n"
    "v_exp_f32 v72, v72\n"
    "v_mfma_f32_16x16x32_bf16 v[60:63], v[180:183], a[180:183], v[60:63]\n"
    "ds_read_b64_tr_b16 v[178:179], v195 offset:512\n"
    "v_exp_f32 v73, v73\n"
    "v_exp_f32 v74, v74\n"
    "v_mfma_f32_16x16x32_bf16 v[64:67], v[180:183], a[184:187], v[64:67]\n"
    "v_exp_f32 v75, v75\n"
    "v_add_f32 v72, 1.0, v72\n"
    "v_mfma_f32_16x16x32_bf16 v[68:71], v[180:183], a[188:191], v[68:71]\n"
    "v_add_f32 v73, 1.0, v73\n"
    "s_waitcnt lgkmcnt(6)\n"
    "v_mfma_f32_16x16x32_bf16 v[56:59], v[16:19], a[192:195], v[56:59]\n"
    "ds_read_b64_tr_b16 v[180:181], v195 offset:4096\n"
    "v_add_f32 v74, 1.0, v74\n"
    "v_add_f32 v75, 1.0, v75\n"
    "v_mfma_f32_16x16x32_bf16 v[60:63], v[16:19], a[196:199], v[60:63]\n"
    "ds_read_b64_tr_b16 v[182:183], v195 offset:4608\n"
    "v_rcp_f32 v72, v72\n"
    "v_rcp_f32 v73, v73\n"
    "v_mfma_f32_16x16x32_bf16 v[64:67], v[16:19], a[200:203], v[64:67]\n"
    "v_rcp_f32 v74, v74\n"
    "v_rcp_f32 v75, v75\n"
    "v_mfma_f32_16x16x32_bf16 v[68:71], v[16:19], a[204:207], v[68:71]\n"
    "v_mul_f32 v72, v48, v72\n"
    "v_mfma_f32_16x16x32_bf16 v[56:59], v[20:23], a[208:211], v[56:59]\n"
    "v_mul_f32 v73, v49, v73\n"
    "v_mul_f32 v74, v50, v74\n"
    "v_mfma_f32_16x16x32_bf16 v[60:63], v[20:23], a[212:215], v[60:63]\n"
    "v_mul_f32 v75, v51, v75\n"
    "v_cvt_pk_bf16_f32 v152, v72, v73\n"
    "v_mfma_f32_16x16x32_bf16 v[64:67], v[20:23], a[216:219], v[64:67]\n"
    "v_cvt_pk_bf16_f32 v153, v74, v75\n"
    "v_mul_f32 v76, v52, v52\n"
    "v_mfma_f32_16x16x32_bf16 v[68:71], v[20:23], a[220:223], v[68:71]\n"
    "v_mul_f32 v77, v53, v53\n"
    "s_waitcnt lgkmcnt(4)\n"
    "v_mfma_f32_16x16x32_bf16 v[56:59], v[24:27], a[224:227], v[56:59]\n"
    "v_mul_f32 v78, v54, v54\n"
    "v_mul_f32 v79, v55, v55\n"
    "v_mfma_f32_16x16x32_bf16 v[60:63], v[24:27], a[228:231], v[60:63]\n"
    "v_fmaak_f32 v76, v76, v15, 0xc0135761\n"
    "v_fmaak_f32 v77, v77, v15, 0xc0135761\n"
    "v_mfma_f32_16x16x32_bf16 v[64:67], v[24:27], a[232:235], v[64:67]\n"
    "v_fmaak_f32 v78, v78, v15, 0xc0135761\n"
    "v_fmaak_f32 v79, v79, v15, 0xc0135761\n"
    "v_mfma_f32_16x16x32_bf16 v[68:71], v[24:27], a[236:239], v[68:71]\n"
    "v_mul_f32 v76, v52, v76\n"
    "s_waitcnt lgkmcnt(0)\n"
    "s_waitcnt vmcnt(0)\n"
    "s_barrier\n"
    "v_mfma_f32_16x16x32_bf16 v[56:59], v[28:31], a[240:243], v[56:59]\n"
    "ds_read_b64_tr_b16 v[16:17], v192 offset:0\n"
    "v_mul_f32 v77, v53, v77\n"
    "v_mul_f32 v78, v54, v78\n"
    "v_mfma_f32_16x16x32_bf16 v[60:63], v[28:31], a[244:247], v[60:63]\n"
    "ds_read_b64_tr_b16 v[18:19], v192 offset:512\n"
    "v_mul_f32 v79, v55, v79\n"
    "v_exp_f32 v76, v76\n"
    "v_mfma_f32_16x16x32_bf16 v[64:67], v[28:31], a[248:251], v[64:67]\n"
    "ds_read_b64_tr_b16 v[20:21], v192 offset:4096\n"
    "v_exp_f32 v77, v77\n"
    "v_exp_f32 v78, v78\n"
    "v_mfma_f32_16x16x32_bf16 v[68:71], v[28:31], a[252:255], v[68:71]\n"
    "ds_read_b64_tr_b16 v[22:23], v192 offset:4608\n"
    "v_exp_f32 v79, v79\n"
    "v_mfma_f32_16x16x32_bf16 v[56:59], v[176:179], v[224:227], v[56:59]\n"
    "ds_read_b64_tr_b16 v[24:25], v192 offset:8192\n"
    "v_add_f32 v76, 1.0, v76\n"
    "v_add_f32 v77, 1.0, v77\n"
    "v_mfma_f32_16x16x32_bf16 v[60:63], v[176:179], v[228:231], v[60:63]\n"
    "ds_read_b64_tr_b16 v[26:27], v192 offset:8704\n"
    "v_add_f32 v78, 1.0, v78\n"
    "v_add_f32 v79, 1.0, v79\n"
    "v_mfma_f32_16x16x32_bf16 v[64:67], v[176:179], v[232:235], v[64:67]\n"
    "ds_read_b64_tr_b16 v[28:29], v192 offset:12288\n"
    "v_rcp_f32 v76, v76\n"
    "v_rcp_f32 v77, v77\n"
    "v_mfma_f32_16x16x32_bf16 v[68:71], v[176:179], v[236:239], v[68:71]\n"
    "ds_read_b64_tr_b16 v[30:31], v192 offset:12800\n"
    "v_rcp_f32 v78, v78\n"
    "v_mfma_f32_16x16x32_bf16 v[56:59], v[180:183], v[240:243], v[56:59]\n"
    "ds_read_b64_tr_b16 v[176:177], v192 offset:16384\n"
    "v_rcp_f32 v79, v79\n"
    "v_mul_f32 v76, v52, v76\n"
    "v_mfma_f32_16x16x32_bf16 v[60:63], v[180:183], v[244:247], v[60:63]\n"
    "ds_read_b64_tr_b16 v[178:179], v192 offset:16896\n"
    "v_mul_f32 v77, v53, v77\n"
    "v_mul_f32 v78, v54, v78\n"
    "v_mfma_f32_16x16x32_bf16 v[64:67], v[180:183], v[248:251], v[64:67]\n"
    "v_mul_f32 v79, v55, v79\n"
    "v_cvt_pk_bf16_f32 v156, v76, v77\n"
    "v_mfma_f32_16x16x32_bf16 v[68:71], v[180:183], v[252:255], v[68:71]\n"
    "v_cvt_pk_bf16_f32 v157, v78, v79\n"
    "s_waitcnt lgkmcnt(6)\n"
    "v_mfma_f32_16x16x32_bf16 v[40:43], v[16:19], a[0:3], v[32:35]\n"
    "ds_read_b64_tr_b16 v[180:181], v192 offset:20480\n"
    "v_add_u32 v194, 73728, v2\n"
    "v_mfma_f32_16x16x32_bf16 v[44:47], v[16:19], a[4:7], v[32:35]\n"
    "ds_read_b64_tr_b16 v[182:183], v192 offset:20992\n"
    "v_add_u32 v195, 139264, v2\n"
    "v_mfma_f32_16x16x32_bf16 v[48:51], v[16:19], a[8:11], v[32:35]\n"
    "v_mfma_f32_16x16x32_bf16 v[52:55], v[16:19], a[12:15], v[32:35]\n"
    "s_add_u32 s40, s15, 0\n"
    "s_cmp_ge_u32 s40, 9\n"
    "s_cselect_b32 s41, 9, 0\n"
    "s_sub_u32 s40, s40, s41\n"
    "s_lshl_b32 s53, s40, 7\n"
    "s_mul_i32 s54, s9, 12288\n"
    "s_add_u32 s54, s54, 0\n"
    "s_add_u32 m0, s54, 0\n"
    "s_add_u32 s47, s50, s53\n"
    "buffer_load_dwordx4 v9, s[20:23], s47 offen offset:0 lds\n"
    "v_mfma_f32_16x16x32_bf16 v[40:43], v[20:23], a[16:19], v[40:43]\n"
    "ds_read_b64_tr_b16 v[16:17], v192 offset:24576\n"
    "v_mul_f32 v72, v56, v56\n"
    "v_mul_f32 v73, v57, v57\n"
    "v_mfma_f32_16x16x32_bf16 v[44:47], v[20:23], a[20:23], v[44:47]\n"
    "ds_read_b64_tr_b16 v[18:19], v192 offset:25088\n"
    "v_mul_f32 v74, v58, v58\n"
    "v_mul_f32 v75, v59, v59\n"
    "v_mfma_f32_16x16x32_bf16 v[48:51], v[20:23], a[24:27], v[48:51]\n"
    "v_fmaak_f32 v72, v72, v15, 0xc0135761\n"
    "v_fmaak_f32 v73, v73, v15, 0xc0135761\n"
    "v_mfma_f32_16x16x32_bf16 v[52:55], v[20:23], a[28:31], v[52:55]\n"
    "v_fmaak_f32 v74, v74, v15, 0xc0135761\n"
    "v_fmaak_f32 v75, v75, v15, 0xc0135761\n"
    "s_waitcnt lgkmcnt(6)\n"
    "v_mfma_f32_16x16x32_bf16 v[40:43], v[24:27], a[32:35], v[40:43]\n"
    "ds_read_b64_tr_b16 v[20:21], v192 offset:28672\n"
    "v_mul_f32 v72, v56, v72\n"
    "v_mul_f32 v73, v57, v73\n"
    "v_mfma_f32_16x16x32_bf16 v[44:47], v[24:27], a[36:39], v[44:47]\n"
    "buffer_load_dwordx4 v10, s[20:23], s47 offen offset:1024 lds\n"
    "ds_read_b64_tr_b16 v[22:23], v192 offset:29184\n"
    "v_mul_f32 v74, v58, v74\n"
    "v_mul_f32 v75, v59, v75\n"
    "v_mfma_f32_16x16x32_bf16 v[48:51], v[24:27], a[40:43], v[48:51]\n"
    "v_exp_f32 v72, v72\n"
    "v_exp_f32 v73, v73\n"
    "v_mfma_f32_16x16x32_bf16 v[52:55], v[24:27], a[44:47], v[52:55]\n"
    "v_exp_f32 v74, v74\n"
    "v_mfma_f32_16x16x32_bf16 v[40:43], v[28:31], a[48:51], v[40:43]\n"
    "ds_read_b64_tr_b16 v[24:25], v192 offset:32768\n"
    "v_exp_f32 v75, v75\n"
    "v_add_f32 v72, 1.0, v72\n"
    "v_mfma_f32_16x16x32_bf16 v[44:47], v[28:31], a[52:55], v[44:47]\n"
    "ds_read_b64_tr_b16 v[26:27], v192 offset:33280\n"
    "v_add_f32 v73, 1.0, v73\n"
    "v_add_f32 v74, 1.0, v74\n"
    "v_mfma_f32_16x16x32_bf16 v[48:51], v[28:31], a[56:59], v[48:51]\n"
    "v_add_f32 v75, 1.0, v75\n"
    "v_rcp_f32 v72, v72\n"
    "v_mfma_f32_16x16x32_bf16 v[52:55], v[28:31], a[60:63], v[52:55]\n"
    "buffer_load_dwordx4 v11, s[20:23], s47 offen offset:2048 lds\n"
    "v_rcp_f32 v73, v73\n"
    "s_waitcnt lgkmcnt(6)\n"
    "v_mfma_f32_16x16x32_bf16 v[40:43], v[176:179], a[64:67], v[40:43]\n"
    "ds_read_b64_tr_b16 v[28:29], v192 offset:36864\n"
    "v_rcp_f32 v74, v74\n"
    "v_rcp_f32 v75, v75\n"
    "v_mfma_f32_16x16x32_bf16 v[44:47], v[176:179], a[68:71], v[44:47]\n"
    "ds_read_b64_tr_b16 v[30:31], v192 offset:37376\n"
    "v_mul_f32 v72, v56, v72\n"
    "v_mul_f32 v73, v57, v73\n"
    "v_mfma_f32_16x16x32_bf16 v[48:51], v[176:179], a[72:75], v[48:51]\n"
    "v_mul_f32 v74, v58, v74\n"
    "v_mul_f32 v75, v59, v75\n"
    "v_mfma_f32_16x16x32_bf16 v[52:55], v[176:179], a[76:79], v[52:55]\n"
    "v_cvt_pk_bf16_f32 v146, v72, v73\n"
    "v_mfma_f32_16x16x32_bf16 v[40:43], v[180:183], a[80:83], v[40:43]\n"
    "ds_read_b64_tr_b16 v[176:177], v192 offset:40960\n"
    "v_cvt_pk_bf16_f32 v147, v74, v75\n"
    "v_mul_f32 v76, v60, v60\n"
    "v_mfma_f32_16x16x32_bf16 v[44:47], v[180:183], a[84:87], v[44:47]\n"
    "buffer_load_dwordx4 v12, s[20:23], s47 offen offset:3072 lds\n"
    "ds_read_b64_tr_b16 v[178:179], v192 offset:41472\n"
    "v_mul_f32 v77, v61, v61\n"
    "v_mul_f32 v78, v62, v62\n"
    "v_mfma_f32_16x16x32_bf16 v[48:51], v[180:183], a[88:91], v[48:51]\n"
    "v_mul_f32 v79, v63, v63\n"
    "v_fmaak_f32 v76, v76, v15, 0xc0135761\n"
    "v_mfma_f32_16x16x32_bf16 v[52:55], v[180:183], a[92:95], v[52:55]\n"
    "v_fmaak_f32 v77, v77, v15, 0xc0135761\n"
    "s_waitcnt lgkmcnt(6)\n"
    "v_mfma_f32_16x16x32_bf16 v[40:43], v[16:19], a[96:99], v[40:43]\n"
    "ds_read_b64_tr_b16 v[180:181], v192 offset:45056\n"
    "v_fmaak_f32 v78, v78, v15, 0xc0135761\n"
    "v_fmaak_f32 v79, v79, v15, 0xc0135761\n"
    "v_mfma_f32_16x16x32_bf16 v[44:47], v[16:19], a[100:103], v[44:47]\n"
    "ds_read_b64_tr_b16 v[182:183], v192 offset:45568\n"
    "v_mul_f32 v76, v60, v76\n"
    "v_mul_f32 v77, v61, v77\n"
    "v_mfma_f32_16x16x32_bf16 v[48:51], v[16:19], a[104:107], v[48:51]\n"
    "v_mul_f32 v78, v62, v78\n"
    "v_mul_f32 v79, v63, v79\n"
    "v_mfma_f32_16x16x32_bf16 v[52:55], v[16:19], a[108:111], v[52:55]\n"
    "s_add_u32 m0, s54, 4096\n"
    "s_add_u32 s47, s51, s53\n"
    "buffer_load_dwordx4 v9, s[20:23], s47 offen offset:0 lds\n"
    "v_exp_f32 v76, v76\n"
    "v_mfma_f32_16x16x32_bf16 v[40:43], v[20:23], a[112:115], v[40:43]\n"
    "ds_read_b64_tr_b16 v[16:17], v192 offset:49152\n"
    "v_exp_f32 v77, v77\n"
    "v_exp_f32 v78, v78\n"
    "v_mfma_f32_16x16x32_bf16 v[44:47], v[20:23], a[116:119], v[44:47]\n"
    "ds_read_b64_tr_b16 v[18:19], v192 offset:49664\n"
    "v_exp_f32 v79, v79\n"
    "v_add_f32 v76, 1.0, v76\n"
    "v_mfma_f32_16x16x32_bf16 v[48:51], v[20:23], a[120:123], v[48:51]\n"
    "v_add_f32 v77, 1.0, v77\n"
    "v_add_f32 v78, 1.0, v78\n"
    "v_mfma_f32_16x16x32_bf16 v[52:55], v[20:23], a[124:127], v[52:55]\n"
    "v_add_f32 v79, 1.0, v79\n"
    "s_waitcnt lgkmcnt(6)\n"
    "v_mfma_f32_16x16x32_bf16 v[40:43], v[24:27], a[128:131], v[40:43]\n"
    "ds_read_b64_tr_b16 v[20:21], v192 offset:53248\n"
    "ds_read_b128 v[36:39], v14 offset:1344\n"
    "v_rcp_f32 v76, v76\n"
    "v_mfma_f32_16x16x32_bf16 v[44:47], v[24:27], a[132:135], v[44:47]\n"
    "buffer_load_dwordx4 v10, s[20:23], s47 offen offset:1024 lds\n"
    "ds_read_b64_tr_b16 v[22:23], v192 offset:53760\n"
    "v_rcp_f32 v77, v77\n"
    "v_rcp_f32 v78, v78\n"
    "v_mfma_f32_16x16x32_bf16 v[48:51], v[24:27], a[136:139], v[48:51]\n"
    "v_rcp_f32 v79, v79\n"
    "v_mul_f32 v76, v60, v76\n"
    "v_mfma_f32_16x16x32_bf16 v[52:55], v[24:27], a[140:143], v[52:55]\n"
    "v_mul_f32 v77, v61, v77\n"
    "v_mul_f32 v78, v62, v78\n"
    "v_mfma_f32_16x16x32_bf16 v[40:43], v[28:31], a[144:147], v[40:43]\n"
    "ds_read_b64_tr_b16 v[24:25], v192 offset:57344\n"
    "v_mul_f32 v79, v63, v79\n"
    "v_cvt_pk_bf16_f32 v150, v76, v77\n"
    "v_mfma_f32_16x16x32_bf16 v[44:47], v[28:31], a[148:151], v[44:47]\n"
    "ds_read_b64_tr_b16 v[26:27], v192 offset:57856\n"
    "v_cvt_pk_bf16_f32 v151, v78, v79\n"
    "v_mul_f32 v72, v64, v64\n"
    "v_mfma_f32_16x16x32_bf16 v[48:51], v[28:31], a[152:155], v[48:51]\n"
    "v_mul_f32 v73, v65, v65\n"
    "v_mul_f32 v74, v66, v66\n"
    "v_mfma_f32_16x16x32_bf16 v[52:55], v[28:31], a[156:159], v[52:55]\n"
    "buffer_load_dwordx4 v11, s[20:23], s47 offen offset:2048 lds\n"
    "v_mul_f32 v75, v67, v67\n"
    "s_waitcnt lgkmcnt(7)\n"
    "v_mfma_f32_16x16x32_bf16 v[40:43], v[176:179], a[160:163], v[40:43]\n"
    "ds_read_b64_tr_b16 v[28:29], v192 offset:61440\n"
    "v_fmaak_f32 v72, v72, v15, 0xc0135761\n"
    "v_fmaak_f32 v73, v73, v15, 0xc0135761\n"
    "v_mfma_f32_16x16x32_bf16 v[44:47], v[176:179], a[164:167], v[44:47]\n"
    "ds_read_b64_tr_b16 v[30:31], v192 offset:61952\n"
    "v_fmaak_f32 v74, v74, v15, 0xc0135761\n"
    "v_fmaak_f32 v75, v75, v15, 0xc0135761\n"
    "v_mfma_f32_16x16x32_bf16 v[48:51], v[176:179], a[168:171], v[48:51]\n"
    "v_mul_f32 v72, v64, v72\n"
    "v_mul_f32 v73, v65, v73\n"
    "v_mfma_f32_16x16x32_bf16 v[52:55], v[176:179], a[172:175], v[52:55]\n"
    "v_mul_f32 v74, v66, v74\n"
    "v_mfma_f32_16x16x32_bf16 v[40:43], v[180:183], a[176:179], v[40:43]\n"
    "ds_read_b64_tr_b16 v[176:177], v193 offset:0\n"
    "v_mul_f32 v75, v67, v75\n"
    "v_exp_f32 v72, v72\n"
    "v_mfma_f32_16x16x32_bf16 v[44:47], v[180:183], a[180:183], v[44:47]\n"
    "buffer_load_dwordx4 v12, s[20:23], s47 offen offset:3072 lds\n"
    "ds_read_b64_tr_b16 v[178:179], v193 offset:512\n"
    "v_exp_f32 v73, v73\n"
    "v_exp_f32 v74, v74\n"
    "v_mfma_f32_16x16x32_bf16 v[48:51], v[180:183], a[184:187], v[48:51]\n"
    "v_exp_f32 v75, v75\n"
    "v_add_f32 v72, 1.0, v72\n"
    "v_mfma_f32_16x16x32_bf16 v[52:55], v[180:183], a[188:191], v[52:55]\n"
    "v_add_f32 v73, 1.0, v73\n"
    "s_waitcnt lgkmcnt(6)\n"
    "v_mfma_f32_16x16x32_bf16 v[40:43], v[16:19], a[192:195], v[40:43]\n"
    "ds_read_b64_tr_b16 v[180:181], v193 offset:4096\n"
    "v_add_f32 v74, 1.0, v74\n"
    "v_add_f32 v75, 1.0, v75\n"
    "v_mfma_f32_16x16x32_bf16 v[44:47], v[16:19], a[196:199], v[44:47]\n"
    "ds_read_b64_tr_b16 v[182:183], v193 offset:4608\n"
    "v_rcp_f32 v72, v72\n"
    "v_rcp_f32 v73, v73\n"
    "v_mfma_f32_16x16x32_bf16 v[48:51], v[16:19], a[200:203], v[48:51]\n"
    "v_rcp_f32 v74, v74\n"
    "v_rcp_f32 v75, v75\n"
    "v_mfma_f32_16x16x32_bf16 v[52:55], v[16:19], a[204:207], v[52:55]\n"
    "s_add_u32 m0, s54, 8192\n"
    "s_add_u32 s47, s52, s53\n"
    "buffer_load_dwordx4 v9, s[20:23], s47 offen offset:0 lds\n"
    "v_mul_f32 v72, v64, v72\n"
    "v_mfma_f32_16x16x32_bf16 v[40:43], v[20:23], a[208:211], v[40:43]\n"
    "ds_read_b64_tr_b16 v[16:17], v194 offset:0\n"
    "v_mul_f32 v73, v65, v73\n"
    "v_mul_f32 v74, v66, v74\n"
    "v_mfma_f32_16x16x32_bf16 v[44:47], v[20:23], a[212:215], v[44:47]\n"
    "ds_read_b64_tr_b16 v[18:19], v194 offset:512\n"
    "v_mul_f32 v75, v67, v75\n"
    "v_cvt_pk_bf16_f32 v154, v72, v73\n"
    "v_mfma_f32_16x16x32_bf16 v[48:51], v[20:23], a[216:219], v[48:51]\n"
    "v_cvt_pk_bf16_f32 v155, v74, v75\n"
    "v_mul_f32 v76, v68, v68\n"
    "v_mfma_f32_16x16x32_bf16 v[52:55], v[20:23], a[220:223], v[52:55]\n"
    "v_mul_f32 v77, v69, v69\n"
    "s_waitcnt lgkmcnt(6)\n"
    "v_mfma_f32_16x16x32_bf16 v[40:43], v[24:27], a[224:227], v[40:43]\n"
    "ds_read_b64_tr_b16 v[20:21], v194 offset:4096\n"
    "v_mul_f32 v78, v70, v70\n"
    "v_mul_f32 v79, v71, v71\n"
    "v_mfma_f32_16x16x32_bf16 v[44:47], v[24:27], a[228:231], v[44:47]\n"
    "buffer_load_dwordx4 v10, s[20:23], s47 offen offset:1024 lds\n"
    "ds_read_b64_tr_b16 v[22:23], v194 offset:4608\n"
    "v_fmaak_f32 v76, v76, v15, 0xc0135761\n"
    "v_fmaak_f32 v77, v77, v15, 0xc0135761\n"
    "v_mfma_f32_16x16x32_bf16 v[48:51], v[24:27], a[232:235], v[48:51]\n"
    "v_fmaak_f32 v78, v78, v15, 0xc0135761\n"
    "v_fmaak_f32 v79, v79, v15, 0xc0135761\n"
    "v_mfma_f32_16x16x32_bf16 v[52:55], v[24:27], a[236:239], v[52:55]\n"
    "v_mul_f32 v76, v68, v76\n"
    "v_mfma_f32_16x16x32_bf16 v[40:43], v[28:31], a[240:243], v[40:43]\n"
    "ds_read_b64_tr_b16 v[24:25], v194 offset:8192\n"
    "v_mul_f32 v77, v69, v77\n"
    "v_mul_f32 v78, v70, v78\n"
    "v_mfma_f32_16x16x32_bf16 v[44:47], v[28:31], a[244:247], v[44:47]\n"
    "ds_read_b64_tr_b16 v[26:27], v194 offset:8704\n"
    "v_mul_f32 v79, v71, v79\n"
    "v_exp_f32 v76, v76\n"
    "v_mfma_f32_16x16x32_bf16 v[48:51], v[28:31], a[248:251], v[48:51]\n"
    "v_exp_f32 v77, v77\n"
    "v_exp_f32 v78, v78\n"
    "v_mfma_f32_16x16x32_bf16 v[52:55], v[28:31], a[252:255], v[52:55]\n"
    "buffer_load_dwordx4 v11, s[20:23], s47 offen offset:2048 lds\n"
    "v_exp_f32 v79, v79\n"
    "s_waitcnt lgkmcnt(6)\n"
    "v_mfma_f32_16x16x32_bf16 v[40:43], v[176:179], v[224:227], v[40:43]\n"
    "ds_read_b64_tr_b16 v[28:29], v194 offset:12288\n"
    "v_add_f32 v76, 1.0, v76\n"
    "v_add_f32 v77, 1.0, v77\n"
    "v_mfma_f32_16x16x32_bf16 v[44:47], v[176:179], v[228:231], v[44:47]\n"
    "ds_read_b64_tr_b16 v[30:31], v194 offset:12800\n"
    "v_add_f32 v78, 1.0, v78\n"
    "v_add_f32 v79, 1.0, v79\n"
    "v_mfma_f32_16x16x32_bf16 v[48:51], v[176:179], v[232:235], v[48:51]\n"
    "v_rcp_f32 v76, v76\n"
    "v_rcp_f32 v77, v77\n"
    "v_mfma_f32_16x16x32_bf16 v[52:55], v[176:179], v[236:239], v[52:55]\n"
    "v_rcp_f32 v78, v78\n"
    "v_mfma_f32_16x16x32_bf16 v[40:43], v[180:183], v[240:243], v[40:43]\n"
    "ds_read_b64_tr_b16 v[176:177], v194 offset:16384\n"
    "v_rcp_f32 v79, v79\n"
    "v_mul_f32 v76, v68, v76\n"
    "v_mfma_f32_16x16x32_bf16 v[44:47], v[180:183], v[244:247], v[44:47]\n"
    "ds_read_b64_tr_b16 v[178:179], v194 offset:16896\n"
    "v_mul_f32 v77, v69, v77\n"
    "v_mul_f32 v78, v70, v78\n"
    "v_mfma_f32_16x16x32_bf16 v[48:51], v[180:183], v[248:251], v[48:51]\n"
    "buffer_load_dwordx4 v12, s[20:23], s47 offen offset:3072 lds\n"
    "v_mul_f32 v79, v71, v79\n"
    "v_cvt_pk_bf16_f32 v158, v76, v77\n"
    "v_mfma_f32_16x16x32_bf16 v[52:55], v[180:183], v[252:255], v[52:55]\n"
    "v_cvt_pk_bf16_f32 v159, v78, v79\n"
    "s_waitcnt lgkmcnt(6)\n"
    "v_mfma_f32_16x16x32_bf16 v[56:59], v[16:19], a[0:3], v[36:39]\n"
    "ds_read_b64_tr_b16 v[180:181], v194 offset:20480\n"
    "v_mul_f32 v72, v40, v40\n"
    "v_mfma_f32_16x16x32_bf16 v[60:63], v[16:19], a[4:7], v[36:39]\n"
    "ds_read_b64_tr_b16 v[182:183], v194 offset:20992\n"
    "v_mul_f32 v73, v41, v41\n"
    "v_mfma_f32_16x16x32_bf16 v[64:67], v[16:19], a[8:11], v[36:39]\n"
    "v_mfma_f32_16x16x32_bf16 v[68:71], v[16:19], a[12:15], v[36:39]\n"
    "v_mfma_f32_16x16x32_bf16 v[56:59], v[20:23], a[16:19], v[56:59]\n"
    "ds_read_b64_tr_b16 v[16:17], v194 offset:24576\n"
    "v_mul_f32 v74, v42, v42\n"
    "v_mul_f32 v75, v43, v43\n"
    "v_mfma_f32_16x16x32_bf16 v[60:63], v[20:23], a[20:23], v[60:63]\n"
    "ds_read_b64_tr_b16 v[18:19], v194 offset:25088\n"
    "v_fmaak_f32 v72, v72, v15, 0xc0135761\n"
    "v_fmaak_f32 v73, v73, v15, 0xc0135761\n"
    "v_mfma_f32_16x16x32_bf16 v[64:67], v[20:23], a[24:27], v[64:67]\n"
    "v_fmaak_f32 v74, v74, v15, 0xc0135761\n"
    "v_fmaak_f32 v75, v75, v15, 0xc0135761\n"
    "v_mfma_f32_16x16x32_bf16 v[68:71], v[20:23], a[28:31], v[68:71]\n"
    "v_mul_f32 v72, v40, v72\n"
    "s_waitcnt lgkmcnt(6)\n"
    "v_mfma_f32_16x16x32_bf16 v[56:59], v[24:27], a[32:35], v[56:59]\n"
    "ds_read_b64_tr_b16 v[20:21], v194 offset:28672\n"
    "v_mul_f32 v73, v41, v73\n"
    "v_mul_f32 v74, v42, v74\n"
    "v_mfma_f32_16x16x32_bf16 v[60:63], v[24:27], a[36:39], v[60:63]\n"
    "ds_read_b64_tr_b16 v[22:23], v194 offset:29184\n"
    "v_mul_f32 v75, v43, v75\n"
    "v_exp_f32 v72, v72\n"
    "v_mfma_f32_16x16x32_bf16 v[64:67], v[24:27], a[40:43], v[64:67]\n"
    "v_exp_f32 v73, v73\n"
    "v_exp_f32 v74, v74\n"
    "v_mfma_f32_16x16x32_bf16 v[68:71], v[24:27], a[44:47], v[68:71]\n"
    "v_exp_f32 v75, v75\n"
    "v_mfma_f32_16x16x32_bf16 v[56:59], v[28:31], a[48:51], v[56:59]\n"
    "ds_read_b64_tr_b16 v[24:25], v194 offset:32768\n"
    "v_add_f32 v72, 1.0, v72\n"
    "v_add_f32 v73, 1.0, v73\n"
    "v_mfma_f32_16x16x32_bf16 v[60:63], v[28:31], a[52:55], v[60:63]\n"
    "ds_read_b64_tr_b16 v[26:27], v194 offset:33280\n"
    "v_add_f32 v74, 1.0, v74\n"
    "v_add_f32 v75, 1.0, v75\n"
    "v_mfma_f32_16x16x32_bf16 v[64:67], v[28:31], a[56:59], v[64:67]\n"
    "v_rcp_f32 v72, v72\n"
    "v_rcp_f32 v73, v73\n"
    "v_mfma_f32_16x16x32_bf16 v[68:71], v[28:31], a[60:63], v[68:71]\n"
    "v_rcp_f32 v74, v74\n"
    "s_waitcnt lgkmcnt(6)\n"
    "v_mfma_f32_16x16x32_bf16 v[56:59], v[176:179], a[64:67], v[56:59]\n"
    "ds_read_b64_tr_b16 v[28:29], v194 offset:36864\n"
    "v_rcp_f32 v75, v75\n"
    "v_mul_f32 v72, v40, v72\n"
    "v_mfma_f32_16x16x32_bf16 v[60:63], v[176:179], a[68:71], v[60:63]\n"
    "ds_read_b64_tr_b16 v[30:31], v194 offset:37376\n"
    "v_mul_f32 v73, v41, v73\n"
    "v_mul_f32 v74, v42, v74\n"
    "v_mfma_f32_16x16x32_bf16 v[64:67], v[176:179], a[72:75], v[64:67]\n"
    "v_mul_f32 v75, v43, v75\n"
    "v_cvt_pk_bf16_f32 v160, v72, v73\n"
    "v_mfma_f32_16x16x32_bf16 v[68:71], v[176:179], a[76:79], v[68:71]\n"
    "v_cvt_pk_bf16_f32 v161, v74, v75\n"
    "v_mfma_f32_16x16x32_bf16 v[56:59], v[180:183], a[80:83], v[56:59]\n"
    "ds_read_b64_tr_b16 v[176:177], v194 offset:40960\n"
    "v_mul_f32 v76, v44, v44\n"
    "v_mul_f32 v77, v45, v45\n"
    "v_mfma_f32_16x16x32_bf16 v[60:63], v[180:183], a[84:87], v[60:63]\n"
    "ds_read_b64_tr_b16 v[178:179], v194 offset:41472\n"
    "v_mul_f32 v78, v46, v46\n"
    "v_mul_f32 v79, v47, v47\n"
    "v_mfma_f32_16x16x32_bf16 v[64:67], v[180:183], a[88:91], v[64:67]\n"
    "v_fmaak_f32 v76, v76, v15, 0xc0135761\n"
    "v_fmaak_f32 v77, v77, v15, 0xc0135761\n"
    "v_mfma_f32_16x16x32_bf16 v[68:71], v[180:183], a[92:95], v[68:71]\n"
    "v_fmaak_f32 v78, v78, v15, 0xc0135761\n"
    "s_waitcnt lgkmcnt(6)\n"
    "v_mfma_f32_16x16x32_bf16 v[56:59], v[16:19], a[96:99], v[56:59]\n"
    "ds_read_b64_tr_b16 v[180:181], v194 offset:45056\n"
    "v_fmaak_f32 v79, v79, v15, 0xc0135761\n"
    "v_mul_f32 v76, v44, v76\n"
    "v_mfma_f32_16x16x32_bf16 v[60:63], v[16:19], a[100:103], v[60:63]\n"
    "ds_read_b64_tr_b16 v[182:183], v194 offset:45568\n"
    "v_mul_f32 v77, v45, v77\n"
    "v_mul_f32 v78, v46, v78\n"
    "v_mfma_f32_16x16x32_bf16 v[64:67], v[16:19], a[104:107], v[64:67]\n"
    "v_mul_f32 v79, v47, v79\n"
    "v_exp_f32 v76, v76\n"
    "v_mfma_f32_16x16x32_bf16 v[68:71], v[16:19], a[108:111], v[68:71]\n"
    "v_exp_f32 v77, v77\n"
    "v_mfma_f32_16x16x32_bf16 v[56:59], v[20:23], a[112:115], v[56:59]\n"
    "ds_read_b64_tr_b16 v[16:17], v194 offset:49152\n"
    "v_exp_f32 v78, v78\n"
    "v_exp_f32 v79, v79\n"
    "v_mfma_f32_16x16x32_bf16 v[60:63], v[20:23], a[116:119], v[60:63]\n"
    "ds_read_b64_tr_b16 v[18:19], v194 offset:49664\n"
    "v_add_f32 v76, 1.0, v76\n"
    "v_add_f32 v77, 1.0, v77\n"
    "v_mfma_f32_16x16x32_bf16 v[64:67], v[20:23], a[120:123], v[64:67]\n"
    "v_add_f32 v78, 1.0, v78\n"
    "v_add_f32 v79, 1.0, v79\n"
    "v_mfma_f32_16x16x32_bf16 v[68:71], v[20:23], a[124:127], v[68:71]\n"
    "v_rcp_f32 v76, v76\n"
    "s_waitcnt lgkmcnt(6)\n"
    "v_mfma_f32_16x16x32_bf16 v[56:59], v[24:27], a[128:131], v[56:59]\n"
    "ds_read_b64_tr_b16 v[20:21], v194 offset:53248\n"
    "v_rcp_f32 v77, v77\n"
    "v_rcp_f32 v78, v78\n"
    "v_mfma_f32_16x16x32_bf16 v[60:63], v[24:27], a[132:135], v[60:63]\n"
    "ds_read_b64_tr_b16 v[22:23], v194 offset:53760\n"
    "v_rcp_f32 v79, v79\n"
    "v_mul_f32 v76, v44, v76\n"
    "v_mfma_f32_16x16x32_bf16 v[64:67], v[24:27], a[136:139], v[64:67]\n"
    "v_mul_f32 v77, v45, v77\n"
    "v_mul_f32 v78, v46, v78\n"
    "v_mfma_f32_16x16x32_bf16 v[68:71], v[24:27], a[140:143], v[68:71]\n"
    "v_mul_f32 v79, v47, v79\n"
    "v_mfma_f32_16x16x32_bf16 v[56:59], v[28:31], a[144:147], v[56:59]\n"
    "ds_read_b64_tr_b16 v[24:25], v194 offset:57344\n"
    "v_cvt_pk_bf16_f32 v164, v76, v77\n"
    "v_cvt_pk_bf16_f32 v165, v78, v79\n"
    "v_mfma_f32_16x16x32_bf16 v[60:63], v[28:31], a[148:151], v[60:63]\n"
    "ds_read_b64_tr_b16 v[26:27], v194 offset:57856\n"
    "v_mul_f32 v72, v48, v48\n"
    "v_mul_f32 v73, v49, v49\n"
    "v_mfma_f32_16x16x32_bf16 v[64:67], v[28:31], a[152:155], v[64:67]\n"
    "v_mul_f32 v74, v50, v50\n"
    "v_mul_f32 v75, v51, v51\n"
    "v_mfma_f32_16x16x32_bf16 v[68:71], v[28:31], a[156:159], v[68:71]\n"
    "v_fmaak_f32 v72, v72, v15, 0xc0135761\n"
    "s_waitcnt lgkmcnt(6)\n"
    "v_mfma_f32_16x16x32_bf16 v[56:59], v[176:179], a[160:163], v[56:59]\n"
    "ds_read_b64_tr_b16 v[28:29], v194 offset:61440\n"
    "v_fmaak_f32 v73, v73, v15, 0xc0135761\n"
    "v_fmaak_f32 v74, v74, v15, 0xc0135761\n"
    "v_mfma_f32_16x16x32_bf16 v[60:63], v[176:179], a[164:167], v[60:63]\n"
    "ds_read_b64_tr_b16 v[30:31], v194 offset:61952\n"
    "v_fmaak_f32 v75, v75, v15, 0xc0135761\n"
    "v_mul_f32 v72, v48, v72\n"
    "v_mfma_f32_16x16x32_bf16 v[64:67], v[176:179], a[168:171], v[64:67]\n"
    "v_mul_f32 v73, v49, v73\n"
    "v_mul_f32 v74, v50, v74\n"
    "v_mfma_f32_16x16x32_bf16 v[68:71], v[176:179], a[172:175], v[68:71]\n"
    "v_mul_f32 v75, v51, v75\n"
    "v_mfma_f32_16x16x32_bf16 v[56:59], v[180:183], a[176:179], v[56:59]\n"
    "ds_read_b64_tr_b16 v[176:177], v195 offset:0\n"
    "v_exp_f32 v72, v72\n"
    "v_exp_f32 v73, v73\n"
    "v_mfma_f32_16x16x32_bf16 v[60:63], v[180:183], a[180:183], v[60:63]\n"
    "ds_read_b64_tr_b16 v[178:179], v195 offset:512\n"
    "v_exp_f32 v74, v74\n"
    "v_exp_f32 v75, v75\n"
    "v_mfma_f32_16x16x32_bf16 v[64:67], v[180:183], a[184:187], v[64:67]\n"
    "v_add_f32 v72, 1.0, v72\n"
    "v_add_f32 v73, 1.0, v73\n"
    "v_mfma_f32_16x16x32_bf16 v[68:71], v[180:183], a[188:191], v[68:71]\n"
    "v_add_f32 v74, 1.0, v74\n"
    "s_waitcnt lgkmcnt(6)\n"
    "v_mfma_f32_16x16x32_bf16 v[56:59], v[16:19], a[192:195], v[56:59]\n"
    "ds_read_b64_tr_b16 v[180:181], v195 offset:4096\n"
    "v_add_f32 v75, 1.0, v75\n"
    "v_rcp_f32 v72, v72\n"
    "v_mfma_f32_16x16x32_bf16 v[60:63], v[16:19], a[196:199], v[60:63]\n"
    "ds_read_b64_tr_b16 v[182:183], v195 offset:4608\n"
    "v_rcp_f32 v73, v73\n"
    "v_rcp_f32 v74, v74\n"
    "v_mfma_f32_16x16x32_bf16 v[64:67], v[16:19], a[200:203], v[64:67]\n"
    "v_rcp_f32 v75, v75\n"
    "v_mul_f32 v72, v48, v72\n"
    "v_mfma_f32_16x16x32_bf16 v[68:71], v[16:19], a[204:207], v[68:71]\n"
    "v_mul_f32 v73, v49, v73\n"
    "v_mfma_f32_16x16x32_bf16 v[56:59], v[20:23], a[208:211], v[56:59]\n"
    "v_mul_f32 v74, v50, v74\n"
    "v_mul_f32 v75, v51, v75\n"
    "v_mfma_f32_16x16x32_bf16 v[60:63], v[20:23], a[212:215], v[60:63]\n"
    "v_cvt_pk_bf16_f32 v168, v72, v73\n"
    "v_cvt_pk_bf16_f32 v169, v74, v75\n"
    "v_mfma_f32_16x16x32_bf16 v[64:67], v[20:23], a[216:219], v[64:67]\n"
    "v_mul_f32 v76, v52, v52\n"
    "v_mul_f32 v77, v53, v53\n"
    "v_mfma_f32_16x16x32_bf16 v[68:71], v[20:23], a[220:223], v[68:71]\n"
    "v_mul_f32 v78, v54, v54\n"
    "s_waitcnt lgkmcnt(4)\n"
    "v_mfma_f32_16x16x32_bf16 v[56:59], v[24:27], a[224:227], v[56:59]\n"
    "v_mul_f32 v79, v55, v55\n"
    "v_fmaak_f32 v76, v76, v15, 0xc0135761\n"
    "v_mfma_f32_16x16x32_bf16 v[60:63], v[24:27], a[228:231], v[60:63]\n"
    "v_fmaak_f32 v77, v77, v15, 0xc0135761\n"
    "v_fmaak_f32 v78, v78, v15, 0xc0135761\n"
    "v_mfma_f32_16x16x32_bf16 v[64:67], v[24:27], a[232:235], v[64:67]\n"
    "v_fmaak_f32 v79, v79, v15, 0xc0135761\n"
    "v_mul_f32 v76, v52, v76\n"
    "v_mfma_f32_16x16x32_bf16 v[68:71], v[24:27], a[236:239], v[68:71]\n"
    "v_mul_f32 v77, v53, v77\n"
    "v_mfma_f32_16x16x32_bf16 v[56:59], v[28:31], a[240:243], v[56:59]\n"
    "v_mul_f32 v78, v54, v78\n"
    "v_mul_f32 v79, v55, v79\n"
    "v_mfma_f32_16x16x32_bf16 v[60:63], v[28:31], a[244:247], v[60:63]\n"
    "v_exp_f32 v76, v76\n"
    "v_exp_f32 v77, v77\n"
    "v_mfma_f32_16x16x32_bf16 v[64:67], v[28:31], a[248:251], v[64:67]\n"
    "v_exp_f32 v78, v78\n"
    "v_exp_f32 v79, v79\n"
    "v_mfma_f32_16x16x32_bf16 v[68:71], v[28:31], a[252:255], v[68:71]\n"
    "v_add_f32 v76, 1.0, v76\n"
    "s_waitcnt lgkmcnt(0)\n"
    "v_mfma_f32_16x16x32_bf16 v[56:59], v[176:179], v[224:227], v[56:59]\n"
    "v_add_f32 v77, 1.0, v77\n"
    "v_add_f32 v78, 1.0, v78\n"
    "v_mfma_f32_16x16x32_bf16 v[60:63], v[176:179], v[228:231], v[60:63]\n"
    "v_add_f32 v79, 1.0, v79\n"
    "v_rcp_f32 v76, v76\n"
    "v_mfma_f32_16x16x32_bf16 v[64:67], v[176:179], v[232:235], v[64:67]\n"
    "v_rcp_f32 v77, v77\n"
    "v_rcp_f32 v78, v78\n"
    "v_mfma_f32_16x16x32_bf16 v[68:71], v[176:179], v[236:239], v[68:71]\n"
    "v_rcp_f32 v79, v79\n"
    "v_mfma_f32_16x16x32_bf16 v[56:59], v[180:183], v[240:243], v[56:59]\n"
    "v_mul_f32 v76, v52, v76\n"
    "v_mul_f32 v77, v53, v77\n"
    "v_mfma_f32_16x16x32_bf16 v[60:63], v[180:183], v[244:247], v[60:63]\n"
    "v_mul_f32 v78, v54, v78\n"
    "v_mul_f32 v79, v55, v79\n"
    "v_mfma_f32_16x16x32_bf16 v[64:67], v[180:183], v[248:251], v[64:67]\n"
    "v_cvt_pk_bf16_f32 v172, v76, v77\n"
    "v_mfma_f32_16x16x32_bf16 v[68:71], v[180:183], v[252:255], v[68:71]\n"
    "v_cvt_pk_bf16_f32 v173, v78, v79\n"
    "s_waitcnt lgkmcnt(0)\n"
    "s_waitcnt vmcnt(0)\n"
    "s_barrier\n"
    "s_lshl_b32 s40, s13, 12\n"
    "v_add_u32 v224, s40, v3\n"
    "v_add_u32 v225, s40, v4\n"
    "s_xor_b32 s40, s13, 1\n"
    "s_lshl_b32 s40, s40, 12\n"
    "v_add_u32 v226, s40, v3\n"
    "v_add_u32 v227, s40, v4\n"
    "s_mul_i32 s40, s10, 9\n"
    "s_lshl_b32 s40, s40, 1\n"
    "s_add_u32 s40, s40, s12\n"
    "s_lshl_b32 s40, s40, 1\n"
    "s_add_u32 s40, s40, s13\n"
    "s_lshl_b32 s56, s40, 16\n"
    "s_lshl_b32 s40, s61, 12\n"
    "s_add_u32 s60, s56, s40\n"
    "s_mul_i32 s40, s10, 36\n"
    "s_add_u32 s40, s40, s9\n"
    "s_lshl_b32 s40, s40, 7\n"
    "s_lshl_b32 s41, s61, 3\n"
    "s_add_u32 s40, s40, s41\n"
    "s_add_u32 s57, s40, 16384\n"
    "v_mov_b32 v228, 0x7fffffff\n"
    "v_cmp_eq_u32 vcc, 0, v0\n"
    "s_nop 1\n"
    "v_cndmask_b32 v228, v228, 0, vcc\n"
    "v_mov_b32 v230, s84\n"
    "v_mov_b32 v231, s85\n"
    "v_add_u32 v216, 0, v224\n"
    "v_add_u32 v217, 0, v225\n"
    "v_add_u32 v218, 0, v226\n"
    "v_add_u32 v219, 0, v227\n"
    "s_lshl_b32 s58, s12, 2\n"
    "s_add_u32 s58, s58, s34\n"
    "v_and_b32 v77, 15, v0\n"
    "s_lshr_b32 s40, s58, 2\n"
    "s_lshl_b32 s40, s40, 1\n"
    "s_add_u32 s40, s40, s13\n"
    "s_lshr_b32 s41, s58, 1\n"
    "v_mov_b32 v78, s41\n"
    "v_mov_b32 v79, s40\n"
    "v_cmp_gt_u32 vcc, 12, v77\n"
    "s_nop 1\n"
    "v_cndmask_b32 v78, v78, v79, vcc\n"
    "v_lshl_add_u32 v78, v78, 4, v77\n"
    "v_lshlrev_b32 v232, 3, v78\n"
    "s_lshl_b32 s40, s10, 7\n"
    "s_lshl_b32 s41, s58, 4\n"
    "s_add_u32 s40, s40, s41\n"
    "v_add_u32 v77, s40, v77\n"
    "v_lshrrev_b32 v78, 4, v0\n"
    "v_lshlrev_b32 v78, 2, v78\n"
    "s_lshl_b32 s40, s13, 5\n"
    "v_add_u32 v78, s40, v78\n"
    "v_mul_u32_u24 v233, 1152, v77\n"
    "v_lshl_add_u32 v233, v78, 1, v233\n"
    "v_lshlrev_b32 v234, 1, v233\n"
    "v_lshlrev_b32 v235, 1, v78\n"
    "v_lshlrev_b32 v77, 3, v77\n"
    "v_add_u32 v77, 131072, v77\n"
    "buffer_load_dwordx2 v[236:237], v77, s[72:75], 0 offen sc1\n"
    "s_mul_i32 s40, s10, 18\n"
    "s_lshr_b32 s41, s58, 2\n"
    "s_add_u32 s40, s40, s41\n"
    "s_lshl_b32 s40, s40, 1\n"
    "s_add_u32 s40, s40, s13\n"
    "s_lshl_b32 s40, s40, 16\n"
    "s_and_b32 s41, s58, 3\n"
    "s_lshl_b32 s41, s41, 10\n"
    "s_add_u32 s45, s40, s41\n"
    "s_mul_i32 s40, s9, 12288\n"
    "s_add_u32 s40, s40, 98304\n"
    "v_add_u32 v214, s40, v13\n"
    "s_xor_b32 s40, s9, 1\n"
    "s_mul_i32 s40, s40, 12288\n"
    "s_add_u32 s40, s40, 98304\n"
    "v_add_u32 v215, s40, v13\n"
    "ds_read_b64_tr_b16 v[16:17], v216 offset:0\n"
    "ds_read_b64_tr_b16 v[18:19], v216 offset:2048\n"
    "ds_read_b64_tr_b16 v[20:21], v217 offset:0\n"
    "ds_read_b64_tr_b16 v[22:23], v217 offset:2048\n"
    "ds_read_b64_tr_b16 v[24:25], v216 offset:8192\n"
    "ds_read_b64_tr_b16 v[26:27], v216 offset:10240\n"
    "ds_read_b64_tr_b16 v[28:29], v217 offset:8192\n"
    "ds_read_b64_tr_b16 v[30:31], v217 offset:10240\n"
    "ds_read_b64_tr_b16 v[32:33], v216 offset:16384\n"
    "ds_read_b64_tr_b16 v[34:35], v216 offset:18432\n"
    "s_waitcnt lgkmcnt(6)\n"
    "v_mfma_f32_16x16x32_bf16 v[176:179], v[16:19], v[80:83], 0\n"
    "ds_write_b128 v214, v[80:83] offset:0\n"
    "ds_read_b64_tr_b16 v[36:37], v217 offset:16384\n"
    "v_add_u32 v220, 49152, v224\n"
    "v_add_u32 v221, 49152, v225\n"
    "v_add_u32 v222, 49152, v226\n"
    "v_add_u32 v223, 49152, v227\n"
    "v_mfma_f32_16x16x32_bf16 v[180:183], v[16:19], v[84:87], 0\n"
    "ds_read_b64_tr_b16 v[38:39], v217 offset:18432\n"
    "v_mul_f32 v72, v56, v56\n"
    "v_mul_f32 v73, v57, v57\n"
    "v_mul_f32 v74, v58, v58\n"
    "v_mfma_f32_16x16x32_bf16 v[184:187], v[16:19], v[88:91], 0\n"
    "ds_write_b128 v214, v[84:87] offset:1024\n"
    "s_add_u32 s40, s15, 1\n"
    "s_cmp_ge_u32 s40, 9\n"
    "s_cselect_b32 s41, 9, 0\n"
    "s_sub_u32 s40, s40, s41\n"
    "s_lshl_b32 s53, s40, 7\n"
    "s_mul_i32 s54, s9, 12288\n"
    "s_add_u32 s54, s54, 49152\n"
    "s_add_u32 m0, s54, 0\n"
    "s_add_u32 s47, s50, s53\n"
    "buffer_load_dwordx4 v9, s[20:23], s47 offen offset:0 lds\n"
    "v_mul_f32 v75, v59, v59\n"
    "v_fmaak_f32 v72, v72, v15, 0xc0135761\n"
    "v_fmaak_f32 v73, v73, v15, 0xc0135761\n"
    "v_mfma_f32_16x16x32_bf16 v[188:191], v[16:19], v[92:95], 0\n"
    "v_fmaak_f32 v74, v74, v15, 0xc0135761\n"
    "v_fmaak_f32 v75, v75, v15, 0xc0135761\n"
    "v_mul_f32 v72, v56, v72\n"
    "v_mfma_f32_16x16x32_bf16 v[192:195], v[20:23], v[80:83], 0\n"
    "ds_write_b128 v214, v[88:91] offset:2048\n"
    "ds_read_b64_tr_b16 v[16:17], v216 offset:24576\n"
    "v_mul_f32 v73, v57, v73\n"
    "v_mul_f32 v74, v58, v74\n"
    "v_mul_f32 v75, v59, v75\n"
    "v_exp_f32 v72, v72\n"
    "v_mfma_f32_16x16x32_bf16 v[196:199], v[20:23], v[84:87], 0\n"
    "ds_read_b64_tr_b16 v[18:19], v216 offset:26624\n"
    "v_exp_f32 v73, v73\n"
    "v_exp_f32 v74, v74\n"
    "v_exp_f32 v75, v75\n"
    "v_mfma_f32_16x16x32_bf16 v[200:203], v[20:23], v[88:91], 0\n"
    "ds_write_b128 v214, v[92:95] offset:3072\n"
    "v_add_f32 v72, 1.0, v72\n"
    "v_add_f32 v73, 1.0, v73\n"
    "v_add_f32 v74, 1.0, v74\n"
    "v_mfma_f32_16x16x32_bf16 v[204:207], v[20:23], v[92:95], 0\n"
    "buffer_load_dwordx4 v10, s[20:23], s47 offen offset:1024 lds\n"
    "v_add_f32 v75, 1.0, v75\n"
    "v_rcp_f32 v72, v72\n"
    "v_rcp_f32 v73, v73\n"
    "s_waitcnt lgkmcnt(10)\n"
    "v_mfma_f32_16x16x32_bf16 v[176:179], v[24:27], v[96:99], v[176:179]\n"
    "ds_write_b128 v214, v[96:99] offset:4096\n"
    "ds_read_b64_tr_b16 v[20:21], v217 offset:24576\n"
    "v_rcp_f32 v74, v74\n"
    "v_rcp_f32 v75, v75\n"
    "v_mul_f32 v72, v56, v72\n"
    "v_mul_f32 v73, v57, v73\n"
    "v_mfma_f32_16x16x32_bf16 v[180:183], v[24:27], v[100:103], v[180:183]\n"
    "ds_read_b64_tr_b16 v[22:23], v217 offset:26624\n"
    "v_mul_f32 v74, v58, v74\n"
    "v_mul_f32 v75, v59, v75\n"
    "v_cvt_pk_bf16_f32 v162, v72, v73\n"
    "v_mfma_f32_16x16x32_bf16 v[184:187], v[24:27], v[104:107], v[184:187]\n"
    "ds_write_b128 v214, v[100:103] offset:5120\n"
    "v_cvt_pk_bf16_f32 v163, v74, v75\n"
    "v_mul_f32 v76, v60, v60\n"
    "v_mul_f32 v77, v61, v61\n"
    "v_mfma_f32_16x16x32_bf16 v[188:191], v[24:27], v[108:111], v[188:191]\n"
    "v_mul_f32 v78, v62, v62\n"
    "v_mul_f32 v79, v63, v63\n"
    "v_fmaak_f32 v76, v76, v15, 0xc0135761\n"
    "v_mfma_f32_16x16x32_bf16 v[192:195], v[28:31], v[96:99], v[192:195]\n"
    "ds_write_b128 v214, v[104:107] offset:6144\n"
    "buffer_load_dwordx4 v11, s[20:23], s47 offen offset:2048 lds\n"
    "ds_read_b64_tr_b16 v[24:25], v216 offset:32768\n"
    "v_fmaak_f32 v77, v77, v15, 0xc0135761\n"
    "v_fmaak_f32 v78, v78, v15, 0xc0135761\n"
    "v_fmaak_f32 v79, v79, v15, 0xc0135761\n"
    "v_mul_f32 v76, v60, v76\n"
    "v_mfma_f32_16x16x32_bf16 v[196:199], v[28:31], v[100:103], v[196:199]\n"
    "ds_read_b64_tr_b16 v[26:27], v216 offset:34816\n"
    "v_mul_f32 v77, v61, v77\n"
    "v_mul_f32 v78, v62, v78\n"
    "v_mul_f32 v79, v63, v79\n"
    "v_mfma_f32_16x16x32_bf16 v[200:203], v[28:31], v[104:107], v[200:203]\n"
    "ds_write_b128 v214, v[108:111] offset:7168\n"
    "v_exp_f32 v76, v76\n"
    "v_exp_f32 v77, v77\n"
    "v_exp_f32 v78, v78\n"
    "v_mfma_f32_16x16x32_bf16 v[204:207], v[28:31], v[108:111], v[204:207]\n"
    "v_exp_f32 v79, v79\n"
    "v_add_f32 v76, 1.0, v76\n"
    "v_add_f32 v77, 1.0, v77\n"
    "s_waitcnt lgkmcnt(13)\n"
    "v_mfma_f32_16x16x32_bf16 v[176:179], v[32:35], v[112:115], v[176:179]\n"
    "ds_write_b128 v214, v[112:115] offset:8192\n"
    "ds_read_b64_tr_b16 v[28:29], v217 offset:32768\n"
    "v_add_f32 v78, 1.0, v78\n"
    "v_add_f32 v79, 1.0, v79\n"
    "v_rcp_f32 v76, v76\n"
    "v_mfma_f32_16x16x32_bf16 v[180:183], v[32:35], v[116:119], v[180:183]\n"
    "buffer_load_dwordx4 v12, s[20:23], s47 offen offset:3072 lds\n"
    "ds_read_b64_tr_b16 v[30:31], v217 offset:34816\n"
    "v_rcp_f32 v77, v77\n"
    "v_rcp_f32 v78, v78\n"
    "v_rcp_f32 v79, v79\n"
    "v_mfma_f32_16x16x32_bf16 v[184:187], v[32:35], v[120:123], v[184:187]\n"
    "ds_write_b128 v214, v[116:119] offset:9216\n"
    "v_mul_f32 v76, v60, v76\n"
    "v_mul_f32 v77, v61, v77\n"
    "v_mul_f32 v78, v62, v78\n"
    "v_mfma_f32_16x16x32_bf16 v[188:191], v[32:35], v[124:127], v[188:191]\n"
    "v_mul_f32 v79, v63, v79\n"
    "v_cvt_pk_bf16_f32 v166, v76, v77\n"
    "v_cvt_pk_bf16_f32 v167, v78, v79\n"
    "v_mfma_f32_16x16x32_bf16 v[192:195], v[36:39], v[112:115], v[192:195]\n"
    "ds_write_b128 v214, v[120:123] offset:10240\n"
    "ds_read_b64_tr_b16 v[32:33], v218 offset:0\n"
    "v_mul_f32 v72, v64, v64\n"
    "v_mul_f32 v73, v65, v65\n"
    "v_mul_f32 v74, v66, v66\n"
    "v_mfma_f32_16x16x32_bf16 v[196:199], v[36:39], v[116:119], v[196:199]\n"
    "ds_read_b64_tr_b16 v[34:35], v218 offset:2048\n"
    "v_mul_f32 v75, v67, v67\n"
    "v_fmaak_f32 v72, v72, v15, 0xc0135761\n"
    "v_fmaak_f32 v73, v73, v15, 0xc0135761\n"
    "v_mfma_f32_16x16x32_bf16 v[200:203], v[36:39], v[120:123], v[200:203]\n"
    "ds_write_b128 v214, v[124:127] offset:11264\n"
    "v_fmaak_f32 v74, v74, v15, 0xc0135761\n"
    "v_fmaak_f32 v75, v75, v15, 0xc0135761\n"
    "v_mul_f32 v72, v64, v72\n"
    "v_mfma_f32_16x16x32_bf16 v[204:207], v[36:39], v[124:127], v[204:207]\n"
    "s_add_u32 m0, s54, 4096\n"
    "s_add_u32 s47, s51, s53\n"
    "buffer_load_dwordx4 v9, s[20:23], s47 offen offset:0 lds\n"
    "v_mul_f32 v73, v65, v73\n"
    "v_mul_f32 v74, v66, v74\n"
    "v_mul_f32 v75, v67, v75\n"
    "s_waitcnt lgkmcnt(0)\n"
    "s_barrier\n"
    "v_mfma_f32_16x16x32_bf16 v[176:179], v[16:19], v[128:131], v[176:179]\n"
    "ds_read_b128 a[0:3], v215 offset:0\n"
    "ds_read_b64_tr_b16 v[36:37], v219 offset:0\n"
    "v_exp_f32 v72, v72\n"
    "v_exp_f32 v73, v73\n"
    "v_exp_f32 v74, v74\n"
    "v_mfma_f32_16x16x32_bf16 v[180:183], v[16:19], v[132:135], v[180:183]\n"
    "ds_read_b128 a[4:7], v215 offset:1024\n"
    "ds_read_b64_tr_b16 v[38:39], v219 offset:2048\n"
    "v_exp_f32 v75, v75\n"
    "v_add_f32 v72, 1.0, v72\n"
    "v_add_f32 v73, 1.0, v73\n"
    "v_mfma_f32_16x16x32_bf16 v[184:187], v[16:19], v[136:139], v[184:187]\n"
    "ds_read_b128 a[8:11], v215 offset:2048\n"
    "v_add_f32 v74, 1.0, v74\n"
    "v_add_f32 v75, 1.0, v75\n"
    "v_rcp_f32 v72, v72\n"
    "v_mfma_f32_16x16x32_bf16 v[188:191], v[16:19], v[140:143], v[188:191]\n"
    "ds_read_b128 a[12:15], v215 offset:3072\n"
    "v_rcp_f32 v73, v73\n"
    "v_rcp_f32 v74, v74\n"
    "v_rcp_f32 v75, v75\n"
    "v_mfma_f32_16x16x32_bf16 v[192:195], v[20:23], v[128:131], v[192:195]\n"
    "buffer_load_dwordx4 v10, s[20:23], s47 offen offset:1024 lds\n"
    "ds_read_b64_tr_b16 v[16:17], v218 offset:8192\n"
    "v_mul_f32 v72, v64, v72\n"
    "v_mul_f32 v73, v65, v73\n"
    "v_mul_f32 v74, v66, v74\n"
    "v_mfma_f32_16x16x32_bf16 v[196:199], v[20:23], v[132:135], v[196:199]\n"
    "ds_read_b128 a[16:19], v215 offset:4096\n"
    "ds_read_b64_tr_b16 v[18:19], v218 offset:10240\n"
    "v_mul_f32 v75, v67, v75\n"
    "v_cvt_pk_bf16_f32 v170, v72, v73\n"
    "v_cvt_pk_bf16_f32 v171, v74, v75\n"
    "v_mfma_f32_16x16x32_bf16 v[200:203], v[20:23], v[136:139], v[200:203]\n"
    "ds_read_b128 a[20:23], v215 offset:5120\n"
    "v_mul_f32 v76, v68, v68\n"
    "v_mul_f32 v77, v69, v69\n"
    "v_mul_f32 v78, v70, v70\n"
    "v_mfma_f32_16x16x32_bf16 v[204:207], v[20:23], v[140:143], v[204:207]\n"
    "ds_read_b128 a[24:27], v215 offset:6144\n"
    "v_mul_f32 v79, v71, v71\n"
    "v_fmaak_f32 v76, v76, v15, 0xc0135761\n"
    "v_fmaak_f32 v77, v77, v15, 0xc0135761\n"
    "v_mfma_f32_16x16x32_bf16 v[176:179], v[24:27], v[144:147], v[176:179]\n"
    "ds_read_b128 a[28:31], v215 offset:7168\n"
    "ds_read_b64_tr_b16 v[20:21], v219 offset:8192\n"
    "v_fmaak_f32 v78, v78, v15, 0xc0135761\n"
    "v_fmaak_f32 v79, v79, v15, 0xc0135761\n"
    "v_mul_f32 v76, v68, v76\n"
    "v_mfma_f32_16x16x32_bf16 v[180:183], v[24:27], v[148:151], v[180:183]\n"
    "buffer_load_dwordx4 v11, s[20:23], s47 offen offset:2048 lds\n"
    "ds_read_b64_tr_b16 v[22:23], v219 offset:10240\n"
    "v_mul_f32 v77, v69, v77\n"
    "v_mul_f32 v78, v70, v78\n"
    "v_mul_f32 v79, v71, v79\n"
    "v_mfma_f32_16x16x32_bf16 v[184:187], v[24:27], v[152:155], v[184:187]\n"
    "ds_read_b128 a[32:35], v215 offset:8192\n"
    "v_exp_f32 v76, v76\n"
    "v_exp_f32 v77, v77\n"
    "v_exp_f32 v78, v78\n"
    "v_mfma_f32_16x16x32_bf16 v[188:191], v[24:27], v[156:159], v[188:191]\n"
    "ds_read_b128 a[36:39], v215 offset:9216\n"
    "v_exp_f32 v79, v79\n"
    "v_add_f32 v76, 1.0, v76\n"
    "v_add_f32 v77, 1.0, v77\n"
    "v_mfma_f32_16x16x32_bf16 v[192:195], v[28:31], v[144:147], v[192:195]\n"
    "ds_read_b128 a[40:43], v215 offset:10240\n"
    "ds_read_b64_tr_b16 v[24:25], v218 offset:16384\n"
    "v_add_f32 v78, 1.0, v78\n"
    "v_add_f32 v79, 1.0, v79\n"
    "v_rcp_f32 v76, v76\n"
    "v_mfma_f32_16x16x32_bf16 v[196:199], v[28:31], v[148:151], v[196:199]\n"
    "ds_read_b64_tr_b16 v[26:27], v218 offset:18432\n"
    "v_rcp_f32 v77, v77\n"
    "v_rcp_f32 v78, v78\n"
    "v_rcp_f32 v79, v79\n"
    "v_mfma_f32_16x16x32_bf16 v[200:203], v[28:31], v[152:155], v[200:203]\n"
    "buffer_load_dwordx4 v12, s[20:23], s47 offen offset:3072 lds\n"
    "ds_read_b128 a[44:47], v215 offset:11264\n"
    "v_mul_f32 v76, v68, v76\n"
    "v_mul_f32 v77, v69, v77\n"
    "v_mul_f32 v78, v70, v78\n"
    "v_mfma_f32_16x16x32_bf16 v[204:207], v[28:31], v[156:159], v[204:207]\n"
    "v_mul_f32 v79, v71, v79\n"
    "v_cvt_pk_bf16_f32 v174, v76, v77\n"
    "v_cvt_pk_bf16_f32 v175, v78, v79\n"
    "s_waitcnt lgkmcnt(0)\n"
    "s_barrier\n"
    "v_mfma_f32_16x16x32_bf16 v[176:179], v[32:35], a[0:3], v[176:179]\n"
    "ds_write_b128 v214, v[128:131] offset:0\n"
    "ds_read_b64_tr_b16 v[28:29], v219 offset:16384\n"
    "v_mfma_f32_16x16x32_bf16 v[180:183], v[32:35], a[4:7], v[180:183]\n"
    "ds_read_b64_tr_b16 v[30:31], v219 offset:18432\n"
    "v_mfma_f32_16x16x32_bf16 v[184:187], v[32:35], a[8:11], v[184:187]\n"
    "ds_write_b128 v214, v[132:135] offset:1024\n"
    "v_mfma_f32_16x16x32_bf16 v[188:191], v[32:35], a[12:15], v[188:191]\n"
    "v_mfma_f32_16x16x32_bf16 v[192:195], v[36:39], a[0:3], v[192:195]\n"
    "s_add_u32 m0, s54, 8192\n"
    "s_add_u32 s47, s52, s53\n"
    "buffer_load_dwordx4 v9, s[20:23], s47 offen offset:0 lds\n"
    "ds_write_b128 v214, v[136:139] offset:2048\n"
    "ds_read_b64_tr_b16 v[32:33], v216 offset:40960\n"
    "v_mfma_f32_16x16x32_bf16 v[196:199], v[36:39], a[4:7], v[196:199]\n"
    "ds_read_b64_tr_b16 v[34:35], v216 offset:43008\n"
    "v_mfma_f32_16x16x32_bf16 v[200:203], v[36:39], a[8:11], v[200:203]\n"
    "ds_write_b128 v214, v[140:143] offset:3072\n"
    "v_mfma_f32_16x16x32_bf16 v[204:207], v[36:39], a[12:15], v[204:207]\n"
    "v_mfma_f32_16x16x32_bf16 v[176:179], v[16:19], a[16:19], v[176:179]\n"
    "ds_write_b128 v214, v[144:147] offset:4096\n"
    "ds_read_b64_tr_b16 v[36:37], v217 offset:40960\n"
    "v_mfma_f32_16x16x32_bf16 v[180:183], v[16:19], a[20:23], v[180:183]\n"
    "buffer_load_dwordx4 v10, s[20:23], s47 offen offset:1024 lds\n"
    "ds_read_b64_tr_b16 v[38:39], v217 offset:43008\n"
    "v_mfma_f32_16x16x32_bf16 v[184:187], v[16:19], a[24:27], v[184:187]\n"
    "ds_write_b128 v214, v[148:151] offset:5120\n"
    "v_mfma_f32_16x16x32_bf16 v[188:191], v[16:19], a[28:31], v[188:191]\n"
    "v_mfma_f32_16x16x32_bf16 v[192:195], v[20:23], a[16:19], v[192:195]\n"
    "ds_write_b128 v214, v[152:155] offset:6144\n"
    "ds_read_b64_tr_b16 v[16:17], v218 offset:24576\n"
    "v_mfma_f32_16x16x32_bf16 v[196:199], v[20:23], a[20:23], v[196:199]\n"
    "ds_read_b64_tr_b16 v[18:19], v218 offset:26624\n"
    "v_mfma_f32_16x16x32_bf16 v[200:203], v[20:23], a[24:27], v[200:203]\n"
    "buffer_load_dwordx4 v11, s[20:23], s47 offen offset:2048 lds\n"
    "ds_write_b128 v214, v[156:159] offset:7168\n"
    "v_mfma_f32_16x16x32_bf16 v[204:207], v[20:23], a[28:31], v[204:207]\n"
    "s_waitcnt lgkmcnt(13)\n"
    "v_mfma_f32_16x16x32_bf16 v[176:179], v[24:27], a[32:35], v[176:179]\n"
    "ds_write_b128 v214, v[160:163] offset:8192\n"
    "ds_read_b64_tr_b16 v[20:21], v219 offset:24576\n"
    "v_mfma_f32_16x16x32_bf16 v[180:183], v[24:27], a[36:39], v[180:183]\n"
    "ds_read_b64_tr_b16 v[22:23], v219 offset:26624\n"
    "v_mfma_f32_16x16x32_bf16 v[184:187], v[24:27], a[40:43], v[184:187]\n"
    "ds_write_b128 v214, v[164:167] offset:9216\n"
    "v_mfma_f32_16x16x32_bf16 v[188:191], v[24:27], a[44:47], v[188:191]\n"
    "v_mfma_f32_16x16x32_bf16 v[192:195], v[28:31], a[32:35], v[192:195]\n"
    "buffer_load_dwordx4 v12, s[20:23], s47 offen offset:3072 lds\n"
    "ds_write_b128 v214, v[168:171] offset:10240\n"
    "ds_read_b64_tr_b16 v[24:25], v218 offset:32768\n"
    "v_mfma_f32_16x16x32_bf16 v[196:199], v[28:31], a[36:39], v[196:199]\n"
    "ds_read_b64_tr_b16 v[26:27], v218 offset:34816\n"
    "v_mfma_f32_16x16x32_bf16 v[200:203], v[28:31], a[40:43], v[200:203]\n"
    "ds_write_b128 v214, v[172:175] offset:11264\n"
    "v_mfma_f32_16x16x32_bf16 v[204:207], v[28:31], a[44:47], v[204:207]\n"
    "s_waitcnt lgkmcnt(0)\n"
    "s_barrier\n"
    "v_mfma_f32_16x16x32_bf16 v[176:179], v[32:35], v[160:163], v[176:179]\n"
    "ds_read_b128 a[48:51], v215 offset:0\n"
    "ds_read_b128 a[52:55], v215 offset:1024\n"
    "ds_read_b64_tr_b16 v[28:29], v219 offset:32768\n"
    "v_mfma_f32_16x16x32_bf16 v[180:183], v[32:35], v[164:167], v[180:183]\n"
    "ds_read_b128 a[56:59], v215 offset:2048\n"
    "ds_read_b128 a[60:63], v215 offset:3072\n"
    "ds_read_b64_tr_b16 v[30:31], v219 offset:34816\n"
    "v_mfma_f32_16x16x32_bf16 v[184:187], v[32:35], v[168:171], v[184:187]\n"
    "ds_read_b128 a[64:67], v215 offset:4096\n"
    "v_mfma_f32_16x16x32_bf16 v[188:191], v[32:35], v[172:175], v[188:191]\n"
    "ds_read_b128 a[68:71], v215 offset:5120\n"
    "ds_read_b128 a[72:75], v215 offset:6144\n"
    "v_mfma_f32_16x16x32_bf16 v[192:195], v[36:39], v[160:163], v[192:195]\n"
    "ds_read_b128 a[76:79], v215 offset:7168\n"
    "ds_read_b64_tr_b16 v[32:33], v218 offset:40960\n"
    "v_mfma_f32_16x16x32_bf16 v[196:199], v[36:39], v[164:167], v[196:199]\n"
    "ds_read_b128 a[80:83], v215 offset:8192\n"
    "ds_read_b128 a[84:87], v215 offset:9216\n"
    "ds_read_b64_tr_b16 v[34:35], v218 offset:43008\n"
    "v_mfma_f32_16x16x32_bf16 v[200:203], v[36:39], v[168:171], v[200:203]\n"
    "ds_read_b128 a[88:91], v215 offset:10240\n"
    "v_mfma_f32_16x16x32_bf16 v[204:207], v[36:39], v[172:175], v[204:207]\n"
    "ds_read_b128 a[92:95], v215 offset:11264\n"
    "s_waitcnt lgkmcnt(0)\n"
    "s_barrier\n"
    "v_mfma_f32_16x16x32_bf16 v[176:179], v[16:19], a[48:51], v[176:179]\n"
    "ds_read_b64_tr_b16 v[36:37], v219 offset:40960\n"
    "v_mfma_f32_16x16x32_bf16 v[180:183], v[16:19], a[52:55], v[180:183]\n"
    "ds_read_b64_tr_b16 v[38:39], v219 offset:43008\n"
    "v_mfma_f32_16x16x32_bf16 v[184:187], v[16:19], a[56:59], v[184:187]\n"
    "s_add_u32 s40, s15, 2\n"
    "s_cmp_ge_u32 s40, 9\n"
    "s_cselect_b32 s41, 9, 0\n"
    "s_sub_u32 s40, s40, s41\n"
    "s_lshl_b32 s53, s40, 7\n"
    "s_mul_i32 s54, s9, 12288\n"
    "s_add_u32 s54, s54, 98304\n"
    "s_add_u32 m0, s54, 0\n"
    "s_add_u32 s47, s50, s53\n"
    "buffer_load_dwordx4 v9, s[20:23], s47 offen offset:0 lds\n"
    "v_mfma_f32_16x16x32_bf16 v[188:191], v[16:19], a[60:63], v[188:191]\n"
    "v_mfma_f32_16x16x32_bf16 v[192:195], v[20:23], a[48:51], v[192:195]\n"
    "v_mfma_f32_16x16x32_bf16 v[196:199], v[20:23], a[52:55], v[196:199]\n"
    "v_mfma_f32_16x16x32_bf16 v[200:203], v[20:23], a[56:59], v[200:203]\n"
    "v_mfma_f32_16x16x32_bf16 v[204:207], v[20:23], a[60:63], v[204:207]\n"
    "buffer_load_dwordx4 v10, s[20:23], s47 offen offset:1024 lds\n"
    "v_mfma_f32_16x16x32_bf16 v[176:179], v[24:27], a[64:67], v[176:179]\n"
    "v_mfma_f32_16x16x32_bf16 v[180:183], v[24:27], a[68:71], v[180:183]\n"
    "v_mfma_f32_16x16x32_bf16 v[184:187], v[24:27], a[72:75], v[184:187]\n"
    "v_mfma_f32_16x16x32_bf16 v[188:191], v[24:27], a[76:79], v[188:191]\n"
    "s_waitcnt lgkmcnt(0)\n"
    "s_waitcnt vmcnt(2)\n"
    "s_barrier\n"
    "v_mfma_f32_16x16x32_bf16 v[192:195], v[28:31], a[64:67], v[192:195]\n"
    "buffer_load_dwordx4 v11, s[20:23], s47 offen offset:2048 lds\n"
    "ds_read_b64_tr_b16 v[16:17], v220 offset:0\n"
    "v_mfma_f32_16x16x32_bf16 v[196:199], v[28:31], a[68:71], v[196:199]\n"
    "ds_read_b64_tr_b16 v[18:19], v220 offset:2048\n"
    "v_mfma_f32_16x16x32_bf16 v[200:203], v[28:31], a[72:75], v[200:203]\n"
    "ds_read_b64_tr_b16 v[20:21], v221 offset:0\n"
    "v_mfma_f32_16x16x32_bf16 v[204:207], v[28:31], a[76:79], v[204:207]\n"
    "ds_read_b64_tr_b16 v[22:23], v221 offset:2048\n"
    "v_mfma_f32_16x16x32_bf16 v[176:179], v[32:35], a[80:83], v[176:179]\n"
    "ds_read_b64_tr_b16 v[24:25], v220 offset:8192\n"
    "v_mfma_f32_16x16x32_bf16 v[180:183], v[32:35], a[84:87], v[180:183]\n"
    "buffer_load_dwordx4 v12, s[20:23], s47 offen offset:3072 lds\n"
    "ds_read_b64_tr_b16 v[26:27], v220 offset:10240\n"
    "v_mfma_f32_16x16x32_bf16 v[184:187], v[32:35], a[88:91], v[184:187]\n"
    "ds_read_b64_tr_b16 v[28:29], v221 offset:8192\n"
    "v_mfma_f32_16x16x32_bf16 v[188:191], v[32:35], a[92:95], v[188:191]\n"
    "ds_read_b64_tr_b16 v[30:31], v221 offset:10240\n"
    "v_mfma_f32_16x16x32_bf16 v[192:195], v[36:39], a[80:83], v[192:195]\n"
    "ds_read_b64_tr_b16 v[32:33], v220 offset:16384\n"
    "v_mfma_f32_16x16x32_bf16 v[196:199], v[36:39], a[84:87], v[196:199]\n"
    "ds_read_b64_tr_b16 v[34:35], v220 offset:18432\n"
    "v_mfma_f32_16x16x32_bf16 v[200:203], v[36:39], a[88:91], v[200:203]\n"
    "v_mfma_f32_16x16x32_bf16 v[204:207], v[36:39], a[92:95], v[204:207]\n"
    "s_add_u32 m0, s54, 4096\n"
    "s_add_u32 s47, s51, s53\n"
    "buffer_load_dwordx4 v9, s[20:23], s47 offen offset:0 lds\n"
    "s_waitcnt lgkmcnt(6)\n"
    "v_cvt_pk_bf16_f32 v240, v176, v177\n"
    "v_cvt_pk_bf16_f32 v241, v178, v179\n"
    "v_mfma_f32_16x16x32_bf16 v[176:179], v[16:19], v[80:83], 0\n"
    "ds_read_b64_tr_b16 v[36:37], v221 offset:16384\n"
    "v_add_u32 v216, 98304, v224\n"
    "v_cvt_pk_bf16_f32 v244, v180, v181\n"
    "v_cvt_pk_bf16_f32 v245, v182, v183\n"
    "v_mfma_f32_16x16x32_bf16 v[180:183], v[16:19], v[84:87], 0\n"
    "buffer_load_dwordx4 v10, s[20:23], s47 offen offset:1024 lds\n"
    "ds_read_b64_tr_b16 v[38:39], v221 offset:18432\n"
    "v_cvt_pk_bf16_f32 v248, v184, v185\n"
    "v_cvt_pk_bf16_f32 v249, v186, v187\n"
    "v_mfma_f32_16x16x32_bf16 v[184:187], v[16:19], v[88:91], 0\n"
    "v_cvt_pk_bf16_f32 v252, v188, v189\n"
    "v_cvt_pk_bf16_f32 v253, v190, v191\n"
    "v_mfma_f32_16x16x32_bf16 v[188:191], v[16:19], v[92:95], 0\n"
    "v_cvt_pk_bf16_f32 v242, v192, v193\n"
    "v_cvt_pk_bf16_f32 v243, v194, v195\n"
    "v_mfma_f32_16x16x32_bf16 v[192:195], v[20:23], v[80:83], 0\n"
    "ds_read_b64_tr_b16 v[16:17], v220 offset:24576\n"
    "v_add_u32 v217, 98304, v225\n"
    "v_cvt_pk_bf16_f32 v246, v196, v197\n"
    "v_cvt_pk_bf16_f32 v247, v198, v199\n"
    "v_mfma_f32_16x16x32_bf16 v[196:199], v[20:23], v[84:87], 0\n"
    "buffer_load_dwordx4 v11, s[20:23], s47 offen offset:2048 lds\n"
    "ds_read_b64_tr_b16 v[18:19], v220 offset:26624\n"
    "v_cvt_pk_bf16_f32 v250, v200, v201\n"
    "v_cvt_pk_bf16_f32 v251, v202, v203\n"
    "v_mfma_f32_16x16x32_bf16 v[200:203], v[20:23], v[88:91], 0\n"
    "v_cvt_pk_bf16_f32 v254, v204, v205\n"
    "v_cvt_pk_bf16_f32 v255, v206, v207\n"
    "v_mfma_f32_16x16x32_bf16 v[204:207], v[20:23], v[92:95], 0\n"
    "s_waitcnt lgkmcnt(6)\n"
    "v_mfma_f32_16x16x32_bf16 v[176:179], v[24:27], v[96:99], v[176:179]\n"
    "ds_read_b64_tr_b16 v[20:21], v221 offset:24576\n"
    "v_add_u32 v218, 98304, v226\n"
    "v_mfma_f32_16x16x32_bf16 v[180:183], v[24:27], v[100:103], v[180:183]\n"
    "buffer_store_dwordx4 v[240:243], v13, s[28:31], s60 offen offset:0 sc1\n"
    "ds_read_b64_tr_b16 v[22:23], v221 offset:26624\n"
    "v_mfma_f32_16x16x32_bf16 v[184:187], v[24:27], v[104:107], v[184:187]\n"
    "buffer_load_dwordx4 v12, s[20:23], s47 offen offset:3072 lds\n"
    "v_mfma_f32_16x16x32_bf16 v[188:191], v[24:27], v[108:111], v[188:191]\n"
    "v_mfma_f32_16x16x32_bf16 v[192:195], v[28:31], v[96:99], v[192:195]\n"
    "ds_read_b64_tr_b16 v[24:25], v220 offset:32768\n"
    "v_add_u32 v219, 98304, v227\n"
    "v_mfma_f32_16x16x32_bf16 v[196:199], v[28:31], v[100:103], v[196:199]\n"
    "buffer_store_dwordx4 v[244:247], v13, s[28:31], s60 offen offset:1024 sc1\n"
    "ds_read_b64_tr_b16 v[26:27], v220 offset:34816\n"
    "v_mfma_f32_16x16x32_bf16 v[200:203], v[28:31], v[104:107], v[200:203]\n"
    "v_mfma_f32_16x16x32_bf16 v[204:207], v[28:31], v[108:111], v[204:207]\n"
    "s_add_u32 m0, s54, 8192\n"
    "s_add_u32 s47, s52, s53\n"
    "buffer_load_dwordx4 v9, s[20:23], s47 offen offset:0 lds\n"
    "s_waitcnt lgkmcnt(6)\n"
    "v_mfma_f32_16x16x32_bf16 v[176:179], v[32:35], v[112:115], v[176:179]\n"
    "ds_read_b64_tr_b16 v[28:29], v221 offset:32768\n"
    "v_mfma_f32_16x16x32_bf16 v[180:183], v[32:35], v[116:119], v[180:183]\n"
    "buffer_store_dwordx4 v[248:251], v13, s[28:31], s60 offen offset:2048 sc1\n"
    "ds_read_b64_tr_b16 v[30:31], v221 offset:34816\n"
    "v_mfma_f32_16x16x32_bf16 v[184:187], v[32:35], v[120:123], v[184:187]\n"
    "v_mfma_f32_16x16x32_bf16 v[188:191], v[32:35], v[124:127], v[188:191]\n"
    "v_mfma_f32_16x16x32_bf16 v[192:195], v[36:39], v[112:115], v[192:195]\n"
    "buffer_load_dwordx4 v10, s[20:23], s47 offen offset:1024 lds\n"
    "ds_read_b64_tr_b16 v[32:33], v220 offset:40960\n"
    "v_mfma_f32_16x16x32_bf16 v[196:199], v[36:39], v[116:119], v[196:199]\n"
    "buffer_store_dwordx4 v[252:255], v13, s[28:31], s60 offen offset:3072 sc1\n"
    "ds_read_b64_tr_b16 v[34:35], v220 offset:43008\n"
    "v_mfma_f32_16x16x32_bf16 v[200:203], v[36:39], v[120:123], v[200:203]\n"
    "v_mfma_f32_16x16x32_bf16 v[204:207], v[36:39], v[124:127], v[204:207]\n"
    "s_waitcnt lgkmcnt(6)\n"
    "v_mfma_f32_16x16x32_bf16 v[176:179], v[16:19], v[128:131], v[176:179]\n"
    "ds_read_b64_tr_b16 v[36:37], v221 offset:40960\n"
    "v_mfma_f32_16x16x32_bf16 v[180:183], v[16:19], v[132:135], v[180:183]\n"
    "buffer_load_dwordx4 v11, s[20:23], s47 offen offset:2048 lds\n"
    "s_add_u32 s60, s60, 262144\n"
    "ds_read_b64_tr_b16 v[38:39], v221 offset:43008\n"
    "v_mfma_f32_16x16x32_bf16 v[184:187], v[16:19], v[136:139], v[184:187]\n"
    "v_mfma_f32_16x16x32_bf16 v[188:191], v[16:19], v[140:143], v[188:191]\n"
    "v_mfma_f32_16x16x32_bf16 v[192:195], v[20:23], v[128:131], v[192:195]\n"
    "ds_read_b64_tr_b16 v[16:17], v222 offset:0\n"
    "v_mfma_f32_16x16x32_bf16 v[196:199], v[20:23], v[132:135], v[196:199]\n"
    "ds_read_b64_tr_b16 v[18:19], v222 offset:2048\n"
    "v_mfma_f32_16x16x32_bf16 v[200:203], v[20:23], v[136:139], v[200:203]\n"
    "buffer_load_dwordx4 v12, s[20:23], s47 offen offset:3072 lds\n"
    "v_mfma_f32_16x16x32_bf16 v[204:207], v[20:23], v[140:143], v[204:207]\n"
    "s_waitcnt lgkmcnt(6)\n"
    "v_mfma_f32_16x16x32_bf16 v[176:179], v[24:27], v[144:147], v[176:179]\n"
    "ds_read_b64_tr_b16 v[20:21], v223 offset:0\n"
    "v_mfma_f32_16x16x32_bf16 v[180:183], v[24:27], v[148:151], v[180:183]\n"
    "ds_read_b64_tr_b16 v[22:23], v223 offset:2048\n"
    "v_mfma_f32_16x16x32_bf16 v[184:187], v[24:27], v[152:155], v[184:187]\n"
    "s_add_u32 s40, s15, 3\n"
    "s_cmp_ge_u32 s40, 9\n"
    "s_cselect_b32 s41, 9, 0\n"
    "s_sub_u32 s40, s40, s41\n"
    "s_lshl_b32 s53, s40, 7\n"
    "s_mul_i32 s54, s9, 12288\n"
    "s_add_u32 s54, s54, 0\n"
    "s_add_u32 m0, s54, 0\n"
    "s_add_u32 s47, s50, s53\n"
    "buffer_load_dwordx4 v9, s[20:23], s47 offen offset:0 lds\n"
    "v_mfma_f32_16x16x32_bf16 v[188:191], v[24:27], v[156:159], v[188:191]\n"
    "v_mfma_f32_16x16x32_bf16 v[192:195], v[28:31], v[144:147], v[192:195]\n"
    "ds_read_b64_tr_b16 v[24:25], v222 offset:8192\n"
    "v_mfma_f32_16x16x32_bf16 v[196:199], v[28:31], v[148:151], v[196:199]\n"
    "ds_read_b64_tr_b16 v[26:27], v222 offset:10240\n"
    "v_mfma_f32_16x16x32_bf16 v[200:203], v[28:31], v[152:155], v[200:203]\n"
    "v_mfma_f32_16x16x32_bf16 v[204:207], v[28:31], v[156:159], v[204:207]\n"
    "buffer_load_dwordx4 v10, s[20:23], s47 offen offset:1024 lds\n"
    "s_waitcnt lgkmcnt(6)\n"
    "v_mfma_f32_16x16x32_bf16 v[176:179], v[32:35], v[160:163], v[176:179]\n"
    "ds_read_b64_tr_b16 v[28:29], v223 offset:8192\n"
    "v_mfma_f32_16x16x32_bf16 v[180:183], v[32:35], v[164:167], v[180:183]\n"
    "ds_read_b64_tr_b16 v[30:31], v223 offset:10240\n"
    "v_mfma_f32_16x16x32_bf16 v[184:187], v[32:35], v[168:171], v[184:187]\n"
    "v_mfma_f32_16x16x32_bf16 v[188:191], v[32:35], v[172:175], v[188:191]\n"
    "v_mfma_f32_16x16x32_bf16 v[192:195], v[36:39], v[160:163], v[192:195]\n"
    "buffer_load_dwordx4 v11, s[20:23], s47 offen offset:2048 lds\n"
    "ds_read_b64_tr_b16 v[32:33], v222 offset:16384\n"
    "v_mfma_f32_16x16x32_bf16 v[196:199], v[36:39], v[164:167], v[196:199]\n"
    "ds_read_b64_tr_b16 v[34:35], v222 offset:18432\n"
    "v_mfma_f32_16x16x32_bf16 v[200:203], v[36:39], v[168:171], v[200:203]\n"
    "v_mfma_f32_16x16x32_bf16 v[204:207], v[36:39], v[172:175], v[204:207]\n"
    "s_waitcnt lgkmcnt(6)\n"
    "v_mfma_f32_16x16x32_bf16 v[176:179], v[16:19], a[0:3], v[176:179]\n"
    "ds_read_b64_tr_b16 v[36:37], v223 offset:16384\n"
    "v_mfma_f32_16x16x32_bf16 v[180:183], v[16:19], a[4:7], v[180:183]\n"
    "ds_read_b64_tr_b16 v[38:39], v223 offset:18432\n"
    "v_mfma_f32_16x16x32_bf16 v[184:187], v[16:19], a[8:11], v[184:187]\n"
    "buffer_load_dwordx4 v12, s[20:23], s47 offen offset:3072 lds\n"
    "v_mfma_f32_16x16x32_bf16 v[188:191], v[16:19], a[12:15], v[188:191]\n"
    "v_mfma_f32_16x16x32_bf16 v[192:195], v[20:23], a[0:3], v[192:195]\n"
    "ds_read_b64_tr_b16 v[16:17], v222 offset:24576\n"
    "v_mfma_f32_16x16x32_bf16 v[196:199], v[20:23], a[4:7], v[196:199]\n"
    "ds_read_b64_tr_b16 v[18:19], v222 offset:26624\n"
    "v_mfma_f32_16x16x32_bf16 v[200:203], v[20:23], a[8:11], v[200:203]\n"
    "v_mfma_f32_16x16x32_bf16 v[204:207], v[20:23], a[12:15], v[204:207]\n"
    "s_add_u32 m0, s54, 4096\n"
    "s_add_u32 s47, s51, s53\n"
    "buffer_load_dwordx4 v9, s[20:23], s47 offen offset:0 lds\n"
    "s_waitcnt lgkmcnt(6)\n"
    "v_mfma_f32_16x16x32_bf16 v[176:179], v[24:27], a[16:19], v[176:179]\n"
    "ds_read_b64_tr_b16 v[20:21], v223 offset:24576\n"
    "v_mfma_f32_16x16x32_bf16 v[180:183], v[24:27], a[20:23], v[180:183]\n"
    "ds_read_b64_tr_b16 v[22:23], v223 offset:26624\n"
    "v_mfma_f32_16x16x32_bf16 v[184:187], v[24:27], a[24:27], v[184:187]\n"
    "v_mfma_f32_16x16x32_bf16 v[188:191], v[24:27], a[28:31], v[188:191]\n"
    "v_mfma_f32_16x16x32_bf16 v[192:195], v[28:31], a[16:19], v[192:195]\n"
    "ds_read_b64_tr_b16 v[24:25], v222 offset:32768\n"
    "v_mfma_f32_16x16x32_bf16 v[196:199], v[28:31], a[20:23], v[196:199]\n"
    "buffer_load_dwordx4 v10, s[20:23], s47 offen offset:1024 lds\n"
    "ds_read_b64_tr_b16 v[26:27], v222 offset:34816\n"
    "v_mfma_f32_16x16x32_bf16 v[200:203], v[28:31], a[24:27], v[200:203]\n"
    "v_mfma_f32_16x16x32_bf16 v[204:207], v[28:31], a[28:31], v[204:207]\n"
    "s_waitcnt lgkmcnt(6)\n"
    "v_mfma_f32_16x16x32_bf16 v[176:179], v[32:35], a[32:35], v[176:179]\n"
    "ds_read_b64_tr_b16 v[28:29], v223 offset:32768\n"
    "v_mfma_f32_16x16x32_bf16 v[180:183], v[32:35], a[36:39], v[180:183]\n"
    "ds_read_b64_tr_b16 v[30:31], v223 offset:34816\n"
    "v_mfma_f32_16x16x32_bf16 v[184:187], v[32:35], a[40:43], v[184:187]\n"
    "buffer_load_dwordx4 v11, s[20:23], s47 offen offset:2048 lds\n"
    "v_mfma_f32_16x16x32_bf16 v[188:191], v[32:35], a[44:47], v[188:191]\n"
    "v_mfma_f32_16x16x32_bf16 v[192:195], v[36:39], a[32:35], v[192:195]\n"
    "ds_read_b64_tr_b16 v[32:33], v222 offset:40960\n"
    "v_mfma_f32_16x16x32_bf16 v[196:199], v[36:39], a[36:39], v[196:199]\n"
    "ds_read_b64_tr_b16 v[34:35], v222 offset:43008\n"
    "v_mfma_f32_16x16x32_bf16 v[200:203], v[36:39], a[40:43], v[200:203]\n"
    "v_mfma_f32_16x16x32_bf16 v[204:207], v[36:39], a[44:47], v[204:207]\n"
    "s_waitcnt lgkmcnt(6)\n"
    "v_mfma_f32_16x16x32_bf16 v[176:179], v[16:19], a[48:51], v[176:179]\n"
    "buffer_load_dwordx4 v12, s[20:23], s47 offen offset:3072 lds\n"
    "ds_read_b64_tr_b16 v[36:37], v223 offset:40960\n"
    "v_mfma_f32_16x16x32_bf16 v[180:183], v[16:19], a[52:55], v[180:183]\n"
    "ds_read_b64_tr_b16 v[38:39], v223 offset:43008\n"
    "v_mfma_f32_16x16x32_bf16 v[184:187], v[16:19], a[56:59], v[184:187]\n"
    "v_mfma_f32_16x16x32_bf16 v[188:191], v[16:19], a[60:63], v[188:191]\n"
    "v_mfma_f32_16x16x32_bf16 v[192:195], v[20:23], a[48:51], v[192:195]\n"
    "v_mfma_f32_16x16x32_bf16 v[196:199], v[20:23], a[52:55], v[196:199]\n"
    "s_add_u32 m0, s54, 8192\n"
    "s_add_u32 s47, s52, s53\n"
    "buffer_load_dwordx4 v9, s[20:23], s47 offen offset:0 lds\n"
    "v_mfma_f32_16x16x32_bf16 v[200:203], v[20:23], a[56:59], v[200:203]\n"
    "v_mfma_f32_16x16x32_bf16 v[204:207], v[20:23], a[60:63], v[204:207]\n"
    "s_waitcnt lgkmcnt(4)\n"
    "v_mfma_f32_16x16x32_bf16 v[176:179], v[24:27], a[64:67], v[176:179]\n"
    "v_mfma_f32_16x16x32_bf16 v[180:183], v[24:27], a[68:71], v[180:183]\n"
    "v_mfma_f32_16x16x32_bf16 v[184:187], v[24:27], a[72:75], v[184:187]\n"
    "v_mfma_f32_16x16x32_bf16 v[188:191], v[24:27], a[76:79], v[188:191]\n"
    "buffer_load_dwordx4 v10, s[20:23], s47 offen offset:1024 lds\n"
    "s_waitcnt lgkmcnt(0)\n"
    "s_waitcnt vmcnt(10)\n"
    "s_barrier\n"
    "v_mfma_f32_16x16x32_bf16 v[192:195], v[28:31], a[64:67], v[192:195]\n"
    "ds_read_b64_tr_b16 v[16:17], v216 offset:0\n"
    "v_mfma_f32_16x16x32_bf16 v[196:199], v[28:31], a[68:71], v[196:199]\n"
    "ds_read_b64_tr_b16 v[18:19], v216 offset:2048\n"
    "v_mfma_f32_16x16x32_bf16 v[200:203], v[28:31], a[72:75], v[200:203]\n"
    "ds_read_b64_tr_b16 v[20:21], v217 offset:0\n"
    "v_mfma_f32_16x16x32_bf16 v[204:207], v[28:31], a[76:79], v[204:207]\n"
    "ds_read_b64_tr_b16 v[22:23], v217 offset:2048\n"
    "v_mfma_f32_16x16x32_bf16 v[176:179], v[32:35], a[80:83], v[176:179]\n"
    "buffer_load_dwordx4 v11, s[20:23], s47 offen offset:2048 lds\n"
    "ds_read_b64_tr_b16 v[24:25], v216 offset:8192\n"
    "v_mfma_f32_16x16x32_bf16 v[180:183], v[32:35], a[84:87], v[180:183]\n"
    "ds_read_b64_tr_b16 v[26:27], v216 offset:10240\n"
    "v_mfma_f32_16x16x32_bf16 v[184:187], v[32:35], a[88:91], v[184:187]\n"
    "ds_read_b64_tr_b16 v[28:29], v217 offset:8192\n"
    "v_mfma_f32_16x16x32_bf16 v[188:191], v[32:35], a[92:95], v[188:191]\n"
    "ds_read_b64_tr_b16 v[30:31], v217 offset:10240\n"
    "v_mfma_f32_16x16x32_bf16 v[192:195], v[36:39], a[80:83], v[192:195]\n"
    "ds_read_b64_tr_b16 v[32:33], v216 offset:16384\n"
    "v_mfma_f32_16x16x32_bf16 v[196:199], v[36:39], a[84:87], v[196:199]\n"
    "ds_read_b64_tr_b16 v[34:35], v216 offset:18432\n"
    "v_mfma_f32_16x16x32_bf16 v[200:203], v[36:39], a[88:91], v[200:203]\n"
    "buffer_load_dwordx4 v12, s[20:23], s47 offen offset:3072 lds\n"
    "v_mfma_f32_16x16x32_bf16 v[204:207], v[36:39], a[92:95], v[204:207]\n"
    "s_waitcnt lgkmcnt(6)\n"
    "v_cvt_pk_bf16_f32 v240, v176, v177\n"
    "v_cvt_pk_bf16_f32 v241, v178, v179\n"
    "v_mfma_f32_16x16x32_bf16 v[176:179], v[16:19], v[80:83], 0\n"
    "ds_read_b64_tr_b16 v[36:37], v217 offset:16384\n"
    "v_add_u32 v220, 0, v224\n"
    "v_cvt_pk_bf16_f32 v244, v180, v181\n"
    "v_cvt_pk_bf16_f32 v245, v182, v183\n"
    "v_mfma_f32_16x16x32_bf16 v[180:183], v[16:19], v[84:87], 0\n"
    "ds_read_b64_tr_b16 v[38:39], v217 offset:18432\n"
    "v_cvt_pk_bf16_f32 v248, v184, v185\n"
    "v_cvt_pk_bf16_f32 v249, v186, v187\n"
    "v_mfma_f32_16x16x32_bf16 v[184:187], v[16:19], v[88:91], 0\n"
    "v_cvt_pk_bf16_f32 v252, v188, v189\n"
    "v_cvt_pk_bf16_f32 v253, v190, v191\n"
    "v_mfma_f32_16x16x32_bf16 v[188:191], v[16:19], v[92:95], 0\n"
    "v_cvt_pk_bf16_f32 v242, v192, v193\n"
    "v_cvt_pk_bf16_f32 v243, v194, v195\n"
    "v_mfma_f32_16x16x32_bf16 v[192:195], v[20:23], v[80:83], 0\n"
    "ds_read_b64_tr_b16 v[16:17], v216 offset:24576\n"
    "v_add_u32 v221, 0, v225\n"
    "v_cvt_pk_bf16_f32 v246, v196, v197\n"
    "v_cvt_pk_bf16_f32 v247, v198, v199\n"
    "v_mfma_f32_16x16x32_bf16 v[196:199], v[20:23], v[84:87], 0\n"
    "ds_read_b64_tr_b16 v[18:19], v216 offset:26624\n"
    "v_cvt_pk_bf16_f32 v250, v200, v201\n"
    "v_cvt_pk_bf16_f32 v251, v202, v203\n"
    "v_mfma_f32_16x16x32_bf16 v[200:203], v[20:23], v[88:91], 0\n"
    "v_cvt_pk_bf16_f32 v254, v204, v205\n"
    "v_cvt_pk_bf16_f32 v255, v206, v207\n"
    "v_mfma_f32_16x16x32_bf16 v[204:207], v[20:23], v[92:95], 0\n"
    "s_waitcnt lgkmcnt(6)\n"
    "v_mfma_f32_16x16x32_bf16 v[176:179], v[24:27], v[96:99], v[176:179]\n"
    "ds_read_b64_tr_b16 v[20:21], v217 offset:24576\n"
    "v_add_u32 v222, 0, v226\n"
    "v_mfma_f32_16x16x32_bf16 v[180:183], v[24:27], v[100:103], v[180:183]\n"
    "buffer_store_dwordx4 v[240:243], v13, s[28:31], s60 offen offset:0 sc1\n"
    "ds_read_b64_tr_b16 v[22:23], v217 offset:26624\n"
    "v_mfma_f32_16x16x32_bf16 v[184:187], v[24:27], v[104:107], v[184:187]\n"
    "v_mfma_f32_16x16x32_bf16 v[188:191], v[24:27], v[108:111], v[188:191]\n"
    "v_mfma_f32_16x16x32_bf16 v[192:195], v[28:31], v[96:99], v[192:195]\n"
    "ds_read_b64_tr_b16 v[24:25], v216 offset:32768\n"
    "v_add_u32 v223, 0, v227\n"
    "v_mfma_f32_16x16x32_bf16 v[196:199], v[28:31], v[100:103], v[196:199]\n"
    "buffer_store_dwordx4 v[244:247], v13, s[28:31], s60 offen offset:1024 sc1\n"
    "ds_read_b64_tr_b16 v[26:27], v216 offset:34816\n"
    "v_mfma_f32_16x16x32_bf16 v[200:203], v[28:31], v[104:107], v[200:203]\n"
    "v_mfma_f32_16x16x32_bf16 v[204:207], v[28:31], v[108:111], v[204:207]\n"
    "s_waitcnt lgkmcnt(6)\n"
    "v_mfma_f32_16x16x32_bf16 v[176:179], v[32:35], v[112:115], v[176:179]\n"
    "ds_read_b64_tr_b16 v[28:29], v217 offset:32768\n"
    "v_mfma_f32_16x16x32_bf16 v[180:183], v[32:35], v[116:119], v[180:183]\n"
    "buffer_store_dwordx4 v[248:251], v13, s[28:31], s60 offen offset:2048 sc1\n"
    "ds_read_b64_tr_b16 v[30:31], v217 offset:34816\n"
    "v_mfma_f32_16x16x32_bf16 v[184:187], v[32:35], v[120:123], v[184:187]\n"
    "v_mfma_f32_16x16x32_bf16 v[188:191], v[32:35], v[124:127], v[188:191]\n"
    "v_mfma_f32_16x16x32_bf16 v[192:195], v[36:39], v[112:115], v[192:195]\n"
    "ds_read_b64_tr_b16 v[32:33], v216 offset:40960\n"
    "v_mfma_f32_16x16x32_bf16 v[196:199], v[36:39], v[116:119], v[196:199]\n"
    "buffer_store_dwordx4 v[252:255], v13, s[28:31], s60 offen offset:3072 sc1\n"
    "ds_read_b64_tr_b16 v[34:35], v216 offset:43008\n"
    "v_mfma_f32_16x16x32_bf16 v[200:203], v[36:39], v[120:123], v[200:203]\n"
    "v_mfma_f32_16x16x32_bf16 v[204:207], v[36:39], v[124:127], v[204:207]\n"
    "s_waitcnt lgkmcnt(6)\n"
    "v_mfma_f32_16x16x32_bf16 v[176:179], v[16:19], v[128:131], v[176:179]\n"
    "ds_read_b64_tr_b16 v[36:37], v217 offset:40960\n"
    "v_mfma_f32_16x16x32_bf16 v[180:183], v[16:19], v[132:135], v[180:183]\n"
    "s_add_u32 s60, s60, 262144\n"
    "ds_read_b64_tr_b16 v[38:39], v217 offset:43008\n"
    "v_mfma_f32_16x16x32_bf16 v[184:187], v[16:19], v[136:139], v[184:187]\n"
    "v_mfma_f32_16x16x32_bf16 v[188:191], v[16:19], v[140:143], v[188:191]\n"
    "v_mfma_f32_16x16x32_bf16 v[192:195], v[20:23], v[128:131], v[192:195]\n"
    "s_add_u32 s40, s15, 4\n"
    "s_cmp_ge_u32 s40, 9\n"
    "s_cselect_b32 s41, 9, 0\n"
    "s_sub_u32 s40, s40, s41\n"
    "s_lshl_b32 s53, s40, 7\n"
    "s_mul_i32 s54, s9, 12288\n"
    "s_add_u32 s54, s54, 49152\n"
    "s_add_u32 m0, s54, 0\n"
    "s_add_u32 s47, s50, s53\n"
    "buffer_load_dwordx4 v9, s[20:23], s47 offen offset:0 lds\n"
    "ds_read_b64_tr_b16 v[16:17], v218 offset:0\n"
    "v_mfma_f32_16x16x32_bf16 v[196:199], v[20:23], v[132:135], v[196:199]\n"
    "ds_read_b64_tr_b16 v[18:19], v218 offset:2048\n"
    "v_mfma_f32_16x16x32_bf16 v[200:203], v[20:23], v[136:139], v[200:203]\n"
    "v_mfma_f32_16x16x32_bf16 v[204:207], v[20:23], v[140:143], v[204:207]\n"
    "s_waitcnt lgkmcnt(6)\n"
    "v_mfma_f32_16x16x32_bf16 v[176:179], v[24:27], v[144:147], v[176:179]\n"
    "ds_read_b64_tr_b16 v[20:21], v219 offset:0\n"
    "v_mfma_f32_16x16x32_bf16 v[180:183], v[24:27], v[148:151], v[180:183]\n"
    "buffer_load_dwordx4 v10, s[20:23], s47 offen offset:1024 lds\n"
    "ds_read_b64_tr_b16 v[22:23], v219 offset:2048\n"
    "v_mfma_f32_16x16x32_bf16 v[184:187], v[24:27], v[152:155], v[184:187]\n"
    "v_mfma_f32_16x16x32_bf16 v[188:191], v[24:27], v[156:159], v[188:191]\n"
    "v_mfma_f32_16x16x32_bf16 v[192:195], v[28:31], v[144:147], v[192:195]\n"
    "ds_read_b64_tr_b16 v[24:25], v218 offset:8192\n"
    "v_mfma_f32_16x16x32_bf16 v[196:199], v[28:31], v[148:151], v[196:199]\n"
    "ds_read_b64_tr_b16 v[26:27], v218 offset:10240\n"
    "v_mfma_f32_16x16x32_bf16 v[200:203], v[28:31], v[152:155], v[200:203]\n"
    "v_mfma_f32_16x16x32_bf16 v[204:207], v[28:31], v[156:159], v[204:207]\n"
    "buffer_load_dwordx4 v11, s[20:23], s47 offen offset:2048 lds\n"
    "s_waitcnt lgkmcnt(6)\n"
    "v_mfma_f32_16x16x32_bf16 v[176:179], v[32:35], v[160:163], v[176:179]\n"
    "ds_read_b64_tr_b16 v[28:29], v219 offset:8192\n"
    "v_mfma_f32_16x16x32_bf16 v[180:183], v[32:35], v[164:167], v[180:183]\n"
    "ds_read_b64_tr_b16 v[30:31], v219 offset:10240\n"
    "v_mfma_f32_16x16x32_bf16 v[184:187], v[32:35], v[168:171], v[184:187]\n"
    "v_mfma_f32_16x16x32_bf16 v[188:191], v[32:35], v[172:175], v[188:191]\n"
    "v_mfma_f32_16x16x32_bf16 v[192:195], v[36:39], v[160:163], v[192:195]\n"
    "ds_read_b64_tr_b16 v[32:33], v218 offset:16384\n"
    "v_mfma_f32_16x16x32_bf16 v[196:199], v[36:39], v[164:167], v[196:199]\n"
    "buffer_load_dwordx4 v12, s[20:23], s47 offen offset:3072 lds\n"
    "ds_read_b64_tr_b16 v[34:35], v218 offset:18432\n"
    "v_mfma_f32_16x16x32_bf16 v[200:203], v[36:39], v[168:171], v[200:203]\n"
    "v_mfma_f32_16x16x32_bf16 v[204:207], v[36:39], v[172:175], v[204:207]\n"
    "s_waitcnt lgkmcnt(6)\n"
    "v_mfma_f32_16x16x32_bf16 v[176:179], v[16:19], a[0:3], v[176:179]\n"
    "ds_read_b64_tr_b16 v[36:37], v219 offset:16384\n"
    "v_mfma_f32_16x16x32_bf16 v[180:183], v[16:19], a[4:7], v[180:183]\n"
    "ds_read_b64_tr_b16 v[38:39], v219 offset:18432\n"
    "v_mfma_f32_16x16x32_bf16 v[184:187], v[16:19], a[8:11], v[184:187]\n"
    "v_mfma_f32_16x16x32_bf16 v[188:191], v[16:19], a[12:15], v[188:191]\n"
    "s_add_u32 m0, s54, 4096\n"
    "s_add_u32 s47, s51, s53\n"
    "buffer_load_dwordx4 v9, s[20:23], s47 offen offset:0 lds\n"
    "v_mfma_f32_16x16x32_bf16 v[192:195], v[20:23], a[0:3], v[192:195]\n"
    "ds_read_b64_tr_b16 v[16:17], v218 offset:24576\n"
    "v_mfma_f32_16x16x32_bf16 v[196:199], v[20:23], a[4:7], v[196:199]\n"
    "ds_read_b64_tr_b16 v[18:19], v218 offset:26624\n"
    "v_mfma_f32_16x16x32_bf16 v[200:203], v[20:23], a[8:11], v[200:203]\n"
    "v_mfma_f32_16x16x32_bf16 v[204:207], v[20:23], a[12:15], v[204:207]\n"
    "s_waitcnt lgkmcnt(6)\n"
    "v_mfma_f32_16x16x32_bf16 v[176:179], v[24:27], a[16:19], v[176:179]\n"
    "ds_read_b64_tr_b16 v[20:21], v219 offset:24576\n"
    "v_mfma_f32_16x16x32_bf16 v[180:183], v[24:27], a[20:23], v[180:183]\n"
    "buffer_load_dwordx4 v10, s[20:23], s47 offen offset:1024 lds\n"
    "ds_read_b64_tr_b16 v[22:23], v219 offset:26624\n"
    "v_mfma_f32_16x16x32_bf16 v[184:187], v[24:27], a[24:27], v[184:187]\n"
    "v_mfma_f32_16x16x32_bf16 v[188:191], v[24:27], a[28:31], v[188:191]\n"
    "v_mfma_f32_16x16x32_bf16 v[192:195], v[28:31], a[16:19], v[192:195]\n"
    "buffer_store_dwordx2 v[230:231], v228, s[72:75], s57 offen sc1\n"
    "s_add_u32 s57, s57, 512\n"
    "ds_read_b64_tr_b16 v[24:25], v218 offset:32768\n"
    "v_mfma_f32_16x16x32_bf16 v[196:199], v[28:31], a[20:23], v[196:199]\n"
    "ds_read_b64_tr_b16 v[26:27], v218 offset:34816\n"
    "v_mfma_f32_16x16x32_bf16 v[200:203], v[28:31], a[24:27], v[200:203]\n"
    "buffer_load_dwordx4 v11, s[20:23], s47 offen offset:2048 lds\n"
    "v_mfma_f32_16x16x32_bf16 v[204:207], v[28:31], a[28:31], v[204:207]\n"
    "s_waitcnt lgkmcnt(6)\n"
    "v_mfma_f32_16x16x32_bf16 v[176:179], v[32:35], a[32:35], v[176:179]\n"
    "ds_read_b64_tr_b16 v[28:29], v219 offset:32768\n"
    "v_mfma_f32_16x16x32_bf16 v[180:183], v[32:35], a[36:39], v[180:183]\n"
    "ds_read_b64_tr_b16 v[30:31], v219 offset:34816\n"
    "v_mfma_f32_16x16x32_bf16 v[184:187], v[32:35], a[40:43], v[184:187]\n"
    "v_mfma_f32_16x16x32_bf16 v[188:191], v[32:35], a[44:47], v[188:191]\n"
    "v_mfma_f32_16x16x32_bf16 v[192:195], v[36:39], a[32:35], v[192:195]\n"
    "buffer_load_dwordx4 v12, s[20:23], s47 offen offset:3072 lds\n"
    "ds_read_b64_tr_b16 v[32:33], v218 offset:40960\n"
    "v_mfma_f32_16x16x32_bf16 v[196:199], v[36:39], a[36:39], v[196:199]\n"
    "ds_read_b64_tr_b16 v[34:35], v218 offset:43008\n"
    "v_mfma_f32_16x16x32_bf16 v[200:203], v[36:39], a[40:43], v[200:203]\n"
    "v_mfma_f32_16x16x32_bf16 v[204:207], v[36:39], a[44:47], v[204:207]\n"
    "s_waitcnt lgkmcnt(6)\n"
    "v_mfma_f32_16x16x32_bf16 v[176:179], v[16:19], a[48:51], v[176:179]\n"
    "ds_read_b64_tr_b16 v[36:37], v219 offset:40960\n"
    "v_mfma_f32_16x16x32_bf16 v[180:183], v[16:19], a[52:55], v[180:183]\n"
    "ds_read_b64_tr_b16 v[38:39], v219 offset:43008\n"
    "v_mfma_f32_16x16x32_bf16 v[184:187], v[16:19], a[56:59], v[184:187]\n"
    "s_add_u32 m0, s54, 8192\n"
    "s_add_u32 s47, s52, s53\n"
    "buffer_load_dwordx4 v9, s[20:23], s47 offen offset:0 lds\n"
    "v_mfma_f32_16x16x32_bf16 v[188:191], v[16:19], a[60:63], v[188:191]\n"
    "v_mfma_f32_16x16x32_bf16 v[192:195], v[20:23], a[48:51], v[192:195]\n"
    "v_mfma_f32_16x16x32_bf16 v[196:199], v[20:23], a[52:55], v[196:199]\n"
    "v_mfma_f32_16x16x32_bf16 v[200:203], v[20:23], a[56:59], v[200:203]\n"
    "v_mfma_f32_16x16x32_bf16 v[204:207], v[20:23], a[60:63], v[204:207]\n"
    "s_waitcnt lgkmcnt(4)\n"
    "v_mfma_f32_16x16x32_bf16 v[176:179], v[24:27], a[64:67], v[176:179]\n"
    "buffer_load_dwordx4 v10, s[20:23], s47 offen offset:1024 lds\n"
    "v_mfma_f32_16x16x32_bf16 v[180:183], v[24:27], a[68:71], v[180:183]\n"
    "v_mfma_f32_16x16x32_bf16 v[184:187], v[24:27], a[72:75], v[184:187]\n"
    "v_mfma_f32_16x16x32_bf16 v[188:191], v[24:27], a[76:79], v[188:191]\n"
    "s_waitcnt lgkmcnt(0)\n"
    "s_waitcnt vmcnt(15)\n"
    "s_barrier\n"
    "v_mfma_f32_16x16x32_bf16 v[192:195], v[28:31], a[64:67], v[192:195]\n"
    "ds_read_b64_tr_b16 v[16:17], v220 offset:0\n"
    "v_mfma_f32_16x16x32_bf16 v[196:199], v[28:31], a[68:71], v[196:199]\n"
    "ds_read_b64_tr_b16 v[18:19], v220 offset:2048\n"
    "v_mfma_f32_16x16x32_bf16 v[200:203], v[28:31], a[72:75], v[200:203]\n"
    "buffer_load_dwordx4 v11, s[20:23], s47 offen offset:2048 lds\n"
    "ds_read_b64_tr_b16 v[20:21], v221 offset:0\n"
    "v_mfma_f32_16x16x32_bf16 v[204:207], v[28:31], a[76:79], v[204:207]\n"
    "ds_read_b64_tr_b16 v[22:23], v221 offset:2048\n"
    "v_mfma_f32_16x16x32_bf16 v[176:179], v[32:35], a[80:83], v[176:179]\n"
    "ds_read_b64_tr_b16 v[24:25], v220 offset:8192\n"
    "v_mfma_f32_16x16x32_bf16 v[180:183], v[32:35], a[84:87], v[180:183]\n"
    "ds_read_b64_tr_b16 v[26:27], v220 offset:10240\n"
    "v_mfma_f32_16x16x32_bf16 v[184:187], v[32:35], a[88:91], v[184:187]\n"
    "ds_read_b64_tr_b16 v[28:29], v221 offset:8192\n"
    "v_mfma_f32_16x16x32_bf16 v[188:191], v[32:35], a[92:95], v[188:191]\n"
    "ds_read_b64_tr_b16 v[30:31], v221 offset:10240\n"
    "v_mfma_f32_16x16x32_bf16 v[192:195], v[36:39], a[80:83], v[192:195]\n"
    "buffer_load_dwordx4 v12, s[20:23], s47 offen offset:3072 lds\n"
    "ds_read_b64_tr_b16 v[32:33], v220 offset:16384\n"
    "v_mfma_f32_16x16x32_bf16 v[196:199], v[36:39], a[84:87], v[196:199]\n"
    "ds_read_b64_tr_b16 v[34:35], v220 offset:18432\n"
    "v_mfma_f32_16x16x32_bf16 v[200:203], v[36:39], a[88:91], v[200:203]\n"
    "v_mfma_f32_16x16x32_bf16 v[204:207], v[36:39], a[92:95], v[204:207]\n"
    "s_waitcnt lgkmcnt(6)\n"
    "v_cvt_pk_bf16_f32 v240, v176, v177\n"
    "v_cvt_pk_bf16_f32 v241, v178, v179\n"
    "v_mfma_f32_16x16x32_bf16 v[176:179], v[16:19], v[80:83], 0\n"
    "ds_read_b64_tr_b16 v[36:37], v221 offset:16384\n"
    "v_add_u32 v216, 49152, v224\n"
    "v_cvt_pk_bf16_f32 v244, v180, v181\n"
    "v_cvt_pk_bf16_f32 v245, v182, v183\n"
    "v_mfma_f32_16x16x32_bf16 v[180:183], v[16:19], v[84:87], 0\n"
    "ds_read_b64_tr_b16 v[38:39], v221 offset:18432\n"
    "v_cvt_pk_bf16_f32 v248, v184, v185\n"
    "v_cvt_pk_bf16_f32 v249, v186, v187\n"
    "v_mfma_f32_16x16x32_bf16 v[184:187], v[16:19], v[88:91], 0\n"
    "v_cvt_pk_bf16_f32 v252, v188, v189\n"
    "v_cvt_pk_bf16_f32 v253, v190, v191\n"
    "v_mfma_f32_16x16x32_bf16 v[188:191], v[16:19], v[92:95], 0\n"
    "v_cvt_pk_bf16_f32 v242, v192, v193\n"
    "v_cvt_pk_bf16_f32 v243, v194, v195\n"
    "v_mfma_f32_16x16x32_bf16 v[192:195], v[20:23], v[80:83], 0\n"
    "ds_read_b64_tr_b16 v[16:17], v220 offset:24576\n"
    "v_add_u32 v217, 49152, v225\n"
    "v_cvt_pk_bf16_f32 v246, v196, v197\n"
    "v_cvt_pk_bf16_f32 v247, v198, v199\n"
    "v_mfma_f32_16x16x32_bf16 v[196:199], v[20:23], v[84:87], 0\n"
    "ds_read_b64_tr_b16 v[18:19], v220 offset:26624\n"
    "v_cvt_pk_bf16_f32 v250, v200, v201\n"
    "v_cvt_pk_bf16_f32 v251, v202, v203\n"
    "v_mfma_f32_16x16x32_bf16 v[200:203], v[20:23], v[88:91], 0\n"
    "v_cvt_pk_bf16_f32 v254, v204, v205\n"
    "v_cvt_pk_bf16_f32 v255, v206, v207\n"
    "v_mfma_f32_16x16x32_bf16 v[204:207], v[20:23], v[92:95], 0\n"
    "s_waitcnt lgkmcnt(6)\n"
    "v_mfma_f32_16x16x32_bf16 v[176:179], v[24:27], v[96:99], v[176:179]\n"
    "ds_read_b64_tr_b16 v[20:21], v221 offset:24576\n"
    "v_add_u32 v218, 49152, v226\n"
    "v_mfma_f32_16x16x32_bf16 v[180:183], v[24:27], v[100:103], v[180:183]\n"
    "buffer_store_dwordx4 v[240:243], v13, s[28:31], s60 offen offset:0 sc1\n"
    "ds_read_b64_tr_b16 v[22:23], v221 offset:26624\n"
    "v_mfma_f32_16x16x32_bf16 v[184:187], v[24:27], v[104:107], v[184:187]\n"
    "v_mfma_f32_16x16x32_bf16 v[188:191], v[24:27], v[108:111], v[188:191]\n"
    "v_mfma_f32_16x16x32_bf16 v[192:195], v[28:31], v[96:99], v[192:195]\n"
    "ds_read_b64_tr_b16 v[24:25], v220 offset:32768\n"
    "v_add_u32 v219, 49152, v227\n"
    "v_mfma_f32_16x16x32_bf16 v[196:199], v[28:31], v[100:103], v[196:199]\n"
    "buffer_store_dwordx4 v[244:247], v13, s[28:31], s60 offen offset:1024 sc1\n"
    "ds_read_b64_tr_b16 v[26:27], v220 offset:34816\n"
    "v_mfma_f32_16x16x32_bf16 v[200:203], v[28:31], v[104:107], v[200:203]\n"
    "v_mfma_f32_16x16x32_bf16 v[204:207], v[28:31], v[108:111], v[204:207]\n"
    "s_waitcnt lgkmcnt(6)\n"
    "v_mfma_f32_16x16x32_bf16 v[176:179], v[32:35], v[112:115], v[176:179]\n"
    "ds_read_b64_tr_b16 v[28:29], v221 offset:32768\n"
    "v_mfma_f32_16x16x32_bf16 v[180:183], v[32:35], v[116:119], v[180:183]\n"
    "buffer_store_dwordx4 v[248:251], v13, s[28:31], s60 offen offset:2048 sc1\n"
    "ds_read_b64_tr_b16 v[30:31], v221 offset:34816\n"
    "v_mfma_f32_16x16x32_bf16 v[184:187], v[32:35], v[120:123], v[184:187]\n"
    "v_mfma_f32_16x16x32_bf16 v[188:191], v[32:35], v[124:127], v[188:191]\n"
    "v_mfma_f32_16x16x32_bf16 v[192:195], v[36:39], v[112:115], v[192:195]\n"
    "ds_read_b64_tr_b16 v[32:33], v220 offset:40960\n"
    "v_mfma_f32_16x16x32_bf16 v[196:199], v[36:39], v[116:119], v[196:199]\n"
    "buffer_store_dwordx4 v[252:255], v13, s[28:31], s60 offen offset:3072 sc1\n"
    "ds_read_b64_tr_b16 v[34:35], v220 offset:43008\n"
    "v_mfma_f32_16x16x32_bf16 v[200:203], v[36:39], v[120:123], v[200:203]\n"
    "v_mfma_f32_16x16x32_bf16 v[204:207], v[36:39], v[124:127], v[204:207]\n"
    "s_waitcnt lgkmcnt(6)\n"
    "v_mfma_f32_16x16x32_bf16 v[176:179], v[16:19], v[128:131], v[176:179]\n"
    "ds_read_b64_tr_b16 v[36:37], v221 offset:40960\n"
    "v_mfma_f32_16x16x32_bf16 v[180:183], v[16:19], v[132:135], v[180:183]\n"
    "s_add_u32 s60, s60, 262144\n"
    "ds_read_b64_tr_b16 v[38:39], v221 offset:43008\n"
    "v_mfma_f32_16x16x32_bf16 v[184:187], v[16:19], v[136:139], v[184:187]\n"
    "v_mfma_f32_16x16x32_bf16 v[188:191], v[16:19], v[140:143], v[188:191]\n"
    "v_mfma_f32_16x16x32_bf16 v[192:195], v[20:23], v[128:131], v[192:195]\n"
    "s_add_u32 s40, s15, 5\n"
    "s_cmp_ge_u32 s40, 9\n"
    "s_cselect_b32 s41, 9, 0\n"
    "s_sub_u32 s40, s40, s41\n"
    "s_lshl_b32 s53, s40, 7\n"
    "s_mul_i32 s54, s9, 12288\n"
    "s_add_u32 s54, s54, 98304\n"
    "s_add_u32 m0, s54, 0\n"
    "s_add_u32 s47, s50, s53\n"
    "buffer_load_dwordx4 v9, s[20:23], s47 offen offset:0 lds\n"
    "ds_read_b64_tr_b16 v[16:17], v222 offset:0\n"
    "v_mfma_f32_16x16x32_bf16 v[196:199], v[20:23], v[132:135], v[196:199]\n"
    "ds_read_b64_tr_b16 v[18:19], v222 offset:2048\n"
    "v_mfma_f32_16x16x32_bf16 v[200:203], v[20:23], v[136:139], v[200:203]\n"
    "v_mfma_f32_16x16x32_bf16 v[204:207], v[20:23], v[140:143], v[204:207]\n"
    "s_waitcnt lgkmcnt(6)\n"
    "v_mfma_f32_16x16x32_bf16 v[176:179], v[24:27], v[144:147], v[176:179]\n"
    "ds_read_b64_tr_b16 v[20:21], v223 offset:0\n"
    "v_mfma_f32_16x16x32_bf16 v[180:183], v[24:27], v[148:151], v[180:183]\n"
    "buffer_load_dwordx4 v10, s[20:23], s47 offen offset:1024 lds\n"
    "ds_read_b64_tr_b16 v[22:23], v223 offset:2048\n"
    "v_mfma_f32_16x16x32_bf16 v[184:187], v[24:27], v[152:155], v[184:187]\n"
    "v_mfma_f32_16x16x32_bf16 v[188:191], v[24:27], v[156:159], v[188:191]\n"
    "v_mfma_f32_16x16x32_bf16 v[192:195], v[28:31], v[144:147], v[192:195]\n"
    "ds_read_b64_tr_b16 v[24:25], v222 offset:8192\n"
    "v_mfma_f32_16x16x32_bf16 v[196:199], v[28:31], v[148:151], v[196:199]\n"
    "ds_read_b64_tr_b16 v[26:27], v222 offset:10240\n"
    "v_mfma_f32_16x16x32_bf16 v[200:203], v[28:31], v[152:155], v[200:203]\n"
    "v_mfma_f32_16x16x32_bf16 v[204:207], v[28:31], v[156:159], v[204:207]\n"
    "buffer_load_dwordx4 v11, s[20:23], s47 offen offset:2048 lds\n"
    "s_waitcnt lgkmcnt(6)\n"
    "v_mfma_f32_16x16x32_bf16 v[176:179], v[32:35], v[160:163], v[176:179]\n"
    "ds_read_b64_tr_b16 v[28:29], v223 offset:8192\n"
    "v_mfma_f32_16x16x32_bf16 v[180:183], v[32:35], v[164:167], v[180:183]\n"
    "ds_read_b64_tr_b16 v[30:31], v223 offset:10240\n"
    "v_mfma_f32_16x16x32_bf16 v[184:187], v[32:35], v[168:171], v[184:187]\n"
    "v_mfma_f32_16x16x32_bf16 v[188:191], v[32:35], v[172:175], v[188:191]\n"
    "v_mfma_f32_16x16x32_bf16 v[192:195], v[36:39], v[160:163], v[192:195]\n"
    "ds_read_b64_tr_b16 v[32:33], v222 offset:16384\n"
    "v_mfma_f32_16x16x32_bf16 v[196:199], v[36:39], v[164:167], v[196:199]\n"
    "buffer_load_dwordx4 v12, s[20:23], s47 offen offset:3072 lds\n"
    "ds_read_b64_tr_b16 v[34:35], v222 offset:18432\n"
    "v_mfma_f32_16x16x32_bf16 v[200:203], v[36:39], v[168:171], v[200:203]\n"
    "v_mfma_f32_16x16x32_bf16 v[204:207], v[36:39], v[172:175], v[204:207]\n"
    "s_waitcnt lgkmcnt(6)\n"
    "v_mfma_f32_16x16x32_bf16 v[176:179], v[16:19], a[0:3], v[176:179]\n"
    "ds_read_b64_tr_b16 v[36:37], v223 offset:16384\n"
    "v_mfma_f32_16x16x32_bf16 v[180:183], v[16:19], a[4:7], v[180:183]\n"
    "ds_read_b64_tr_b16 v[38:39], v223 offset:18432\n"
    "v_mfma_f32_16x16x32_bf16 v[184:187], v[16:19], a[8:11], v[184:187]\n"
    "v_mfma_f32_16x16x32_bf16 v[188:191], v[16:19], a[12:15], v[188:191]\n"
    "s_add_u32 m0, s54, 4096\n"
    "s_add_u32 s47, s51, s53\n"
    "buffer_load_dwordx4 v9, s[20:23], s47 offen offset:0 lds\n"
    "v_mfma_f32_16x16x32_bf16 v[192:195], v[20:23], a[0:3], v[192:195]\n"
    "ds_read_b64_tr_b16 v[16:17], v222 offset:24576\n"
    "v_mfma_f32_16x16x32_bf16 v[196:199], v[20:23], a[4:7], v[196:199]\n"
    "ds_read_b64_tr_b16 v[18:19], v222 offset:26624\n"
    "v_mfma_f32_16x16x32_bf16 v[200:203], v[20:23], a[8:11], v[200:203]\n"
    "v_mfma_f32_16x16x32_bf16 v[204:207], v[20:23], a[12:15], v[204:207]\n"
    "s_waitcnt lgkmcnt(6)\n"
    "v_mfma_f32_16x16x32_bf16 v[176:179], v[24:27], a[16:19], v[176:179]\n"
    "ds_read_b64_tr_b16 v[20:21], v223 offset:24576\n"
    "v_mfma_f32_16x16x32_bf16 v[180:183], v[24:27], a[20:23], v[180:183]\n"
    "buffer_load_dwordx4 v10, s[20:23], s47 offen offset:1024 lds\n"
    "ds_read_b64_tr_b16 v[22:23], v223 offset:26624\n"
    "v_mfma_f32_16x16x32_bf16 v[184:187], v[24:27], a[24:27], v[184:187]\n"
    "v_mfma_f32_16x16x32_bf16 v[188:191], v[24:27], a[28:31], v[188:191]\n"
    "v_mfma_f32_16x16x32_bf16 v[192:195], v[28:31], a[16:19], v[192:195]\n"
    "s_waitcnt vmcnt(23)\n"
    "buffer_store_dwordx2 v[230:231], v228, s[72:75], s57 offen sc1\n"
    "s_add_u32 s57, s57, 512\n"
    "ds_read_b64_tr_b16 v[24:25], v222 offset:32768\n"
    "v_mfma_f32_16x16x32_bf16 v[196:199], v[28:31], a[20:23], v[196:199]\n"
    "ds_read_b64_tr_b16 v[26:27], v222 offset:34816\n"
    "v_mfma_f32_16x16x32_bf16 v[200:203], v[28:31], a[24:27], v[200:203]\n"
    "buffer_load_dwordx4 v11, s[20:23], s47 offen offset:2048 lds\n"
    "v_mfma_f32_16x16x32_bf16 v[204:207], v[28:31], a[28:31], v[204:207]\n"
    "s_waitcnt lgkmcnt(6)\n"
    "v_mfma_f32_16x16x32_bf16 v[176:179], v[32:35], a[32:35], v[176:179]\n"
    "ds_read_b64_tr_b16 v[28:29], v223 offset:32768\n"
    "v_mfma_f32_16x16x32_bf16 v[180:183], v[32:35], a[36:39], v[180:183]\n"
    "ds_read_b64_tr_b16 v[30:31], v223 offset:34816\n"
    "v_mfma_f32_16x16x32_bf16 v[184:187], v[32:35], a[40:43], v[184:187]\n"
    "v_mfma_f32_16x16x32_bf16 v[188:191], v[32:35], a[44:47], v[188:191]\n"
    "v_mfma_f32_16x16x32_bf16 v[192:195], v[36:39], a[32:35], v[192:195]\n"
    "buffer_load_dwordx4 v12, s[20:23], s47 offen offset:3072 lds\n"
    "ds_read_b64_tr_b16 v[32:33], v222 offset:40960\n"
    "v_mfma_f32_16x16x32_bf16 v[196:199], v[36:39], a[36:39], v[196:199]\n"
    "ds_read_b64_tr_b16 v[34:35], v222 offset:43008\n"
    "v_mfma_f32_16x16x32_bf16 v[200:203], v[36:39], a[40:43], v[200:203]\n"
    "v_mfma_f32_16x16x32_bf16 v[204:207], v[36:39], a[44:47], v[204:207]\n"
    "s_waitcnt lgkmcnt(6)\n"
    "v_mfma_f32_16x16x32_bf16 v[176:179], v[16:19], a[48:51], v[176:179]\n"
    "ds_read_b64_tr_b16 v[36:37], v223 offset:40960\n"
    "v_mfma_f32_16x16x32_bf16 v[180:183], v[16:19], a[52:55], v[180:183]\n"
    "ds_read_b64_tr_b16 v[38:39], v223 offset:43008\n"
    "v_mfma_f32_16x16x32_bf16 v[184:187], v[16:19], a[56:59], v[184:187]\n"
    "s_add_u32 m0, s54, 8192\n"
    "s_add_u32 s47, s52, s53\n"
    "buffer_load_dwordx4 v9, s[20:23], s47 offen offset:0 lds\n"
    "v_mfma_f32_16x16x32_bf16 v[188:191], v[16:19], a[60:63], v[188:191]\n"
    "v_mfma_f32_16x16x32_bf16 v[192:195], v[20:23], a[48:51], v[192:195]\n"
    "v_mfma_f32_16x16x32_bf16 v[196:199], v[20:23], a[52:55], v[196:199]\n"
    "v_mfma_f32_16x16x32_bf16 v[200:203], v[20:23], a[56:59], v[200:203]\n"
    "v_mfma_f32_16x16x32_bf16 v[204:207], v[20:23], a[60:63], v[204:207]\n"
    "s_waitcnt lgkmcnt(4)\n"
    "v_mfma_f32_16x16x32_bf16 v[176:179], v[24:27], a[64:67], v[176:179]\n"
    "buffer_load_dwordx4 v10, s[20:23], s47 offen offset:1024 lds\n"
    "v_mfma_f32_16x16x32_bf16 v[180:183], v[24:27], a[68:71], v[180:183]\n"
    "v_mfma_f32_16x16x32_bf16 v[184:187], v[24:27], a[72:75], v[184:187]\n"
    "v_mfma_f32_16x16x32_bf16 v[188:191], v[24:27], a[76:79], v[188:191]\n"
    "s_waitcnt lgkmcnt(0)\n"
    "s_waitcnt vmcnt(15)\n"
    "s_barrier\n"
    "v_mfma_f32_16x16x32_bf16 v[192:195], v[28:31], a[64:67], v[192:195]\n"
    "ds_read_b64_tr_b16 v[16:17], v216 offset:0\n"
    "v_mfma_f32_16x16x32_bf16 v[196:199], v[28:31], a[68:71], v[196:199]\n"
    "ds_read_b64_tr_b16 v[18:19], v216 offset:2048\n"
    "v_mfma_f32_16x16x32_bf16 v[200:203], v[28:31], a[72:75], v[200:203]\n"
    "buffer_load_dwordx4 v11, s[20:23], s47 offen offset:2048 lds\n"
    "ds_read_b64_tr_b16 v[20:21], v217 offset:0\n"
    "v_mfma_f32_16x16x32_bf16 v[204:207], v[28:31], a[76:79], v[204:207]\n"
    "ds_read_b64_tr_b16 v[22:23], v217 offset:2048\n"
    "v_mfma_f32_16x16x32_bf16 v[176:179], v[32:35], a[80:83], v[176:179]\n"
    "ds_read_b64_tr_b16 v[24:25], v216 offset:8192\n"
    "v_mfma_f32_16x16x32_bf16 v[180:183], v[32:35], a[84:87], v[180:183]\n"
    "ds_read_b64_tr_b16 v[26:27], v216 offset:10240\n"
    "v_mfma_f32_16x16x32_bf16 v[184:187], v[32:35], a[88:91], v[184:187]\n"
    "ds_read_b64_tr_b16 v[28:29], v217 offset:8192\n"
    "v_mfma_f32_16x16x32_bf16 v[188:191], v[32:35], a[92:95], v[188:191]\n"
    "ds_read_b64_tr_b16 v[30:31], v217 offset:10240\n"
    "v_mfma_f32_16x16x32_bf16 v[192:195], v[36:39], a[80:83], v[192:195]\n"
    "buffer_load_dwordx4 v12, s[20:23], s47 offen offset:3072 lds\n"
    "ds_read_b64_tr_b16 v[32:33], v216 offset:16384\n"
    "v_mfma_f32_16x16x32_bf16 v[196:199], v[36:39], a[84:87], v[196:199]\n"
    "ds_read_b64_tr_b16 v[34:35], v216 offset:18432\n"
    "v_mfma_f32_16x16x32_bf16 v[200:203], v[36:39], a[88:91], v[200:203]\n"
    "v_mfma_f32_16x16x32_bf16 v[204:207], v[36:39], a[92:95], v[204:207]\n"
    "s_waitcnt lgkmcnt(6)\n"
    "v_cvt_pk_bf16_f32 v240, v176, v177\n"
    "v_cvt_pk_bf16_f32 v241, v178, v179\n"
    "v_mfma_f32_16x16x32_bf16 v[176:179], v[16:19], v[80:83], 0\n"
    "ds_read_b64_tr_b16 v[36:37], v217 offset:16384\n"
    "v_add_u32 v220, 98304, v224\n"
    "v_cvt_pk_bf16_f32 v244, v180, v181\n"
    "v_cvt_pk_bf16_f32 v245, v182, v183\n"
    "v_mfma_f32_16x16x32_bf16 v[180:183], v[16:19], v[84:87], 0\n"
    "ds_read_b64_tr_b16 v[38:39], v217 offset:18432\n"
    "v_cvt_pk_bf16_f32 v248, v184, v185\n"
    "v_cvt_pk_bf16_f32 v249, v186, v187\n"
    "v_mfma_f32_16x16x32_bf16 v[184:187], v[16:19], v[88:91], 0\n"
    "v_cvt_pk_bf16_f32 v252, v188, v189\n"
    "v_cvt_pk_bf16_f32 v253, v190, v191\n"
    "v_mfma_f32_16x16x32_bf16 v[188:191], v[16:19], v[92:95], 0\n"
    "v_cvt_pk_bf16_f32 v242, v192, v193\n"
    "v_cvt_pk_bf16_f32 v243, v194, v195\n"
    "v_mfma_f32_16x16x32_bf16 v[192:195], v[20:23], v[80:83], 0\n"
    "ds_read_b64_tr_b16 v[16:17], v216 offset:24576\n"
    "v_add_u32 v221, 98304, v225\n"
    "v_cvt_pk_bf16_f32 v246, v196, v197\n"
    "v_cvt_pk_bf16_f32 v247, v198, v199\n"
    "v_mfma_f32_16x16x32_bf16 v[196:199], v[20:23], v[84:87], 0\n"
    "ds_read_b64_tr_b16 v[18:19], v216 offset:26624\n"
    "v_cvt_pk_bf16_f32 v250, v200, v201\n"
    "v_cvt_pk_bf16_f32 v251, v202, v203\n"
    "v_mfma_f32_16x16x32_bf16 v[200:203], v[20:23], v[88:91], 0\n"
    "v_cvt_pk_bf16_f32 v254, v204, v205\n"
    "v_cvt_pk_bf16_f32 v255, v206, v207\n"
    "v_mfma_f32_16x16x32_bf16 v[204:207], v[20:23], v[92:95], 0\n"
    "s_waitcnt lgkmcnt(6)\n"
    "v_mfma_f32_16x16x32_bf16 v[176:179], v[24:27], v[96:99], v[176:179]\n"
    "ds_read_b64_tr_b16 v[20:21], v217 offset:24576\n"
    "v_add_u32 v222, 98304, v226\n"
    "v_mfma_f32_16x16x32_bf16 v[180:183], v[24:27], v[100:103], v[180:183]\n"
    "buffer_store_dwordx4 v[240:243], v13, s[28:31], s60 offen offset:0 sc1\n"
    "ds_read_b64_tr_b16 v[22:23], v217 offset:26624\n"
    "v_mfma_f32_16x16x32_bf16 v[184:187], v[24:27], v[104:107], v[184:187]\n"
    "v_mfma_f32_16x16x32_bf16 v[188:191], v[24:27], v[108:111], v[188:191]\n"
    "v_mfma_f32_16x16x32_bf16 v[192:195], v[28:31], v[96:99], v[192:195]\n"
    "ds_read_b64_tr_b16 v[24:25], v216 offset:32768\n"
    "v_add_u32 v223, 98304, v227\n"
    "v_mfma_f32_16x16x32_bf16 v[196:199], v[28:31], v[100:103], v[196:199]\n"
    "buffer_store_dwordx4 v[244:247], v13, s[28:31], s60 offen offset:1024 sc1\n"
    "ds_read_b64_tr_b16 v[26:27], v216 offset:34816\n"
    "v_mfma_f32_16x16x32_bf16 v[200:203], v[28:31], v[104:107], v[200:203]\n"
    "v_mfma_f32_16x16x32_bf16 v[204:207], v[28:31], v[108:111], v[204:207]\n"
    "s_waitcnt lgkmcnt(6)\n"
    "v_mfma_f32_16x16x32_bf16 v[176:179], v[32:35], v[112:115], v[176:179]\n"
    "ds_read_b64_tr_b16 v[28:29], v217 offset:32768\n"
    "v_mfma_f32_16x16x32_bf16 v[180:183], v[32:35], v[116:119], v[180:183]\n"
    "buffer_store_dwordx4 v[248:251], v13, s[28:31], s60 offen offset:2048 sc1\n"
    "ds_read_b64_tr_b16 v[30:31], v217 offset:34816\n"
    "v_mfma_f32_16x16x32_bf16 v[184:187], v[32:35], v[120:123], v[184:187]\n"
    "v_mfma_f32_16x16x32_bf16 v[188:191], v[32:35], v[124:127], v[188:191]\n"
    "v_mfma_f32_16x16x32_bf16 v[192:195], v[36:39], v[112:115], v[192:195]\n"
    "ds_read_b64_tr_b16 v[32:33], v216 offset:40960\n"
    "v_mfma_f32_16x16x32_bf16 v[196:199], v[36:39], v[116:119], v[196:199]\n"
    "buffer_store_dwordx4 v[252:255], v13, s[28:31], s60 offen offset:3072 sc1\n"
    "ds_read_b64_tr_b16 v[34:35], v216 offset:43008\n"
    "v_mfma_f32_16x16x32_bf16 v[200:203], v[36:39], v[120:123], v[200:203]\n"
    "v_mfma_f32_16x16x32_bf16 v[204:207], v[36:39], v[124:127], v[204:207]\n"
    "s_waitcnt lgkmcnt(6)\n"
    "v_mfma_f32_16x16x32_bf16 v[176:179], v[16:19], v[128:131], v[176:179]\n"
    "ds_read_b64_tr_b16 v[36:37], v217 offset:40960\n"
    "v_mfma_f32_16x16x32_bf16 v[180:183], v[16:19], v[132:135], v[180:183]\n"
    "s_add_u32 s60, s60, 262144\n"
    "ds_read_b64_tr_b16 v[38:39], v217 offset:43008\n"
    "v_mfma_f32_16x16x32_bf16 v[184:187], v[16:19], v[136:139], v[184:187]\n"
    "v_mfma_f32_16x16x32_bf16 v[188:191], v[16:19], v[140:143], v[188:191]\n"
    "v_mfma_f32_16x16x32_bf16 v[192:195], v[20:23], v[128:131], v[192:195]\n"
    "s_add_u32 s40, s15, 6\n"
    "s_cmp_ge_u32 s40, 9\n"
    "s_cselect_b32 s41, 9, 0\n"
    "s_sub_u32 s40, s40, s41\n"
    "s_lshl_b32 s53, s40, 7\n"
    "s_mul_i32 s54, s9, 12288\n"
    "s_add_u32 s54, s54, 0\n"
    "s_add_u32 m0, s54, 0\n"
    "s_add_u32 s47, s50, s53\n"
    "buffer_load_dwordx4 v9, s[20:23], s47 offen offset:0 lds\n"
    "ds_read_b64_tr_b16 v[16:17], v218 offset:0\n"
    "v_mfma_f32_16x16x32_bf16 v[196:199], v[20:23], v[132:135], v[196:199]\n"
    "ds_read_b64_tr_b16 v[18:19], v218 offset:2048\n"
    "v_mfma_f32_16x16x32_bf16 v[200:203], v[20:23], v[136:139], v[200:203]\n"
    "v_mfma_f32_16x16x32_bf16 v[204:207], v[20:23], v[140:143], v[204:207]\n"
    "s_waitcnt lgkmcnt(6)\n"
    "v_mfma_f32_16x16x32_bf16 v[176:179], v[24:27], v[144:147], v[176:179]\n"
    "ds_read_b64_tr_b16 v[20:21], v219 offset:0\n"
    "v_mfma_f32_16x16x32_bf16 v[180:183], v[24:27], v[148:151], v[180:183]\n"
    "buffer_load_dwordx4 v10, s[20:23], s47 offen offset:1024 lds\n"
    "ds_read_b64_tr_b16 v[22:23], v219 offset:2048\n"
    "v_mfma_f32_16x16x32_bf16 v[184:187], v[24:27], v[152:155], v[184:187]\n"
    "v_mfma_f32_16x16x32_bf16 v[188:191], v[24:27], v[156:159], v[188:191]\n"
    "v_mfma_f32_16x16x32_bf16 v[192:195], v[28:31], v[144:147], v[192:195]\n"
    "ds_read_b64_tr_b16 v[24:25], v218 offset:8192\n"
    "v_mfma_f32_16x16x32_bf16 v[196:199], v[28:31], v[148:151], v[196:199]\n"
    "ds_read_b64_tr_b16 v[26:27], v218 offset:10240\n"
    "v_mfma_f32_16x16x32_bf16 v[200:203], v[28:31], v[152:155], v[200:203]\n"
    "v_mfma_f32_16x16x32_bf16 v[204:207], v[28:31], v[156:159], v[204:207]\n"
    "buffer_load_dwordx4 v11, s[20:23], s47 offen offset:2048 lds\n"
    "s_waitcnt lgkmcnt(6)\n"
    "v_mfma_f32_16x16x32_bf16 v[176:179], v[32:35], v[160:163], v[176:179]\n"
    "ds_read_b64_tr_b16 v[28:29], v219 offset:8192\n"
    "v_mfma_f32_16x16x32_bf16 v[180:183], v[32:35], v[164:167], v[180:183]\n"
    "ds_read_b64_tr_b16 v[30:31], v219 offset:10240\n"
    "v_mfma_f32_16x16x32_bf16 v[184:187], v[32:35], v[168:171], v[184:187]\n"
    "v_mfma_f32_16x16x32_bf16 v[188:191], v[32:35], v[172:175], v[188:191]\n"
    "v_mfma_f32_16x16x32_bf16 v[192:195], v[36:39], v[160:163], v[192:195]\n"
    "ds_read_b64_tr_b16 v[32:33], v218 offset:16384\n"
    "v_mfma_f32_16x16x32_bf16 v[196:199], v[36:39], v[164:167], v[196:199]\n"
    "buffer_load_dwordx4 v12, s[20:23], s47 offen offset:3072 lds\n"
    "ds_read_b64_tr_b16 v[34:35], v218 offset:18432\n"
    "v_mfma_f32_16x16x32_bf16 v[200:203], v[36:39], v[168:171], v[200:203]\n"
    "v_mfma_f32_16x16x32_bf16 v[204:207], v[36:39], v[172:175], v[204:207]\n"
    "s_waitcnt lgkmcnt(6)\n"
    "v_mfma_f32_16x16x32_bf16 v[176:179], v[16:19], a[0:3], v[176:179]\n"
    "ds_read_b64_tr_b16 v[36:37], v219 offset:16384\n"
    "v_mfma_f32_16x16x32_bf16 v[180:183], v[16:19], a[4:7], v[180:183]\n"
    "ds_read_b64_tr_b16 v[38:39], v219 offset:18432\n"
    "v_mfma_f32_16x16x32_bf16 v[184:187], v[16:19], a[8:11], v[184:187]\n"
    "v_mfma_f32_16x16x32_bf16 v[188:191], v[16:19], a[12:15], v[188:191]\n"
    "s_add_u32 m0, s54, 4096\n"
    "s_add_u32 s47, s51, s53\n"
    "buffer_load_dwordx4 v9, s[20:23], s47 offen offset:0 lds\n"
    "v_mfma_f32_16x16x32_bf16 v[192:195], v[20:23], a[0:3], v[192:195]\n"
    "ds_read_b64_tr_b16 v[16:17], v218 offset:24576\n"
    "v_mfma_f32_16x16x32_bf16 v[196:199], v[20:23], a[4:7], v[196:199]\n"
    "ds_read_b64_tr_b16 v[18:19], v218 offset:26624\n"
    "v_mfma_f32_16x16x32_bf16 v[200:203], v[20:23], a[8:11], v[200:203]\n"
    "v_mfma_f32_16x16x32_bf16 v[204:207], v[20:23], a[12:15], v[204:207]\n"
    "s_waitcnt lgkmcnt(6)\n"
    "v_mfma_f32_16x16x32_bf16 v[176:179], v[24:27], a[16:19], v[176:179]\n"
    "ds_read_b64_tr_b16 v[20:21], v219 offset:24576\n"
    "v_mfma_f32_16x16x32_bf16 v[180:183], v[24:27], a[20:23], v[180:183]\n"
    "buffer_load_dwordx4 v10, s[20:23], s47 offen offset:1024 lds\n"
    "ds_read_b64_tr_b16 v[22:23], v219 offset:26624\n"
    "v_mfma_f32_16x16x32_bf16 v[184:187], v[24:27], a[24:27], v[184:187]\n"
    "v_mfma_f32_16x16x32_bf16 v[188:191], v[24:27], a[28:31], v[188:191]\n"
    "v_mfma_f32_16x16x32_bf16 v[192:195], v[28:31], a[16:19], v[192:195]\n"
    "s_waitcnt vmcnt(23)\n"
    "buffer_store_dwordx2 v[230:231], v228, s[72:75], s57 offen sc1\n"
    "s_add_u32 s57, s57, 512\n"
    "ds_read_b64_tr_b16 v[24:25], v218 offset:32768\n"
    "v_mfma_f32_16x16x32_bf16 v[196:199], v[28:31], a[20:23], v[196:199]\n"
    "ds_read_b64_tr_b16 v[26:27], v218 offset:34816\n"
    "v_mfma_f32_16x16x32_bf16 v[200:203], v[28:31], a[24:27], v[200:203]\n"
    "buffer_load_dwordx4 v11, s[20:23], s47 offen offset:2048 lds\n"
    "v_mfma_f32_16x16x32_bf16 v[204:207], v[28:31], a[28:31], v[204:207]\n"
    "s_waitcnt lgkmcnt(6)\n"
    "v_mfma_f32_16x16x32_bf16 v[176:179], v[32:35], a[32:35], v[176:179]\n"
    "ds_read_b64_tr_b16 v[28:29], v219 offset:32768\n"
    "v_mfma_f32_16x16x32_bf16 v[180:183], v[32:35], a[36:39], v[180:183]\n"
    "ds_read_b64_tr_b16 v[30:31], v219 offset:34816\n"
    "v_mfma_f32_16x16x32_bf16 v[184:187], v[32:35], a[40:43], v[184:187]\n"
    "v_mfma_f32_16x16x32_bf16 v[188:191], v[32:35], a[44:47], v[188:191]\n"
    "v_mfma_f32_16x16x32_bf16 v[192:195], v[36:39], a[32:35], v[192:195]\n"
    "buffer_load_dwordx4 v12, s[20:23], s47 offen offset:3072 lds\n"
    "ds_read_b64_tr_b16 v[32:33], v218 offset:40960\n"
    "v_mfma_f32_16x16x32_bf16 v[196:199], v[36:39], a[36:39], v[196:199]\n"
    "ds_read_b64_tr_b16 v[34:35], v218 offset:43008\n"
    "v_mfma_f32_16x16x32_bf16 v[200:203], v[36:39], a[40:43], v[200:203]\n"
    "v_mfma_f32_16x16x32_bf16 v[204:207], v[36:39], a[44:47], v[204:207]\n"
    "s_waitcnt lgkmcnt(6)\n"
    "v_mfma_f32_16x16x32_bf16 v[176:179], v[16:19], a[48:51], v[176:179]\n"
    "ds_read_b64_tr_b16 v[36:37], v219 offset:40960\n"
    "v_mfma_f32_16x16x32_bf16 v[180:183], v[16:19], a[52:55], v[180:183]\n"
    "ds_read_b64_tr_b16 v[38:39], v219 offset:43008\n"
    "v_mfma_f32_16x16x32_bf16 v[184:187], v[16:19], a[56:59], v[184:187]\n"
    "s_add_u32 m0, s54, 8192\n"
    "s_add_u32 s47, s52, s53\n"
    "buffer_load_dwordx4 v9, s[20:23], s47 offen offset:0 lds\n"
    "v_mfma_f32_16x16x32_bf16 v[188:191], v[16:19], a[60:63], v[188:191]\n"
    "v_mfma_f32_16x16x32_bf16 v[192:195], v[20:23], a[48:51], v[192:195]\n"
    "v_mfma_f32_16x16x32_bf16 v[196:199], v[20:23], a[52:55], v[196:199]\n"
    "v_mfma_f32_16x16x32_bf16 v[200:203], v[20:23], a[56:59], v[200:203]\n"
    "v_mfma_f32_16x16x32_bf16 v[204:207], v[20:23], a[60:63], v[204:207]\n"
    "s_waitcnt lgkmcnt(4)\n"
    "v_mfma_f32_16x16x32_bf16 v[176:179], v[24:27], a[64:67], v[176:179]\n"
    "buffer_load_dwordx4 v10, s[20:23], s47 offen offset:1024 lds\n"
    "v_mfma_f32_16x16x32_bf16 v[180:183], v[24:27], a[68:71], v[180:183]\n"
    "v_mfma_f32_16x16x32_bf16 v[184:187], v[24:27], a[72:75], v[184:187]\n"
    "v_mfma_f32_16x16x32_bf16 v[188:191], v[24:27], a[76:79], v[188:191]\n"
    "s_waitcnt lgkmcnt(0)\n"
    "s_waitcnt vmcnt(15)\n"
    "s_barrier\n"
    "v_mfma_f32_16x16x32_bf16 v[192:195], v[28:31], a[64:67], v[192:195]\n"
    "ds_read_b64_tr_b16 v[16:17], v220 offset:0\n"
    "v_mfma_f32_16x16x32_bf16 v[196:199], v[28:31], a[68:71], v[196:199]\n"
    "ds_read_b64_tr_b16 v[18:19], v220 offset:2048\n"
    "v_mfma_f32_16x16x32_bf16 v[200:203], v[28:31], a[72:75], v[200:203]\n"
    "buffer_load_dwordx4 v11, s[20:23], s47 offen offset:2048 lds\n"
    "ds_read_b64_tr_b16 v[20:21], v221 offset:0\n"
    "v_mfma_f32_16x16x32_bf16 v[204:207], v[28:31], a[76:79], v[204:207]\n"
    "ds_read_b64_tr_b16 v[22:23], v221 offset:2048\n"
    "v_mfma_f32_16x16x32_bf16 v[176:179], v[32:35], a[80:83], v[176:179]\n"
    "ds_read_b64_tr_b16 v[24:25], v220 offset:8192\n"
    "v_mfma_f32_16x16x32_bf16 v[180:183], v[32:35], a[84:87], v[180:183]\n"
    "ds_read_b64_tr_b16 v[26:27], v220 offset:10240\n"
    "v_mfma_f32_16x16x32_bf16 v[184:187], v[32:35], a[88:91], v[184:187]\n"
    "ds_read_b64_tr_b16 v[28:29], v221 offset:8192\n"
    "v_mfma_f32_16x16x32_bf16 v[188:191], v[32:35], a[92:95], v[188:191]\n"
    "ds_read_b64_tr_b16 v[30:31], v221 offset:10240\n"
    "v_mfma_f32_16x16x32_bf16 v[192:195], v[36:39], a[80:83], v[192:195]\n"
    "buffer_load_dwordx4 v12, s[20:23], s47 offen offset:3072 lds\n"
    "ds_read_b64_tr_b16 v[32:33], v220 offset:16384\n"
    "v_mfma_f32_16x16x32_bf16 v[196:199], v[36:39], a[84:87], v[196:199]\n"
    "ds_read_b64_tr_b16 v[34:35], v220 offset:18432\n"
    "v_mfma_f32_16x16x32_bf16 v[200:203], v[36:39], a[88:91], v[200:203]\n"
    "v_mfma_f32_16x16x32_bf16 v[204:207], v[36:39], a[92:95], v[204:207]\n"
    "s_waitcnt lgkmcnt(6)\n"
    "v_cvt_pk_bf16_f32 v240, v176, v177\n"
    "v_cvt_pk_bf16_f32 v241, v178, v179\n"
    "v_mfma_f32_16x16x32_bf16 v[176:179], v[16:19], v[80:83], 0\n"
    "ds_read_b64_tr_b16 v[36:37], v221 offset:16384\n"
    "v_add_u32 v216, 0, v224\n"
    "v_cvt_pk_bf16_f32 v244, v180, v181\n"
    "v_cvt_pk_bf16_f32 v245, v182, v183\n"
    "v_mfma_f32_16x16x32_bf16 v[180:183], v[16:19], v[84:87], 0\n"
    "ds_read_b64_tr_b16 v[38:39], v221 offset:18432\n"
    "v_cvt_pk_bf16_f32 v248, v184, v185\n"
    "v_cvt_pk_bf16_f32 v249, v186, v187\n"
    "v_mfma_f32_16x16x32_bf16 v[184:187], v[16:19], v[88:91], 0\n"
    "v_cvt_pk_bf16_f32 v252, v188, v189\n"
    "v_cvt_pk_bf16_f32 v253, v190, v191\n"
    "v_mfma_f32_16x16x32_bf16 v[188:191], v[16:19], v[92:95], 0\n"
    "v_cvt_pk_bf16_f32 v242, v192, v193\n"
    "v_cvt_pk_bf16_f32 v243, v194, v195\n"
    "v_mfma_f32_16x16x32_bf16 v[192:195], v[20:23], v[80:83], 0\n"
    "ds_read_b64_tr_b16 v[16:17], v220 offset:24576\n"
    "v_add_u32 v217, 0, v225\n"
    "v_cvt_pk_bf16_f32 v246, v196, v197\n"
    "v_cvt_pk_bf16_f32 v247, v198, v199\n"
    "v_mfma_f32_16x16x32_bf16 v[196:199], v[20:23], v[84:87], 0\n"
    "ds_read_b64_tr_b16 v[18:19], v220 offset:26624\n"
    "v_cvt_pk_bf16_f32 v250, v200, v201\n"
    "v_cvt_pk_bf16_f32 v251, v202, v203\n"
    "v_mfma_f32_16x16x32_bf16 v[200:203], v[20:23], v[88:91], 0\n"
    "v_cvt_pk_bf16_f32 v254, v204, v205\n"
    "v_cvt_pk_bf16_f32 v255, v206, v207\n"
    "v_mfma_f32_16x16x32_bf16 v[204:207], v[20:23], v[92:95], 0\n"
    "s_waitcnt lgkmcnt(6)\n"
    "v_mfma_f32_16x16x32_bf16 v[176:179], v[24:27], v[96:99], v[176:179]\n"
    "ds_read_b64_tr_b16 v[20:21], v221 offset:24576\n"
    "v_add_u32 v218, 0, v226\n"
    "v_mfma_f32_16x16x32_bf16 v[180:183], v[24:27], v[100:103], v[180:183]\n"
    "buffer_store_dwordx4 v[240:243], v13, s[28:31], s60 offen offset:0 sc1\n"
    "ds_read_b64_tr_b16 v[22:23], v221 offset:26624\n"
    "v_mfma_f32_16x16x32_bf16 v[184:187], v[24:27], v[104:107], v[184:187]\n"
    "v_mfma_f32_16x16x32_bf16 v[188:191], v[24:27], v[108:111], v[188:191]\n"
    "v_mfma_f32_16x16x32_bf16 v[192:195], v[28:31], v[96:99], v[192:195]\n"
    "ds_read_b64_tr_b16 v[24:25], v220 offset:32768\n"
    "v_add_u32 v219, 0, v227\n"
    "v_mfma_f32_16x16x32_bf16 v[196:199], v[28:31], v[100:103], v[196:199]\n"
    "buffer_store_dwordx4 v[244:247], v13, s[28:31], s60 offen offset:1024 sc1\n"
    "ds_read_b64_tr_b16 v[26:27], v220 offset:34816\n"
    "v_mfma_f32_16x16x32_bf16 v[200:203], v[28:31], v[104:107], v[200:203]\n"
    "v_mfma_f32_16x16x32_bf16 v[204:207], v[28:31], v[108:111], v[204:207]\n"
    "s_waitcnt lgkmcnt(6)\n"
    "v_mfma_f32_16x16x32_bf16 v[176:179], v[32:35], v[112:115], v[176:179]\n"
    "ds_read_b64_tr_b16 v[28:29], v221 offset:32768\n"
    "v_mfma_f32_16x16x32_bf16 v[180:183], v[32:35], v[116:119], v[180:183]\n"
    "buffer_store_dwordx4 v[248:251], v13, s[28:31], s60 offen offset:2048 sc1\n"
    "ds_read_b64_tr_b16 v[30:31], v221 offset:34816\n"
    "v_mfma_f32_16x16x32_bf16 v[184:187], v[32:35], v[120:123], v[184:187]\n"
    "v_mfma_f32_16x16x32_bf16 v[188:191], v[32:35], v[124:127], v[188:191]\n"
    "v_mfma_f32_16x16x32_bf16 v[192:195], v[36:39], v[112:115], v[192:195]\n"
    "ds_read_b64_tr_b16 v[32:33], v220 offset:40960\n"
    "v_mfma_f32_16x16x32_bf16 v[196:199], v[36:39], v[116:119], v[196:199]\n"
    "buffer_store_dwordx4 v[252:255], v13, s[28:31], s60 offen offset:3072 sc1\n"
    "ds_read_b64_tr_b16 v[34:35], v220 offset:43008\n"
    "v_mfma_f32_16x16x32_bf16 v[200:203], v[36:39], v[120:123], v[200:203]\n"
    "v_mfma_f32_16x16x32_bf16 v[204:207], v[36:39], v[124:127], v[204:207]\n"
    "s_waitcnt lgkmcnt(6)\n"
    "v_mfma_f32_16x16x32_bf16 v[176:179], v[16:19], v[128:131], v[176:179]\n"
    "ds_read_b64_tr_b16 v[36:37], v221 offset:40960\n"
    "v_mfma_f32_16x16x32_bf16 v[180:183], v[16:19], v[132:135], v[180:183]\n"
    "s_add_u32 s60, s60, 262144\n"
    "ds_read_b64_tr_b16 v[38:39], v221 offset:43008\n"
    "v_mfma_f32_16x16x32_bf16 v[184:187], v[16:19], v[136:139], v[184:187]\n"
    "v_mfma_f32_16x16x32_bf16 v[188:191], v[16:19], v[140:143], v[188:191]\n"
    "v_mfma_f32_16x16x32_bf16 v[192:195], v[20:23], v[128:131], v[192:195]\n"
    "s_add_u32 s40, s15, 7\n"
    "s_cmp_ge_u32 s40, 9\n"
    "s_cselect_b32 s41, 9, 0\n"
    "s_sub_u32 s40, s40, s41\n"
    "s_lshl_b32 s53, s40, 7\n"
    "s_mul_i32 s54, s9, 12288\n"
    "s_add_u32 s54, s54, 49152\n"
    "s_add_u32 m0, s54, 0\n"
    "s_add_u32 s47, s50, s53\n"
    "buffer_load_dwordx4 v9, s[20:23], s47 offen offset:0 lds\n"
    "ds_read_b64_tr_b16 v[16:17], v222 offset:0\n"
    "v_mfma_f32_16x16x32_bf16 v[196:199], v[20:23], v[132:135], v[196:199]\n"
    "ds_read_b64_tr_b16 v[18:19], v222 offset:2048\n"
    "v_mfma_f32_16x16x32_bf16 v[200:203], v[20:23], v[136:139], v[200:203]\n"
    "v_mfma_f32_16x16x32_bf16 v[204:207], v[20:23], v[140:143], v[204:207]\n"
    "s_waitcnt lgkmcnt(6)\n"
    "v_mfma_f32_16x16x32_bf16 v[176:179], v[24:27], v[144:147], v[176:179]\n"
    "ds_read_b64_tr_b16 v[20:21], v223 offset:0\n"
    "v_mfma_f32_16x16x32_bf16 v[180:183], v[24:27], v[148:151], v[180:183]\n"
    "buffer_load_dwordx4 v10, s[20:23], s47 offen offset:1024 lds\n"
    "ds_read_b64_tr_b16 v[22:23], v223 offset:2048\n"
    "v_mfma_f32_16x16x32_bf16 v[184:187], v[24:27], v[152:155], v[184:187]\n"
    "v_mfma_f32_16x16x32_bf16 v[188:191], v[24:27], v[156:159], v[188:191]\n"
    "v_mfma_f32_16x16x32_bf16 v[192:195], v[28:31], v[144:147], v[192:195]\n"
    "ds_read_b64_tr_b16 v[24:25], v222 offset:8192\n"
    "v_mfma_f32_16x16x32_bf16 v[196:199], v[28:31], v[148:151], v[196:199]\n"
    "ds_read_b64_tr_b16 v[26:27], v222 offset:10240\n"
    "v_mfma_f32_16x16x32_bf16 v[200:203], v[28:31], v[152:155], v[200:203]\n"
    "v_mfma_f32_16x16x32_bf16 v[204:207], v[28:31], v[156:159], v[204:207]\n"
    "buffer_load_dwordx4 v11, s[20:23], s47 offen offset:2048 lds\n"
    "s_waitcnt lgkmcnt(6)\n"
    "v_mfma_f32_16x16x32_bf16 v[176:179], v[32:35], v[160:163], v[176:179]\n"
    "ds_read_b64_tr_b16 v[28:29], v223 offset:8192\n"
    "v_mfma_f32_16x16x32_bf16 v[180:183], v[32:35], v[164:167], v[180:183]\n"
    "ds_read_b64_tr_b16 v[30:31], v223 offset:10240\n"
    "v_mfma_f32_16x16x32_bf16 v[184:187], v[32:35], v[168:171], v[184:187]\n"
    "v_mfma_f32_16x16x32_bf16 v[188:191], v[32:35], v[172:175], v[188:191]\n"
    "v_mfma_f32_16x16x32_bf16 v[192:195], v[36:39], v[160:163], v[192:195]\n"
    "ds_read_b64_tr_b16 v[32:33], v222 offset:16384\n"
    "v_mfma_f32_16x16x32_bf16 v[196:199], v[36:39], v[164:167], v[196:199]\n"
    "buffer_load_dwordx4 v12, s[20:23], s47 offen offset:3072 lds\n"
    "ds_read_b64_tr_b16 v[34:35], v222 offset:18432\n"
    "v_mfma_f32_16x16x32_bf16 v[200:203], v[36:39], v[168:171], v[200:203]\n"
    "v_mfma_f32_16x16x32_bf16 v[204:207], v[36:39], v[172:175], v[204:207]\n"
    "s_waitcnt lgkmcnt(6)\n"
    "v_mfma_f32_16x16x32_bf16 v[176:179], v[16:19], a[0:3], v[176:179]\n"
    "ds_read_b64_tr_b16 v[36:37], v223 offset:16384\n"
    "v_mfma_f32_16x16x32_bf16 v[180:183], v[16:19], a[4:7], v[180:183]\n"
    "ds_read_b64_tr_b16 v[38:39], v223 offset:18432\n"
    "v_mfma_f32_16x16x32_bf16 v[184:187], v[16:19], a[8:11], v[184:187]\n"
    "v_mfma_f32_16x16x32_bf16 v[188:191], v[16:19], a[12:15], v[188:191]\n"
    "s_add_u32 m0, s54, 4096\n"
    "s_add_u32 s47, s51, s53\n"
    "buffer_load_dwordx4 v9, s[20:23], s47 offen offset:0 lds\n"
    "v_mfma_f32_16x16x32_bf16 v[192:195], v[20:23], a[0:3], v[192:195]\n"
    "ds_read_b64_tr_b16 v[16:17], v222 offset:24576\n"
    "v_mfma_f32_16x16x32_bf16 v[196:199], v[20:23], a[4:7], v[196:199]\n"
    "ds_read_b64_tr_b16 v[18:19], v222 offset:26624\n"
    "v_mfma_f32_16x16x32_bf16 v[200:203], v[20:23], a[8:11], v[200:203]\n"
    "v_mfma_f32_16x16x32_bf16 v[204:207], v[20:23], a[12:15], v[204:207]\n"
    "s_waitcnt lgkmcnt(6)\n"
    "v_mfma_f32_16x16x32_bf16 v[176:179], v[24:27], a[16:19], v[176:179]\n"
    "ds_read_b64_tr_b16 v[20:21], v223 offset:24576\n"
    "v_mfma_f32_16x16x32_bf16 v[180:183], v[24:27], a[20:23], v[180:183]\n"
    "buffer_load_dwordx4 v10, s[20:23], s47 offen offset:1024 lds\n"
    "ds_read_b64_tr_b16 v[22:23], v223 offset:26624\n"
    "v_mfma_f32_16x16x32_bf16 v[184:187], v[24:27], a[24:27], v[184:187]\n"
    "v_mfma_f32_16x16x32_bf16 v[188:191], v[24:27], a[28:31], v[188:191]\n"
    "v_mfma_f32_16x16x32_bf16 v[192:195], v[28:31], a[16:19], v[192:195]\n"
    "s_waitcnt vmcnt(23)\n"
    "buffer_store_dwordx2 v[230:231], v228, s[72:75], s57 offen sc1\n"
    "s_add_u32 s57, s57, 512\n"
    "ds_read_b64_tr_b16 v[24:25], v222 offset:32768\n"
    "v_mfma_f32_16x16x32_bf16 v[196:199], v[28:31], a[20:23], v[196:199]\n"
    "ds_read_b64_tr_b16 v[26:27], v222 offset:34816\n"
    "v_mfma_f32_16x16x32_bf16 v[200:203], v[28:31], a[24:27], v[200:203]\n"
    "buffer_load_dwordx4 v11, s[20:23], s47 offen offset:2048 lds\n"
    "v_mfma_f32_16x16x32_bf16 v[204:207], v[28:31], a[28:31], v[204:207]\n"
    "s_waitcnt lgkmcnt(6)\n"
    "v_mfma_f32_16x16x32_bf16 v[176:179], v[32:35], a[32:35], v[176:179]\n"
    "ds_read_b64_tr_b16 v[28:29], v223 offset:32768\n"
    "v_mfma_f32_16x16x32_bf16 v[180:183], v[32:35], a[36:39], v[180:183]\n"
    "ds_read_b64_tr_b16 v[30:31], v223 offset:34816\n"
    "v_mfma_f32_16x16x32_bf16 v[184:187], v[32:35], a[40:43], v[184:187]\n"
    "v_mfma_f32_16x16x32_bf16 v[188:191], v[32:35], a[44:47], v[188:191]\n"
    "v_mfma_f32_16x16x32_bf16 v[192:195], v[36:39], a[32:35], v[192:195]\n"
    "buffer_load_dwordx4 v12, s[20:23], s47 offen offset:3072 lds\n"
    "ds_read_b64_tr_b16 v[32:33], v222 offset:40960\n"
    "v_mfma_f32_16x16x32_bf16 v[196:199], v[36:39], a[36:39], v[196:199]\n"
    "ds_read_b64_tr_b16 v[34:35], v222 offset:43008\n"
    "v_mfma_f32_16x16x32_bf16 v[200:203], v[36:39], a[40:43], v[200:203]\n"
    "v_mfma_f32_16x16x32_bf16 v[204:207], v[36:39], a[44:47], v[204:207]\n"
    "s_waitcnt lgkmcnt(6)\n"
    "v_mfma_f32_16x16x32_bf16 v[176:179], v[16:19], a[48:51], v[176:179]\n"
    "ds_read_b64_tr_b16 v[36:37], v223 offset:40960\n"
    "v_mfma_f32_16x16x32_bf16 v[180:183], v[16:19], a[52:55], v[180:183]\n"
    "ds_read_b64_tr_b16 v[38:39], v223 offset:43008\n"
    "v_mfma_f32_16x16x32_bf16 v[184:187], v[16:19], a[56:59], v[184:187]\n"
    "s_add_u32 m0, s54, 8192\n"
    "s_add_u32 s47, s52, s53\n"
    "buffer_load_dwordx4 v9, s[20:23], s47 offen offset:0 lds\n"
    "v_mfma_f32_16x16x32_bf16 v[188:191], v[16:19], a[60:63], v[188:191]\n"
    "v_mfma_f32_16x16x32_bf16 v[192:195], v[20:23], a[48:51], v[192:195]\n"
    "v_mfma_f32_16x16x32_bf16 v[196:199], v[20:23], a[52:55], v[196:199]\n"
    "v_mfma_f32_16x16x32_bf16 v[200:203], v[20:23], a[56:59], v[200:203]\n"
    "v_mfma_f32_16x16x32_bf16 v[204:207], v[20:23], a[60:63], v[204:207]\n"
    "s_waitcnt lgkmcnt(4)\n"
    "v_mfma_f32_16x16x32_bf16 v[176:179], v[24:27], a[64:67], v[176:179]\n"
    "buffer_load_dwordx4 v10, s[20:23], s47 offen offset:1024 lds\n"
    "v_mfma_f32_16x16x32_bf16 v[180:183], v[24:27], a[68:71], v[180:183]\n"
    "v_mfma_f32_16x16x32_bf16 v[184:187], v[24:27], a[72:75], v[184:187]\n"
    "v_mfma_f32_16x16x32_bf16 v[188:191], v[24:27], a[76:79], v[188:191]\n"
    "s_waitcnt lgkmcnt(0)\n"
    "s_waitcnt vmcnt(15)\n"
    "s_barrier\n"
    "v_mfma_f32_16x16x32_bf16 v[192:195], v[28:31], a[64:67], v[192:195]\n"
    "ds_read_b64_tr_b16 v[16:17], v216 offset:0\n"
    "v_mfma_f32_16x16x32_bf16 v[196:199], v[28:31], a[68:71], v[196:199]\n"
    "ds_read_b64_tr_b16 v[18:19], v216 offset:2048\n"
    "v_mfma_f32_16x16x32_bf16 v[200:203], v[28:31], a[72:75], v[200:203]\n"
    "buffer_load_dwordx4 v11, s[20:23], s47 offen offset:2048 lds\n"
    "ds_read_b64_tr_b16 v[20:21], v217 offset:0\n"
    "v_mfma_f32_16x16x32_bf16 v[204:207], v[28:31], a[76:79], v[204:207]\n"
    "ds_read_b64_tr_b16 v[22:23], v217 offset:2048\n"
    "v_mfma_f32_16x16x32_bf16 v[176:179], v[32:35], a[80:83], v[176:179]\n"
    "ds_read_b64_tr_b16 v[24:25], v216 offset:8192\n"
    "v_mfma_f32_16x16x32_bf16 v[180:183], v[32:35], a[84:87], v[180:183]\n"
    "ds_read_b64_tr_b16 v[26:27], v216 offset:10240\n"
    "v_mfma_f32_16x16x32_bf16 v[184:187], v[32:35], a[88:91], v[184:187]\n"
    "ds_read_b64_tr_b16 v[28:29], v217 offset:8192\n"
    "v_mfma_f32_16x16x32_bf16 v[188:191], v[32:35], a[92:95], v[188:191]\n"
    "ds_read_b64_tr_b16 v[30:31], v217 offset:10240\n"
    "v_mfma_f32_16x16x32_bf16 v[192:195], v[36:39], a[80:83], v[192:195]\n"
    "buffer_load_dwordx4 v12, s[20:23], s47 offen offset:3072 lds\n"
    "ds_read_b64_tr_b16 v[32:33], v216 offset:16384\n"
    "v_mfma_f32_16x16x32_bf16 v[196:199], v[36:39], a[84:87], v[196:199]\n"
    "ds_read_b64_tr_b16 v[34:35], v216 offset:18432\n"
    "v_mfma_f32_16x16x32_bf16 v[200:203], v[36:39], a[88:91], v[200:203]\n"
    "v_mfma_f32_16x16x32_bf16 v[204:207], v[36:39], a[92:95], v[204:207]\n"
    "s_waitcnt lgkmcnt(6)\n"
    "v_cvt_pk_bf16_f32 v240, v176, v177\n"
    "v_cvt_pk_bf16_f32 v241, v178, v179\n"
    "v_mfma_f32_16x16x32_bf16 v[176:179], v[16:19], v[80:83], 0\n"
    "ds_read_b64_tr_b16 v[36:37], v217 offset:16384\n"
    "v_add_u32 v220, 49152, v224\n"
    "v_cvt_pk_bf16_f32 v244, v180, v181\n"
    "v_cvt_pk_bf16_f32 v245, v182, v183\n"
    "v_mfma_f32_16x16x32_bf16 v[180:183], v[16:19], v[84:87], 0\n"
    "ds_read_b64_tr_b16 v[38:39], v217 offset:18432\n"
    "v_cvt_pk_bf16_f32 v248, v184, v185\n"
    "v_cvt_pk_bf16_f32 v249, v186, v187\n"
    "v_mfma_f32_16x16x32_bf16 v[184:187], v[16:19], v[88:91], 0\n"
    "v_cvt_pk_bf16_f32 v252, v188, v189\n"
    "v_cvt_pk_bf16_f32 v253, v190, v191\n"
    "v_mfma_f32_16x16x32_bf16 v[188:191], v[16:19], v[92:95], 0\n"
    "v_cvt_pk_bf16_f32 v242, v192, v193\n"
    "v_cvt_pk_bf16_f32 v243, v194, v195\n"
    "v_mfma_f32_16x16x32_bf16 v[192:195], v[20:23], v[80:83], 0\n"
    "ds_read_b64_tr_b16 v[16:17], v216 offset:24576\n"
    "v_add_u32 v221, 49152, v225\n"
    "v_cvt_pk_bf16_f32 v246, v196, v197\n"
    "v_cvt_pk_bf16_f32 v247, v198, v199\n"
    "v_mfma_f32_16x16x32_bf16 v[196:199], v[20:23], v[84:87], 0\n"
    "ds_read_b64_tr_b16 v[18:19], v216 offset:26624\n"
    "v_cvt_pk_bf16_f32 v250, v200, v201\n"
    "v_cvt_pk_bf16_f32 v251, v202, v203\n"
    "v_mfma_f32_16x16x32_bf16 v[200:203], v[20:23], v[88:91], 0\n"
    "v_cvt_pk_bf16_f32 v254, v204, v205\n"
    "v_cvt_pk_bf16_f32 v255, v206, v207\n"
    "v_mfma_f32_16x16x32_bf16 v[204:207], v[20:23], v[92:95], 0\n"
    "s_waitcnt lgkmcnt(6)\n"
    "v_mfma_f32_16x16x32_bf16 v[176:179], v[24:27], v[96:99], v[176:179]\n"
    "ds_read_b64_tr_b16 v[20:21], v217 offset:24576\n"
    "v_add_u32 v222, 49152, v226\n"
    "v_mfma_f32_16x16x32_bf16 v[180:183], v[24:27], v[100:103], v[180:183]\n"
    "buffer_store_dwordx4 v[240:243], v13, s[28:31], s60 offen offset:0 sc1\n"
    "ds_read_b64_tr_b16 v[22:23], v217 offset:26624\n"
    "v_mfma_f32_16x16x32_bf16 v[184:187], v[24:27], v[104:107], v[184:187]\n"
    "v_mfma_f32_16x16x32_bf16 v[188:191], v[24:27], v[108:111], v[188:191]\n"
    "v_mfma_f32_16x16x32_bf16 v[192:195], v[28:31], v[96:99], v[192:195]\n"
    "ds_read_b64_tr_b16 v[24:25], v216 offset:32768\n"
    "v_add_u32 v223, 49152, v227\n"
    "v_mfma_f32_16x16x32_bf16 v[196:199], v[28:31], v[100:103], v[196:199]\n"
    "buffer_store_dwordx4 v[244:247], v13, s[28:31], s60 offen offset:1024 sc1\n"
    "ds_read_b64_tr_b16 v[26:27], v216 offset:34816\n"
    "v_mfma_f32_16x16x32_bf16 v[200:203], v[28:31], v[104:107], v[200:203]\n"
    "v_mfma_f32_16x16x32_bf16 v[204:207], v[28:31], v[108:111], v[204:207]\n"
    "s_waitcnt lgkmcnt(6)\n"
    "v_mfma_f32_16x16x32_bf16 v[176:179], v[32:35], v[112:115], v[176:179]\n"
    "ds_read_b64_tr_b16 v[28:29], v217 offset:32768\n"
    "v_mfma_f32_16x16x32_bf16 v[180:183], v[32:35], v[116:119], v[180:183]\n"
    "buffer_store_dwordx4 v[248:251], v13, s[28:31], s60 offen offset:2048 sc1\n"
    "ds_read_b64_tr_b16 v[30:31], v217 offset:34816\n"
    "v_mfma_f32_16x16x32_bf16 v[184:187], v[32:35], v[120:123], v[184:187]\n"
    "v_mfma_f32_16x16x32_bf16 v[188:191], v[32:35], v[124:127], v[188:191]\n"
    "v_mfma_f32_16x16x32_bf16 v[192:195], v[36:39], v[112:115], v[192:195]\n"
    "ds_read_b64_tr_b16 v[32:33], v216 offset:40960\n"
    "v_mfma_f32_16x16x32_bf16 v[196:199], v[36:39], v[116:119], v[196:199]\n"
    "buffer_store_dwordx4 v[252:255], v13, s[28:31], s60 offen offset:3072 sc1\n"
    "ds_read_b64_tr_b16 v[34:35], v216 offset:43008\n"
    "v_mfma_f32_16x16x32_bf16 v[200:203], v[36:39], v[120:123], v[200:203]\n"
    "v_mfma_f32_16x16x32_bf16 v[204:207], v[36:39], v[124:127], v[204:207]\n"
    "s_waitcnt lgkmcnt(6)\n"
    "v_mfma_f32_16x16x32_bf16 v[176:179], v[16:19], v[128:131], v[176:179]\n"
    "ds_read_b64_tr_b16 v[36:37], v217 offset:40960\n"
    "v_mfma_f32_16x16x32_bf16 v[180:183], v[16:19], v[132:135], v[180:183]\n"
    "s_add_u32 s60, s60, 262144\n"
    "ds_read_b64_tr_b16 v[38:39], v217 offset:43008\n"
    "v_mfma_f32_16x16x32_bf16 v[184:187], v[16:19], v[136:139], v[184:187]\n"
    "v_mfma_f32_16x16x32_bf16 v[188:191], v[16:19], v[140:143], v[188:191]\n"
    "v_mfma_f32_16x16x32_bf16 v[192:195], v[20:23], v[128:131], v[192:195]\n"
    "s_add_u32 s40, s15, 8\n"
    "s_cmp_ge_u32 s40, 9\n"
    "s_cselect_b32 s41, 9, 0\n"
    "s_sub_u32 s40, s40, s41\n"
    "s_lshl_b32 s53, s40, 7\n"
    "s_mul_i32 s54, s9, 12288\n"
    "s_add_u32 s54, s54, 98304\n"
    "s_add_u32 m0, s54, 0\n"
    "s_add_u32 s47, s50, s53\n"
    "buffer_load_dwordx4 v9, s[20:23], s47 offen offset:0 lds\n"
    "ds_read_b64_tr_b16 v[16:17], v218 offset:0\n"
    "v_mfma_f32_16x16x32_bf16 v[196:199], v[20:23], v[132:135], v[196:199]\n"
    "ds_read_b64_tr_b16 v[18:19], v218 offset:2048\n"
    "v_mfma_f32_16x16x32_bf16 v[200:203], v[20:23], v[136:139], v[200:203]\n"
    "v_mfma_f32_16x16x32_bf16 v[204:207], v[20:23], v[140:143], v[204:207]\n"
    "s_waitcnt lgkmcnt(6)\n"
    "v_mfma_f32_16x16x32_bf16 v[176:179], v[24:27], v[144:147], v[176:179]\n"
    "ds_read_b64_tr_b16 v[20:21], v219 offset:0\n"
    "v_mfma_f32_16x16x32_bf16 v[180:183], v[24:27], v[148:151], v[180:183]\n"
    "buffer_load_dwordx4 v10, s[20:23], s47 offen offset:1024 lds\n"
    "ds_read_b64_tr_b16 v[22:23], v219 offset:2048\n"
    "v_mfma_f32_16x16x32_bf16 v[184:187], v[24:27], v[152:155], v[184:187]\n"
    "v_mfma_f32_16x16x32_bf16 v[188:191], v[24:27], v[156:159], v[188:191]\n"
    "v_mfma_f32_16x16x32_bf16 v[192:195], v[28:31], v[144:147], v[192:195]\n"
    "ds_read_b64_tr_b16 v[24:25], v218 offset:8192\n"
    "v_mfma_f32_16x16x32_bf16 v[196:199], v[28:31], v[148:151], v[196:199]\n"
    "ds_read_b64_tr_b16 v[26:27], v218 offset:10240\n"
    "v_mfma_f32_16x16x32_bf16 v[200:203], v[28:31], v[152:155], v[200:203]\n"
    "v_mfma_f32_16x16x32_bf16 v[204:207], v[28:31], v[156:159], v[204:207]\n"
    "buffer_load_dwordx4 v11, s[20:23], s47 offen offset:2048 lds\n"
    "s_waitcnt lgkmcnt(6)\n"
    "v_mfma_f32_16x16x32_bf16 v[176:179], v[32:35], v[160:163], v[176:179]\n"
    "ds_read_b64_tr_b16 v[28:29], v219 offset:8192\n"
    "v_mfma_f32_16x16x32_bf16 v[180:183], v[32:35], v[164:167], v[180:183]\n"
    "ds_read_b64_tr_b16 v[30:31], v219 offset:10240\n"
    "v_mfma_f32_16x16x32_bf16 v[184:187], v[32:35], v[168:171], v[184:187]\n"
    "v_mfma_f32_16x16x32_bf16 v[188:191], v[32:35], v[172:175], v[188:191]\n"
    "v_mfma_f32_16x16x32_bf16 v[192:195], v[36:39], v[160:163], v[192:195]\n"
    "ds_read_b64_tr_b16 v[32:33], v218 offset:16384\n"
    "v_mfma_f32_16x16x32_bf16 v[196:199], v[36:39], v[164:167], v[196:199]\n"
    "buffer_load_dwordx4 v12, s[20:23], s47 offen offset:3072 lds\n"
    "ds_read_b64_tr_b16 v[34:35], v218 offset:18432\n"
    "v_mfma_f32_16x16x32_bf16 v[200:203], v[36:39], v[168:171], v[200:203]\n"
    "v_mfma_f32_16x16x32_bf16 v[204:207], v[36:39], v[172:175], v[204:207]\n"
    "s_waitcnt lgkmcnt(6)\n"
    "v_mfma_f32_16x16x32_bf16 v[176:179], v[16:19], a[0:3], v[176:179]\n"
    "ds_read_b64_tr_b16 v[36:37], v219 offset:16384\n"
    "v_mfma_f32_16x16x32_bf16 v[180:183], v[16:19], a[4:7], v[180:183]\n"
    "ds_read_b64_tr_b16 v[38:39], v219 offset:18432\n"
    "v_mfma_f32_16x16x32_bf16 v[184:187], v[16:19], a[8:11], v[184:187]\n"
    "v_mfma_f32_16x16x32_bf16 v[188:191], v[16:19], a[12:15], v[188:191]\n"
    "s_add_u32 m0, s54, 4096\n"
    "s_add_u32 s47, s51, s53\n"
    "buffer_load_dwordx4 v9, s[20:23], s47 offen offset:0 lds\n"
    "v_mfma_f32_16x16x32_bf16 v[192:195], v[20:23], a[0:3], v[192:195]\n"
    "ds_read_b64_tr_b16 v[16:17], v218 offset:24576\n"
    "v_mfma_f32_16x16x32_bf16 v[196:199], v[20:23], a[4:7], v[196:199]\n"
    "ds_read_b64_tr_b16 v[18:19], v218 offset:26624\n"
    "v_mfma_f32_16x16x32_bf16 v[200:203], v[20:23], a[8:11], v[200:203]\n"
    "v_mfma_f32_16x16x32_bf16 v[204:207], v[20:23], a[12:15], v[204:207]\n"
    "s_waitcnt lgkmcnt(6)\n"
    "v_mfma_f32_16x16x32_bf16 v[176:179], v[24:27], a[16:19], v[176:179]\n"
    "ds_read_b64_tr_b16 v[20:21], v219 offset:24576\n"
    "v_mfma_f32_16x16x32_bf16 v[180:183], v[24:27], a[20:23], v[180:183]\n"
    "buffer_load_dwordx4 v10, s[20:23], s47 offen offset:1024 lds\n"
    "ds_read_b64_tr_b16 v[22:23], v219 offset:26624\n"
    "v_mfma_f32_16x16x32_bf16 v[184:187], v[24:27], a[24:27], v[184:187]\n"
    "v_mfma_f32_16x16x32_bf16 v[188:191], v[24:27], a[28:31], v[188:191]\n"
    "v_mfma_f32_16x16x32_bf16 v[192:195], v[28:31], a[16:19], v[192:195]\n"
    "s_waitcnt vmcnt(23)\n"
    "buffer_store_dwordx2 v[230:231], v228, s[72:75], s57 offen sc1\n"
    "s_add_u32 s57, s57, 512\n"
    "ds_read_b64_tr_b16 v[24:25], v218 offset:32768\n"
    "v_mfma_f32_16x16x32_bf16 v[196:199], v[28:31], a[20:23], v[196:199]\n"
    "ds_read_b64_tr_b16 v[26:27], v218 offset:34816\n"
    "v_mfma_f32_16x16x32_bf16 v[200:203], v[28:31], a[24:27], v[200:203]\n"
    "buffer_load_dwordx4 v11, s[20:23], s47 offen offset:2048 lds\n"
    "v_mfma_f32_16x16x32_bf16 v[204:207], v[28:31], a[28:31], v[204:207]\n"
    "s_waitcnt lgkmcnt(6)\n"
    "v_mfma_f32_16x16x32_bf16 v[176:179], v[32:35], a[32:35], v[176:179]\n"
    "ds_read_b64_tr_b16 v[28:29], v219 offset:32768\n"
    "v_mfma_f32_16x16x32_bf16 v[180:183], v[32:35], a[36:39], v[180:183]\n"
    "ds_read_b64_tr_b16 v[30:31], v219 offset:34816\n"
    "v_mfma_f32_16x16x32_bf16 v[184:187], v[32:35], a[40:43], v[184:187]\n"
    "v_mfma_f32_16x16x32_bf16 v[188:191], v[32:35], a[44:47], v[188:191]\n"
    "v_mfma_f32_16x16x32_bf16 v[192:195], v[36:39], a[32:35], v[192:195]\n"
    "buffer_load_dwordx4 v12, s[20:23], s47 offen offset:3072 lds\n"
    "ds_read_b64_tr_b16 v[32:33], v218 offset:40960\n"
    "v_mfma_f32_16x16x32_bf16 v[196:199], v[36:39], a[36:39], v[196:199]\n"
    "ds_read_b64_tr_b16 v[34:35], v218 offset:43008\n"
    "v_mfma_f32_16x16x32_bf16 v[200:203], v[36:39], a[40:43], v[200:203]\n"
    "v_mfma_f32_16x16x32_bf16 v[204:207], v[36:39], a[44:47], v[204:207]\n"
    "s_waitcnt lgkmcnt(6)\n"
    "v_mfma_f32_16x16x32_bf16 v[176:179], v[16:19], a[48:51], v[176:179]\n"
    "ds_read_b64_tr_b16 v[36:37], v219 offset:40960\n"
    "v_mfma_f32_16x16x32_bf16 v[180:183], v[16:19], a[52:55], v[180:183]\n"
    "ds_read_b64_tr_b16 v[38:39], v219 offset:43008\n"
    "v_mfma_f32_16x16x32_bf16 v[184:187], v[16:19], a[56:59], v[184:187]\n"
    "s_add_u32 m0, s54, 8192\n"
    "s_add_u32 s47, s52, s53\n"
    "buffer_load_dwordx4 v9, s[20:23], s47 offen offset:0 lds\n"
    "v_mfma_f32_16x16x32_bf16 v[188:191], v[16:19], a[60:63], v[188:191]\n"
    "v_mfma_f32_16x16x32_bf16 v[192:195], v[20:23], a[48:51], v[192:195]\n"
    "v_mfma_f32_16x16x32_bf16 v[196:199], v[20:23], a[52:55], v[196:199]\n"
    "v_mfma_f32_16x16x32_bf16 v[200:203], v[20:23], a[56:59], v[200:203]\n"
    "v_mfma_f32_16x16x32_bf16 v[204:207], v[20:23], a[60:63], v[204:207]\n"
    "s_waitcnt lgkmcnt(4)\n"
    "v_mfma_f32_16x16x32_bf16 v[176:179], v[24:27], a[64:67], v[176:179]\n"
    "buffer_load_dwordx4 v10, s[20:23], s47 offen offset:1024 lds\n"
    "v_mfma_f32_16x16x32_bf16 v[180:183], v[24:27], a[68:71], v[180:183]\n"
    "v_mfma_f32_16x16x32_bf16 v[184:187], v[24:27], a[72:75], v[184:187]\n"
    "v_mfma_f32_16x16x32_bf16 v[188:191], v[24:27], a[76:79], v[188:191]\n"
    "s_waitcnt lgkmcnt(0)\n"
    "s_waitcnt vmcnt(15)\n"
    "s_barrier\n"
    "v_mfma_f32_16x16x32_bf16 v[192:195], v[28:31], a[64:67], v[192:195]\n"
    "ds_read_b64_tr_b16 v[16:17], v220 offset:0\n"
    "v_mfma_f32_16x16x32_bf16 v[196:199], v[28:31], a[68:71], v[196:199]\n"
    "ds_read_b64_tr_b16 v[18:19], v220 offset:2048\n"
    "v_mfma_f32_16x16x32_bf16 v[200:203], v[28:31], a[72:75], v[200:203]\n"
    "buffer_load_dwordx4 v11, s[20:23], s47 offen offset:2048 lds\n"
    "ds_read_b64_tr_b16 v[20:21], v221 offset:0\n"
    "v_mfma_f32_16x16x32_bf16 v[204:207], v[28:31], a[76:79], v[204:207]\n"
    "ds_read_b64_tr_b16 v[22:23], v221 offset:2048\n"
    "v_mfma_f32_16x16x32_bf16 v[176:179], v[32:35], a[80:83], v[176:179]\n"
    "ds_read_b64_tr_b16 v[24:25], v220 offset:8192\n"
    "v_mfma_f32_16x16x32_bf16 v[180:183], v[32:35], a[84:87], v[180:183]\n"
    "ds_read_b64_tr_b16 v[26:27], v220 offset:10240\n"
    "v_mfma_f32_16x16x32_bf16 v[184:187], v[32:35], a[88:91], v[184:187]\n"
    "ds_read_b64_tr_b16 v[28:29], v221 offset:8192\n"
    "v_mfma_f32_16x16x32_bf16 v[188:191], v[32:35], a[92:95], v[188:191]\n"
    "ds_read_b64_tr_b16 v[30:31], v221 offset:10240\n"
    "v_mfma_f32_16x16x32_bf16 v[192:195], v[36:39], a[80:83], v[192:195]\n"
    "buffer_load_dwordx4 v12, s[20:23], s47 offen offset:3072 lds\n"
    "ds_read_b64_tr_b16 v[32:33], v220 offset:16384\n"
    "v_mfma_f32_16x16x32_bf16 v[196:199], v[36:39], a[84:87], v[196:199]\n"
    "ds_read_b64_tr_b16 v[34:35], v220 offset:18432\n"
    "v_mfma_f32_16x16x32_bf16 v[200:203], v[36:39], a[88:91], v[200:203]\n"
    "v_mfma_f32_16x16x32_bf16 v[204:207], v[36:39], a[92:95], v[204:207]\n"
    "s_waitcnt lgkmcnt(6)\n"
    "v_cvt_pk_bf16_f32 v240, v176, v177\n"
    "v_cvt_pk_bf16_f32 v241, v178, v179\n"
    "v_mfma_f32_16x16x32_bf16 v[176:179], v[16:19], v[80:83], 0\n"
    "ds_read_b64_tr_b16 v[36:37], v221 offset:16384\n"
    "v_add_u32 v216, 98304, v224\n"
    "v_cvt_pk_bf16_f32 v244, v180, v181\n"
    "v_cvt_pk_bf16_f32 v245, v182, v183\n"
    "v_mfma_f32_16x16x32_bf16 v[180:183], v[16:19], v[84:87], 0\n"
    "ds_read_b64_tr_b16 v[38:39], v221 offset:18432\n"
    "v_cvt_pk_bf16_f32 v248, v184, v185\n"
    "v_cvt_pk_bf16_f32 v249, v186, v187\n"
    "v_mfma_f32_16x16x32_bf16 v[184:187], v[16:19], v[88:91], 0\n"
    "v_cvt_pk_bf16_f32 v252, v188, v189\n"
    "v_cvt_pk_bf16_f32 v253, v190, v191\n"
    "v_mfma_f32_16x16x32_bf16 v[188:191], v[16:19], v[92:95], 0\n"
    "v_cvt_pk_bf16_f32 v242, v192, v193\n"
    "v_cvt_pk_bf16_f32 v243, v194, v195\n"
    "v_mfma_f32_16x16x32_bf16 v[192:195], v[20:23], v[80:83], 0\n"
    "ds_read_b64_tr_b16 v[16:17], v220 offset:24576\n"
    "v_add_u32 v217, 98304, v225\n"
    "v_cvt_pk_bf16_f32 v246, v196, v197\n"
    "v_cvt_pk_bf16_f32 v247, v198, v199\n"
    "v_mfma_f32_16x16x32_bf16 v[196:199], v[20:23], v[84:87], 0\n"
    "ds_read_b64_tr_b16 v[18:19], v220 offset:26624\n"
    "v_cvt_pk_bf16_f32 v250, v200, v201\n"
    "v_cvt_pk_bf16_f32 v251, v202, v203\n"
    "v_mfma_f32_16x16x32_bf16 v[200:203], v[20:23], v[88:91], 0\n"
    "v_cvt_pk_bf16_f32 v254, v204, v205\n"
    "v_cvt_pk_bf16_f32 v255, v206, v207\n"
    "v_mfma_f32_16x16x32_bf16 v[204:207], v[20:23], v[92:95], 0\n"
    "s_waitcnt lgkmcnt(6)\n"
    "v_mfma_f32_16x16x32_bf16 v[176:179], v[24:27], v[96:99], v[176:179]\n"
    "ds_read_b64_tr_b16 v[20:21], v221 offset:24576\n"
    "v_add_u32 v218, 98304, v226\n"
    "v_mfma_f32_16x16x32_bf16 v[180:183], v[24:27], v[100:103], v[180:183]\n"
    "buffer_store_dwordx4 v[240:243], v13, s[28:31], s60 offen offset:0 sc1\n"
    "ds_read_b64_tr_b16 v[22:23], v221 offset:26624\n"
    "v_mfma_f32_16x16x32_bf16 v[184:187], v[24:27], v[104:107], v[184:187]\n"
    "v_mfma_f32_16x16x32_bf16 v[188:191], v[24:27], v[108:111], v[188:191]\n"
    "v_mfma_f32_16x16x32_bf16 v[192:195], v[28:31], v[96:99], v[192:195]\n"
    "ds_read_b64_tr_b16 v[24:25], v220 offset:32768\n"
    "v_add_u32 v219, 98304, v227\n"
    "v_mfma_f32_16x16x32_bf16 v[196:199], v[28:31], v[100:103], v[196:199]\n"
    "buffer_store_dwordx4 v[244:247], v13, s[28:31], s60 offen offset:1024 sc1\n"
    "ds_read_b64_tr_b16 v[26:27], v220 offset:34816\n"
    "v_mfma_f32_16x16x32_bf16 v[200:203], v[28:31], v[104:107], v[200:203]\n"
    "v_mfma_f32_16x16x32_bf16 v[204:207], v[28:31], v[108:111], v[204:207]\n"
    "s_waitcnt lgkmcnt(6)\n"
    "v_mfma_f32_16x16x32_bf16 v[176:179], v[32:35], v[112:115], v[176:179]\n"
    "ds_read_b64_tr_b16 v[28:29], v221 offset:32768\n"
    "v_mfma_f32_16x16x32_bf16 v[180:183], v[32:35], v[116:119], v[180:183]\n"
    "buffer_store_dwordx4 v[248:251], v13, s[28:31], s60 offen offset:2048 sc1\n"
    "ds_read_b64_tr_b16 v[30:31], v221 offset:34816\n"
    "v_mfma_f32_16x16x32_bf16 v[184:187], v[32:35], v[120:123], v[184:187]\n"
    "v_mfma_f32_16x16x32_bf16 v[188:191], v[32:35], v[124:127], v[188:191]\n"
    "v_mfma_f32_16x16x32_bf16 v[192:195], v[36:39], v[112:115], v[192:195]\n"
    "ds_read_b64_tr_b16 v[32:33], v220 offset:40960\n"
    "v_mfma_f32_16x16x32_bf16 v[196:199], v[36:39], v[116:119], v[196:199]\n"
    "buffer_store_dwordx4 v[252:255], v13, s[28:31], s60 offen offset:3072 sc1\n"
    "ds_read_b64_tr_b16 v[34:35], v220 offset:43008\n"
    "v_mfma_f32_16x16x32_bf16 v[200:203], v[36:39], v[120:123], v[200:203]\n"
    "v_mfma_f32_16x16x32_bf16 v[204:207], v[36:39], v[124:127], v[204:207]\n"
    "s_waitcnt lgkmcnt(6)\n"
    "v_mfma_f32_16x16x32_bf16 v[176:179], v[16:19], v[128:131], v[176:179]\n"
    "ds_read_b64_tr_b16 v[36:37], v221 offset:40960\n"
    "v_mfma_f32_16x16x32_bf16 v[180:183], v[16:19], v[132:135], v[180:183]\n"
    "s_add_u32 s60, s60, 262144\n"
    "ds_read_b64_tr_b16 v[38:39], v221 offset:43008\n"
    "v_mfma_f32_16x16x32_bf16 v[184:187], v[16:19], v[136:139], v[184:187]\n"
    "v_mfma_f32_16x16x32_bf16 v[188:191], v[16:19], v[140:143], v[188:191]\n"
    "v_mfma_f32_16x16x32_bf16 v[192:195], v[20:23], v[128:131], v[192:195]\n"
    "ds_read_b64_tr_b16 v[16:17], v222 offset:0\n"
    "v_mfma_f32_16x16x32_bf16 v[196:199], v[20:23], v[132:135], v[196:199]\n"
    "ds_read_b64_tr_b16 v[18:19], v222 offset:2048\n"
    "v_mfma_f32_16x16x32_bf16 v[200:203], v[20:23], v[136:139], v[200:203]\n"
    "s_add_u32 s40, s35, 3\n"
    "s_add_u32 s40, s40, s15\n"
    "s_cmp_ge_u32 s40, 18\n"
    "s_cselect_b32 s41, 9, 0\n"
    "s_sub_u32 s40, s40, s41\n"
    "s_cmp_ge_u32 s40, 9\n"
    "s_cselect_b32 s41, 9, 0\n"
    "s_sub_u32 s40, s40, s41\n"
    "s_lshl_b32 s92, s40, 7\n"
    "buffer_load_dwordx2 a[192:193], v233, s[76:79], s92 offen offset:0\n"
    "v_mfma_f32_16x16x32_bf16 v[204:207], v[20:23], v[140:143], v[204:207]\n"
    "s_waitcnt lgkmcnt(6)\n"
    "v_mfma_f32_16x16x32_bf16 v[176:179], v[24:27], v[144:147], v[176:179]\n"
    "ds_read_b64_tr_b16 v[20:21], v223 offset:0\n"
    "v_mfma_f32_16x16x32_bf16 v[180:183], v[24:27], v[148:151], v[180:183]\n"
    "ds_read_b64_tr_b16 v[22:23], v223 offset:2048\n"
    "v_mfma_f32_16x16x32_bf16 v[184:187], v[24:27], v[152:155], v[184:187]\n"
    "v_mfma_f32_16x16x32_bf16 v[188:191], v[24:27], v[156:159], v[188:191]\n"
    "v_mfma_f32_16x16x32_bf16 v[192:195], v[28:31], v[144:147], v[192:195]\n"
    "ds_read_b64_tr_b16 v[24:25], v222 offset:8192\n"
    "v_mfma_f32_16x16x32_bf16 v[196:199], v[28:31], v[148:151], v[196:199]\n"
    "ds_read_b64_tr_b16 v[26:27], v222 offset:10240\n"
    "v_mfma_f32_16x16x32_bf16 v[200:203], v[28:31], v[152:155], v[200:203]\n"
    "v_mfma_f32_16x16x32_bf16 v[204:207], v[28:31], v[156:159], v[204:207]\n"
    "s_waitcnt lgkmcnt(6)\n"
    "v_mfma_f32_16x16x32_bf16 v[176:179], v[32:35], v[160:163], v[176:179]\n"
    "ds_read_b64_tr_b16 v[28:29], v223 offset:8192\n"
    "v_mfma_f32_16x16x32_bf16 v[180:183], v[32:35], v[164:167], v[180:183]\n"
    "ds_read_b64_tr_b16 v[30:31], v223 offset:10240\n"
    "v_mfma_f32_16x16x32_bf16 v[184:187], v[32:35], v[168:171], v[184:187]\n"
    "v_mfma_f32_16x16x32_bf16 v[188:191], v[32:35], v[172:175], v[188:191]\n"
    "v_mfma_f32_16x16x32_bf16 v[192:195], v[36:39], v[160:163], v[192:195]\n"
    "ds_read_b64_tr_b16 v[32:33], v222 offset:16384\n"
    "v_mfma_f32_16x16x32_bf16 v[196:199], v[36:39], v[164:167], v[196:199]\n"
    "ds_read_b64_tr_b16 v[34:35], v222 offset:18432\n"
    "v_mfma_f32_16x16x32_bf16 v[200:203], v[36:39], v[168:171], v[200:203]\n"
    "v_mfma_f32_16x16x32_bf16 v[204:207], v[36:39], v[172:175], v[204:207]\n"
    "s_waitcnt lgkmcnt(6)\n"
    "v_mfma_f32_16x16x32_bf16 v[176:179], v[16:19], a[0:3], v[176:179]\n"
    "ds_read_b64_tr_b16 v[36:37], v223 offset:16384\n"
    "v_mfma_f32_16x16x32_bf16 v[180:183], v[16:19], a[4:7], v[180:183]\n"
    "ds_read_b64_tr_b16 v[38:39], v223 offset:18432\n"
    "v_mfma_f32_16x16x32_bf16 v[184:187], v[16:19], a[8:11], v[184:187]\n"
    "buffer_load_dwordx2 a[194:195], v233, s[76:79], s92 offen offset:32\n"
    "v_mfma_f32_16x16x32_bf16 v[188:191], v[16:19], a[12:15], v[188:191]\n"
    "v_mfma_f32_16x16x32_bf16 v[192:195], v[20:23], a[0:3], v[192:195]\n"
    "ds_read_b64_tr_b16 v[16:17], v222 offset:24576\n"
    "v_mfma_f32_16x16x32_bf16 v[196:199], v[20:23], a[4:7], v[196:199]\n"
    "ds_read_b64_tr_b16 v[18:19], v222 offset:26624\n"
    "v_mfma_f32_16x16x32_bf16 v[200:203], v[20:23], a[8:11], v[200:203]\n"
    "v_mfma_f32_16x16x32_bf16 v[204:207], v[20:23], a[12:15], v[204:207]\n"
    "s_waitcnt lgkmcnt(6)\n"
    "v_mfma_f32_16x16x32_bf16 v[176:179], v[24:27], a[16:19], v[176:179]\n"
    "ds_read_b64_tr_b16 v[20:21], v223 offset:24576\n"
    "v_mfma_f32_16x16x32_bf16 v[180:183], v[24:27], a[20:23], v[180:183]\n"
    "ds_read_b64_tr_b16 v[22:23], v223 offset:26624\n"
    "v_mfma_f32_16x16x32_bf16 v[184:187], v[24:27], a[24:27], v[184:187]\n"
    "v_mfma_f32_16x16x32_bf16 v[188:191], v[24:27], a[28:31], v[188:191]\n"
    "v_mfma_f32_16x16x32_bf16 v[192:195], v[28:31], a[16:19], v[192:195]\n"
    "s_waitcnt vmcnt(19)\n"
    "buffer_store_dwordx2 v[230:231], v228, s[72:75], s57 offen sc1\n"
    "s_add_u32 s57, s57, 512\n"
    "ds_read_b64_tr_b16 v[24:25], v222 offset:32768\n"
    "v_mfma_f32_16x16x32_bf16 v[196:199], v[28:31], a[20:23], v[196:199]\n"
    "ds_read_b64_tr_b16 v[26:27], v222 offset:34816\n"
    "v_mfma_f32_16x16x32_bf16 v[200:203], v[28:31], a[24:27], v[200:203]\n"
    "v_mfma_f32_16x16x32_bf16 v[204:207], v[28:31], a[28:31], v[204:207]\n"
    "s_waitcnt lgkmcnt(6)\n"
    "v_mfma_f32_16x16x32_bf16 v[176:179], v[32:35], a[32:35], v[176:179]\n"
    "ds_read_b64_tr_b16 v[28:29], v223 offset:32768\n"
    "v_mfma_f32_16x16x32_bf16 v[180:183], v[32:35], a[36:39], v[180:183]\n"
    "ds_read_b64_tr_b16 v[30:31], v223 offset:34816\n"
    "v_mfma_f32_16x16x32_bf16 v[184:187], v[32:35], a[40:43], v[184:187]\n"
    "v_mfma_f32_16x16x32_bf16 v[188:191], v[32:35], a[44:47], v[188:191]\n"
    "v_mfma_f32_16x16x32_bf16 v[192:195], v[36:39], a[32:35], v[192:195]\n"
    "ds_read_b64_tr_b16 v[32:33], v222 offset:40960\n"
    "v_mfma_f32_16x16x32_bf16 v[196:199], v[36:39], a[36:39], v[196:199]\n"
    "ds_read_b64_tr_b16 v[34:35], v222 offset:43008\n"
    "v_mfma_f32_16x16x32_bf16 v[200:203], v[36:39], a[40:43], v[200:203]\n"
    "s_add_u32 s40, s35, 6\n"
    "s_add_u32 s40, s40, s15\n"
    "s_cmp_ge_u32 s40, 18\n"
    "s_cselect_b32 s41, 9, 0\n"
    "s_sub_u32 s40, s40, s41\n"
    "s_cmp_ge_u32 s40, 9\n"
    "s_cselect_b32 s41, 9, 0\n"
    "s_sub_u32 s40, s40, s41\n"
    "s_lshl_b32 s93, s40, 7\n"
    "buffer_load_dwordx2 a[196:197], v233, s[76:79], s93 offen offset:0\n"
    "v_mfma_f32_16x16x32_bf16 v[204:207], v[36:39], a[44:47], v[204:207]\n"
    "s_waitcnt lgkmcnt(6)\n"
    "v_mfma_f32_16x16x32_bf16 v[176:179], v[16:19], a[48:51], v[176:179]\n"
    "ds_read_b64_tr_b16 v[36:37], v223 offset:40960\n"
    "v_mfma_f32_16x16x32_bf16 v[180:183], v[16:19], a[52:55], v[180:183]\n"
    "ds_read_b64_tr_b16 v[38:39], v223 offset:43008\n"
    "v_mfma_f32_16x16x32_bf16 v[184:187], v[16:19], a[56:59], v[184:187]\n"
    "v_mfma_f32_16x16x32_bf16 v[188:191], v[16:19], a[60:63], v[188:191]\n"
    "v_mfma_f32_16x16x32_bf16 v[192:195], v[20:23], a[48:51], v[192:195]\n"
    "v_mfma_f32_16x16x32_bf16 v[196:199], v[20:23], a[52:55], v[196:199]\n"
    "v_mfma_f32_16x16x32_bf16 v[200:203], v[20:23], a[56:59], v[200:203]\n"
    "v_mfma_f32_16x16x32_bf16 v[204:207], v[20:23], a[60:63], v[204:207]\n"
    "s_waitcnt lgkmcnt(4)\n"
    "v_mfma_f32_16x16x32_bf16 v[176:179], v[24:27], a[64:67], v[176:179]\n"
    "v_mfma_f32_16x16x32_bf16 v[180:183], v[24:27], a[68:71], v[180:183]\n"
    "v_mfma_f32_16x16x32_bf16 v[184:187], v[24:27], a[72:75], v[184:187]\n"
    "v_mfma_f32_16x16x32_bf16 v[188:191], v[24:27], a[76:79], v[188:191]\n"
    "s_waitcnt lgkmcnt(0)\n"
    "s_waitcnt vmcnt(8)\n"
    "s_barrier\n"
    "v_mfma_f32_16x16x32_bf16 v[192:195], v[28:31], a[64:67], v[192:195]\n"
    "ds_read_b64_tr_b16 v[16:17], v216 offset:0\n"
    "v_mfma_f32_16x16x32_bf16 v[196:199], v[28:31], a[68:71], v[196:199]\n"
    "ds_read_b64_tr_b16 v[18:19], v216 offset:2048\n"
    "v_mfma_f32_16x16x32_bf16 v[200:203], v[28:31], a[72:75], v[200:203]\n"
    "ds_read_b64_tr_b16 v[20:21], v217 offset:0\n"
    "v_mfma_f32_16x16x32_bf16 v[204:207], v[28:31], a[76:79], v[204:207]\n"
    "ds_read_b64_tr_b16 v[22:23], v217 offset:2048\n"
    "v_mfma_f32_16x16x32_bf16 v[176:179], v[32:35], a[80:83], v[176:179]\n"
    "ds_read_b64_tr_b16 v[24:25], v216 offset:8192\n"
    "v_mfma_f32_16x16x32_bf16 v[180:183], v[32:35], a[84:87], v[180:183]\n"
    "ds_read_b64_tr_b16 v[26:27], v216 offset:10240\n"
    "v_mfma_f32_16x16x32_bf16 v[184:187], v[32:35], a[88:91], v[184:187]\n"
    "buffer_load_dwordx2 a[198:199], v233, s[76:79], s93 offen offset:32\n"
    "ds_read_b64_tr_b16 v[28:29], v217 offset:8192\n"
    "v_mfma_f32_16x16x32_bf16 v[188:191], v[32:35], a[92:95], v[188:191]\n"
    "ds_read_b64_tr_b16 v[30:31], v217 offset:10240\n"
    "v_mfma_f32_16x16x32_bf16 v[192:195], v[36:39], a[80:83], v[192:195]\n"
    "ds_read_b64_tr_b16 v[32:33], v216 offset:16384\n"
    "v_mfma_f32_16x16x32_bf16 v[196:199], v[36:39], a[84:87], v[196:199]\n"
    "ds_read_b64_tr_b16 v[34:35], v216 offset:18432\n"
    "v_mfma_f32_16x16x32_bf16 v[200:203], v[36:39], a[88:91], v[200:203]\n"
    "v_mfma_f32_16x16x32_bf16 v[204:207], v[36:39], a[92:95], v[204:207]\n"
    "s_waitcnt lgkmcnt(6)\n"
    "v_cvt_pk_bf16_f32 v240, v176, v177\n"
    "v_cvt_pk_bf16_f32 v241, v178, v179\n"
    "v_mfma_f32_16x16x32_bf16 v[176:179], v[16:19], v[80:83], 0\n"
    "ds_read_b64_tr_b16 v[36:37], v217 offset:16384\n"
    "v_cvt_pk_bf16_f32 v244, v180, v181\n"
    "v_cvt_pk_bf16_f32 v245, v182, v183\n"
    "v_mfma_f32_16x16x32_bf16 v[180:183], v[16:19], v[84:87], 0\n"
    "ds_read_b64_tr_b16 v[38:39], v217 offset:18432\n"
    "v_cvt_pk_bf16_f32 v248, v184, v185\n"
    "v_cvt_pk_bf16_f32 v249, v186, v187\n"
    "v_mfma_f32_16x16x32_bf16 v[184:187], v[16:19], v[88:91], 0\n"
    "v_cvt_pk_bf16_f32 v252, v188, v189\n"
    "v_cvt_pk_bf16_f32 v253, v190, v191\n"
    "v_mfma_f32_16x16x32_bf16 v[188:191], v[16:19], v[92:95], 0\n"
    "v_cvt_pk_bf16_f32 v242, v192, v193\n"
    "v_cvt_pk_bf16_f32 v243, v194, v195\n"
    "v_mfma_f32_16x16x32_bf16 v[192:195], v[20:23], v[80:83], 0\n"
    "ds_read_b64_tr_b16 v[16:17], v216 offset:24576\n"
    "v_cvt_pk_bf16_f32 v246, v196, v197\n"
    "v_cvt_pk_bf16_f32 v247, v198, v199\n"
    "v_mfma_f32_16x16x32_bf16 v[196:199], v[20:23], v[84:87], 0\n"
    "ds_read_b64_tr_b16 v[18:19], v216 offset:26624\n"
    "v_cvt_pk_bf16_f32 v250, v200, v201\n"
    "v_cvt_pk_bf16_f32 v251, v202, v203\n"
    "v_mfma_f32_16x16x32_bf16 v[200:203], v[20:23], v[88:91], 0\n"
    "v_cvt_pk_bf16_f32 v254, v204, v205\n"
    "v_cvt_pk_bf16_f32 v255, v206, v207\n"
    "v_mfma_f32_16x16x32_bf16 v[204:207], v[20:23], v[92:95], 0\n"
    "s_waitcnt lgkmcnt(6)\n"
    "v_mfma_f32_16x16x32_bf16 v[176:179], v[24:27], v[96:99], v[176:179]\n"
    "ds_read_b64_tr_b16 v[20:21], v217 offset:24576\n"
    "v_mfma_f32_16x16x32_bf16 v[180:183], v[24:27], v[100:103], v[180:183]\n"
    "buffer_store_dwordx4 v[240:243], v13, s[28:31], s60 offen offset:0 sc1\n"
    "ds_read_b64_tr_b16 v[22:23], v217 offset:26624\n"
    "v_mfma_f32_16x16x32_bf16 v[184:187], v[24:27], v[104:107], v[184:187]\n"
    "v_mfma_f32_16x16x32_bf16 v[188:191], v[24:27], v[108:111], v[188:191]\n"
    "v_mfma_f32_16x16x32_bf16 v[192:195], v[28:31], v[96:99], v[192:195]\n"
    "ds_read_b64_tr_b16 v[24:25], v216 offset:32768\n"
    "v_mfma_f32_16x16x32_bf16 v[196:199], v[28:31], v[100:103], v[196:199]\n"
    "buffer_store_dwordx4 v[244:247], v13, s[28:31], s60 offen offset:1024 sc1\n"
    "ds_read_b64_tr_b16 v[26:27], v216 offset:34816\n"
    "v_mfma_f32_16x16x32_bf16 v[200:203], v[28:31], v[104:107], v[200:203]\n"
    "v_mfma_f32_16x16x32_bf16 v[204:207], v[28:31], v[108:111], v[204:207]\n"
    "s_waitcnt lgkmcnt(6)\n"
    "v_mfma_f32_16x16x32_bf16 v[176:179], v[32:35], v[112:115], v[176:179]\n"
    "ds_read_b64_tr_b16 v[28:29], v217 offset:32768\n"
    "v_mfma_f32_16x16x32_bf16 v[180:183], v[32:35], v[116:119], v[180:183]\n"
    "buffer_store_dwordx4 v[248:251], v13, s[28:31], s60 offen offset:2048 sc1\n"
    "ds_read_b64_tr_b16 v[30:31], v217 offset:34816\n"
    "v_mfma_f32_16x16x32_bf16 v[184:187], v[32:35], v[120:123], v[184:187]\n"
    "v_mfma_f32_16x16x32_bf16 v[188:191], v[32:35], v[124:127], v[188:191]\n"
    "v_mfma_f32_16x16x32_bf16 v[192:195], v[36:39], v[112:115], v[192:195]\n"
    "ds_read_b64_tr_b16 v[32:33], v216 offset:40960\n"
    "v_mfma_f32_16x16x32_bf16 v[196:199], v[36:39], v[116:119], v[196:199]\n"
    "buffer_store_dwordx4 v[252:255], v13, s[28:31], s60 offen offset:3072 sc1\n"
    "ds_read_b64_tr_b16 v[34:35], v216 offset:43008\n"
    "v_mfma_f32_16x16x32_bf16 v[200:203], v[36:39], v[120:123], v[200:203]\n"
    "v_mfma_f32_16x16x32_bf16 v[204:207], v[36:39], v[124:127], v[204:207]\n"
    "s_waitcnt lgkmcnt(6)\n"
    "v_mfma_f32_16x16x32_bf16 v[176:179], v[16:19], v[128:131], v[176:179]\n"
    "ds_read_b64_tr_b16 v[36:37], v217 offset:40960\n"
    "v_mfma_f32_16x16x32_bf16 v[180:183], v[16:19], v[132:135], v[180:183]\n"
    "s_add_u32 s60, s60, 262144\n"
    "ds_read_b64_tr_b16 v[38:39], v217 offset:43008\n"
    "v_mfma_f32_16x16x32_bf16 v[184:187], v[16:19], v[136:139], v[184:187]\n"
    "v_mfma_f32_16x16x32_bf16 v[188:191], v[16:19], v[140:143], v[188:191]\n"
    "v_mfma_f32_16x16x32_bf16 v[192:195], v[20:23], v[128:131], v[192:195]\n"
    "ds_read_b64_tr_b16 v[16:17], v218 offset:0\n"
    "v_mfma_f32_16x16x32_bf16 v[196:199], v[20:23], v[132:135], v[196:199]\n"
    "ds_read_b64_tr_b16 v[18:19], v218 offset:2048\n"
    "v_mfma_f32_16x16x32_bf16 v[200:203], v[20:23], v[136:139], v[200:203]\n"
    "v_mfma_f32_16x16x32_bf16 v[204:207], v[20:23], v[140:143], v[204:207]\n"
    "s_waitcnt lgkmcnt(6)\n"
    "v_mfma_f32_16x16x32_bf16 v[176:179], v[24:27], v[144:147], v[176:179]\n"
    "ds_read_b64_tr_b16 v[20:21], v219 offset:0\n"
    "v_mfma_f32_16x16x32_bf16 v[180:183], v[24:27], v[148:151], v[180:183]\n"
    "ds_read_b64_tr_b16 v[22:23], v219 offset:2048\n"
    "v_mfma_f32_16x16x32_bf16 v[184:187], v[24:27], v[152:155], v[184:187]\n"
    "v_mfma_f32_16x16x32_bf16 v[188:191], v[24:27], v[156:159], v[188:191]\n"
    "v_mfma_f32_16x16x32_bf16 v[192:195], v[28:31], v[144:147], v[192:195]\n"
    "ds_read_b64_tr_b16 v[24:25], v218 offset:8192\n"
    "v_mfma_f32_16x16x32_bf16 v[196:199], v[28:31], v[148:151], v[196:199]\n"
    "ds_read_b64_tr_b16 v[26:27], v218 offset:10240\n"
    "v_mfma_f32_16x16x32_bf16 v[200:203], v[28:31], v[152:155], v[200:203]\n"
    "v_mfma_f32_16x16x32_bf16 v[204:207], v[28:31], v[156:159], v[204:207]\n"
    "s_waitcnt lgkmcnt(6)\n"
    "v_mfma_f32_16x16x32_bf16 v[176:179], v[32:35], v[160:163], v[176:179]\n"
    "ds_read_b64_tr_b16 v[28:29], v219 offset:8192\n"
    "v_mfma_f32_16x16x32_bf16 v[180:183], v[32:35], v[164:167], v[180:183]\n"
    "ds_read_b64_tr_b16 v[30:31], v219 offset:10240\n"
    "v_mfma_f32_16x16x32_bf16 v[184:187], v[32:35], v[168:171], v[184:187]\n"
    "v_mfma_f32_16x16x32_bf16 v[188:191], v[32:35], v[172:175], v[188:191]\n"
    "v_mfma_f32_16x16x32_bf16 v[192:195], v[36:39], v[160:163], v[192:195]\n"
    "ds_read_b64_tr_b16 v[32:33], v218 offset:16384\n"
    "v_mfma_f32_16x16x32_bf16 v[196:199], v[36:39], v[164:167], v[196:199]\n"
    "ds_read_b64_tr_b16 v[34:35], v218 offset:18432\n"
    "v_mfma_f32_16x16x32_bf16 v[200:203], v[36:39], v[168:171], v[200:203]\n"
    "v_mfma_f32_16x16x32_bf16 v[204:207], v[36:39], v[172:175], v[204:207]\n"
    "s_waitcnt lgkmcnt(6)\n"
    "v_mfma_f32_16x16x32_bf16 v[176:179], v[16:19], a[0:3], v[176:179]\n"
    "ds_read_b64_tr_b16 v[36:37], v219 offset:16384\n"
    "v_mfma_f32_16x16x32_bf16 v[180:183], v[16:19], a[4:7], v[180:183]\n"
    "ds_read_b64_tr_b16 v[38:39], v219 offset:18432\n"
    "v_mfma_f32_16x16x32_bf16 v[184:187], v[16:19], a[8:11], v[184:187]\n"
    "v_mfma_f32_16x16x32_bf16 v[188:191], v[16:19], a[12:15], v[188:191]\n"
    "v_mfma_f32_16x16x32_bf16 v[192:195], v[20:23], a[0:3], v[192:195]\n"
    "ds_read_b64_tr_b16 v[16:17], v218 offset:24576\n"
    "v_mfma_f32_16x16x32_bf16 v[196:199], v[20:23], a[4:7], v[196:199]\n"
    "ds_read_b64_tr_b16 v[18:19], v218 offset:26624\n"
    "v_mfma_f32_16x16x32_bf16 v[200:203], v[20:23], a[8:11], v[200:203]\n"
    "v_mfma_f32_16x16x32_bf16 v[204:207], v[20:23], a[12:15], v[204:207]\n"
    "s_waitcnt lgkmcnt(6)\n"
    "v_mfma_f32_16x16x32_bf16 v[176:179], v[24:27], a[16:19], v[176:179]\n"
    "ds_read_b64_tr_b16 v[20:21], v219 offset:24576\n"
    "v_mfma_f32_16x16x32_bf16 v[180:183], v[24:27], a[20:23], v[180:183]\n"
    "ds_read_b64_tr_b16 v[22:23], v219 offset:26624\n"
    "v_mfma_f32_16x16x32_bf16 v[184:187], v[24:27], a[24:27], v[184:187]\n"
    "v_mfma_f32_16x16x32_bf16 v[188:191], v[24:27], a[28:31], v[188:191]\n"
    "v_mfma_f32_16x16x32_bf16 v[192:195], v[28:31], a[16:19], v[192:195]\n"
    "s_waitcnt vmcnt(9)\n"
    "buffer_store_dwordx2 v[230:231], v228, s[72:75], s57 offen sc1\n"
    "s_add_u32 s57, s57, 512\n"
    "ds_read_b64_tr_b16 v[24:25], v218 offset:32768\n"
    "v_mfma_f32_16x16x32_bf16 v[196:199], v[28:31], a[20:23], v[196:199]\n"
    "ds_read_b64_tr_b16 v[26:27], v218 offset:34816\n"
    "v_mfma_f32_16x16x32_bf16 v[200:203], v[28:31], a[24:27], v[200:203]\n"
    "v_mfma_f32_16x16x32_bf16 v[204:207], v[28:31], a[28:31], v[204:207]\n"
    "s_waitcnt lgkmcnt(6)\n"
    "v_mfma_f32_16x16x32_bf16 v[176:179], v[32:35], a[32:35], v[176:179]\n"
    "ds_read_b64_tr_b16 v[28:29], v219 offset:32768\n"
    "v_mfma_f32_16x16x32_bf16 v[180:183], v[32:35], a[36:39], v[180:183]\n"
    "ds_read_b64_tr_b16 v[30:31], v219 offset:34816\n"
    "v_mfma_f32_16x16x32_bf16 v[184:187], v[32:35], a[40:43], v[184:187]\n"
    "v_mfma_f32_16x16x32_bf16 v[188:191], v[32:35], a[44:47], v[188:191]\n"
    "v_mfma_f32_16x16x32_bf16 v[192:195], v[36:39], a[32:35], v[192:195]\n"
    "ds_read_b64_tr_b16 v[32:33], v218 offset:40960\n"
    "v_mfma_f32_16x16x32_bf16 v[196:199], v[36:39], a[36:39], v[196:199]\n"
    "ds_read_b64_tr_b16 v[34:35], v218 offset:43008\n"
    "v_mfma_f32_16x16x32_bf16 v[200:203], v[36:39], a[40:43], v[200:203]\n"
    "v_mfma_f32_16x16x32_bf16 v[204:207], v[36:39], a[44:47], v[204:207]\n"
    "s_waitcnt lgkmcnt(6)\n"
    "v_mfma_f32_16x16x32_bf16 v[176:179], v[16:19], a[48:51], v[176:179]\n"
    "ds_read_b64_tr_b16 v[36:37], v219 offset:40960\n"
    "v_mfma_f32_16x16x32_bf16 v[180:183], v[16:19], a[52:55], v[180:183]\n"
    "ds_read_b64_tr_b16 v[38:39], v219 offset:43008\n"
    "v_mfma_f32_16x16x32_bf16 v[184:187], v[16:19], a[56:59], v[184:187]\n"
    "v_mfma_f32_16x16x32_bf16 v[188:191], v[16:19], a[60:63], v[188:191]\n"
    "v_mfma_f32_16x16x32_bf16 v[192:195], v[20:23], a[48:51], v[192:195]\n"
    "v_mfma_f32_16x16x32_bf16 v[196:199], v[20:23], a[52:55], v[196:199]\n"
    "v_mfma_f32_16x16x32_bf16 v[200:203], v[20:23], a[56:59], v[200:203]\n"
    "v_mfma_f32_16x16x32_bf16 v[204:207], v[20:23], a[60:63], v[204:207]\n"
    "s_waitcnt lgkmcnt(4)\n"
    "v_mfma_f32_16x16x32_bf16 v[176:179], v[24:27], a[64:67], v[176:179]\n"
    "v_mfma_f32_16x16x32_bf16 v[180:183], v[24:27], a[68:71], v[180:183]\n"
    "v_mfma_f32_16x16x32_bf16 v[184:187], v[24:27], a[72:75], v[184:187]\n"
    "v_mfma_f32_16x16x32_bf16 v[188:191], v[24:27], a[76:79], v[188:191]\n"
    "v_mfma_f32_16x16x32_bf16 v[192:195], v[28:31], a[64:67], v[192:195]\n"
    "v_mfma_f32_16x16x32_bf16 v[196:199], v[28:31], a[68:71], v[196:199]\n"
    "v_mfma_f32_16x16x32_bf16 v[200:203], v[28:31], a[72:75], v[200:203]\n"
    "v_mfma_f32_16x16x32_bf16 v[204:207], v[28:31], a[76:79], v[204:207]\n"
    "s_waitcnt lgkmcnt(0)\n"
    "v_mfma_f32_16x16x32_bf16 v[176:179], v[32:35], a[80:83], v[176:179]\n"
    "v_mfma_f32_16x16x32_bf16 v[180:183], v[32:35], a[84:87], v[180:183]\n"
    "v_mfma_f32_16x16x32_bf16 v[184:187], v[32:35], a[88:91], v[184:187]\n"
    "v_mfma_f32_16x16x32_bf16 v[188:191], v[32:35], a[92:95], v[188:191]\n"
    "v_mfma_f32_16x16x32_bf16 v[192:195], v[36:39], a[80:83], v[192:195]\n"
    "v_mfma_f32_16x16x32_bf16 v[196:199], v[36:39], a[84:87], v[196:199]\n"
    "v_mfma_f32_16x16x32_bf16 v[200:203], v[36:39], a[88:91], v[200:203]\n"
    "v_mfma_f32_16x16x32_bf16 v[204:207], v[36:39], a[92:95], v[204:207]\n"
    "s_nop 0\n"
    "v_cvt_pk_bf16_f32 v240, v176, v177\n"
    "v_cvt_pk_bf16_f32 v241, v178, v179\n"
    "s_nop 1\n"
    "v_cvt_pk_bf16_f32 v242, v192, v193\n"
    "v_cvt_pk_bf16_f32 v243, v194, v195\n"
    "v_cvt_pk_bf16_f32 v244, v180, v181\n"
    "v_cvt_pk_bf16_f32 v245, v182, v183\n"
    "v_cvt_pk_bf16_f32 v246, v196, v197\n"
    "v_cvt_pk_bf16_f32 v247, v198, v199\n"
    "v_cvt_pk_bf16_f32 v248, v184, v185\n"
    "v_cvt_pk_bf16_f32 v249, v186, v187\n"
    "v_cvt_pk_bf16_f32 v250, v200, v201\n"
    "v_cvt_pk_bf16_f32 v251, v202, v203\n"
    "v_cvt_pk_bf16_f32 v252, v188, v189\n"
    "v_cvt_pk_bf16_f32 v253, v190, v191\n"
    "v_cvt_pk_bf16_f32 v254, v204, v205\n"
    "v_cvt_pk_bf16_f32 v255, v206, v207\n"
    "buffer_store_dwordx4 v[240:243], v13, s[28:31], s60 offen offset:0 sc1\n"
    "buffer_store_dwordx4 v[244:247], v13, s[28:31], s60 offen offset:1024 sc1\n"
    "buffer_store_dwordx4 v[248:251], v13, s[28:31], s60 offen offset:2048 sc1\n"
    "buffer_store_dwordx4 v[252:255], v13, s[28:31], s60 offen offset:3072 sc1\n"
    "s_add_u32 s60, s60, 262144\n"
    "s_mov_b32 s100, 0x00003f80\n"
    "s_mov_b32 s101, 0x3f800000\n"
    "s_add_u32 s59, s35, 3\n"
    "s_mul_i32 s40, s10, 9\n"
    "s_add_u32 s40, s40, s59\n"
    "s_lshl_b32 s40, s40, 9\n"
    "s_add_u32 s55, s40, 16384\n"
    "s_waitcnt vmcnt(5)\n"
    "buffer_store_dwordx2 v[230:231], v228, s[72:75], s57 offen sc1\n"
    "s_add_u32 s57, s57, 512\n"
    "s_mov_b32 s52, 0\n"
    "TL_4:\n"
    "s_cmp_eq_u32 s52, 1\n"
    "s_cbranch_scc1 TA1_7\n"
    "buffer_load_dwordx2 v[238:239], v232, s[72:75], s55 offen sc1\n"
    "v_accvgpr_read_b32 v16, a192\n"
    "v_accvgpr_read_b32 v17, a193\n"
    "v_accvgpr_read_b32 v18, a194\n"
    "v_accvgpr_read_b32 v19, a195\n"
    "s_mov_b32 s53, s92\n"
    "s_branch TAD_8\n"
    "TA1_7:\n"
    "buffer_load_dwordx2 v[238:239], v232, s[72:75], s55 offen sc1\n"
    "v_accvgpr_read_b32 v16, a196\n"
    "v_accvgpr_read_b32 v17, a197\n"
    "v_accvgpr_read_b32 v18, a198\n"
    "v_accvgpr_read_b32 v19, a199\n"
    "s_mov_b32 s53, s93\n"
    "TAD_8:\n"
    "v_add_u32 v36, s53, v235\n"
    "v_add_u32 v36, 157696, v36\n"
    "ds_read_b64 v[20:21], v36 offset:0\n"
    "ds_read_b64 v[24:25], v36 offset:2048\n"
    "ds_read_b64 v[22:23], v36 offset:32\n"
    "ds_read_b64 v[26:27], v36 offset:2080\n"
    "s_waitcnt lgkmcnt(0)\n"
    "v_lshlrev_b32 v56, 16, v16\n"
    "v_and_b32 v57, 0xffff0000, v16\n"
    "v_lshlrev_b32 v64, 16, v20\n"
    "v_and_b32 v65, 0xffff0000, v20\n"
    "v_lshlrev_b32 v58, 16, v17\n"
    "v_and_b32 v59, 0xffff0000, v17\n"
    "v_lshlrev_b32 v66, 16, v21\n"
    "v_and_b32 v67, 0xffff0000, v21\n"
    "v_lshlrev_b32 v60, 16, v18\n"
    "v_and_b32 v61, 0xffff0000, v18\n"
    "v_lshlrev_b32 v68, 16, v22\n"
    "v_and_b32 v69, 0xffff0000, v22\n"
    "v_lshlrev_b32 v62, 16, v19\n"
    "v_and_b32 v63, 0xffff0000, v19\n"
    "v_lshlrev_b32 v70, 16, v23\n"
    "v_and_b32 v71, 0xffff0000, v23\n"
    "v_pk_add_f32 v[28:29], v[56:57], v[236:237] op_sel_hi:[1,0] neg_lo:[0,1] neg_hi:[0,1]\n"
    "v_pk_add_f32 v[30:31], v[58:59], v[236:237] op_sel_hi:[1,0] neg_lo:[0,1] neg_hi:[0,1]\n"
    "v_pk_add_f32 v[32:33], v[60:61], v[236:237] op_sel_hi:[1,0] neg_lo:[0,1] neg_hi:[0,1]\n"
    "v_pk_add_f32 v[34:35], v[62:63], v[236:237] op_sel_hi:[1,0] neg_lo:[0,1] neg_hi:[0,1]\n"
    "v_lshlrev_b32 v20, 16, v24\n"
    "v_and_b32 v21, 0xffff0000, v24\n"
    "v_pk_mul_f32 v[28:29], v[28:29], v[236:237] op_sel:[0,1] op_sel_hi:[1,1]\n"
    "v_pk_mul_f32 v[28:29], v[28:29], v[20:21]\n"
    "v_lshlrev_b32 v20, 16, v25\n"
    "v_and_b32 v21, 0xffff0000, v25\n"
    "v_pk_mul_f32 v[30:31], v[30:31], v[236:237] op_sel:[0,1] op_sel_hi:[1,1]\n"
    "v_pk_mul_f32 v[30:31], v[30:31], v[20:21]\n"
    "v_lshlrev_b32 v20, 16, v26\n"
    "v_and_b32 v21, 0xffff0000, v26\n"
    "v_pk_mul_f32 v[32:33], v[32:33], v[236:237] op_sel:[0,1] op_sel_hi:[1,1]\n"
    "v_pk_mul_f32 v[32:33], v[32:33], v[20:21]\n"
    "v_lshlrev_b32 v20, 16, v27\n"
    "v_and_b32 v21, 0xffff0000, v27\n"
    "v_pk_mul_f32 v[34:35], v[34:35], v[236:237] op_sel:[0,1] op_sel_hi:[1,1]\n"
    "v_pk_mul_f32 v[34:35], v[34:35], v[20:21]\n"
    "s_waitcnt vmcnt(0)\n"
    "s_cmp_eq_u32 s52, 0\n"
    "s_cbranch_scc0 TNF_9\n"
    "buffer_store_dwordx2 v[230:231], v228, s[72:75], s57 offen sc1\n"
    "TNF_9:\n"
    "v_cmp_eq_u32 vcc, s84, v238\n"
    "v_cmp_eq_u32 s[40:41], s85, v239\n"
    "s_nop 1\n"
    "s_and_b64 vcc, vcc, s[40:41]\n"
    "s_cmp_eq_u64 vcc, -1\n"
    "s_cbranch_scc1 TOK_5\n"
    "s_mov_b32 s42, 0\n"
    "TLP_6:\n"
    "s_sleep 1\n"
    "buffer_load_dwordx2 v[238:239], v232, s[72:75], s55 offen sc1\n"
    "s_waitcnt vmcnt(0)\n"
    "v_cmp_eq_u32 vcc, s84, v238\n"
    "v_cmp_eq_u32 s[40:41], s85, v239\n"
    "s_nop 1\n"
    "s_and_b64 vcc, vcc, s[40:41]\n"
    "s_cmp_eq_u64 vcc, -1\n"
    "s_cbranch_scc1 TOK_5\n"
    "s_add_u32 s42, s42, 1\n"
    "s_cmp_lt_u32 s42, 20000\n"
    "s_cbranch_scc1 TLP_6\n"
    "TOK_5:\n"
    "s_lshl_b32 s40, s59, 18\n"
    "s_add_u32 s43, s45, s40\n"
    "buffer_load_dwordx4 v[80:83], v13, s[28:31], s43 offen sc1\n"
    "s_add_u32 s43, s43, 4096\n"
    "buffer_load_dwordx4 v[84:87], v13, s[28:31], s43 offen sc1\n"
    "s_add_u32 s43, s43, 4096\n"
    "buffer_load_dwordx4 v[88:91], v13, s[28:31], s43 offen sc1\n"
    "s_add_u32 s43, s43, 4096\n"
    "buffer_load_dwordx4 v[92:95], v13, s[28:31], s43 offen sc1\n"
    "s_add_u32 s43, s43, 4096\n"
    "buffer_load_dwordx4 v[96:99], v13, s[28:31], s43 offen sc1\n"
    "s_add_u32 s43, s43, 4096\n"
    "buffer_load_dwordx4 v[100:103], v13, s[28:31], s43 offen sc1\n"
    "s_add_u32 s43, s43, 4096\n"
    "buffer_load_dwordx4 v[104:107], v13, s[28:31], s43 offen sc1\n"
    "s_add_u32 s43, s43, 4096\n"
    "buffer_load_dwordx4 v[108:111], v13, s[28:31], s43 offen sc1\n"
    "s_add_u32 s43, s43, 4096\n"
    "buffer_load_dwordx4 v[112:115], v13, s[28:31], s43 offen sc1\n"
    "s_add_u32 s43, s43, 4096\n"
    "buffer_load_dwordx4 v[116:119], v13, s[28:31], s43 offen sc1\n"
    "s_add_u32 s43, s43, 4096\n"
    "buffer_load_dwordx4 v[120:123], v13, s[28:31], s43 offen sc1\n"
    "s_add_u32 s43, s43, 4096\n"
    "buffer_load_dwordx4 v[124:127], v13, s[28:31], s43 offen sc1\n"
    "s_add_u32 s43, s43, 4096\n"
    "buffer_load_dwordx4 v[128:131], v13, s[28:31], s43 offen sc1\n"
    "s_add_u32 s43, s43, 4096\n"
    "buffer_load_dwordx4 v[132:135], v13, s[28:31], s43 offen sc1\n"
    "s_add_u32 s43, s43, 4096\n"
    "buffer_load_dwordx4 v[136:139], v13, s[28:31], s43 offen sc1\n"
    "s_add_u32 s43, s43, 4096\n"
    "buffer_load_dwordx4 v[140:143], v13, s[28:31], s43 offen sc1\n"
    "s_waitcnt vmcnt(15)\n"
    "v_lshlrev_b32 v40, 16, v80\n"
    "v_and_b32 v41, 0xffff0000, v80\n"
    "v_lshlrev_b32 v42, 16, v81\n"
    "v_and_b32 v43, 0xffff0000, v81\n"
    "v_lshlrev_b32 v44, 16, v82\n"
    "v_and_b32 v45, 0xffff0000, v82\n"
    "v_lshlrev_b32 v46, 16, v83\n"
    "v_and_b32 v47, 0xffff0000, v83\n"
    "s_waitcnt vmcnt(14)\n"
    "v_dot2_f32_bf16 v40, v84, s100, v40\n"
    "v_dot2_f32_bf16 v41, v84, s101, v41\n"
    "v_dot2_f32_bf16 v42, v85, s100, v42\n"
    "v_dot2_f32_bf16 v43, v85, s101, v43\n"
    "v_dot2_f32_bf16 v44, v86, s100, v44\n"
    "v_dot2_f32_bf16 v45, v86, s101, v45\n"
    "v_dot2_f32_bf16 v46, v87, s100, v46\n"
    "v_dot2_f32_bf16 v47, v87, s101, v47\n"
    "s_waitcnt vmcnt(13)\n"
    "v_dot2_f32_bf16 v40, v88, s100, v40\n"
    "v_dot2_f32_bf16 v41, v88, s101, v41\n"
    "v_dot2_f32_bf16 v42, v89, s100, v42\n"
    "v_dot2_f32_bf16 v43, v89, s101, v43\n"
    "v_dot2_f32_bf16 v44, v90, s100, v44\n"
    "v_dot2_f32_bf16 v45, v90, s101, v45\n"
    "v_dot2_f32_bf16 v46, v91, s100, v46\n"
    "v_dot2_f32_bf16 v47, v91, s101, v47\n"
    "s_waitcnt vmcnt(12)\n"
    "v_dot2_f32_bf16 v40, v92, s100, v40\n"
    "v_dot2_f32_bf16 v41, v92, s101, v41\n"
    "v_dot2_f32_bf16 v42, v93, s100, v42\n"
    "v_dot2_f32_bf16 v43, v93, s101, v43\n"
    "v_dot2_f32_bf16 v44, v94, s100, v44\n"
    "v_dot2_f32_bf16 v45, v94, s101, v45\n"
    "v_dot2_f32_bf16 v46, v95, s100, v46\n"
    "v_dot2_f32_bf16 v47, v95, s101, v47\n"
    "s_waitcnt vmcnt(11)\n"
    "v_dot2_f32_bf16 v40, v96, s100, v40\n"
    "v_dot2_f32_bf16 v41, v96, s101, v41\n"
    "v_dot2_f32_bf16 v42, v97, s100, v42\n"
    "v_dot2_f32_bf16 v43, v97, s101, v43\n"
    "v_dot2_f32_bf16 v44, v98, s100, v44\n"
    "v_dot2_f32_bf16 v45, v98, s101, v45\n"
    "v_dot2_f32_bf16 v46, v99, s100, v46\n"
    "v_dot2_f32_bf16 v47, v99, s101, v47\n"
    "s_waitcnt vmcnt(10)\n"
    "v_dot2_f32_bf16 v40, v100, s100, v40\n"
    "v_dot2_f32_bf16 v41, v100, s101, v41\n"
    "v_dot2_f32_bf16 v42, v101, s100, v42\n"
    "v_dot2_f32_bf16 v43, v101, s101, v43\n"
    "v_dot2_f32_bf16 v44, v102, s100, v44\n"
    "v_dot2_f32_bf16 v45, v102, s101, v45\n"
    "v_dot2_f32_bf16 v46, v103, s100, v46\n"
    "v_dot2_f32_bf16 v47, v103, s101, v47\n"
    "s_waitcnt vmcnt(9)\n"
    "v_dot2_f32_bf16 v40, v104, s100, v40\n"
    "v_dot2_f32_bf16 v41, v104, s101, v41\n"
    "v_dot2_f32_bf16 v42, v105, s100, v42\n"
    "v_dot2_f32_bf16 v43, v105, s101, v43\n"
    "v_dot2_f32_bf16 v44, v106, s100, v44\n"
    "v_dot2_f32_bf16 v45, v106, s101, v45\n"
    "v_dot2_f32_bf16 v46, v107, s100, v46\n"
    "v_dot2_f32_bf16 v47, v107, s101, v47\n"
    "s_waitcnt vmcnt(8)\n"
    "v_dot2_f32_bf16 v40, v108, s100, v40\n"
    "v_dot2_f32_bf16 v41, v108, s101, v41\n"
    "v_dot2_f32_bf16 v42, v109, s100, v42\n"
    "v_dot2_f32_bf16 v43, v109, s101, v43\n"
    "v_dot2_f32_bf16 v44, v110, s100, v44\n"
    "v_dot2_f32_bf16 v45, v110, s101, v45\n"
    "v_dot2_f32_bf16 v46, v111, s100, v46\n"
    "v_dot2_f32_bf16 v47, v111, s101, v47\n"
    "s_waitcnt vmcnt(7)\n"
    "v_dot2_f32_bf16 v40, v112, s100, v40\n"
    "v_dot2_f32_bf16 v41, v112, s101, v41\n"
    "v_dot2_f32_bf16 v42, v113, s100, v42\n"
    "v_dot2_f32_bf16 v43, v113, s101, v43\n"
    "v_dot2_f32_bf16 v44, v114, s100, v44\n"
    "v_dot2_f32_bf16 v45, v114, s101, v45\n"
    "v_dot2_f32_bf16 v46, v115, s100, v46\n"
    "v_dot2_f32_bf16 v47, v115, s101, v47\n"
    "s_waitcnt vmcnt(6)\n"
    "v_dot2_f32_bf16 v40, v116, s100, v40\n"
    "v_dot2_f32_bf16 v41, v116, s101, v41\n"
    "v_dot2_f32_bf16 v42, v117, s100, v42\n"
    "v_dot2_f32_bf16 v43, v117, s101, v43\n"
    "v_dot2_f32_bf16 v44, v118, s100, v44\n"
    "v_dot2_f32_bf16 v45, v118, s101, v45\n"
    "v_dot2_f32_bf16 v46, v119, s100, v46\n"
    "v_dot2_f32_bf16 v47, v119, s101, v47\n"
    "s_waitcnt vmcnt(5)\n"
    "v_dot2_f32_bf16 v40, v120, s100, v40\n"
    "v_dot2_f32_bf16 v41, v120, s101, v41\n"
    "v_dot2_f32_bf16 v42, v121, s100, v42\n"
    "v_dot2_f32_bf16 v43, v121, s101, v43\n"
    "v_dot2_f32_bf16 v44, v122, s100, v44\n"
    "v_dot2_f32_bf16 v45, v122, s101, v45\n"
    "v_dot2_f32_bf16 v46, v123, s100, v46\n"
    "v_dot2_f32_bf16 v47, v123, s101, v47\n"
    "s_waitcnt vmcnt(4)\n"
    "v_dot2_f32_bf16 v40, v124, s100, v40\n"
    "v_dot2_f32_bf16 v41, v124, s101, v41\n"
    "v_dot2_f32_bf16 v42, v125, s100, v42\n"
    "v_dot2_f32_bf16 v43, v125, s101, v43\n"
    "v_dot2_f32_bf16 v44, v126, s100, v44\n"
    "v_dot2_f32_bf16 v45, v126, s101, v45\n"
    "v_dot2_f32_bf16 v46, v127, s100, v46\n"
    "v_dot2_f32_bf16 v47, v127, s101, v47\n"
    "s_waitcnt vmcnt(3)\n"
    "v_lshlrev_b32 v48, 16, v128\n"
    "v_and_b32 v49, 0xffff0000, v128\n"
    "v_lshlrev_b32 v50, 16, v129\n"
    "v_and_b32 v51, 0xffff0000, v129\n"
    "v_lshlrev_b32 v52, 16, v130\n"
    "v_and_b32 v53, 0xffff0000, v130\n"
    "v_lshlrev_b32 v54, 16, v131\n"
    "v_and_b32 v55, 0xffff0000, v131\n"
    "s_waitcnt vmcnt(2)\n"
    "v_dot2_f32_bf16 v48, v132, s100, v48\n"
    "v_dot2_f32_bf16 v49, v132, s101, v49\n"
    "v_dot2_f32_bf16 v50, v133, s100, v50\n"
    "v_dot2_f32_bf16 v51, v133, s101, v51\n"
    "v_dot2_f32_bf16 v52, v134, s100, v52\n"
    "v_dot2_f32_bf16 v53, v134, s101, v53\n"
    "v_dot2_f32_bf16 v54, v135, s100, v54\n"
    "v_dot2_f32_bf16 v55, v135, s101, v55\n"
    "s_waitcnt vmcnt(1)\n"
    "v_dot2_f32_bf16 v48, v136, s100, v48\n"
    "v_dot2_f32_bf16 v49, v136, s101, v49\n"
    "v_dot2_f32_bf16 v50, v137, s100, v50\n"
    "v_dot2_f32_bf16 v51, v137, s101, v51\n"
    "v_dot2_f32_bf16 v52, v138, s100, v52\n"
    "v_dot2_f32_bf16 v53, v138, s101, v53\n"
    "v_dot2_f32_bf16 v54, v139, s100, v54\n"
    "v_dot2_f32_bf16 v55, v139, s101, v55\n"
    "s_waitcnt vmcnt(0)\n"
    "v_dot2_f32_bf16 v48, v140, s100, v48\n"
    "v_dot2_f32_bf16 v49, v140, s101, v49\n"
    "v_dot2_f32_bf16 v50, v141, s100, v50\n"
    "v_dot2_f32_bf16 v51, v141, s101, v51\n"
    "v_dot2_f32_bf16 v52, v142, s100, v52\n"
    "v_dot2_f32_bf16 v53, v142, s101, v53\n"
    "v_dot2_f32_bf16 v54, v143, s100, v54\n"
    "v_dot2_f32_bf16 v55, v143, s101, v55\n"
    "v_pk_add_f32 v[40:41], v[40:41], v[64:65]\n"
    "v_pk_add_f32 v[42:43], v[42:43], v[66:67]\n"
    "v_pk_add_f32 v[44:45], v[44:45], v[68:69]\n"
    "v_pk_add_f32 v[46:47], v[46:47], v[70:71]\n"
    "v_mul_f32 v208, 0xbfb8aa3b, v40\n"
    "v_mul_f32 v209, 0xbfb8aa3b, v41\n"
    "v_mul_f32 v210, 0xbfb8aa3b, v42\n"
    "v_mul_f32 v211, 0xbfb8aa3b, v43\n"
    "v_mul_f32 v212, 0xbfb8aa3b, v44\n"
    "v_mul_f32 v213, 0xbfb8aa3b, v45\n"
    "v_mul_f32 v214, 0xbfb8aa3b, v46\n"
    "v_mul_f32 v215, 0xbfb8aa3b, v47\n"
    "v_exp_f32 v208, v208\n"
    "v_exp_f32 v209, v209\n"
    "v_exp_f32 v210, v210\n"
    "v_exp_f32 v211, v211\n"
    "v_exp_f32 v212, v212\n"
    "v_exp_f32 v213, v213\n"
    "v_exp_f32 v214, v214\n"
    "v_exp_f32 v215, v215\n"
    "v_pk_add_f32 v[48:49], v[48:49], v[28:29]\n"
    "v_pk_add_f32 v[50:51], v[50:51], v[30:31]\n"
    "v_pk_add_f32 v[52:53], v[52:53], v[32:33]\n"
    "v_pk_add_f32 v[54:55], v[54:55], v[34:35]\n"
    "v_add_f32 v208, 1.0, v208\n"
    "v_add_f32 v209, 1.0, v209\n"
    "v_add_f32 v210, 1.0, v210\n"
    "v_add_f32 v211, 1.0, v211\n"
    "v_add_f32 v212, 1.0, v212\n"
    "v_add_f32 v213, 1.0, v213\n"
    "v_add_f32 v214, 1.0, v214\n"
    "v_add_f32 v215, 1.0, v215\n"
    "v_rcp_f32 v208, v208\n"
    "v_rcp_f32 v209, v209\n"
    "v_rcp_f32 v210, v210\n"
    "v_rcp_f32 v211, v211\n"
    "v_rcp_f32 v212, v212\n"
    "v_rcp_f32 v213, v213\n"
    "v_rcp_f32 v214, v214\n"
    "v_rcp_f32 v215, v215\n"
    "v_pk_mul_f32 v[48:49], v[48:49], v[40:41]\n"
    "v_pk_mul_f32 v[50:51], v[50:51], v[42:43]\n"
    "v_pk_mul_f32 v[52:53], v[52:53], v[44:45]\n"
    "v_pk_mul_f32 v[54:55], v[54:55], v[46:47]\n"
    "v_pk_fma_f32 v[56:57], v[48:49], v[208:209], v[56:57]\n"
    "v_pk_fma_f32 v[58:59], v[50:51], v[210:211], v[58:59]\n"
    "v_pk_fma_f32 v[60:61], v[52:53], v[212:213], v[60:61]\n"
    "v_pk_fma_f32 v[62:63], v[54:55], v[214:215], v[62:63]\n"
    "s_lshl_b32 s41, s53, 1\n"
    "buffer_store_dwordx4 v[56:59], v234, s[80:83], s41 offen offset:0\n"
    "buffer_store_dwordx4 v[60:63], v234, s[80:83], s41 offen offset:64\n"
    "s_add_u32 s59, s59, 3\n"
    "s_cmp_gt_u32 s59, 8\n"
    "s_cbranch_scc1 K_END\n"
    "s_cmp_eq_u32 s52, 1\n"
    "s_cbranch_scc1 K_END\n"
    "s_mul_i32 s40, s10, 9\n"
    "s_add_u32 s40, s40, s59\n"
    "s_lshl_b32 s40, s40, 9\n"
    "s_add_u32 s55, s40, 16384\n"
    "s_mov_b32 s52, 1\n"
    "s_branch TL_4\n"
    "s_branch K_END\n"
    "K_SSM:\n"
    "s_waitcnt vmcnt(0)\n"
    "s_lshl_b32 s40, s10, 6\n"
    "s_lshl_b32 s41, s61, 2\n"
    "s_add_u32 s40, s40, s41\n"
    "s_add_u32 s40, s40, s9\n"
    "s_lshl_b32 s40, s40, 3\n"
    "s_add_u32 s40, s40, 4096\n"
    "v_mov_b32 v95, 0x7fffffff\n"
    "v_cmp_eq_u32 vcc, 0, v0\n"
    "s_nop 1\n"
    "v_cndmask_b32 v95, v95, 0, vcc\n"
    "v_mov_b32 v96, s84\n"
    "v_mov_b32 v97, s85\n"
    "buffer_store_dwordx2 v[96:97], v95, s[72:75], s40 offen sc1\n"
    "s_mov_b32 s16, s88\n"
    "s_and_b32 s17, s89, 0xffff\n"
    "s_mov_b32 s18, 663552\n"
    "s_mov_b32 s19, 0x20000\n"
    "s_mov_b32 s20, s90\n"
    "s_and_b32 s21, s91, 0xffff\n"
    "s_mov_b32 s22, 663552\n"
    "s_mov_b32 s23, 0x20000\n"
    "v_lshrrev_b32 v40, 3, v0\n"
    "v_add_u32 v40, 0, v40\n"
    "v_mul_u32_u24 v40, 1152, v40\n"
    "v_lshrrev_b32 v41, 4, v0\n"
    "v_add_u32 v41, 0, v41\n"
    "v_and_b32 v42, 7, v0\n"
    "v_xor_b32 v41, v41, v42\n"
    "v_and_b32 v41, 7, v41\n"
    "v_lshl_add_u32 v6, v41, 4, v40\n"
    "v_lshrrev_b32 v40, 3, v0\n"
    "v_add_u32 v40, 8, v40\n"
    "v_mul_u32_u24 v40, 1152, v40\n"
    "v_lshrrev_b32 v41, 4, v0\n"
    "v_add_u32 v41, 4, v41\n"
    "v_and_b32 v42, 7, v0\n"
    "v_xor_b32 v41, v41, v42\n"
    "v_and_b32 v41, 7, v41\n"
    "v_lshl_add_u32 v7, v41, 4, v40\n"
    "s_cmp_lg_u32 s12, 0\n"
    "s_cselect_b32 s48, s20, s16\n"
    "s_cselect_b32 s49, s21, s17\n"
    "s_cselect_b32 s50, s22, s18\n"
    "s_cselect_b32 s51, s23, s19\n"
    "s_cmp_lg_u32 s13, 0\n"
    "v_mov_b32 v8, v6\n"
    "s_cbranch_scc0 SH_10\n"
    "v_mov_b32 v8, v7\n"
    "SH_10:\n"
    "s_mul_i32 s52, s11, 144\n"
    "s_add_u32 s52, s52, 0\n"
    "s_mul_i32 s52, s52, 1152\n"
    "s_mul_i32 s53, s9, 9216\n"
    "s_add_u32 s53, s53, 0\n"
    "s_add_u32 m0, s53, 0\n"
    "s_nop 0\n"
    "buffer_load_dwordx4 v8, s[48:51], s52 offen lds\n"
    "s_add_u32 m0, s53, 1024\n"
    "s_add_u32 s52, s52, 128\n"
    "buffer_load_dwordx4 v8, s[48:51], s52 offen lds\n"
    "s_add_u32 m0, s53, 2048\n"
    "s_add_u32 s52, s52, 128\n"
    "buffer_load_dwordx4 v8, s[48:51], s52 offen lds\n"
    "s_add_u32 m0, s53, 3072\n"
    "s_add_u32 s52, s52, 128\n"
    "buffer_load_dwordx4 v8, s[48:51], s52 offen lds\n"
    "s_add_u32 m0, s53, 4096\n"
    "s_add_u32 s52, s52, 128\n"
    "buffer_load_dwordx4 v8, s[48:51], s52 offen lds\n"
    "s_add_u32 m0, s53, 5120\n"
    "s_add_u32 s52, s52, 128\n"
    "buffer_load_dwordx4 v8, s[48:51], s52 offen lds\n"
    "s_add_u32 m0, s53, 6144\n"
    "s_add_u32 s52, s52, 128\n"
    "buffer_load_dwordx4 v8, s[48:51], s52 offen lds\n"
    "s_add_u32 m0, s53, 7168\n"
    "s_add_u32 s52, s52, 128\n"
    "buffer_load_dwordx4 v8, s[48:51], s52 offen lds\n"
    "s_add_u32 m0, s53, 8192\n"
    "s_add_u32 s52, s52, 128\n"
    "buffer_load_dwordx4 v8, s[48:51], s52 offen lds\n"
    "s_mul_i32 s52, s11, 144\n"
    "s_add_u32 s52, s52, 16\n"
    "s_mul_i32 s52, s52, 1152\n"
    "s_mul_i32 s53, s9, 9216\n"
    "s_add_u32 s53, s53, 36864\n"
    "s_add_u32 m0, s53, 0\n"
    "s_nop 0\n"
    "buffer_load_dwordx4 v8, s[48:51], s52 offen lds\n"
    "s_add_u32 m0, s53, 1024\n"
    "s_add_u32 s52, s52, 128\n"
    "buffer_load_dwordx4 v8, s[48:51], s52 offen lds\n"
    "s_add_u32 m0, s53, 2048\n"
    "s_add_u32 s52, s52, 128\n"
    "buffer_load_dwordx4 v8, s[48:51], s52 offen lds\n"
    "s_add_u32 m0, s53, 3072\n"
    "s_add_u32 s52, s52, 128\n"
    "buffer_load_dwordx4 v8, s[48:51], s52 offen lds\n"
    "s_add_u32 m0, s53, 4096\n"
    "s_add_u32 s52, s52, 128\n"
    "buffer_load_dwordx4 v8, s[48:51], s52 offen lds\n"
    "s_add_u32 m0, s53, 5120\n"
    "s_add_u32 s52, s52, 128\n"
    "buffer_load_dwordx4 v8, s[48:51], s52 offen lds\n"
    "s_add_u32 m0, s53, 6144\n"
    "s_add_u32 s52, s52, 128\n"
    "buffer_load_dwordx4 v8, s[48:51], s52 offen lds\n"
    "s_add_u32 m0, s53, 7168\n"
    "s_add_u32 s52, s52, 128\n"
    "buffer_load_dwordx4 v8, s[48:51], s52 offen lds\n"
    "s_add_u32 m0, s53, 8192\n"
    "s_add_u32 s52, s52, 128\n"
    "buffer_load_dwordx4 v8, s[48:51], s52 offen lds\n"
    "s_load_dwordx4 s[92:95], %0, 8\n"
    "s_load_dwordx2 s[96:97], %0, 56\n"
    "s_lshl_b32 s40, s9, 6\n"
    "v_add_u32 v189, s40, v0\n"
    "v_min_u32 v189, 143, v189\n"
    "s_mul_i32 s40, s11, 144\n"
    "v_add_u32 v191, s40, v189\n"
    "v_lshlrev_b32 v191, 1, v191\n"
    "s_waitcnt lgkmcnt(0)\n"
    "s_mov_b32 s44, s92\n"
    "s_and_b32 s45, s93, 0xffff\n"
    "s_mov_b32 s46, 1152\n"
    "s_mov_b32 s47, 0x20000\n"
    "buffer_load_ushort v192, v191, s[44:47], 0 offen\n"
    "s_nop 4\n"
    "s_mov_b32 s44, s94\n"
    "s_and_b32 s45, s95, 0xffff\n"
    "s_mov_b32 s46, 1152\n"
    "s_mov_b32 s47, 0x20000\n"
    "buffer_load_ushort v193, v191, s[44:47], 0 offen\n"
    "s_nop 4\n"
    "s_mov_b32 s44, s96\n"
    "s_and_b32 s45, s97, 0xffff\n"
    "s_mov_b32 s46, 1152\n"
    "s_mov_b32 s47, 0x20000\n"
    "buffer_load_ushort v194, v191, s[44:47], 0 offen\n"
    "s_nop 4\n"
    "s_waitcnt vmcnt(0)\n"
    "s_load_dwordx4 s[88:91], %0, 40\n"
    "s_lshl_b32 s40, s10, 9\n"
    "s_add_u32 s55, s40, 4096\n"
    "v_lshlrev_b32 v160, 3, v0\n"
    "buffer_load_dwordx2 v[162:163], v160, s[72:75], s55 offen sc1\n"
    "v_lshlrev_b32 v192, 16, v192\n"
    "v_lshlrev_b32 v193, 16, v193\n"
    "v_lshlrev_b32 v194, 16, v194\n"
    "v_mul_f32 v194, 0x3fb8aa3b, v194\n"
    "v_exp_f32 v194, v194\n"
    "s_nop 0\n"
    "v_cvt_pk_bf16_f32 v194, v194, v194\n"
    "v_lshlrev_b32 v194, 16, v194\n"
    "v_mul_f32 v195, v194, v192\n"
    "v_mul_f32 v196, v194, v193\n"
    "v_mul_f32 v195, 0x3fb8aa3b, v195\n"
    "v_exp_f32 v195, v195\n"
    "v_mul_f32 v196, 0x3e22f983, v196\n"
    "v_fract_f32 v196, v196\n"
    "v_cos_f32 v197, v196\n"
    "v_sin_f32 v198, v196\n"
    "v_mul_f32 v40, v195, v197\n"
    "v_mul_f32 v41, v195, v198\n"
    "v_add_f32 v44, -1.0, v40\n"
    "v_mul_f32 v45, v192, v192\n"
    "v_fmac_f32 v45, v193, v193\n"
    "v_rcp_f32 v45, v45\n"
    "s_nop 0\n"
    "v_add_f32 v45, v45, v45\n"
    "v_mul_f32 v42, v44, v192\n"
    "v_fmac_f32 v42, v41, v193\n"
    "v_mul_f32 v42, v42, v45\n"
    "v_mul_f32 v43, v41, v192\n"
    "v_fma_f32 v43, -v44, v193, v43\n"
    "v_mul_f32 v43, v43, v45\n"
    "v_mov_b32 v248, v40\n"
    "v_mov_b32 v249, v41\n"
    "v_mul_f32 v50, v248, v248\n"
    "v_fma_f32 v50, -v249, v249, v50\n"
    "v_add_f32 v51, v248, v248\n"
    "v_mul_f32 v249, v51, v249\n"
    "v_mov_b32 v248, v50\n"
    "v_mul_f32 v50, v248, v248\n"
    "v_fma_f32 v50, -v249, v249, v50\n"
    "v_add_f32 v51, v248, v248\n"
    "v_mul_f32 v249, v51, v249\n"
    "v_mov_b32 v248, v50\n"
    "v_mul_f32 v50, v248, v248\n"
    "v_fma_f32 v50, -v249, v249, v50\n"
    "v_add_f32 v51, v248, v248\n"
    "v_mul_f32 v249, v51, v249\n"
    "v_mov_b32 v248, v50\n"
    "v_mov_b32 v250, v248\n"
    "v_mov_b32 v251, v249\n"
    "v_mul_f32 v50, v250, v250\n"
    "v_fma_f32 v50, -v251, v251, v50\n"
    "v_add_f32 v51, v250, v250\n"
    "v_mul_f32 v251, v51, v251\n"
    "v_mov_b32 v250, v50\n"
    "v_mul_f32 v50, v250, v250\n"
    "v_fma_f32 v50, -v251, v251, v50\n"
    "v_add_f32 v51, v250, v250\n"
    "v_mul_f32 v251, v51, v251\n"
    "v_mov_b32 v250, v50\n"
    "v_mul_f32 v50, v250, v250\n"
    "v_fma_f32 v50, -v251, v251, v50\n"
    "v_add_f32 v51, v250, v250\n"
    "v_mul_f32 v251, v51, v251\n"
    "v_mov_b32 v250, v50\n"
    "v_mul_f32 v50, v250, v250\n"
    "v_fma_f32 v50, -v251, v251, v50\n"
    "v_add_f32 v51, v250, v250\n"
    "v_mul_f32 v251, v51, v251\n"
    "v_mov_b32 v250, v50\n"
    "v_lshlrev_b32 v191, 4, v189\n"
    "v_add_u32 v191, 147456, v191\n"
    "ds_write_b128 v191, v[40:43]\n"
    "v_and_b32 v40, 15, v0\n"
    "v_lshrrev_b32 v41, 3, v40\n"
    "v_mul_u32_u24 v41, 9216, v41\n"
    "v_and_b32 v42, 7, v40\n"
    "v_lshl_add_u32 v41, v42, 7, v41\n"
    "v_lshrrev_b32 v42, 1, v40\n"
    "v_lshrrev_b32 v43, 4, v0\n"
    "v_xor_b32 v42, v42, v43\n"
    "v_lshl_add_u32 v1, v42, 4, v41\n"
    "v_xor_b32 v2, 64, v1\n"
    "v_lshlrev_b32 v9, 4, v40\n"
    "v_add_u32 v9, 147456, v9\n"
    "s_lshl_b32 s40, s9, 2\n"
    "v_add_u32 v10, s40, v43\n"
    "v_mul_u32_u24 v10, 144, v10\n"
    "v_add_u32 v10, v10, v40\n"
    "v_lshlrev_b32 v10, 3, v10\n"
    "v_add_u32 v11, 129024, v10\n"
    "v_add_u32 v10, 110592, v10\n"
    "v_add_u32 v40, 0, v0\n"
    "v_mul_u32_u24 v41, 3641, v40\n"
    "v_lshrrev_b32 v41, 16, v41\n"
    "v_mul_u32_u24 v42, 18, v41\n"
    "v_sub_u32 v42, v40, v42\n"
    "v_mul_u32_u24 v41, 1152, v41\n"
    "v_lshl_add_u32 v180, v42, 4, v41\n"
    "v_add_u32 v40, 64, v0\n"
    "v_mul_u32_u24 v41, 3641, v40\n"
    "v_lshrrev_b32 v41, 16, v41\n"
    "v_mul_u32_u24 v42, 18, v41\n"
    "v_sub_u32 v42, v40, v42\n"
    "v_mul_u32_u24 v41, 1152, v41\n"
    "v_lshl_add_u32 v181, v42, 4, v41\n"
    "v_add_u32 v40, 128, v0\n"
    "v_mul_u32_u24 v41, 3641, v40\n"
    "v_lshrrev_b32 v41, 16, v41\n"
    "v_mul_u32_u24 v42, 18, v41\n"
    "v_sub_u32 v42, v40, v42\n"
    "v_mul_u32_u24 v41, 1152, v41\n"
    "v_lshl_add_u32 v182, v42, 4, v41\n"
    "v_add_u32 v40, 192, v0\n"
    "v_mul_u32_u24 v41, 3641, v40\n"
    "v_lshrrev_b32 v41, 16, v41\n"
    "v_mul_u32_u24 v42, 18, v41\n"
    "v_sub_u32 v42, v40, v42\n"
    "v_mul_u32_u24 v41, 1152, v41\n"
    "v_lshl_add_u32 v183, v42, 4, v41\n"
    "v_add_u32 v40, 256, v0\n"
    "v_mul_u32_u24 v41, 3641, v40\n"
    "v_lshrrev_b32 v41, 16, v41\n"
    "v_mul_u32_u24 v42, 18, v41\n"
    "v_sub_u32 v42, v40, v42\n"
    "v_mul_u32_u24 v41, 1152, v41\n"
    "v_lshl_add_u32 v184, v42, 4, v41\n"
    "v_add_u32 v40, 320, v0\n"
    "v_mul_u32_u24 v41, 3641, v40\n"
    "v_lshrrev_b32 v41, 16, v41\n"
    "v_mul_u32_u24 v42, 18, v41\n"
    "v_sub_u32 v42, v40, v42\n"
    "v_mul_u32_u24 v41, 1152, v41\n"
    "v_lshl_add_u32 v185, v42, 4, v41\n"
    "v_add_u32 v40, 384, v0\n"
    "v_mul_u32_u24 v41, 3641, v40\n"
    "v_lshrrev_b32 v41, 16, v41\n"
    "v_mul_u32_u24 v42, 18, v41\n"
    "v_sub_u32 v42, v40, v42\n"
    "v_mul_u32_u24 v41, 1152, v41\n"
    "v_lshl_add_u32 v186, v42, 4, v41\n"
    "v_add_u32 v40, 448, v0\n"
    "v_mul_u32_u24 v41, 3641, v40\n"
    "v_lshrrev_b32 v41, 16, v41\n"
    "v_mul_u32_u24 v42, 18, v41\n"
    "v_sub_u32 v42, v40, v42\n"
    "v_mul_u32_u24 v41, 1152, v41\n"
    "v_lshl_add_u32 v187, v42, 4, v41\n"
    "v_add_u32 v40, 512, v0\n"
    "v_mul_u32_u24 v41, 3641, v40\n"
    "v_lshrrev_b32 v41, 16, v41\n"
    "v_mul_u32_u24 v42, 18, v41\n"
    "v_sub_u32 v42, v40, v42\n"
    "v_mul_u32_u24 v41, 1152, v41\n"
    "v_lshl_add_u32 v188, v42, 4, v41\n"
    "s_waitcnt lgkmcnt(0)\n"
    "s_mov_b32 s44, s90\n"
    "s_mov_b32 s45, s91\n"
    "s_mov_b32 s92, s44\n"
    "s_and_b32 s93, s45, 0xffff\n"
    "s_mov_b32 s94, 663552\n"
    "s_mov_b32 s95, 0x20000\n"
    "s_mov_b32 s44, s88\n"
    "s_mov_b32 s45, s89\n"
    "s_mov_b32 s88, s44\n"
    "s_and_b32 s89, s45, 0xffff\n"
    "s_mov_b32 s90, 663552\n"
    "s_mov_b32 s91, 0x20000\n"
    "s_cmp_lg_u32 s12, 0\n"
    "s_cselect_b32 s36, s92, s88\n"
    "s_cselect_b32 s37, s93, s89\n"
    "s_cselect_b32 s38, s94, s90\n"
    "s_cselect_b32 s39, s95, s91\n"
    "s_add_u32 s24, s66, 4194304\n"
    "s_addc_u32 s25, s67, 0\n"
    "s_and_b32 s25, s25, 0xffff\n"
    "s_mov_b32 s26, 2359296\n"
    "s_mov_b32 s27, 0x20000\n"
    "s_add_u32 s28, s66, 16777216\n"
    "s_addc_u32 s29, s67, 0\n"
    "s_and_b32 s29, s29, 0xffff\n"
    "s_mov_b32 s30, 37748736\n"
    "s_mov_b32 s31, 0x20000\n"
    "s_barrier\n"
    "s_waitcnt vmcnt(0)\n"
    "v_cmp_eq_u32 vcc, s84, v162\n"
    "v_cmp_eq_u32 s[40:41], s85, v163\n"
    "s_nop 1\n"
    "s_and_b64 vcc, vcc, s[40:41]\n"
    "s_cmp_eq_u64 vcc, -1\n"
    "s_cbranch_scc1 POK_11\n"
    "s_mov_b32 s42, 0\n"
    "PLP_12:\n"
    "s_sleep 1\n"
    "buffer_load_dwordx2 v[162:163], v160, s[72:75], s55 offen sc1\n"
    "s_waitcnt vmcnt(0)\n"
    "v_cmp_eq_u32 vcc, s84, v162\n"
    "v_cmp_eq_u32 s[40:41], s85, v163\n"
    "s_nop 1\n"
    "s_and_b64 vcc, vcc, s[40:41]\n"
    "s_cmp_eq_u64 vcc, -1\n"
    "s_cbranch_scc1 POK_11\n"
    "s_add_u32 s42, s42, 1\n"
    "s_cmp_lt_u32 s42, 20000\n"
    "s_cbranch_scc1 PLP_12\n"
    "POK_11:\n"
    "s_mul_i32 s40, s10, 147456\n"
    "s_mul_i32 s41, s9, 36864\n"
    "s_add_u32 s40, s40, s41\n"
    "s_add_u32 s56, s40, 0\n"
    "s_add_u32 s57, s40, 18432\n"
    "buffer_load_dwordx4 a[0:3], v13, s[24:27], s56 offen offset:0 sc1\n"
    "buffer_load_dwordx4 a[4:7], v13, s[24:27], s57 offen offset:0 sc1\n"
    "buffer_load_dwordx4 a[8:11], v13, s[24:27], s56 offen offset:1024 sc1\n"
    "buffer_load_dwordx4 a[12:15], v13, s[24:27], s57 offen offset:1024 sc1\n"
    "buffer_load_dwordx4 a[16:19], v13, s[24:27], s56 offen offset:2048 sc1\n"
    "buffer_load_dwordx4 a[20:23], v13, s[24:27], s57 offen offset:2048 sc1\n"
    "buffer_load_dwordx4 a[24:27], v13, s[24:27], s56 offen offset:3072 sc1\n"
    "buffer_load_dwordx4 a[28:31], v13, s[24:27], s57 offen offset:3072 sc1\n"
    "s_add_u32 s56, s56, 4096\n"
    "buffer_load_dwordx4 a[32:35], v13, s[24:27], s56 offen offset:0 sc1\n"
    "s_add_u32 s57, s57, 4096\n"
    "buffer_load_dwordx4 a[36:39], v13, s[24:27], s57 offen offset:0 sc1\n"
    "buffer_load_dwordx4 a[40:43], v13, s[24:27], s56 offen offset:1024 sc1\n"
    "buffer_load_dwordx4 a[44:47], v13, s[24:27], s57 offen offset:1024 sc1\n"
    "buffer_load_dwordx4 a[48:51], v13, s[24:27], s56 offen offset:2048 sc1\n"
    "buffer_load_dwordx4 a[52:55], v13, s[24:27], s57 offen offset:2048 sc1\n"
    "buffer_load_dwordx4 a[56:59], v13, s[24:27], s56 offen offset:3072 sc1\n"
    "buffer_load_dwordx4 a[60:63], v13, s[24:27], s57 offen offset:3072 sc1\n"
    "s_add_u32 s56, s56, 4096\n"
    "buffer_load_dwordx4 a[64:67], v13, s[24:27], s56 offen offset:0 sc1\n"
    "s_add_u32 s57, s57, 4096\n"
    "buffer_load_dwordx4 a[68:71], v13, s[24:27], s57 offen offset:0 sc1\n"
    "buffer_load_dwordx4 a[72:75], v13, s[24:27], s56 offen offset:1024 sc1\n"
    "buffer_load_dwordx4 a[76:79], v13, s[24:27], s57 offen offset:1024 sc1\n"
    "buffer_load_dwordx4 a[80:83], v13, s[24:27], s56 offen offset:2048 sc1\n"
    "buffer_load_dwordx4 a[84:87], v13, s[24:27], s57 offen offset:2048 sc1\n"
    "buffer_load_dwordx4 a[88:91], v13, s[24:27], s56 offen offset:3072 sc1\n"
    "buffer_load_dwordx4 a[92:95], v13, s[24:27], s57 offen offset:3072 sc1\n"
    "s_add_u32 s56, s56, 4096\n"
    "buffer_load_dwordx4 a[96:99], v13, s[24:27], s56 offen offset:0 sc1\n"
    "s_add_u32 s57, s57, 4096\n"
    "buffer_load_dwordx4 a[100:103], v13, s[24:27], s57 offen offset:0 sc1\n"
    "buffer_load_dwordx4 a[104:107], v13, s[24:27], s56 offen offset:1024 sc1\n"
    "buffer_load_dwordx4 a[108:111], v13, s[24:27], s57 offen offset:1024 sc1\n"
    "buffer_load_dwordx4 a[112:115], v13, s[24:27], s56 offen offset:2048 sc1\n"
    "buffer_load_dwordx4 a[116:119], v13, s[24:27], s57 offen offset:2048 sc1\n"
    "buffer_load_dwordx4 a[120:123], v13, s[24:27], s56 offen offset:3072 sc1\n"
    "buffer_load_dwordx4 a[124:127], v13, s[24:27], s57 offen offset:3072 sc1\n"
    "s_add_u32 s56, s56, 4096\n"
    "buffer_load_dwordx4 a[128:131], v13, s[24:27], s56 offen offset:0 sc1\n"
    "s_add_u32 s57, s57, 4096\n"
    "buffer_load_dwordx4 a[132:135], v13, s[24:27], s57 offen offset:0 sc1\n"
    "buffer_load_dwordx4 a[136:139], v13, s[24:27], s56 offen offset:1024 sc1\n"
    "buffer_load_dwordx4 a[140:143], v13, s[24:27], s57 offen offset:1024 sc1\n"
    "s_mul_i32 s52, s11, 144\n"
    "s_add_u32 s52, s52, 32\n"
    "s_mul_i32 s52, s52, 1152\n"
    "s_mul_i32 s53, s9, 9216\n"
    "s_add_u32 s53, s53, 73728\n"
    "s_add_u32 m0, s53, 0\n"
    "s_nop 0\n"
    "buffer_load_dwordx4 v8, s[48:51], s52 offen lds\n"
    "s_add_u32 m0, s53, 1024\n"
    "s_add_u32 s52, s52, 128\n"
    "buffer_load_dwordx4 v8, s[48:51], s52 offen lds\n"
    "s_add_u32 m0, s53, 2048\n"
    "s_add_u32 s52, s52, 128\n"
    "buffer_load_dwordx4 v8, s[48:51], s52 offen lds\n"
    "s_add_u32 m0, s53, 3072\n"
    "s_add_u32 s52, s52, 128\n"
    "buffer_load_dwordx4 v8, s[48:51], s52 offen lds\n"
    "s_add_u32 m0, s53, 4096\n"
    "s_add_u32 s52, s52, 128\n"
    "buffer_load_dwordx4 v8, s[48:51], s52 offen lds\n"
    "s_add_u32 m0, s53, 5120\n"
    "s_add_u32 s52, s52, 128\n"
    "buffer_load_dwordx4 v8, s[48:51], s52 offen lds\n"
    "s_add_u32 m0, s53, 6144\n"
    "s_add_u32 s52, s52, 128\n"
    "buffer_load_dwordx4 v8, s[48:51], s52 offen lds\n"
    "s_add_u32 m0, s53, 7168\n"
    "s_add_u32 s52, s52, 128\n"
    "buffer_load_dwordx4 v8, s[48:51], s52 offen lds\n"
    "s_add_u32 m0, s53, 8192\n"
    "s_add_u32 s52, s52, 128\n"
    "buffer_load_dwordx4 v8, s[48:51], s52 offen lds\n"
    "v_add_u32 v14, 36864, v1\n"
    "v_add_u32 v15, 36864, v2\n"
    "v_mov_b32 v3, v1\n"
    "v_mov_b32 v4, v2\n"
    "ds_read_b128 v[16:19], v3 offset:0\n"
    "ds_read_b128 v[20:23], v3 offset:18432\n"
    "ds_read_b128 v[24:27], v14 offset:0\n"
    "ds_read_b128 v[28:31], v14 offset:18432\n"
    "s_waitcnt lgkmcnt(0)\n"
    "s_waitcnt vmcnt(44)\n"
    "v_mfma_f32_16x16x32_bf16 v[32:35], a[0:3], v[16:19], 0\n"
    "s_waitcnt vmcnt(43)\n"
    "v_mfma_f32_16x16x32_bf16 v[36:39], a[4:7], v[16:19], 0\n"
    "ds_read_b128 v[200:203], v4 offset:0\n"
    "v_mfma_f32_16x16x32_bf16 v[40:43], a[0:3], v[20:23], 0\n"
    "v_mfma_f32_16x16x32_bf16 v[44:47], a[4:7], v[20:23], 0\n"
    "ds_read_b128 v[204:207], v4 offset:18432\n"
    "v_mfma_f32_16x16x32_bf16 v[48:51], a[0:3], v[24:27], 0\n"
    "v_mfma_f32_16x16x32_bf16 v[52:55], a[4:7], v[24:27], 0\n"
    "ds_read_b128 v[208:211], v15 offset:0\n"
    "v_mfma_f32_16x16x32_bf16 v[56:59], a[0:3], v[28:31], 0\n"
    "v_mfma_f32_16x16x32_bf16 v[60:63], a[4:7], v[28:31], 0\n"
    "ds_read_b128 v[212:215], v15 offset:18432\n"
    "s_waitcnt lgkmcnt(0)\n"
    "s_waitcnt vmcnt(42)\n"
    "v_mfma_f32_16x16x32_bf16 v[32:35], a[8:11], v[200:203], v[32:35]\n"
    "s_waitcnt vmcnt(41)\n"
    "v_mfma_f32_16x16x32_bf16 v[36:39], a[12:15], v[200:203], v[36:39]\n"
    "ds_read_b128 v[16:19], v3 offset:1024\n"
    "v_mfma_f32_16x16x32_bf16 v[40:43], a[8:11], v[204:207], v[40:43]\n"
    "v_mfma_f32_16x16x32_bf16 v[44:47], a[12:15], v[204:207], v[44:47]\n"
    "ds_read_b128 v[20:23], v3 offset:19456\n"
    "v_mfma_f32_16x16x32_bf16 v[48:51], a[8:11], v[208:211], v[48:51]\n"
    "v_mfma_f32_16x16x32_bf16 v[52:55], a[12:15], v[208:211], v[52:55]\n"
    "ds_read_b128 v[24:27], v14 offset:1024\n"
    "v_mfma_f32_16x16x32_bf16 v[56:59], a[8:11], v[212:215], v[56:59]\n"
    "v_mfma_f32_16x16x32_bf16 v[60:63], a[12:15], v[212:215], v[60:63]\n"
    "ds_read_b128 v[28:31], v14 offset:19456\n"
    "s_waitcnt lgkmcnt(0)\n"
    "s_waitcnt vmcnt(40)\n"
    "v_mfma_f32_16x16x32_bf16 v[32:35], a[16:19], v[16:19], v[32:35]\n"
    "s_waitcnt vmcnt(39)\n"
    "v_mfma_f32_16x16x32_bf16 v[36:39], a[20:23], v[16:19], v[36:39]\n"
    "ds_read_b128 v[200:203], v4 offset:1024\n"
    "v_mfma_f32_16x16x32_bf16 v[40:43], a[16:19], v[20:23], v[40:43]\n"
    "v_mfma_f32_16x16x32_bf16 v[44:47], a[20:23], v[20:23], v[44:47]\n"
    "ds_read_b128 v[204:207], v4 offset:19456\n"
    "v_mfma_f32_16x16x32_bf16 v[48:51], a[16:19], v[24:27], v[48:51]\n"
    "v_mfma_f32_16x16x32_bf16 v[52:55], a[20:23], v[24:27], v[52:55]\n"
    "ds_read_b128 v[208:211], v15 offset:1024\n"
    "v_mfma_f32_16x16x32_bf16 v[56:59], a[16:19], v[28:31], v[56:59]\n"
    "v_mfma_f32_16x16x32_bf16 v[60:63], a[20:23], v[28:31], v[60:63]\n"
    "ds_read_b128 v[212:215], v15 offset:19456\n"
    "s_waitcnt lgkmcnt(0)\n"
    "s_waitcnt vmcnt(38)\n"
    "v_mfma_f32_16x16x32_bf16 v[32:35], a[24:27], v[200:203], v[32:35]\n"
    "s_waitcnt vmcnt(37)\n"
    "v_mfma_f32_16x16x32_bf16 v[36:39], a[28:31], v[200:203], v[36:39]\n"
    "ds_read_b128 v[16:19], v3 offset:2048\n"
    "v_mfma_f32_16x16x32_bf16 v[40:43], a[24:27], v[204:207], v[40:43]\n"
    "v_mfma_f32_16x16x32_bf16 v[44:47], a[28:31], v[204:207], v[44:47]\n"
    "ds_read_b128 v[20:23], v3 offset:20480\n"
    "v_mfma_f32_16x16x32_bf16 v[48:51], a[24:27], v[208:211], v[48:51]\n"
    "v_mfma_f32_16x16x32_bf16 v[52:55], a[28:31], v[208:211], v[52:55]\n"
    "ds_read_b128 v[24:27], v14 offset:2048\n"
    "v_mfma_f32_16x16x32_bf16 v[56:59], a[24:27], v[212:215], v[56:59]\n"
    "v_mfma_f32_16x16x32_bf16 v[60:63], a[28:31], v[212:215], v[60:63]\n"
    "ds_read_b128 v[28:31], v14 offset:20480\n"
    "s_waitcnt lgkmcnt(0)\n"
    "s_waitcnt vmcnt(36)\n"
    "v_mfma_f32_16x16x32_bf16 v[32:35], a[32:35], v[16:19], v[32:35]\n"
    "s_waitcnt vmcnt(35)\n"
    "v_mfma_f32_16x16x32_bf16 v[36:39], a[36:39], v[16:19], v[36:39]\n"
    "ds_read_b128 v[200:203], v4 offset:2048\n"
    "v_mfma_f32_16x16x32_bf16 v[40:43], a[32:35], v[20:23], v[40:43]\n"
    "v_mfma_f32_16x16x32_bf16 v[44:47], a[36:39], v[20:23], v[44:47]\n"
    "ds_read_b128 v[204:207], v4 offset:20480\n"
    "v_mfma_f32_16x16x32_bf16 v[48:51], a[32:35], v[24:27], v[48:51]\n"
    "v_mfma_f32_16x16x32_bf16 v[52:55], a[36:39], v[24:27], v[52:55]\n"
    "ds_read_b128 v[208:211], v15 offset:2048\n"
    "v_mfma_f32_16x16x32_bf16 v[56:59], a[32:35], v[28:31], v[56:59]\n"
    "v_mfma_f32_16x16x32_bf16 v[60:63], a[36:39], v[28:31], v[60:63]\n"
    "ds_read_b128 v[212:215], v15 offset:20480\n"
    "s_waitcnt lgkmcnt(0)\n"
    "s_waitcnt vmcnt(34)\n"
    "v_mfma_f32_16x16x32_bf16 v[32:35], a[40:43], v[200:203], v[32:35]\n"
    "s_waitcnt vmcnt(33)\n"
    "v_mfma_f32_16x16x32_bf16 v[36:39], a[44:47], v[200:203], v[36:39]\n"
    "ds_read_b128 v[16:19], v3 offset:3072\n"
    "v_mfma_f32_16x16x32_bf16 v[40:43], a[40:43], v[204:207], v[40:43]\n"
    "v_mfma_f32_16x16x32_bf16 v[44:47], a[44:47], v[204:207], v[44:47]\n"
    "ds_read_b128 v[20:23], v3 offset:21504\n"
    "v_mfma_f32_16x16x32_bf16 v[48:51], a[40:43], v[208:211], v[48:51]\n"
    "v_mfma_f32_16x16x32_bf16 v[52:55], a[44:47], v[208:211], v[52:55]\n"
    "ds_read_b128 v[24:27], v14 offset:3072\n"
    "v_mfma_f32_16x16x32_bf16 v[56:59], a[40:43], v[212:215], v[56:59]\n"
    "v_mfma_f32_16x16x32_bf16 v[60:63], a[44:47], v[212:215], v[60:63]\n"
    "ds_read_b128 v[28:31], v14 offset:21504\n"
    "s_waitcnt lgkmcnt(0)\n"
    "s_waitcnt vmcnt(32)\n"
    "v_mfma_f32_16x16x32_bf16 v[32:35], a[48:51], v[16:19], v[32:35]\n"
    "s_waitcnt vmcnt(31)\n"
    "v_mfma_f32_16x16x32_bf16 v[36:39], a[52:55], v[16:19], v[36:39]\n"
    "ds_read_b128 v[200:203], v4 offset:3072\n"
    "v_mfma_f32_16x16x32_bf16 v[40:43], a[48:51], v[20:23], v[40:43]\n"
    "v_mfma_f32_16x16x32_bf16 v[44:47], a[52:55], v[20:23], v[44:47]\n"
    "ds_read_b128 v[204:207], v4 offset:21504\n"
    "v_mfma_f32_16x16x32_bf16 v[48:51], a[48:51], v[24:27], v[48:51]\n"
    "v_mfma_f32_16x16x32_bf16 v[52:55], a[52:55], v[24:27], v[52:55]\n"
    "ds_read_b128 v[208:211], v15 offset:3072\n"
    "v_mfma_f32_16x16x32_bf16 v[56:59], a[48:51], v[28:31], v[56:59]\n"
    "v_mfma_f32_16x16x32_bf16 v[60:63], a[52:55], v[28:31], v[60:63]\n"
    "ds_read_b128 v[212:215], v15 offset:21504\n"
    "s_waitcnt lgkmcnt(0)\n"
    "s_waitcnt vmcnt(30)\n"
    "v_mfma_f32_16x16x32_bf16 v[32:35], a[56:59], v[200:203], v[32:35]\n"
    "s_waitcnt vmcnt(29)\n"
    "v_mfma_f32_16x16x32_bf16 v[36:39], a[60:63], v[200:203], v[36:39]\n"
    "ds_read_b128 v[16:19], v3 offset:4096\n"
    "v_mfma_f32_16x16x32_bf16 v[40:43], a[56:59], v[204:207], v[40:43]\n"
    "v_mfma_f32_16x16x32_bf16 v[44:47], a[60:63], v[204:207], v[44:47]\n"
    "ds_read_b128 v[20:23], v3 offset:22528\n"
    "v_mfma_f32_16x16x32_bf16 v[48:51], a[56:59], v[208:211], v[48:51]\n"
    "v_mfma_f32_16x16x32_bf16 v[52:55], a[60:63], v[208:211], v[52:55]\n"
    "ds_read_b128 v[24:27], v14 offset:4096\n"
    "v_mfma_f32_16x16x32_bf16 v[56:59], a[56:59], v[212:215], v[56:59]\n"
    "v_mfma_f32_16x16x32_bf16 v[60:63], a[60:63], v[212:215], v[60:63]\n"
    "ds_read_b128 v[28:31], v14 offset:22528\n"
    "s_waitcnt lgkmcnt(0)\n"
    "s_waitcnt vmcnt(28)\n"
    "v_mfma_f32_16x16x32_bf16 v[32:35], a[64:67], v[16:19], v[32:35]\n"
    "s_waitcnt vmcnt(27)\n"
    "v_mfma_f32_16x16x32_bf16 v[36:39], a[68:71], v[16:19], v[36:39]\n"
    "ds_read_b128 v[200:203], v4 offset:4096\n"
    "v_mfma_f32_16x16x32_bf16 v[40:43], a[64:67], v[20:23], v[40:43]\n"
    "v_mfma_f32_16x16x32_bf16 v[44:47], a[68:71], v[20:23], v[44:47]\n"
    "ds_read_b128 v[204:207], v4 offset:22528\n"
    "v_mfma_f32_16x16x32_bf16 v[48:51], a[64:67], v[24:27], v[48:51]\n"
    "v_mfma_f32_16x16x32_bf16 v[52:55], a[68:71], v[24:27], v[52:55]\n"
    "ds_read_b128 v[208:211], v15 offset:4096\n"
    "v_mfma_f32_16x16x32_bf16 v[56:59], a[64:67], v[28:31], v[56:59]\n"
    "v_mfma_f32_16x16x32_bf16 v[60:63], a[68:71], v[28:31], v[60:63]\n"
    "ds_read_b128 v[212:215], v15 offset:22528\n"
    "s_waitcnt lgkmcnt(0)\n"
    "s_waitcnt vmcnt(26)\n"
    "v_mfma_f32_16x16x32_bf16 v[32:35], a[72:75], v[200:203], v[32:35]\n"
    "s_waitcnt vmcnt(25)\n"
    "v_mfma_f32_16x16x32_bf16 v[36:39], a[76:79], v[200:203], v[36:39]\n"
    "ds_read_b128 v[16:19], v3 offset:5120\n"
    "v_mfma_f32_16x16x32_bf16 v[40:43], a[72:75], v[204:207], v[40:43]\n"
    "v_mfma_f32_16x16x32_bf16 v[44:47], a[76:79], v[204:207], v[44:47]\n"
    "ds_read_b128 v[20:23], v3 offset:23552\n"
    "v_mfma_f32_16x16x32_bf16 v[48:51], a[72:75], v[208:211], v[48:51]\n"
    "v_mfma_f32_16x16x32_bf16 v[52:55], a[76:79], v[208:211], v[52:55]\n"
    "ds_read_b128 v[24:27], v14 offset:5120\n"
    "v_mfma_f32_16x16x32_bf16 v[56:59], a[72:75], v[212:215], v[56:59]\n"
    "v_mfma_f32_16x16x32_bf16 v[60:63], a[76:79], v[212:215], v[60:63]\n"
    "ds_read_b128 v[28:31], v14 offset:23552\n"
    "s_waitcnt lgkmcnt(0)\n"
    "s_waitcnt vmcnt(24)\n"
    "v_mfma_f32_16x16x32_bf16 v[32:35], a[80:83], v[16:19], v[32:35]\n"
    "s_waitcnt vmcnt(23)\n"
    "v_mfma_f32_16x16x32_bf16 v[36:39], a[84:87], v[16:19], v[36:39]\n"
    "ds_read_b128 v[200:203], v4 offset:5120\n"
    "v_mfma_f32_16x16x32_bf16 v[40:43], a[80:83], v[20:23], v[40:43]\n"
    "v_mfma_f32_16x16x32_bf16 v[44:47], a[84:87], v[20:23], v[44:47]\n"
    "ds_read_b128 v[204:207], v4 offset:23552\n"
    "v_mfma_f32_16x16x32_bf16 v[48:51], a[80:83], v[24:27], v[48:51]\n"
    "v_mfma_f32_16x16x32_bf16 v[52:55], a[84:87], v[24:27], v[52:55]\n"
    "ds_read_b128 v[208:211], v15 offset:5120\n"
    "v_mfma_f32_16x16x32_bf16 v[56:59], a[80:83], v[28:31], v[56:59]\n"
    "v_mfma_f32_16x16x32_bf16 v[60:63], a[84:87], v[28:31], v[60:63]\n"
    "ds_read_b128 v[212:215], v15 offset:23552\n"
    "s_waitcnt lgkmcnt(0)\n"
    "s_waitcnt vmcnt(22)\n"
    "v_mfma_f32_16x16x32_bf16 v[32:35], a[88:91], v[200:203], v[32:35]\n"
    "s_waitcnt vmcnt(21)\n"
    "v_mfma_f32_16x16x32_bf16 v[36:39], a[92:95], v[200:203], v[36:39]\n"
    "ds_read_b128 v[16:19], v3 offset:6144\n"
    "v_mfma_f32_16x16x32_bf16 v[40:43], a[88:91], v[204:207], v[40:43]\n"
    "v_mfma_f32_16x16x32_bf16 v[44:47], a[92:95], v[204:207], v[44:47]\n"
    "ds_read_b128 v[20:23], v3 offset:24576\n"
    "v_mfma_f32_16x16x32_bf16 v[48:51], a[88:91], v[208:211], v[48:51]\n"
    "v_mfma_f32_16x16x32_bf16 v[52:55], a[92:95], v[208:211], v[52:55]\n"
    "ds_read_b128 v[24:27], v14 offset:6144\n"
    "v_mfma_f32_16x16x32_bf16 v[56:59], a[88:91], v[212:215], v[56:59]\n"
    "v_mfma_f32_16x16x32_bf16 v[60:63], a[92:95], v[212:215], v[60:63]\n"
    "ds_read_b128 v[28:31], v14 offset:24576\n"
    "s_waitcnt lgkmcnt(0)\n"
    "s_waitcnt vmcnt(20)\n"
    "v_mfma_f32_16x16x32_bf16 v[32:35], a[96:99], v[16:19], v[32:35]\n"
    "s_waitcnt vmcnt(19)\n"
    "v_mfma_f32_16x16x32_bf16 v[36:39], a[100:103], v[16:19], v[36:39]\n"
    "ds_read_b128 v[200:203], v4 offset:6144\n"
    "v_mfma_f32_16x16x32_bf16 v[40:43], a[96:99], v[20:23], v[40:43]\n"
    "v_mfma_f32_16x16x32_bf16 v[44:47], a[100:103], v[20:23], v[44:47]\n"
    "ds_read_b128 v[204:207], v4 offset:24576\n"
    "v_mfma_f32_16x16x32_bf16 v[48:51], a[96:99], v[24:27], v[48:51]\n"
    "v_mfma_f32_16x16x32_bf16 v[52:55], a[100:103], v[24:27], v[52:55]\n"
    "ds_read_b128 v[208:211], v15 offset:6144\n"
    "v_mfma_f32_16x16x32_bf16 v[56:59], a[96:99], v[28:31], v[56:59]\n"
    "v_mfma_f32_16x16x32_bf16 v[60:63], a[100:103], v[28:31], v[60:63]\n"
    "ds_read_b128 v[212:215], v15 offset:24576\n"
    "s_waitcnt lgkmcnt(0)\n"
    "s_waitcnt vmcnt(18)\n"
    "v_mfma_f32_16x16x32_bf16 v[32:35], a[104:107], v[200:203], v[32:35]\n"
    "s_waitcnt vmcnt(17)\n"
    "v_mfma_f32_16x16x32_bf16 v[36:39], a[108:111], v[200:203], v[36:39]\n"
    "ds_read_b128 v[16:19], v3 offset:7168\n"
    "v_mfma_f32_16x16x32_bf16 v[40:43], a[104:107], v[204:207], v[40:43]\n"
    "v_mfma_f32_16x16x32_bf16 v[44:47], a[108:111], v[204:207], v[44:47]\n"
    "ds_read_b128 v[20:23], v3 offset:25600\n"
    "v_mfma_f32_16x16x32_bf16 v[48:51], a[104:107], v[208:211], v[48:51]\n"
    "v_mfma_f32_16x16x32_bf16 v[52:55], a[108:111], v[208:211], v[52:55]\n"
    "ds_read_b128 v[24:27], v14 offset:7168\n"
    "v_mfma_f32_16x16x32_bf16 v[56:59], a[104:107], v[212:215], v[56:59]\n"
    "v_mfma_f32_16x16x32_bf16 v[60:63], a[108:111], v[212:215], v[60:63]\n"
    "ds_read_b128 v[28:31], v14 offset:25600\n"
    "s_waitcnt lgkmcnt(0)\n"
    "s_waitcnt vmcnt(16)\n"
    "v_mfma_f32_16x16x32_bf16 v[32:35], a[112:115], v[16:19], v[32:35]\n"
    "s_waitcnt vmcnt(15)\n"
    "v_mfma_f32_16x16x32_bf16 v[36:39], a[116:119], v[16:19], v[36:39]\n"
    "ds_read_b128 v[200:203], v4 offset:7168\n"
    "v_mfma_f32_16x16x32_bf16 v[40:43], a[112:115], v[20:23], v[40:43]\n"
    "v_mfma_f32_16x16x32_bf16 v[44:47], a[116:119], v[20:23], v[44:47]\n"
    "ds_read_b128 v[204:207], v4 offset:25600\n"
    "v_mfma_f32_16x16x32_bf16 v[48:51], a[112:115], v[24:27], v[48:51]\n"
    "v_mfma_f32_16x16x32_bf16 v[52:55], a[116:119], v[24:27], v[52:55]\n"
    "ds_read_b128 v[208:211], v15 offset:7168\n"
    "v_mfma_f32_16x16x32_bf16 v[56:59], a[112:115], v[28:31], v[56:59]\n"
    "v_mfma_f32_16x16x32_bf16 v[60:63], a[116:119], v[28:31], v[60:63]\n"
    "ds_read_b128 v[212:215], v15 offset:25600\n"
    "s_waitcnt lgkmcnt(0)\n"
    "s_waitcnt vmcnt(14)\n"
    "v_mfma_f32_16x16x32_bf16 v[32:35], a[120:123], v[200:203], v[32:35]\n"
    "s_waitcnt vmcnt(13)\n"
    "v_mfma_f32_16x16x32_bf16 v[36:39], a[124:127], v[200:203], v[36:39]\n"
    "ds_read_b128 v[16:19], v3 offset:8192\n"
    "v_mfma_f32_16x16x32_bf16 v[40:43], a[120:123], v[204:207], v[40:43]\n"
    "v_mfma_f32_16x16x32_bf16 v[44:47], a[124:127], v[204:207], v[44:47]\n"
    "ds_read_b128 v[20:23], v3 offset:26624\n"
    "v_mfma_f32_16x16x32_bf16 v[48:51], a[120:123], v[208:211], v[48:51]\n"
    "v_mfma_f32_16x16x32_bf16 v[52:55], a[124:127], v[208:211], v[52:55]\n"
    "ds_read_b128 v[24:27], v14 offset:8192\n"
    "v_mfma_f32_16x16x32_bf16 v[56:59], a[120:123], v[212:215], v[56:59]\n"
    "v_mfma_f32_16x16x32_bf16 v[60:63], a[124:127], v[212:215], v[60:63]\n"
    "ds_read_b128 v[28:31], v14 offset:26624\n"
    "s_waitcnt lgkmcnt(0)\n"
    "s_waitcnt vmcnt(12)\n"
    "v_mfma_f32_16x16x32_bf16 v[32:35], a[128:131], v[16:19], v[32:35]\n"
    "s_waitcnt vmcnt(11)\n"
    "v_mfma_f32_16x16x32_bf16 v[36:39], a[132:135], v[16:19], v[36:39]\n"
    "ds_read_b128 v[200:203], v4 offset:8192\n"
    "v_mfma_f32_16x16x32_bf16 v[40:43], a[128:131], v[20:23], v[40:43]\n"
    "v_mfma_f32_16x16x32_bf16 v[44:47], a[132:135], v[20:23], v[44:47]\n"
    "ds_read_b128 v[204:207], v4 offset:26624\n"
    "v_mfma_f32_16x16x32_bf16 v[48:51], a[128:131], v[24:27], v[48:51]\n"
    "v_mfma_f32_16x16x32_bf16 v[52:55], a[132:135], v[24:27], v[52:55]\n"
    "ds_read_b128 v[208:211], v15 offset:8192\n"
    "v_mfma_f32_16x16x32_bf16 v[56:59], a[128:131], v[28:31], v[56:59]\n"
    "v_mfma_f32_16x16x32_bf16 v[60:63], a[132:135], v[28:31], v[60:63]\n"
    "ds_read_b128 v[212:215], v15 offset:26624\n"
    "s_waitcnt lgkmcnt(0)\n"
    "s_waitcnt vmcnt(10)\n"
    "v_mfma_f32_16x16x32_bf16 v[32:35], a[136:139], v[200:203], v[32:35]\n"
    "s_waitcnt vmcnt(9)\n"
    "v_mfma_f32_16x16x32_bf16 v[36:39], a[140:143], v[200:203], v[36:39]\n"
    "v_mfma_f32_16x16x32_bf16 v[40:43], a[136:139], v[204:207], v[40:43]\n"
    "v_mfma_f32_16x16x32_bf16 v[44:47], a[140:143], v[204:207], v[44:47]\n"
    "v_mfma_f32_16x16x32_bf16 v[48:51], a[136:139], v[208:211], v[48:51]\n"
    "v_mfma_f32_16x16x32_bf16 v[52:55], a[140:143], v[208:211], v[52:55]\n"
    "v_mfma_f32_16x16x32_bf16 v[56:59], a[136:139], v[212:215], v[56:59]\n"
    "v_mfma_f32_16x16x32_bf16 v[60:63], a[140:143], v[212:215], v[60:63]\n"
    "s_waitcnt lgkmcnt(0)\n"
    "s_waitcnt vmcnt(0)\n"
    "s_barrier\n"
    "v_add_u32 v3, 73728, v1\n"
    "v_add_u32 v4, 73728, v2\n"
    "ds_read_b128 v[16:19], v3 offset:0\n"
    "ds_read_b128 v[20:23], v4 offset:0\n"
    "ds_read_b128 v[24:27], v3 offset:1024\n"
    "ds_read_b128 v[28:31], v4 offset:1024\n"
    "ds_read_b128 v[200:203], v3 offset:2048\n"
    "ds_read_b128 v[204:207], v4 offset:2048\n"
    "ds_read_b128 v[208:211], v3 offset:3072\n"
    "s_waitcnt lgkmcnt(5)\n"
    "v_mfma_f32_16x16x32_bf16 v[64:67], a[0:3], v[16:19], 0\n"
    "ds_read_b128 v[212:215], v4 offset:3072\n"
    "v_mfma_f32_16x16x32_bf16 v[68:71], a[4:7], v[16:19], 0\n"
    "s_mul_i32 s52, s11, 144\n"
    "s_add_u32 s52, s52, 48\n"
    "s_mul_i32 s52, s52, 1152\n"
    "s_mul_i32 s53, s9, 9216\n"
    "s_add_u32 s53, s53, 0\n"
    "s_add_u32 m0, s53, 0\n"
    "s_nop 0\n"
    "buffer_load_dwordx4 v8, s[48:51], s52 offen lds\n"
    "v_mfma_f32_16x16x32_bf16 v[64:67], a[8:11], v[20:23], v[64:67]\n"
    "ds_read_b128 v[16:19], v3 offset:4096\n"
    "v_mfma_f32_16x16x32_bf16 v[68:71], a[12:15], v[20:23], v[68:71]\n"
    "s_waitcnt lgkmcnt(5)\n"
    "v_mfma_f32_16x16x32_bf16 v[64:67], a[16:19], v[24:27], v[64:67]\n"
    "s_add_u32 m0, s53, 1024\n"
    "s_add_u32 s52, s52, 128\n"
    "buffer_load_dwordx4 v8, s[48:51], s52 offen lds\n"
    "ds_read_b128 v[20:23], v4 offset:4096\n"
    "v_add_u32 v14, 0, v1\n"
    "v_mfma_f32_16x16x32_bf16 v[68:71], a[20:23], v[24:27], v[68:71]\n"
    "v_mfma_f32_16x16x32_bf16 v[64:67], a[24:27], v[28:31], v[64:67]\n"
    "ds_read_b128 v[24:27], v3 offset:5120\n"
    "v_add_u32 v15, 0, v2\n"
    "ds_read_b64 v[176:177], v9 offset:0\n"
    "v_mfma_f32_16x16x32_bf16 v[68:71], a[28:31], v[28:31], v[68:71]\n"
    "s_waitcnt lgkmcnt(0)\n"
    "v_mfma_f32_16x16x32_bf16 v[64:67], a[32:35], v[200:203], v[64:67]\n"
    "s_add_u32 m0, s53, 2048\n"
    "s_add_u32 s52, s52, 128\n"
    "buffer_load_dwordx4 v8, s[48:51], s52 offen lds\n"
    "ds_read_b128 v[28:31], v4 offset:5120\n"
    "v_fma_f32 v192, v176, v32, v33\n"
    "v_fma_f32 v193, v176, v40, v41\n"
    "v_mfma_f32_16x16x32_bf16 v[68:71], a[36:39], v[200:203], v[68:71]\n"
    "v_fma_f32 v190, -v177, v40, v192\n"
    "v_mfma_f32_16x16x32_bf16 v[64:67], a[40:43], v[204:207], v[64:67]\n"
    "ds_read_b128 v[200:203], v3 offset:6144\n"
    "v_fma_f32 v191, v177, v32, v193\n"
    "v_fma_f32 v192, v176, v190, v34\n"
    "v_mfma_f32_16x16x32_bf16 v[68:71], a[44:47], v[204:207], v[68:71]\n"
    "v_fma_f32 v193, v176, v191, v42\n"
    "v_mfma_f32_16x16x32_bf16 v[64:67], a[48:51], v[208:211], v[64:67]\n"
    "s_add_u32 m0, s53, 3072\n"
    "s_add_u32 s52, s52, 128\n"
    "buffer_load_dwordx4 v8, s[48:51], s52 offen lds\n"
    "ds_read_b128 v[204:207], v4 offset:6144\n"
    "v_fma_f32 v194, -v177, v191, v192\n"
    "v_fma_f32 v195, v177, v190, v193\n"
    "v_mfma_f32_16x16x32_bf16 v[68:71], a[52:55], v[208:211], v[68:71]\n"
    "v_fma_f32 v192, v176, v194, v35\n"
    "v_mfma_f32_16x16x32_bf16 v[64:67], a[56:59], v[212:215], v[64:67]\n"
    "ds_read_b128 v[208:211], v3 offset:7168\n"
    "v_fma_f32 v193, v176, v195, v43\n"
    "v_fma_f32 v190, -v177, v195, v192\n"
    "v_mfma_f32_16x16x32_bf16 v[68:71], a[60:63], v[212:215], v[68:71]\n"
    "v_fma_f32 v191, v177, v194, v193\n"
    "v_mfma_f32_16x16x32_bf16 v[64:67], a[64:67], v[16:19], v[64:67]\n"
    "s_add_u32 m0, s53, 4096\n"
    "s_add_u32 s52, s52, 128\n"
    "buffer_load_dwordx4 v8, s[48:51], s52 offen lds\n"
    "ds_read_b128 v[212:215], v4 offset:7168\n"
    "v_fma_f32 v192, v176, v190, v36\n"
    "v_fma_f32 v193, v176, v191, v44\n"
    "v_mfma_f32_16x16x32_bf16 v[68:71], a[68:71], v[16:19], v[68:71]\n"
    "v_fma_f32 v194, -v177, v191, v192\n"
    "v_mfma_f32_16x16x32_bf16 v[64:67], a[72:75], v[20:23], v[64:67]\n"
    "ds_read_b128 v[16:19], v3 offset:8192\n"
    "v_fma_f32 v195, v177, v190, v193\n"
    "v_fma_f32 v192, v176, v194, v37\n"
    "v_mfma_f32_16x16x32_bf16 v[68:71], a[76:79], v[20:23], v[68:71]\n"
    "v_fma_f32 v193, v176, v195, v45\n"
    "s_waitcnt lgkmcnt(5)\n"
    "v_mfma_f32_16x16x32_bf16 v[64:67], a[80:83], v[24:27], v[64:67]\n"
    "s_add_u32 m0, s53, 5120\n"
    "s_add_u32 s52, s52, 128\n"
    "buffer_load_dwordx4 v8, s[48:51], s52 offen lds\n"
    "ds_read_b128 v[20:23], v4 offset:8192\n"
    "v_fma_f32 v190, -v177, v195, v192\n"
    "v_mfma_f32_16x16x32_bf16 v[68:71], a[84:87], v[24:27], v[68:71]\n"
    "v_fma_f32 v191, v177, v194, v193\n"
    "v_mfma_f32_16x16x32_bf16 v[64:67], a[88:91], v[28:31], v[64:67]\n"
    "ds_read_b128 v[24:27], v3 offset:18432\n"
    "v_fma_f32 v192, v176, v190, v38\n"
    "v_mfma_f32_16x16x32_bf16 v[68:71], a[92:95], v[28:31], v[68:71]\n"
    "s_add_u32 m0, s53, 6144\n"
    "s_add_u32 s52, s52, 128\n"
    "buffer_load_dwordx4 v8, s[48:51], s52 offen lds\n"
    "v_fma_f32 v193, v176, v191, v46\n"
    "s_waitcnt lgkmcnt(5)\n"
    "v_mfma_f32_16x16x32_bf16 v[64:67], a[96:99], v[200:203], v[64:67]\n"
    "ds_read_b128 v[28:31], v4 offset:18432\n"
    "v_fma_f32 v194, -v177, v191, v192\n"
    "v_mfma_f32_16x16x32_bf16 v[68:71], a[100:103], v[200:203], v[68:71]\n"
    "v_fma_f32 v195, v177, v190, v193\n"
    "v_mfma_f32_16x16x32_bf16 v[64:67], a[104:107], v[204:207], v[64:67]\n"
    "ds_read_b128 v[200:203], v3 offset:19456\n"
    "v_fma_f32 v192, v176, v194, v39\n"
    "v_mfma_f32_16x16x32_bf16 v[68:71], a[108:111], v[204:207], v[68:71]\n"
    "s_add_u32 m0, s53, 7168\n"
    "s_add_u32 s52, s52, 128\n"
    "buffer_load_dwordx4 v8, s[48:51], s52 offen lds\n"
    "v_fma_f32 v193, v176, v195, v47\n"
    "s_waitcnt lgkmcnt(5)\n"
    "v_mfma_f32_16x16x32_bf16 v[64:67], a[112:115], v[208:211], v[64:67]\n"
    "ds_read_b128 v[204:207], v4 offset:19456\n"
    "v_fma_f32 v196, -v177, v195, v192\n"
    "v_mfma_f32_16x16x32_bf16 v[68:71], a[116:119], v[208:211], v[68:71]\n"
    "v_fma_f32 v197, v177, v194, v193\n"
    "v_mfma_f32_16x16x32_bf16 v[64:67], a[120:123], v[212:215], v[64:67]\n"
    "ds_read_b128 v[208:211], v3 offset:20480\n"
    "ds_write_b64 v10, v[196:197] offset:0\n"
    "v_mfma_f32_16x16x32_bf16 v[68:71], a[124:127], v[212:215], v[68:71]\n"
    "s_add_u32 m0, s53, 8192\n"
    "s_add_u32 s52, s52, 128\n"
    "buffer_load_dwordx4 v8, s[48:51], s52 offen lds\n"
    "ds_read_b64 v[176:177], v9 offset:256\n"
    "s_waitcnt lgkmcnt(7)\n"
    "v_mfma_f32_16x16x32_bf16 v[64:67], a[128:131], v[16:19], v[64:67]\n"
    "ds_read_b128 v[212:215], v4 offset:20480\n"
    "s_waitcnt lgkmcnt(1)\n"
    "v_mfma_f32_16x16x32_bf16 v[68:71], a[132:135], v[16:19], v[68:71]\n"
    "v_fma_f32 v192, v176, v48, v49\n"
    "v_mfma_f32_16x16x32_bf16 v[64:67], a[136:139], v[20:23], v[64:67]\n"
    "ds_read_b128 v[16:19], v3 offset:21504\n"
    "v_fma_f32 v193, v176, v56, v57\n"
    "v_mfma_f32_16x16x32_bf16 v[68:71], a[140:143], v[20:23], v[68:71]\n"
    "s_mul_i32 s52, s11, 144\n"
    "s_add_u32 s52, s52, 64\n"
    "s_mul_i32 s52, s52, 1152\n"
    "s_mul_i32 s53, s9, 9216\n"
    "s_add_u32 s53, s53, 36864\n"
    "s_add_u32 m0, s53, 0\n"
    "s_nop 0\n"
    "buffer_load_dwordx4 v8, s[48:51], s52 offen lds\n"
    "v_fma_f32 v190, -v177, v56, v192\n"
    "v_mfma_f32_16x16x32_bf16 v[72:75], a[0:3], v[24:27], 0\n"
    "ds_read_b128 v[20:23], v4 offset:21504\n"
    "v_fma_f32 v191, v177, v48, v193\n"
    "v_mfma_f32_16x16x32_bf16 v[76:79], a[4:7], v[24:27], 0\n"
    "v_fma_f32 v192, v176, v190, v50\n"
    "v_mfma_f32_16x16x32_bf16 v[72:75], a[8:11], v[28:31], v[72:75]\n"
    "ds_read_b128 v[24:27], v3 offset:22528\n"
    "v_fma_f32 v193, v176, v191, v58\n"
    "v_mfma_f32_16x16x32_bf16 v[76:79], a[12:15], v[28:31], v[76:79]\n"
    "s_add_u32 m0, s53, 1024\n"
    "s_add_u32 s52, s52, 128\n"
    "buffer_load_dwordx4 v8, s[48:51], s52 offen lds\n"
    "v_fma_f32 v194, -v177, v191, v192\n"
    "v_mfma_f32_16x16x32_bf16 v[72:75], a[16:19], v[200:203], v[72:75]\n"
    "ds_read_b128 v[28:31], v4 offset:22528\n"
    "v_fma_f32 v195, v177, v190, v193\n"
    "v_mfma_f32_16x16x32_bf16 v[76:79], a[20:23], v[200:203], v[76:79]\n"
    "v_fma_f32 v192, v176, v194, v51\n"
    "v_mfma_f32_16x16x32_bf16 v[72:75], a[24:27], v[204:207], v[72:75]\n"
    "ds_read_b128 v[200:203], v3 offset:23552\n"
    "v_fma_f32 v193, v176, v195, v59\n"
    "v_mfma_f32_16x16x32_bf16 v[76:79], a[28:31], v[204:207], v[76:79]\n"
    "s_add_u32 m0, s53, 2048\n"
    "s_add_u32 s52, s52, 128\n"
    "buffer_load_dwordx4 v8, s[48:51], s52 offen lds\n"
    "v_fma_f32 v190, -v177, v195, v192\n"
    "s_waitcnt lgkmcnt(5)\n"
    "v_mfma_f32_16x16x32_bf16 v[72:75], a[32:35], v[208:211], v[72:75]\n"
    "ds_read_b128 v[204:207], v4 offset:23552\n"
    "v_fma_f32 v191, v177, v194, v193\n"
    "v_mfma_f32_16x16x32_bf16 v[76:79], a[36:39], v[208:211], v[76:79]\n"
    "v_fma_f32 v192, v176, v190, v52\n"
    "v_mfma_f32_16x16x32_bf16 v[72:75], a[40:43], v[212:215], v[72:75]\n"
    "s_add_u32 m0, s53, 3072\n"
    "s_add_u32 s52, s52, 128\n"
    "buffer_load_dwordx4 v8, s[48:51], s52 offen lds\n"
    "ds_read_b128 v[208:211], v3 offset:24576\n"
    "v_fma_f32 v193, v176, v191, v60\n"
    "v_mfma_f32_16x16x32_bf16 v[76:79], a[44:47], v[212:215], v[76:79]\n"
    "v_fma_f32 v194, -v177, v191, v192\n"
    "s_waitcnt lgkmcnt(5)\n"
    "v_mfma_f32_16x16x32_bf16 v[72:75], a[48:51], v[16:19], v[72:75]\n"
    "ds_read_b128 v[212:215], v4 offset:24576\n"
    "v_fma_f32 v195, v177, v190, v193\n"
    "v_mfma_f32_16x16x32_bf16 v[76:79], a[52:55], v[16:19], v[76:79]\n"
    "v_fma_f32 v192, v176, v194, v53\n"
    "v_mfma_f32_16x16x32_bf16 v[72:75], a[56:59], v[20:23], v[72:75]\n"
    "s_add_u32 m0, s53, 4096\n"
    "s_add_u32 s52, s52, 128\n"
    "buffer_load_dwordx4 v8, s[48:51], s52 offen lds\n"
    "ds_read_b128 v[16:19], v3 offset:25600\n"
    "v_fma_f32 v193, v176, v195, v61\n"
    "v_mfma_f32_16x16x32_bf16 v[76:79], a[60:63], v[20:23], v[76:79]\n"
    "v_fma_f32 v190, -v177, v195, v192\n"
    "s_waitcnt lgkmcnt(5)\n"
    "v_mfma_f32_16x16x32_bf16 v[72:75], a[64:67], v[24:27], v[72:75]\n"
    "ds_read_b128 v[20:23], v4 offset:25600\n"
    "v_fma_f32 v191, v177, v194, v193\n"
    "v_mfma_f32_16x16x32_bf16 v[76:79], a[68:71], v[24:27], v[76:79]\n"
    "v_fma_f32 v192, v176, v190, v54\n"
    "v_mfma_f32_16x16x32_bf16 v[72:75], a[72:75], v[28:31], v[72:75]\n"
    "s_add_u32 m0, s53, 5120\n"
    "s_add_u32 s52, s52, 128\n"
    "buffer_load_dwordx4 v8, s[48:51], s52 offen lds\n"
    "ds_read_b128 v[24:27], v3 offset:26624\n"
    "v_fma_f32 v193, v176, v191, v62\n"
    "v_mfma_f32_16x16x32_bf16 v[76:79], a[76:79], v[28:31], v[76:79]\n"
    "v_fma_f32 v194, -v177, v191, v192\n"
    "s_waitcnt lgkmcnt(5)\n"
    "v_mfma_f32_16x16x32_bf16 v[72:75], a[80:83], v[200:203], v[72:75]\n"
    "ds_read_b128 v[28:31], v4 offset:26624\n"
    "v_fma_f32 v195, v177, v190, v193\n"
    "v_mfma_f32_16x16x32_bf16 v[76:79], a[84:87], v[200:203], v[76:79]\n"
    "v_fma_f32 v192, v176, v194, v55\n"
    "v_mfma_f32_16x16x32_bf16 v[72:75], a[88:91], v[204:207], v[72:75]\n"
    "s_add_u32 m0, s53, 6144\n"
    "s_add_u32 s52, s52, 128\n"
    "buffer_load_dwordx4 v8, s[48:51], s52 offen lds\n"
    "v_fma_f32 v193, v176, v195, v63\n"
    "v_mfma_f32_16x16x32_bf16 v[76:79], a[92:95], v[204:207], v[76:79]\n"
    "v_fma_f32 v196, -v177, v195, v192\n"
    "s_waitcnt lgkmcnt(4)\n"
    "v_mfma_f32_16x16x32_bf16 v[72:75], a[96:99], v[208:211], v[72:75]\n"
    "v_fma_f32 v197, v177, v194, v193\n"
    "v_mfma_f32_16x16x32_bf16 v[76:79], a[100:103], v[208:211], v[76:79]\n"
    "ds_write_b64 v10, v[196:197] offset:128\n"
    "v_mfma_f32_16x16x32_bf16 v[72:75], a[104:107], v[212:215], v[72:75]\n"
    "s_add_u32 m0, s53, 7168\n"
    "s_add_u32 s52, s52, 128\n"
    "buffer_load_dwordx4 v8, s[48:51], s52 offen lds\n"
    "v_mfma_f32_16x16x32_bf16 v[76:79], a[108:111], v[212:215], v[76:79]\n"
    "s_waitcnt lgkmcnt(3)\n"
    "s_waitcnt lgkmcnt(0)\n"
    "s_waitcnt vmcnt(8)\n"
    "s_barrier\n"
    "v_mfma_f32_16x16x32_bf16 v[72:75], a[112:115], v[16:19], v[72:75]\n"
    "ds_read_b128 v[200:203], v14 offset:0\n"
    "v_mfma_f32_16x16x32_bf16 v[76:79], a[116:119], v[16:19], v[76:79]\n"
    "ds_read_b128 v[204:207], v15 offset:0\n"
    "v_mfma_f32_16x16x32_bf16 v[72:75], a[120:123], v[20:23], v[72:75]\n"
    "s_add_u32 m0, s53, 8192\n"
    "s_add_u32 s52, s52, 128\n"
    "buffer_load_dwordx4 v8, s[48:51], s52 offen lds\n"
    "ds_read_b128 v[208:211], v14 offset:1024\n"
    "v_mfma_f32_16x16x32_bf16 v[76:79], a[124:127], v[20:23], v[76:79]\n"
    "ds_read_b128 v[212:215], v15 offset:1024\n"
    "v_mfma_f32_16x16x32_bf16 v[72:75], a[128:131], v[24:27], v[72:75]\n"
    "ds_read_b128 v[16:19], v14 offset:2048\n"
    "v_mfma_f32_16x16x32_bf16 v[76:79], a[132:135], v[24:27], v[76:79]\n"
    "ds_read_b128 v[20:23], v15 offset:2048\n"
    "v_mfma_f32_16x16x32_bf16 v[72:75], a[136:139], v[28:31], v[72:75]\n"
    "ds_read_b128 v[24:27], v14 offset:3072\n"
    "v_mfma_f32_16x16x32_bf16 v[76:79], a[140:143], v[28:31], v[76:79]\n"
    "s_waitcnt lgkmcnt(5)\n"
    "v_mfma_f32_16x16x32_bf16 v[80:83], a[0:3], v[200:203], 0\n"
    "ds_read_b128 v[28:31], v15 offset:3072\n"
    "v_mfma_f32_16x16x32_bf16 v[84:87], a[4:7], v[200:203], 0\n"
    "s_mul_i32 s52, s11, 144\n"
    "s_add_u32 s52, s52, 80\n"
    "s_mul_i32 s52, s52, 1152\n"
    "s_mul_i32 s53, s9, 9216\n"
    "s_add_u32 s53, s53, 73728\n"
    "s_add_u32 m0, s53, 0\n"
    "s_nop 0\n"
    "buffer_load_dwordx4 v8, s[48:51], s52 offen lds\n"
    "v_mfma_f32_16x16x32_bf16 v[80:83], a[8:11], v[204:207], v[80:83]\n"
    "ds_read_b128 v[200:203], v14 offset:4096\n"
    "v_mfma_f32_16x16x32_bf16 v[84:87], a[12:15], v[204:207], v[84:87]\n"
    "s_waitcnt lgkmcnt(5)\n"
    "v_mfma_f32_16x16x32_bf16 v[80:83], a[16:19], v[208:211], v[80:83]\n"
    "ds_read_b128 v[204:207], v15 offset:4096\n"
    "v_add_u32 v3, 36864, v1\n"
    "v_mfma_f32_16x16x32_bf16 v[84:87], a[20:23], v[208:211], v[84:87]\n"
    "v_mfma_f32_16x16x32_bf16 v[80:83], a[24:27], v[212:215], v[80:83]\n"
    "ds_read_b128 v[208:211], v14 offset:5120\n"
    "v_add_u32 v4, 36864, v2\n"
    "v_mfma_f32_16x16x32_bf16 v[84:87], a[28:31], v[212:215], v[84:87]\n"
    "ds_read_b64 v[176:177], v9 offset:512\n"
    "s_waitcnt lgkmcnt(6)\n"
    "v_mfma_f32_16x16x32_bf16 v[80:83], a[32:35], v[16:19], v[80:83]\n"
    "ds_read_b128 v[212:215], v15 offset:5120\n"
    "s_waitcnt lgkmcnt(1)\n"
    "v_mfma_f32_16x16x32_bf16 v[84:87], a[36:39], v[16:19], v[84:87]\n"
    "s_add_u32 m0, s53, 1024\n"
    "s_add_u32 s52, s52, 128\n"
    "buffer_load_dwordx4 v8, s[48:51], s52 offen lds\n"
    "v_fma_f32 v192, v176, v64, v65\n"
    "v_mfma_f32_16x16x32_bf16 v[80:83], a[40:43], v[20:23], v[80:83]\n"
    "ds_read_b128 v[16:19], v14 offset:6144\n"
    "v_fma_f32 v193, v176, v72, v73\n"
    "v_mfma_f32_16x16x32_bf16 v[84:87], a[44:47], v[20:23], v[84:87]\n"
    "v_fma_f32 v190, -v177, v72, v192\n"
    "v_mfma_f32_16x16x32_bf16 v[80:83], a[48:51], v[24:27], v[80:83]\n"
    "ds_read_b128 v[20:23], v15 offset:6144\n"
    "v_fma_f32 v191, v177, v64, v193\n"
    "v_mfma_f32_16x16x32_bf16 v[84:87], a[52:55], v[24:27], v[84:87]\n"
    "v_fma_f32 v192, v176, v190, v66\n"
    "v_mfma_f32_16x16x32_bf16 v[80:83], a[56:59], v[28:31], v[80:83]\n"
    "ds_read_b128 v[24:27], v14 offset:7168\n"
    "v_fma_f32 v193, v176, v191, v74\n"
    "v_mfma_f32_16x16x32_bf16 v[84:87], a[60:63], v[28:31], v[84:87]\n"
    "v_mfma_f32_16x16x32_bf16 v[80:83], a[64:67], v[200:203], v[80:83]\n"
    "ds_read_b128 v[28:31], v15 offset:7168\n"
    "v_fma_f32 v194, -v177, v191, v192\n"
    "v_mfma_f32_16x16x32_bf16 v[84:87], a[68:71], v[200:203], v[84:87]\n"
    "s_add_u32 m0, s53, 2048\n"
    "s_add_u32 s52, s52, 128\n"
    "buffer_load_dwordx4 v8, s[48:51], s52 offen lds\n"
    "v_mfma_f32_16x16x32_bf16 v[80:83], a[72:75], v[204:207], v[80:83]\n"
    "ds_read_b128 v[200:203], v14 offset:8192\n"
    "v_fma_f32 v195, v177, v190, v193\n"
    "v_mfma_f32_16x16x32_bf16 v[84:87], a[76:79], v[204:207], v[84:87]\n"
    "s_waitcnt lgkmcnt(5)\n"
    "v_mfma_f32_16x16x32_bf16 v[80:83], a[80:83], v[208:211], v[80:83]\n"
    "ds_read_b128 v[204:207], v15 offset:8192\n"
    "v_fma_f32 v192, v176, v194, v67\n"
    "v_mfma_f32_16x16x32_bf16 v[84:87], a[84:87], v[208:211], v[84:87]\n"
    "v_mfma_f32_16x16x32_bf16 v[80:83], a[88:91], v[212:215], v[80:83]\n"
    "ds_read_b128 v[208:211], v14 offset:18432\n"
    "v_fma_f32 v193, v176, v195, v75\n"
    "v_mfma_f32_16x16x32_bf16 v[84:87], a[92:95], v[212:215], v[84:87]\n"
    "s_waitcnt lgkmcnt(5)\n"
    "v_mfma_f32_16x16x32_bf16 v[80:83], a[96:99], v[16:19], v[80:83]\n"
    "ds_read_b128 v[212:215], v15 offset:18432\n"
    "v_fma_f32 v190, -v177, v195, v192\n"
    "v_mfma_f32_16x16x32_bf16 v[84:87], a[100:103], v[16:19], v[84:87]\n"
    "s_add_u32 m0, s53, 3072\n"
    "s_add_u32 s52, s52, 128\n"
    "buffer_load_dwordx4 v8, s[48:51], s52 offen lds\n"
    "v_mfma_f32_16x16x32_bf16 v[80:83], a[104:107], v[20:23], v[80:83]\n"
    "ds_read_b128 v[16:19], v14 offset:19456\n"
    "v_fma_f32 v191, v177, v194, v193\n"
    "v_mfma_f32_16x16x32_bf16 v[84:87], a[108:111], v[20:23], v[84:87]\n"
    "s_waitcnt lgkmcnt(5)\n"
    "v_mfma_f32_16x16x32_bf16 v[80:83], a[112:115], v[24:27], v[80:83]\n"
    "ds_read_b128 v[20:23], v15 offset:19456\n"
    "v_fma_f32 v192, v176, v190, v68\n"
    "v_mfma_f32_16x16x32_bf16 v[84:87], a[116:119], v[24:27], v[84:87]\n"
    "v_mfma_f32_16x16x32_bf16 v[80:83], a[120:123], v[28:31], v[80:83]\n"
    "ds_read_b128 v[24:27], v14 offset:20480\n"
    "v_fma_f32 v193, v176, v191, v76\n"
    "v_mfma_f32_16x16x32_bf16 v[84:87], a[124:127], v[28:31], v[84:87]\n"
    "s_waitcnt lgkmcnt(5)\n"
    "v_mfma_f32_16x16x32_bf16 v[80:83], a[128:131], v[200:203], v[80:83]\n"
    "ds_read_b128 v[28:31], v15 offset:20480\n"
    "v_fma_f32 v194, -v177, v191, v192\n"
    "v_mfma_f32_16x16x32_bf16 v[84:87], a[132:135], v[200:203], v[84:87]\n"
    "s_add_u32 m0, s53, 4096\n"
    "s_add_u32 s52, s52, 128\n"
    "buffer_load_dwordx4 v8, s[48:51], s52 offen lds\n"
    "v_mfma_f32_16x16x32_bf16 v[80:83], a[136:139], v[204:207], v[80:83]\n"
    "ds_read_b128 v[200:203], v14 offset:21504\n"
    "v_fma_f32 v195, v177, v190, v193\n"
    "v_mfma_f32_16x16x32_bf16 v[84:87], a[140:143], v[204:207], v[84:87]\n"
    "s_waitcnt lgkmcnt(5)\n"
    "v_mfma_f32_16x16x32_bf16 v[88:91], a[0:3], v[208:211], 0\n"
    "ds_read_b128 v[204:207], v15 offset:21504\n"
    "v_fma_f32 v192, v176, v194, v69\n"
    "v_mfma_f32_16x16x32_bf16 v[92:95], a[4:7], v[208:211], 0\n"
    "v_mfma_f32_16x16x32_bf16 v[88:91], a[8:11], v[212:215], v[88:91]\n"
    "ds_read_b128 v[208:211], v14 offset:22528\n"
    "v_fma_f32 v193, v176, v195, v77\n"
    "v_mfma_f32_16x16x32_bf16 v[92:95], a[12:15], v[212:215], v[92:95]\n"
    "s_waitcnt lgkmcnt(5)\n"
    "v_mfma_f32_16x16x32_bf16 v[88:91], a[16:19], v[16:19], v[88:91]\n"
    "ds_read_b128 v[212:215], v15 offset:22528\n"
    "v_fma_f32 v190, -v177, v195, v192\n"
    "v_mfma_f32_16x16x32_bf16 v[92:95], a[20:23], v[16:19], v[92:95]\n"
    "s_add_u32 m0, s53, 5120\n"
    "s_add_u32 s52, s52, 128\n"
    "buffer_load_dwordx4 v8, s[48:51], s52 offen lds\n"
    "v_mfma_f32_16x16x32_bf16 v[88:91], a[24:27], v[20:23], v[88:91]\n"
    "ds_read_b128 v[16:19], v14 offset:23552\n"
    "v_fma_f32 v191, v177, v194, v193\n"
    "v_mfma_f32_16x16x32_bf16 v[92:95], a[28:31], v[20:23], v[92:95]\n"
    "s_waitcnt lgkmcnt(5)\n"
    "v_mfma_f32_16x16x32_bf16 v[88:91], a[32:35], v[24:27], v[88:91]\n"
    "ds_read_b128 v[20:23], v15 offset:23552\n"
    "v_fma_f32 v192, v176, v190, v70\n"
    "v_mfma_f32_16x16x32_bf16 v[92:95], a[36:39], v[24:27], v[92:95]\n"
    "v_mfma_f32_16x16x32_bf16 v[88:91], a[40:43], v[28:31], v[88:91]\n"
    "ds_read_b128 v[24:27], v14 offset:24576\n"
    "v_fma_f32 v193, v176, v191, v78\n"
    "v_mfma_f32_16x16x32_bf16 v[92:95], a[44:47], v[28:31], v[92:95]\n"
    "s_waitcnt lgkmcnt(5)\n"
    "v_mfma_f32_16x16x32_bf16 v[88:91], a[48:51], v[200:203], v[88:91]\n"
    "ds_read_b128 v[28:31], v15 offset:24576\n"
    "v_fma_f32 v194, -v177, v191, v192\n"
    "v_mfma_f32_16x16x32_bf16 v[92:95], a[52:55], v[200:203], v[92:95]\n"
    "s_add_u32 m0, s53, 6144\n"
    "s_add_u32 s52, s52, 128\n"
    "buffer_load_dwordx4 v8, s[48:51], s52 offen lds\n"
    "v_mfma_f32_16x16x32_bf16 v[88:91], a[56:59], v[204:207], v[88:91]\n"
    "ds_read_b128 v[200:203], v14 offset:25600\n"
    "v_fma_f32 v195, v177, v190, v193\n"
    "v_mfma_f32_16x16x32_bf16 v[92:95], a[60:63], v[204:207], v[92:95]\n"
    "s_waitcnt lgkmcnt(5)\n"
    "v_mfma_f32_16x16x32_bf16 v[88:91], a[64:67], v[208:211], v[88:91]\n"
    "ds_read_b128 v[204:207], v15 offset:25600\n"
    "v_fma_f32 v192, v176, v194, v71\n"
    "v_mfma_f32_16x16x32_bf16 v[92:95], a[68:71], v[208:211], v[92:95]\n"
    "v_mfma_f32_16x16x32_bf16 v[88:91], a[72:75], v[212:215], v[88:91]\n"
    "ds_read_b128 v[208:211], v14 offset:26624\n"
    "v_fma_f32 v193, v176, v195, v79\n"
    "v_mfma_f32_16x16x32_bf16 v[92:95], a[76:79], v[212:215], v[92:95]\n"
    "s_waitcnt lgkmcnt(5)\n"
    "v_mfma_f32_16x16x32_bf16 v[88:91], a[80:83], v[16:19], v[88:91]\n"
    "ds_read_b128 v[212:215], v15 offset:26624\n"
    "v_fma_f32 v196, -v177, v195, v192\n"
    "v_mfma_f32_16x16x32_bf16 v[92:95], a[84:87], v[16:19], v[92:95]\n"
    "s_add_u32 m0, s53, 7168\n"
    "s_add_u32 s52, s52, 128\n"
    "buffer_load_dwordx4 v8, s[48:51], s52 offen lds\n"
    "v_mfma_f32_16x16x32_bf16 v[88:91], a[88:91], v[20:23], v[88:91]\n"
    "v_fma_f32 v197, v177, v194, v193\n"
    "v_mfma_f32_16x16x32_bf16 v[92:95], a[92:95], v[20:23], v[92:95]\n"
    "s_waitcnt lgkmcnt(4)\n"
    "v_mfma_f32_16x16x32_bf16 v[88:91], a[96:99], v[24:27], v[88:91]\n"
    "ds_write_b64 v10, v[196:197] offset:256\n"
    "v_mfma_f32_16x16x32_bf16 v[92:95], a[100:103], v[24:27], v[92:95]\n"
    "v_mfma_f32_16x16x32_bf16 v[88:91], a[104:107], v[28:31], v[88:91]\n"
    "v_mfma_f32_16x16x32_bf16 v[92:95], a[108:111], v[28:31], v[92:95]\n"
    "s_waitcnt lgkmcnt(3)\n"
    "s_waitcnt lgkmcnt(0)\n"
    "s_waitcnt vmcnt(8)\n"
    "s_barrier\n"
    "v_mfma_f32_16x16x32_bf16 v[88:91], a[112:115], v[200:203], v[88:91]\n"
    "ds_read_b128 v[16:19], v3 offset:0\n"
    "v_mfma_f32_16x16x32_bf16 v[92:95], a[116:119], v[200:203], v[92:95]\n"
    "ds_read_b128 v[20:23], v4 offset:0\n"
    "v_mfma_f32_16x16x32_bf16 v[88:91], a[120:123], v[204:207], v[88:91]\n"
    "s_add_u32 m0, s53, 8192\n"
    "s_add_u32 s52, s52, 128\n"
    "buffer_load_dwordx4 v8, s[48:51], s52 offen lds\n"
    "ds_read_b128 v[24:27], v3 offset:1024\n"
    "v_mfma_f32_16x16x32_bf16 v[92:95], a[124:127], v[204:207], v[92:95]\n"
    "ds_read_b128 v[28:31], v4 offset:1024\n"
    "v_mfma_f32_16x16x32_bf16 v[88:91], a[128:131], v[208:211], v[88:91]\n"
    "ds_read_b128 v[200:203], v3 offset:2048\n"
    "v_mfma_f32_16x16x32_bf16 v[92:95], a[132:135], v[208:211], v[92:95]\n"
    "ds_read_b128 v[204:207], v4 offset:2048\n"
    "v_mfma_f32_16x16x32_bf16 v[88:91], a[136:139], v[212:215], v[88:91]\n"
    "ds_read_b128 v[208:211], v3 offset:3072\n"
    "v_mfma_f32_16x16x32_bf16 v[92:95], a[140:143], v[212:215], v[92:95]\n"
    "s_waitcnt lgkmcnt(5)\n"
    "v_mfma_f32_16x16x32_bf16 v[96:99], a[0:3], v[16:19], 0\n"
    "ds_read_b128 v[212:215], v4 offset:3072\n"
    "v_mfma_f32_16x16x32_bf16 v[100:103], a[4:7], v[16:19], 0\n"
    "s_mul_i32 s52, s11, 144\n"
    "s_add_u32 s52, s52, 96\n"
    "s_mul_i32 s52, s52, 1152\n"
    "s_mul_i32 s53, s9, 9216\n"
    "s_add_u32 s53, s53, 0\n"
    "s_add_u32 m0, s53, 0\n"
    "s_nop 0\n"
    "buffer_load_dwordx4 v8, s[48:51], s52 offen lds\n"
    "v_mfma_f32_16x16x32_bf16 v[96:99], a[8:11], v[20:23], v[96:99]\n"
    "ds_read_b128 v[16:19], v3 offset:4096\n"
    "v_mfma_f32_16x16x32_bf16 v[100:103], a[12:15], v[20:23], v[100:103]\n"
    "s_waitcnt lgkmcnt(5)\n"
    "v_mfma_f32_16x16x32_bf16 v[96:99], a[16:19], v[24:27], v[96:99]\n"
    "ds_read_b128 v[20:23], v4 offset:4096\n"
    "v_add_u32 v14, 73728, v1\n"
    "v_mfma_f32_16x16x32_bf16 v[100:103], a[20:23], v[24:27], v[100:103]\n"
    "v_mfma_f32_16x16x32_bf16 v[96:99], a[24:27], v[28:31], v[96:99]\n"
    "ds_read_b128 v[24:27], v3 offset:5120\n"
    "v_add_u32 v15, 73728, v2\n"
    "v_mfma_f32_16x16x32_bf16 v[100:103], a[28:31], v[28:31], v[100:103]\n"
    "ds_read_b64 v[176:177], v9 offset:768\n"
    "s_waitcnt lgkmcnt(6)\n"
    "v_mfma_f32_16x16x32_bf16 v[96:99], a[32:35], v[200:203], v[96:99]\n"
    "ds_read_b128 v[28:31], v4 offset:5120\n"
    "s_waitcnt lgkmcnt(1)\n"
    "v_mfma_f32_16x16x32_bf16 v[100:103], a[36:39], v[200:203], v[100:103]\n"
    "s_add_u32 m0, s53, 1024\n"
    "s_add_u32 s52, s52, 128\n"
    "buffer_load_dwordx4 v8, s[48:51], s52 offen lds\n"
    "v_fma_f32 v192, v176, v80, v81\n"
    "v_mfma_f32_16x16x32_bf16 v[96:99], a[40:43], v[204:207], v[96:99]\n"
    "ds_read_b128 v[200:203], v3 offset:6144\n"
    "v_fma_f32 v193, v176, v88, v89\n"
    "v_mfma_f32_16x16x32_bf16 v[100:103], a[44:47], v[204:207], v[100:103]\n"
    "v_fma_f32 v190, -v177, v88, v192\n"
    "v_mfma_f32_16x16x32_bf16 v[96:99], a[48:51], v[208:211], v[96:99]\n"
    "ds_read_b128 v[204:207], v4 offset:6144\n"
    "v_fma_f32 v191, v177, v80, v193\n"
    "v_mfma_f32_16x16x32_bf16 v[100:103], a[52:55], v[208:211], v[100:103]\n"
    "v_fma_f32 v192, v176, v190, v82\n"
    "v_mfma_f32_16x16x32_bf16 v[96:99], a[56:59], v[212:215], v[96:99]\n"
    "ds_read_b128 v[208:211], v3 offset:7168\n"
    "v_fma_f32 v193, v176, v191, v90\n"
    "v_mfma_f32_16x16x32_bf16 v[100:103], a[60:63], v[212:215], v[100:103]\n"
    "v_mfma_f32_16x16x32_bf16 v[96:99], a[64:67], v[16:19], v[96:99]\n"
    "ds_read_b128 v[212:215], v4 offset:7168\n"
    "v_fma_f32 v194, -v177, v191, v192\n"
    "v_mfma_f32_16x16x32_bf16 v[100:103], a[68:71], v[16:19], v[100:103]\n"
    "s_add_u32 m0, s53, 2048\n"
    "s_add_u32 s52, s52, 128\n"
    "buffer_load_dwordx4 v8, s[48:51], s52 offen lds\n"
    "v_mfma_f32_16x16x32_bf16 v[96:99], a[72:75], v[20:23], v[96:99]\n"
    "ds_read_b128 v[16:19], v3 offset:8192\n"
    "v_fma_f32 v195, v177, v190, v193\n"
    "v_mfma_f32_16x16x32_bf16 v[100:103], a[76:79], v[20:23], v[100:103]\n"
    "s_waitcnt lgkmcnt(5)\n"
    "v_mfma_f32_16x16x32_bf16 v[96:99], a[80:83], v[24:27], v[96:99]\n"
    "ds_read_b128 v[20:23], v4 offset:8192\n"
    "v_fma_f32 v192, v176, v194, v83\n"
    "v_mfma_f32_16x16x32_bf16 v[100:103], a[84:87], v[24:27], v[100:103]\n"
    "v_mfma_f32_16x16x32_bf16 v[96:99], a[88:91], v[28:31], v[96:99]\n"
    "ds_read_b128 v[24:27], v3 offset:18432\n"
    "v_fma_f32 v193, v176, v195, v91\n"
    "v_mfma_f32_16x16x32_bf16 v[100:103], a[92:95], v[28:31], v[100:103]\n"
    "s_waitcnt lgkmcnt(5)\n"
    "v_mfma_f32_16x16x32_bf16 v[96:99], a[96:99], v[200:203], v[96:99]\n"
    "ds_read_b128 v[28:31], v4 offset:18432\n"
    "v_fma_f32 v190, -v177, v195, v192\n"
    "v_mfma_f32_16x16x32_bf16 v[100:103], a[100:103], v[200:203], v[100:103]\n"
    "s_add_u32 m0, s53, 3072\n"
    "s_add_u32 s52, s52, 128\n"
    "buffer_load_dwordx4 v8, s[48:51], s52 offen lds\n"
    "v_mfma_f32_16x16x32_bf16 v[96:99], a[104:107], v[204:207], v[96:99]\n"
    "ds_read_b128 v[200:203], v3 offset:19456\n"
    "v_fma_f32 v191, v177, v194, v193\n"
    "v_mfma_f32_16x16x32_bf16 v[100:103], a[108:111], v[204:207], v[100:103]\n"
    "s_waitcnt lgkmcnt(5)\n"
    "v_mfma_f32_16x16x32_bf16 v[96:99], a[112:115], v[208:211], v[96:99]\n"
    "ds_read_b128 v[204:207], v4 offset:19456\n"
    "v_fma_f32 v192, v176, v190, v84\n"
    "v_mfma_f32_16x16x32_bf16 v[100:103], a[116:119], v[208:211], v[100:103]\n"
    "v_mfma_f32_16x16x32_bf16 v[96:99], a[120:123], v[212:215], v[96:99]\n"
    "ds_read_b128 v[208:211], v3 offset:20480\n"
    "v_fma_f32 v193, v176, v191, v92\n"
    "v_mfma_f32_16x16x32_bf16 v[100:103], a[124:127], v[212:215], v[100:103]\n"
    "s_waitcnt lgkmcnt(5)\n"
    "v_mfma_f32_16x16x32_bf16 v[96:99], a[128:131], v[16:19], v[96:99]\n"
    "ds_read_b128 v[212:215], v4 offset:20480\n"
    "v_fma_f32 v194, -v177, v191, v192\n"
    "v_mfma_f32_16x16x32_bf16 v[100:103], a[132:135], v[16:19], v[100:103]\n"
    "s_add_u32 m0, s53, 4096\n"
    "s_add_u32 s52, s52, 128\n"
    "buffer_load_dwordx4 v8, s[48:51], s52 offen lds\n"
    "v_mfma_f32_16x16x32_bf16 v[96:99], a[136:139], v[20:23], v[96:99]\n"
    "ds_read_b128 v[16:19], v3 offset:21504\n"
    "v_fma_f32 v195, v177, v190, v193\n"
    "v_mfma_f32_16x16x32_bf16 v[100:103], a[140:143], v[20:23], v[100:103]\n"
    "s_waitcnt lgkmcnt(5)\n"
    "v_mfma_f32_16x16x32_bf16 v[104:107], a[0:3], v[24:27], 0\n"
    "ds_read_b128 v[20:23], v4 offset:21504\n"
    "v_fma_f32 v192, v176, v194, v85\n"
    "v_mfma_f32_16x16x32_bf16 v[108:111], a[4:7], v[24:27], 0\n"
    "v_mfma_f32_16x16x32_bf16 v[104:107], a[8:11], v[28:31], v[104:107]\n"
    "ds_read_b128 v[24:27], v3 offset:22528\n"
    "v_fma_f32 v193, v176, v195, v93\n"
    "v_mfma_f32_16x16x32_bf16 v[108:111], a[12:15], v[28:31], v[108:111]\n"
    "s_waitcnt lgkmcnt(5)\n"
    "v_mfma_f32_16x16x32_bf16 v[104:107], a[16:19], v[200:203], v[104:107]\n"
    "ds_read_b128 v[28:31], v4 offset:22528\n"
    "v_fma_f32 v190, -v177, v195, v192\n"
    "v_mfma_f32_16x16x32_bf16 v[108:111], a[20:23], v[200:203], v[108:111]\n"
    "s_add_u32 m0, s53, 5120\n"
    "s_add_u32 s52, s52, 128\n"
    "buffer_load_dwordx4 v8, s[48:51], s52 offen lds\n"
    "v_mfma_f32_16x16x32_bf16 v[104:107], a[24:27], v[204:207], v[104:107]\n"
    "ds_read_b128 v[200:203], v3 offset:23552\n"
    "v_fma_f32 v191, v177, v194, v193\n"
    "v_mfma_f32_16x16x32_bf16 v[108:111], a[28:31], v[204:207], v[108:111]\n"
    "s_waitcnt lgkmcnt(5)\n"
    "v_mfma_f32_16x16x32_bf16 v[104:107], a[32:35], v[208:211], v[104:107]\n"
    "ds_read_b128 v[204:207], v4 offset:23552\n"
    "v_fma_f32 v192, v176, v190, v86\n"
    "v_mfma_f32_16x16x32_bf16 v[108:111], a[36:39], v[208:211], v[108:111]\n"
    "v_mfma_f32_16x16x32_bf16 v[104:107], a[40:43], v[212:215], v[104:107]\n"
    "ds_read_b128 v[208:211], v3 offset:24576\n"
    "v_fma_f32 v193, v176, v191, v94\n"
    "v_mfma_f32_16x16x32_bf16 v[108:111], a[44:47], v[212:215], v[108:111]\n"
    "s_waitcnt lgkmcnt(5)\n"
    "v_mfma_f32_16x16x32_bf16 v[104:107], a[48:51], v[16:19], v[104:107]\n"
    "ds_read_b128 v[212:215], v4 offset:24576\n"
    "v_fma_f32 v194, -v177, v191, v192\n"
    "v_mfma_f32_16x16x32_bf16 v[108:111], a[52:55], v[16:19], v[108:111]\n"
    "s_add_u32 m0, s53, 6144\n"
    "s_add_u32 s52, s52, 128\n"
    "buffer_load_dwordx4 v8, s[48:51], s52 offen lds\n"
    "v_mfma_f32_16x16x32_bf16 v[104:107], a[56:59], v[20:23], v[104:107]\n"
    "ds_read_b128 v[16:19], v3 offset:25600\n"
    "v_fma_f32 v195, v177, v190, v193\n"
    "v_mfma_f32_16x16x32_bf16 v[108:111], a[60:63], v[20:23], v[108:111]\n"
    "s_waitcnt lgkmcnt(5)\n"
    "v_mfma_f32_16x16x32_bf16 v[104:107], a[64:67], v[24:27], v[104:107]\n"
    "ds_read_b128 v[20:23], v4 offset:25600\n"
    "v_fma_f32 v192, v176, v194, v87\n"
    "v_mfma_f32_16x16x32_bf16 v[108:111], a[68:71], v[24:27], v[108:111]\n"
    "v_mfma_f32_16x16x32_bf16 v[104:107], a[72:75], v[28:31], v[104:107]\n"
    "ds_read_b128 v[24:27], v3 offset:26624\n"
    "v_fma_f32 v193, v176, v195, v95\n"
    "v_mfma_f32_16x16x32_bf16 v[108:111], a[76:79], v[28:31], v[108:111]\n"
    "s_waitcnt lgkmcnt(5)\n"
    "v_mfma_f32_16x16x32_bf16 v[104:107], a[80:83], v[200:203], v[104:107]\n"
    "ds_read_b128 v[28:31], v4 offset:26624\n"
    "v_fma_f32 v196, -v177, v195, v192\n"
    "v_mfma_f32_16x16x32_bf16 v[108:111], a[84:87], v[200:203], v[108:111]\n"
    "s_add_u32 m0, s53, 7168\n"
    "s_add_u32 s52, s52, 128\n"
    "buffer_load_dwordx4 v8, s[48:51], s52 offen lds\n"
    "v_mfma_f32_16x16x32_bf16 v[104:107], a[88:91], v[204:207], v[104:107]\n"
    "v_fma_f32 v197, v177, v194, v193\n"
    "v_mfma_f32_16x16x32_bf16 v[108:111], a[92:95], v[204:207], v[108:111]\n"
    "s_waitcnt lgkmcnt(4)\n"
    "v_mfma_f32_16x16x32_bf16 v[104:107], a[96:99], v[208:211], v[104:107]\n"
    "ds_write_b64 v10, v[196:197] offset:384\n"
    "v_mfma_f32_16x16x32_bf16 v[108:111], a[100:103], v[208:211], v[108:111]\n"
    "v_mfma_f32_16x16x32_bf16 v[104:107], a[104:107], v[212:215], v[104:107]\n"
    "v_mfma_f32_16x16x32_bf16 v[108:111], a[108:111], v[212:215], v[108:111]\n"
    "s_waitcnt lgkmcnt(3)\n"
    "s_waitcnt lgkmcnt(0)\n"
    "s_waitcnt vmcnt(8)\n"
    "s_barrier\n"
    "v_mfma_f32_16x16x32_bf16 v[104:107], a[112:115], v[16:19], v[104:107]\n"
    "ds_read_b128 v[200:203], v14 offset:0\n"
    "v_mfma_f32_16x16x32_bf16 v[108:111], a[116:119], v[16:19], v[108:111]\n"
    "ds_read_b128 v[204:207], v15 offset:0\n"
    "v_mfma_f32_16x16x32_bf16 v[104:107], a[120:123], v[20:23], v[104:107]\n"
    "s_add_u32 m0, s53, 8192\n"
    "s_add_u32 s52, s52, 128\n"
    "buffer_load_dwordx4 v8, s[48:51], s52 offen lds\n"
    "ds_read_b128 v[208:211], v14 offset:1024\n"
    "v_mfma_f32_16x16x32_bf16 v[108:111], a[124:127], v[20:23], v[108:111]\n"
    "ds_read_b128 v[212:215], v15 offset:1024\n"
    "v_mfma_f32_16x16x32_bf16 v[104:107], a[128:131], v[24:27], v[104:107]\n"
    "ds_read_b128 v[16:19], v14 offset:2048\n"
    "v_mfma_f32_16x16x32_bf16 v[108:111], a[132:135], v[24:27], v[108:111]\n"
    "ds_read_b128 v[20:23], v15 offset:2048\n"
    "v_mfma_f32_16x16x32_bf16 v[104:107], a[136:139], v[28:31], v[104:107]\n"
    "ds_read_b128 v[24:27], v14 offset:3072\n"
    "v_mfma_f32_16x16x32_bf16 v[108:111], a[140:143], v[28:31], v[108:111]\n"
    "s_waitcnt lgkmcnt(5)\n"
    "v_mfma_f32_16x16x32_bf16 v[112:115], a[0:3], v[200:203], 0\n"
    "ds_read_b128 v[28:31], v15 offset:3072\n"
    "v_mfma_f32_16x16x32_bf16 v[116:119], a[4:7], v[200:203], 0\n"
    "s_mul_i32 s52, s11, 144\n"
    "s_add_u32 s52, s52, 112\n"
    "s_mul_i32 s52, s52, 1152\n"
    "s_mul_i32 s53, s9, 9216\n"
    "s_add_u32 s53, s53, 36864\n"
    "s_add_u32 m0, s53, 0\n"
    "s_nop 0\n"
    "buffer_load_dwordx4 v8, s[48:51], s52 offen lds\n"
    "v_mfma_f32_16x16x32_bf16 v[112:115], a[8:11], v[204:207], v[112:115]\n"
    "ds_read_b128 v[200:203], v14 offset:4096\n"
    "v_mfma_f32_16x16x32_bf16 v[116:119], a[12:15], v[204:207], v[116:119]\n"
    "s_waitcnt lgkmcnt(5)\n"
    "v_mfma_f32_16x16x32_bf16 v[112:115], a[16:19], v[208:211], v[112:115]\n"
    "ds_read_b128 v[204:207], v15 offset:4096\n"
    "v_add_u32 v3, 0, v1\n"
    "v_mfma_f32_16x16x32_bf16 v[116:119], a[20:23], v[208:211], v[116:119]\n"
    "v_mfma_f32_16x16x32_bf16 v[112:115], a[24:27], v[212:215], v[112:115]\n"
    "ds_read_b128 v[208:211], v14 offset:5120\n"
    "v_add_u32 v4, 0, v2\n"
    "v_mfma_f32_16x16x32_bf16 v[116:119], a[28:31], v[212:215], v[116:119]\n"
    "ds_read_b64 v[176:177], v9 offset:1024\n"
    "s_waitcnt lgkmcnt(6)\n"
    "v_mfma_f32_16x16x32_bf16 v[112:115], a[32:35], v[16:19], v[112:115]\n"
    "ds_read_b128 v[212:215], v15 offset:5120\n"
    "s_waitcnt lgkmcnt(1)\n"
    "v_mfma_f32_16x16x32_bf16 v[116:119], a[36:39], v[16:19], v[116:119]\n"
    "s_add_u32 m0, s53, 1024\n"
    "s_add_u32 s52, s52, 128\n"
    "buffer_load_dwordx4 v8, s[48:51], s52 offen lds\n"
    "v_fma_f32 v192, v176, v96, v97\n"
    "v_mfma_f32_16x16x32_bf16 v[112:115], a[40:43], v[20:23], v[112:115]\n"
    "ds_read_b128 v[16:19], v14 offset:6144\n"
    "v_fma_f32 v193, v176, v104, v105\n"
    "v_mfma_f32_16x16x32_bf16 v[116:119], a[44:47], v[20:23], v[116:119]\n"
    "v_fma_f32 v190, -v177, v104, v192\n"
    "v_mfma_f32_16x16x32_bf16 v[112:115], a[48:51], v[24:27], v[112:115]\n"
    "ds_read_b128 v[20:23], v15 offset:6144\n"
    "v_fma_f32 v191, v177, v96, v193\n"
    "v_mfma_f32_16x16x32_bf16 v[116:119], a[52:55], v[24:27], v[116:119]\n"
    "v_fma_f32 v192, v176, v190, v98\n"
    "v_mfma_f32_16x16x32_bf16 v[112:115], a[56:59], v[28:31], v[112:115]\n"
    "ds_read_b128 v[24:27], v14 offset:7168\n"
    "v_fma_f32 v193, v176, v191, v106\n"
    "v_mfma_f32_16x16x32_bf16 v[116:119], a[60:63], v[28:31], v[116:119]\n"
    "v_mfma_f32_16x16x32_bf16 v[112:115], a[64:67], v[200:203], v[112:115]\n"
    "ds_read_b128 v[28:31], v15 offset:7168\n"
    "v_fma_f32 v194, -v177, v191, v192\n"
    "v_mfma_f32_16x16x32_bf16 v[116:119], a[68:71], v[200:203], v[116:119]\n"
    "s_add_u32 m0, s53, 2048\n"
    "s_add_u32 s52, s52, 128\n"
    "buffer_load_dwordx4 v8, s[48:51], s52 offen lds\n"
    "v_mfma_f32_16x16x32_bf16 v[112:115], a[72:75], v[204:207], v[112:115]\n"
    "ds_read_b128 v[200:203], v14 offset:8192\n"
    "v_fma_f32 v195, v177, v190, v193\n"
    "v_mfma_f32_16x16x32_bf16 v[116:119], a[76:79], v[204:207], v[116:119]\n"
    "s_waitcnt lgkmcnt(5)\n"
    "v_mfma_f32_16x16x32_bf16 v[112:115], a[80:83], v[208:211], v[112:115]\n"
    "ds_read_b128 v[204:207], v15 offset:8192\n"
    "v_fma_f32 v192, v176, v194, v99\n"
    "v_mfma_f32_16x16x32_bf16 v[116:119], a[84:87], v[208:211], v[116:119]\n"
    "v_mfma_f32_16x16x32_bf16 v[112:115], a[88:91], v[212:215], v[112:115]\n"
    "ds_read_b128 v[208:211], v14 offset:18432\n"
    "v_fma_f32 v193, v176, v195, v107\n"
    "v_mfma_f32_16x16x32_bf16 v[116:119], a[92:95], v[212:215], v[116:119]\n"
    "s_waitcnt lgkmcnt(5)\n"
    "v_mfma_f32_16x16x32_bf16 v[112:115], a[96:99], v[16:19], v[112:115]\n"
    "ds_read_b128 v[212:215], v15 offset:18432\n"
    "v_fma_f32 v190, -v177, v195, v192\n"
    "v_mfma_f32_16x16x32_bf16 v[116:119], a[100:103], v[16:19], v[116:119]\n"
    "s_add_u32 m0, s53, 3072\n"
    "s_add_u32 s52, s52, 128\n"
    "buffer_load_dwordx4 v8, s[48:51], s52 offen lds\n"
    "v_mfma_f32_16x16x32_bf16 v[112:115], a[104:107], v[20:23], v[112:115]\n"
    "ds_read_b128 v[16:19], v14 offset:19456\n"
    "v_fma_f32 v191, v177, v194, v193\n"
    "v_mfma_f32_16x16x32_bf16 v[116:119], a[108:111], v[20:23], v[116:119]\n"
    "s_waitcnt lgkmcnt(5)\n"
    "v_mfma_f32_16x16x32_bf16 v[112:115], a[112:115], v[24:27], v[112:115]\n"
    "ds_read_b128 v[20:23], v15 offset:19456\n"
    "v_fma_f32 v192, v176, v190, v100\n"
    "v_mfma_f32_16x16x32_bf16 v[116:119], a[116:119], v[24:27], v[116:119]\n"
    "v_mfma_f32_16x16x32_bf16 v[112:115], a[120:123], v[28:31], v[112:115]\n"
    "ds_read_b128 v[24:27], v14 offset:20480\n"
    "v_fma_f32 v193, v176, v191, v108\n"
    "v_mfma_f32_16x16x32_bf16 v[116:119], a[124:127], v[28:31], v[116:119]\n"
    "s_waitcnt lgkmcnt(5)\n"
    "v_mfma_f32_16x16x32_bf16 v[112:115], a[128:131], v[200:203], v[112:115]\n"
    "ds_read_b128 v[28:31], v15 offset:20480\n"
    "v_fma_f32 v194, -v177, v191, v192\n"
    "v_mfma_f32_16x16x32_bf16 v[116:119], a[132:135], v[200:203], v[116:119]\n"
    "s_add_u32 m0, s53, 4096\n"
    "s_add_u32 s52, s52, 128\n"
    "buffer_load_dwordx4 v8, s[48:51], s52 offen lds\n"
    "v_mfma_f32_16x16x32_bf16 v[112:115], a[136:139], v[204:207], v[112:115]\n"
    "ds_read_b128 v[200:203], v14 offset:21504\n"
    "v_fma_f32 v195, v177, v190, v193\n"
    "v_mfma_f32_16x16x32_bf16 v[116:119], a[140:143], v[204:207], v[116:119]\n"
    "s_waitcnt lgkmcnt(5)\n"
    "v_mfma_f32_16x16x32_bf16 v[120:123], a[0:3], v[208:211], 0\n"
    "ds_read_b128 v[204:207], v15 offset:21504\n"
    "v_fma_f32 v192, v176, v194, v101\n"
    "v_mfma_f32_16x16x32_bf16 v[124:127], a[4:7], v[208:211], 0\n"
    "v_mfma_f32_16x16x32_bf16 v[120:123], a[8:11], v[212:215], v[120:123]\n"
    "ds_read_b128 v[208:211], v14 offset:22528\n"
    "v_fma_f32 v193, v176, v195, v109\n"
    "v_mfma_f32_16x16x32_bf16 v[124:127], a[12:15], v[212:215], v[124:127]\n"
    "s_waitcnt lgkmcnt(5)\n"
    "v_mfma_f32_16x16x32_bf16 v[120:123], a[16:19], v[16:19], v[120:123]\n"
    "ds_read_b128 v[212:215], v15 offset:22528\n"
    "v_fma_f32 v190, -v177, v195, v192\n"
    "v_mfma_f32_16x16x32_bf16 v[124:127], a[20:23], v[16:19], v[124:127]\n"
    "s_add_u32 m0, s53, 5120\n"
    "s_add_u32 s52, s52, 128\n"
    "buffer_load_dwordx4 v8, s[48:51], s52 offen lds\n"
    "v_mfma_f32_16x16x32_bf16 v[120:123], a[24:27], v[20:23], v[120:123]\n"
    "ds_read_b128 v[16:19], v14 offset:23552\n"
    "v_fma_f32 v191, v177, v194, v193\n"
    "v_mfma_f32_16x16x32_bf16 v[124:127], a[28:31], v[20:23], v[124:127]\n"
    "s_waitcnt lgkmcnt(5)\n"
    "v_mfma_f32_16x16x32_bf16 v[120:123], a[32:35], v[24:27], v[120:123]\n"
    "ds_read_b128 v[20:23], v15 offset:23552\n"
    "v_fma_f32 v192, v176, v190, v102\n"
    "v_mfma_f32_16x16x32_bf16 v[124:127], a[36:39], v[24:27], v[124:127]\n"
    "v_mfma_f32_16x16x32_bf16 v[120:123], a[40:43], v[28:31], v[120:123]\n"
    "ds_read_b128 v[24:27], v14 offset:24576\n"
    "v_fma_f32 v193, v176, v191, v110\n"
    "v_mfma_f32_16x16x32_bf16 v[124:127], a[44:47], v[28:31], v[124:127]\n"
    "s_waitcnt lgkmcnt(5)\n"
    "v_mfma_f32_16x16x32_bf16 v[120:123], a[48:51], v[200:203], v[120:123]\n"
    "ds_read_b128 v[28:31], v15 offset:24576\n"
    "v_fma_f32 v194, -v177, v191, v192\n"
    "v_mfma_f32_16x16x32_bf16 v[124:127], a[52:55], v[200:203], v[124:127]\n"
    "s_add_u32 m0, s53, 6144\n"
    "s_add_u32 s52, s52, 128\n"
    "buffer_load_dwordx4 v8, s[48:51], s52 offen lds\n"
    "v_mfma_f32_16x16x32_bf16 v[120:123], a[56:59], v[204:207], v[120:123]\n"
    "ds_read_b128 v[200:203], v14 offset:25600\n"
    "v_fma_f32 v195, v177, v190, v193\n"
    "v_mfma_f32_16x16x32_bf16 v[124:127], a[60:63], v[204:207], v[124:127]\n"
    "s_waitcnt lgkmcnt(5)\n"
    "v_mfma_f32_16x16x32_bf16 v[120:123], a[64:67], v[208:211], v[120:123]\n"
    "ds_read_b128 v[204:207], v15 offset:25600\n"
    "v_fma_f32 v192, v176, v194, v103\n"
    "v_mfma_f32_16x16x32_bf16 v[124:127], a[68:71], v[208:211], v[124:127]\n"
    "v_mfma_f32_16x16x32_bf16 v[120:123], a[72:75], v[212:215], v[120:123]\n"
    "ds_read_b128 v[208:211], v14 offset:26624\n"
    "v_fma_f32 v193, v176, v195, v111\n"
    "v_mfma_f32_16x16x32_bf16 v[124:127], a[76:79], v[212:215], v[124:127]\n"
    "s_waitcnt lgkmcnt(5)\n"
    "v_mfma_f32_16x16x32_bf16 v[120:123], a[80:83], v[16:19], v[120:123]\n"
    "ds_read_b128 v[212:215], v15 offset:26624\n"
    "v_fma_f32 v196, -v177, v195, v192\n"
    "v_mfma_f32_16x16x32_bf16 v[124:127], a[84:87], v[16:19], v[124:127]\n"
    "s_add_u32 m0, s53, 7168\n"
    "s_add_u32 s52, s52, 128\n"
    "buffer_load_dwordx4 v8, s[48:51], s52 offen lds\n"
    "v_mfma_f32_16x16x32_bf16 v[120:123], a[88:91], v[20:23], v[120:123]\n"
    "v_fma_f32 v197, v177, v194, v193\n"
    "v_mfma_f32_16x16x32_bf16 v[124:127], a[92:95], v[20:23], v[124:127]\n"
    "s_waitcnt lgkmcnt(4)\n"
    "v_mfma_f32_16x16x32_bf16 v[120:123], a[96:99], v[24:27], v[120:123]\n"
    "ds_write_b64 v10, v[196:197] offset:512\n"
    "v_mfma_f32_16x16x32_bf16 v[124:127], a[100:103], v[24:27], v[124:127]\n"
    "v_mfma_f32_16x16x32_bf16 v[120:123], a[104:107], v[28:31], v[120:123]\n"
    "v_mfma_f32_16x16x32_bf16 v[124:127], a[108:111], v[28:31], v[124:127]\n"
    "s_waitcnt lgkmcnt(3)\n"
    "s_waitcnt lgkmcnt(0)\n"
    "s_waitcnt vmcnt(8)\n"
    "s_barrier\n"
    "v_mfma_f32_16x16x32_bf16 v[120:123], a[112:115], v[200:203], v[120:123]\n"
    "ds_read_b128 v[16:19], v3 offset:0\n"
    "v_mfma_f32_16x16x32_bf16 v[124:127], a[116:119], v[200:203], v[124:127]\n"
    "ds_read_b128 v[20:23], v4 offset:0\n"
    "v_mfma_f32_16x16x32_bf16 v[120:123], a[120:123], v[204:207], v[120:123]\n"
    "s_add_u32 m0, s53, 8192\n"
    "s_add_u32 s52, s52, 128\n"
    "buffer_load_dwordx4 v8, s[48:51], s52 offen lds\n"
    "ds_read_b128 v[24:27], v3 offset:1024\n"
    "v_mfma_f32_16x16x32_bf16 v[124:127], a[124:127], v[204:207], v[124:127]\n"
    "ds_read_b128 v[28:31], v4 offset:1024\n"
    "v_mfma_f32_16x16x32_bf16 v[120:123], a[128:131], v[208:211], v[120:123]\n"
    "ds_read_b128 v[200:203], v3 offset:2048\n"
    "v_mfma_f32_16x16x32_bf16 v[124:127], a[132:135], v[208:211], v[124:127]\n"
    "ds_read_b128 v[204:207], v4 offset:2048\n"
    "v_mfma_f32_16x16x32_bf16 v[120:123], a[136:139], v[212:215], v[120:123]\n"
    "ds_read_b128 v[208:211], v3 offset:3072\n"
    "v_mfma_f32_16x16x32_bf16 v[124:127], a[140:143], v[212:215], v[124:127]\n"
    "s_waitcnt lgkmcnt(5)\n"
    "v_mfma_f32_16x16x32_bf16 v[128:131], a[0:3], v[16:19], 0\n"
    "ds_read_b128 v[212:215], v4 offset:3072\n"
    "v_mfma_f32_16x16x32_bf16 v[132:135], a[4:7], v[16:19], 0\n"
    "s_mul_i32 s52, s11, 144\n"
    "s_add_u32 s52, s52, 128\n"
    "s_mul_i32 s52, s52, 1152\n"
    "s_mul_i32 s53, s9, 9216\n"
    "s_add_u32 s53, s53, 73728\n"
    "s_add_u32 m0, s53, 0\n"
    "s_nop 0\n"
    "buffer_load_dwordx4 v8, s[48:51], s52 offen lds\n"
    "v_mfma_f32_16x16x32_bf16 v[128:131], a[8:11], v[20:23], v[128:131]\n"
    "ds_read_b128 v[16:19], v3 offset:4096\n"
    "v_mfma_f32_16x16x32_bf16 v[132:135], a[12:15], v[20:23], v[132:135]\n"
    "s_waitcnt lgkmcnt(5)\n"
    "v_mfma_f32_16x16x32_bf16 v[128:131], a[16:19], v[24:27], v[128:131]\n"
    "ds_read_b128 v[20:23], v4 offset:4096\n"
    "v_add_u32 v14, 36864, v1\n"
    "v_mfma_f32_16x16x32_bf16 v[132:135], a[20:23], v[24:27], v[132:135]\n"
    "v_mfma_f32_16x16x32_bf16 v[128:131], a[24:27], v[28:31], v[128:131]\n"
    "ds_read_b128 v[24:27], v3 offset:5120\n"
    "v_add_u32 v15, 36864, v2\n"
    "v_mfma_f32_16x16x32_bf16 v[132:135], a[28:31], v[28:31], v[132:135]\n"
    "ds_read_b64 v[176:177], v9 offset:1280\n"
    "s_waitcnt lgkmcnt(6)\n"
    "v_mfma_f32_16x16x32_bf16 v[128:131], a[32:35], v[200:203], v[128:131]\n"
    "ds_read_b128 v[28:31], v4 offset:5120\n"
    "s_waitcnt lgkmcnt(1)\n"
    "v_mfma_f32_16x16x32_bf16 v[132:135], a[36:39], v[200:203], v[132:135]\n"
    "s_add_u32 m0, s53, 1024\n"
    "s_add_u32 s52, s52, 128\n"
    "buffer_load_dwordx4 v8, s[48:51], s52 offen lds\n"
    "v_fma_f32 v192, v176, v112, v113\n"
    "v_mfma_f32_16x16x32_bf16 v[128:131], a[40:43], v[204:207], v[128:131]\n"
    "ds_read_b128 v[200:203], v3 offset:6144\n"
    "v_fma_f32 v193, v176, v120, v121\n"
    "v_mfma_f32_16x16x32_bf16 v[132:135], a[44:47], v[204:207], v[132:135]\n"
    "v_fma_f32 v190, -v177, v120, v192\n"
    "v_mfma_f32_16x16x32_bf16 v[128:131], a[48:51], v[208:211], v[128:131]\n"
    "ds_read_b128 v[204:207], v4 offset:6144\n"
    "v_fma_f32 v191, v177, v112, v193\n"
    "v_mfma_f32_16x16x32_bf16 v[132:135], a[52:55], v[208:211], v[132:135]\n"
    "v_fma_f32 v192, v176, v190, v114\n"
    "v_mfma_f32_16x16x32_bf16 v[128:131], a[56:59], v[212:215], v[128:131]\n"
    "ds_read_b128 v[208:211], v3 offset:7168\n"
    "v_fma_f32 v193, v176, v191, v122\n"
    "v_mfma_f32_16x16x32_bf16 v[132:135], a[60:63], v[212:215], v[132:135]\n"
    "v_mfma_f32_16x16x32_bf16 v[128:131], a[64:67], v[16:19], v[128:131]\n"
    "ds_read_b128 v[212:215], v4 offset:7168\n"
    "v_fma_f32 v194, -v177, v191, v192\n"
    "v_mfma_f32_16x16x32_bf16 v[132:135], a[68:71], v[16:19], v[132:135]\n"
    "s_add_u32 m0, s53, 2048\n"
    "s_add_u32 s52, s52, 128\n"
    "buffer_load_dwordx4 v8, s[48:51], s52 offen lds\n"
    "v_mfma_f32_16x16x32_bf16 v[128:131], a[72:75], v[20:23], v[128:131]\n"
    "ds_read_b128 v[16:19], v3 offset:8192\n"
    "v_fma_f32 v195, v177, v190, v193\n"
    "v_mfma_f32_16x16x32_bf16 v[132:135], a[76:79], v[20:23], v[132:135]\n"
    "s_waitcnt lgkmcnt(5)\n"
    "v_mfma_f32_16x16x32_bf16 v[128:131], a[80:83], v[24:27], v[128:131]\n"
    "ds_read_b128 v[20:23], v4 offset:8192\n"
    "v_fma_f32 v192, v176, v194, v115\n"
    "v_mfma_f32_16x16x32_bf16 v[132:135], a[84:87], v[24:27], v[132:135]\n"
    "v_mfma_f32_16x16x32_bf16 v[128:131], a[88:91], v[28:31], v[128:131]\n"
    "ds_read_b128 v[24:27], v3 offset:18432\n"
    "v_fma_f32 v193, v176, v195, v123\n"
    "v_mfma_f32_16x16x32_bf16 v[132:135], a[92:95], v[28:31], v[132:135]\n"
    "s_waitcnt lgkmcnt(5)\n"
    "v_mfma_f32_16x16x32_bf16 v[128:131], a[96:99], v[200:203], v[128:131]\n"
    "ds_read_b128 v[28:31], v4 offset:18432\n"
    "v_fma_f32 v190, -v177, v195, v192\n"
    "v_mfma_f32_16x16x32_bf16 v[132:135], a[100:103], v[200:203], v[132:135]\n"
    "s_add_u32 m0, s53, 3072\n"
    "s_add_u32 s52, s52, 128\n"
    "buffer_load_dwordx4 v8, s[48:51], s52 offen lds\n"
    "v_mfma_f32_16x16x32_bf16 v[128:131], a[104:107], v[204:207], v[128:131]\n"
    "ds_read_b128 v[200:203], v3 offset:19456\n"
    "v_fma_f32 v191, v177, v194, v193\n"
    "v_mfma_f32_16x16x32_bf16 v[132:135], a[108:111], v[204:207], v[132:135]\n"
    "s_waitcnt lgkmcnt(5)\n"
    "v_mfma_f32_16x16x32_bf16 v[128:131], a[112:115], v[208:211], v[128:131]\n"
    "ds_read_b128 v[204:207], v4 offset:19456\n"
    "v_fma_f32 v192, v176, v190, v116\n"
    "v_mfma_f32_16x16x32_bf16 v[132:135], a[116:119], v[208:211], v[132:135]\n"
    "v_mfma_f32_16x16x32_bf16 v[128:131], a[120:123], v[212:215], v[128:131]\n"
    "ds_read_b128 v[208:211], v3 offset:20480\n"
    "v_fma_f32 v193, v176, v191, v124\n"
    "v_mfma_f32_16x16x32_bf16 v[132:135], a[124:127], v[212:215], v[132:135]\n"
    "s_waitcnt lgkmcnt(5)\n"
    "v_mfma_f32_16x16x32_bf16 v[128:131], a[128:131], v[16:19], v[128:131]\n"
    "ds_read_b128 v[212:215], v4 offset:20480\n"
    "v_fma_f32 v194, -v177, v191, v192\n"
    "v_mfma_f32_16x16x32_bf16 v[132:135], a[132:135], v[16:19], v[132:135]\n"
    "s_add_u32 m0, s53, 4096\n"
    "s_add_u32 s52, s52, 128\n"
    "buffer_load_dwordx4 v8, s[48:51], s52 offen lds\n"
    "v_mfma_f32_16x16x32_bf16 v[128:131], a[136:139], v[20:23], v[128:131]\n"
    "ds_read_b128 v[16:19], v3 offset:21504\n"
    "v_fma_f32 v195, v177, v190, v193\n"
    "v_mfma_f32_16x16x32_bf16 v[132:135], a[140:143], v[20:23], v[132:135]\n"
    "s_waitcnt lgkmcnt(5)\n"
    "v_mfma_f32_16x16x32_bf16 v[136:139], a[0:3], v[24:27], 0\n"
    "ds_read_b128 v[20:23], v4 offset:21504\n"
    "v_fma_f32 v192, v176, v194, v117\n"
    "v_mfma_f32_16x16x32_bf16 v[140:143], a[4:7], v[24:27], 0\n"
    "v_mfma_f32_16x16x32_bf16 v[136:139], a[8:11], v[28:31], v[136:139]\n"
    "ds_read_b128 v[24:27], v3 offset:22528\n"
    "v_fma_f32 v193, v176, v195, v125\n"
    "v_mfma_f32_16x16x32_bf16 v[140:143], a[12:15], v[28:31], v[140:143]\n"
    "s_waitcnt lgkmcnt(5)\n"
    "v_mfma_f32_16x16x32_bf16 v[136:139], a[16:19], v[200:203], v[136:139]\n"
    "ds_read_b128 v[28:31], v4 offset:22528\n"
    "v_fma_f32 v190, -v177, v195, v192\n"
    "v_mfma_f32_16x16x32_bf16 v[140:143], a[20:23], v[200:203], v[140:143]\n"
    "s_add_u32 m0, s53, 5120\n"
    "s_add_u32 s52, s52, 128\n"
    "buffer_load_dwordx4 v8, s[48:51], s52 offen lds\n"
    "v_mfma_f32_16x16x32_bf16 v[136:139], a[24:27], v[204:207], v[136:139]\n"
    "ds_read_b128 v[200:203], v3 offset:23552\n"
    "v_fma_f32 v191, v177, v194, v193\n"
    "v_mfma_f32_16x16x32_bf16 v[140:143], a[28:31], v[204:207], v[140:143]\n"
    "s_waitcnt lgkmcnt(5)\n"
    "v_mfma_f32_16x16x32_bf16 v[136:139], a[32:35], v[208:211], v[136:139]\n"
    "ds_read_b128 v[204:207], v4 offset:23552\n"
    "v_fma_f32 v192, v176, v190, v118\n"
    "v_mfma_f32_16x16x32_bf16 v[140:143], a[36:39], v[208:211], v[140:143]\n"
    "v_mfma_f32_16x16x32_bf16 v[136:139], a[40:43], v[212:215], v[136:139]\n"
    "ds_read_b128 v[208:211], v3 offset:24576\n"
    "v_fma_f32 v193, v176, v191, v126\n"
    "v_mfma_f32_16x16x32_bf16 v[140:143], a[44:47], v[212:215], v[140:143]\n"
    "s_waitcnt lgkmcnt(5)\n"
    "v_mfma_f32_16x16x32_bf16 v[136:139], a[48:51], v[16:19], v[136:139]\n"
    "ds_read_b128 v[212:215], v4 offset:24576\n"
    "v_fma_f32 v194, -v177, v191, v192\n"
    "v_mfma_f32_16x16x32_bf16 v[140:143], a[52:55], v[16:19], v[140:143]\n"
    "s_add_u32 m0, s53, 6144\n"
    "s_add_u32 s52, s52, 128\n"
    "buffer_load_dwordx4 v8, s[48:51], s52 offen lds\n"
    "v_mfma_f32_16x16x32_bf16 v[136:139], a[56:59], v[20:23], v[136:139]\n"
    "ds_read_b128 v[16:19], v3 offset:25600\n"
    "v_fma_f32 v195, v177, v190, v193\n"
    "v_mfma_f32_16x16x32_bf16 v[140:143], a[60:63], v[20:23], v[140:143]\n"
    "s_waitcnt lgkmcnt(5)\n"
    "v_mfma_f32_16x16x32_bf16 v[136:139], a[64:67], v[24:27], v[136:139]\n"
    "ds_read_b128 v[20:23], v4 offset:25600\n"
    "v_fma_f32 v192, v176, v194, v119\n"
    "v_mfma_f32_16x16x32_bf16 v[140:143], a[68:71], v[24:27], v[140:143]\n"
    "v_mfma_f32_16x16x32_bf16 v[136:139], a[72:75], v[28:31], v[136:139]\n"
    "ds_read_b128 v[24:27], v3 offset:26624\n"
    "v_fma_f32 v193, v176, v195, v127\n"
    "v_mfma_f32_16x16x32_bf16 v[140:143], a[76:79], v[28:31], v[140:143]\n"
    "s_waitcnt lgkmcnt(5)\n"
    "v_mfma_f32_16x16x32_bf16 v[136:139], a[80:83], v[200:203], v[136:139]\n"
    "ds_read_b128 v[28:31], v4 offset:26624\n"
    "v_fma_f32 v196, -v177, v195, v192\n"
    "v_mfma_f32_16x16x32_bf16 v[140:143], a[84:87], v[200:203], v[140:143]\n"
    "s_add_u32 m0, s53, 7168\n"
    "s_add_u32 s52, s52, 128\n"
    "buffer_load_dwordx4 v8, s[48:51], s52 offen lds\n"
    "v_mfma_f32_16x16x32_bf16 v[136:139], a[88:91], v[204:207], v[136:139]\n"
    "v_fma_f32 v197, v177, v194, v193\n"
    "v_mfma_f32_16x16x32_bf16 v[140:143], a[92:95], v[204:207], v[140:143]\n"
    "s_waitcnt lgkmcnt(4)\n"
    "v_mfma_f32_16x16x32_bf16 v[136:139], a[96:99], v[208:211], v[136:139]\n"
    "ds_write_b64 v10, v[196:197] offset:640\n"
    "v_mfma_f32_16x16x32_bf16 v[140:143], a[100:103], v[208:211], v[140:143]\n"
    "v_mfma_f32_16x16x32_bf16 v[136:139], a[104:107], v[212:215], v[136:139]\n"
    "v_mfma_f32_16x16x32_bf16 v[140:143], a[108:111], v[212:215], v[140:143]\n"
    "s_waitcnt lgkmcnt(3)\n"
    "s_waitcnt lgkmcnt(0)\n"
    "s_waitcnt vmcnt(8)\n"
    "s_barrier\n"
    "v_mfma_f32_16x16x32_bf16 v[136:139], a[112:115], v[16:19], v[136:139]\n"
    "ds_read_b128 v[200:203], v14 offset:0\n"
    "v_mfma_f32_16x16x32_bf16 v[140:143], a[116:119], v[16:19], v[140:143]\n"
    "ds_read_b128 v[204:207], v15 offset:0\n"
    "v_mfma_f32_16x16x32_bf16 v[136:139], a[120:123], v[20:23], v[136:139]\n"
    "s_add_u32 m0, s53, 8192\n"
    "s_add_u32 s52, s52, 128\n"
    "buffer_load_dwordx4 v8, s[48:51], s52 offen lds\n"
    "ds_read_b128 v[208:211], v14 offset:1024\n"
    "v_mfma_f32_16x16x32_bf16 v[140:143], a[124:127], v[20:23], v[140:143]\n"
    "ds_read_b128 v[212:215], v15 offset:1024\n"
    "v_mfma_f32_16x16x32_bf16 v[136:139], a[128:131], v[24:27], v[136:139]\n"
    "ds_read_b128 v[16:19], v14 offset:2048\n"
    "v_mfma_f32_16x16x32_bf16 v[140:143], a[132:135], v[24:27], v[140:143]\n"
    "ds_read_b128 v[20:23], v15 offset:2048\n"
    "v_mfma_f32_16x16x32_bf16 v[136:139], a[136:139], v[28:31], v[136:139]\n"
    "ds_read_b128 v[24:27], v14 offset:3072\n"
    "v_mfma_f32_16x16x32_bf16 v[140:143], a[140:143], v[28:31], v[140:143]\n"
    "s_waitcnt lgkmcnt(5)\n"
    "v_mfma_f32_16x16x32_bf16 v[144:147], a[0:3], v[200:203], 0\n"
    "ds_read_b128 v[28:31], v15 offset:3072\n"
    "v_mfma_f32_16x16x32_bf16 v[148:151], a[4:7], v[200:203], 0\n"
    "s_add_u32 s40, s15, 0\n"
    "s_cmp_ge_u32 s40, 9\n"
    "s_cselect_b32 s41, 9, 0\n"
    "s_sub_u32 s40, s40, s41\n"
    "s_lshl_b32 s40, s40, 6\n"
    "s_lshl_b32 s41, s13, 5\n"
    "s_add_u32 s40, s40, s41\n"
    "s_mul_i32 s40, s40, 1152\n"
    "s_mul_i32 s41, s11, 288\n"
    "s_add_u32 s52, s40, s41\n"
    "s_mul_i32 s53, s9, 9216\n"
    "s_add_u32 m0, s53, 0\n"
    "s_nop 0\n"
    "buffer_load_dwordx4 v180, s[36:39], s52 offen lds\n"
    "s_add_u32 m0, m0, 1024\n"
    "v_mfma_f32_16x16x32_bf16 v[144:147], a[8:11], v[204:207], v[144:147]\n"
    "ds_read_b128 v[200:203], v14 offset:4096\n"
    "v_mfma_f32_16x16x32_bf16 v[148:151], a[12:15], v[204:207], v[148:151]\n"
    "s_waitcnt lgkmcnt(5)\n"
    "v_mfma_f32_16x16x32_bf16 v[144:147], a[16:19], v[208:211], v[144:147]\n"
    "ds_read_b128 v[204:207], v15 offset:4096\n"
    "v_add_u32 v3, 73728, v1\n"
    "v_mfma_f32_16x16x32_bf16 v[148:151], a[20:23], v[208:211], v[148:151]\n"
    "v_mfma_f32_16x16x32_bf16 v[144:147], a[24:27], v[212:215], v[144:147]\n"
    "ds_read_b128 v[208:211], v14 offset:5120\n"
    "v_add_u32 v4, 73728, v2\n"
    "v_mfma_f32_16x16x32_bf16 v[148:151], a[28:31], v[212:215], v[148:151]\n"
    "ds_read_b64 v[176:177], v9 offset:1536\n"
    "s_waitcnt lgkmcnt(6)\n"
    "v_mfma_f32_16x16x32_bf16 v[144:147], a[32:35], v[16:19], v[144:147]\n"
    "ds_read_b128 v[212:215], v15 offset:5120\n"
    "s_waitcnt lgkmcnt(1)\n"
    "v_mfma_f32_16x16x32_bf16 v[148:151], a[36:39], v[16:19], v[148:151]\n"
    "buffer_load_dwordx4 v181, s[36:39], s52 offen lds\n"
    "s_add_u32 m0, m0, 1024\n"
    "v_fma_f32 v192, v176, v128, v129\n"
    "v_mfma_f32_16x16x32_bf16 v[144:147], a[40:43], v[20:23], v[144:147]\n"
    "ds_read_b128 v[16:19], v14 offset:6144\n"
    "v_fma_f32 v193, v176, v136, v137\n"
    "v_mfma_f32_16x16x32_bf16 v[148:151], a[44:47], v[20:23], v[148:151]\n"
    "v_fma_f32 v190, -v177, v136, v192\n"
    "v_mfma_f32_16x16x32_bf16 v[144:147], a[48:51], v[24:27], v[144:147]\n"
    "ds_read_b128 v[20:23], v15 offset:6144\n"
    "v_fma_f32 v191, v177, v128, v193\n"
    "v_mfma_f32_16x16x32_bf16 v[148:151], a[52:55], v[24:27], v[148:151]\n"
    "v_fma_f32 v192, v176, v190, v130\n"
    "v_mfma_f32_16x16x32_bf16 v[144:147], a[56:59], v[28:31], v[144:147]\n"
    "ds_read_b128 v[24:27], v14 offset:7168\n"
    "v_fma_f32 v193, v176, v191, v138\n"
    "v_mfma_f32_16x16x32_bf16 v[148:151], a[60:63], v[28:31], v[148:151]\n"
    "v_mfma_f32_16x16x32_bf16 v[144:147], a[64:67], v[200:203], v[144:147]\n"
    "ds_read_b128 v[28:31], v15 offset:7168\n"
    "v_fma_f32 v194, -v177, v191, v192\n"
    "v_mfma_f32_16x16x32_bf16 v[148:151], a[68:71], v[200:203], v[148:151]\n"
    "buffer_load_dwordx4 v182, s[36:39], s52 offen lds\n"
    "s_add_u32 m0, m0, 1024\n"
    "v_mfma_f32_16x16x32_bf16 v[144:147], a[72:75], v[204:207], v[144:147]\n"
    "ds_read_b128 v[200:203], v14 offset:8192\n"
    "v_fma_f32 v195, v177, v190, v193\n"
    "v_mfma_f32_16x16x32_bf16 v[148:151], a[76:79], v[204:207], v[148:151]\n"
    "s_waitcnt lgkmcnt(5)\n"
    "v_mfma_f32_16x16x32_bf16 v[144:147], a[80:83], v[208:211], v[144:147]\n"
    "ds_read_b128 v[204:207], v15 offset:8192\n"
    "v_fma_f32 v192, v176, v194, v131\n"
    "v_mfma_f32_16x16x32_bf16 v[148:151], a[84:87], v[208:211], v[148:151]\n"
    "v_mfma_f32_16x16x32_bf16 v[144:147], a[88:91], v[212:215], v[144:147]\n"
    "ds_read_b128 v[208:211], v14 offset:18432\n"
    "v_fma_f32 v193, v176, v195, v139\n"
    "v_mfma_f32_16x16x32_bf16 v[148:151], a[92:95], v[212:215], v[148:151]\n"
    "s_waitcnt lgkmcnt(5)\n"
    "v_mfma_f32_16x16x32_bf16 v[144:147], a[96:99], v[16:19], v[144:147]\n"
    "ds_read_b128 v[212:215], v15 offset:18432\n"
    "v_fma_f32 v190, -v177, v195, v192\n"
    "v_mfma_f32_16x16x32_bf16 v[148:151], a[100:103], v[16:19], v[148:151]\n"
    "buffer_load_dwordx4 v183, s[36:39], s52 offen lds\n"
    "s_add_u32 m0, m0, 1024\n"
    "v_mfma_f32_16x16x32_bf16 v[144:147], a[104:107], v[20:23], v[144:147]\n"
    "ds_read_b128 v[16:19], v14 offset:19456\n"
    "v_fma_f32 v191, v177, v194, v193\n"
    "v_mfma_f32_16x16x32_bf16 v[148:151], a[108:111], v[20:23], v[148:151]\n"
    "s_waitcnt lgkmcnt(5)\n"
    "v_mfma_f32_16x16x32_bf16 v[144:147], a[112:115], v[24:27], v[144:147]\n"
    "ds_read_b128 v[20:23], v15 offset:19456\n"
    "v_fma_f32 v192, v176, v190, v132\n"
    "v_mfma_f32_16x16x32_bf16 v[148:151], a[116:119], v[24:27], v[148:151]\n"
    "v_mfma_f32_16x16x32_bf16 v[144:147], a[120:123], v[28:31], v[144:147]\n"
    "ds_read_b128 v[24:27], v14 offset:20480\n"
    "v_fma_f32 v193, v176, v191, v140\n"
    "v_mfma_f32_16x16x32_bf16 v[148:151], a[124:127], v[28:31], v[148:151]\n"
    "s_waitcnt lgkmcnt(5)\n"
    "v_mfma_f32_16x16x32_bf16 v[144:147], a[128:131], v[200:203], v[144:147]\n"
    "ds_read_b128 v[28:31], v15 offset:20480\n"
    "v_fma_f32 v194, -v177, v191, v192\n"
    "v_mfma_f32_16x16x32_bf16 v[148:151], a[132:135], v[200:203], v[148:151]\n"
    "buffer_load_dwordx4 v184, s[36:39], s52 offen lds\n"
    "s_add_u32 m0, m0, 1024\n"
    "v_mfma_f32_16x16x32_bf16 v[144:147], a[136:139], v[204:207], v[144:147]\n"
    "ds_read_b128 v[200:203], v14 offset:21504\n"
    "v_fma_f32 v195, v177, v190, v193\n"
    "v_mfma_f32_16x16x32_bf16 v[148:151], a[140:143], v[204:207], v[148:151]\n"
    "s_waitcnt lgkmcnt(5)\n"
    "v_mfma_f32_16x16x32_bf16 v[152:155], a[0:3], v[208:211], 0\n"
    "ds_read_b128 v[204:207], v15 offset:21504\n"
    "v_fma_f32 v192, v176, v194, v133\n"
    "v_mfma_f32_16x16x32_bf16 v[156:159], a[4:7], v[208:211], 0\n"
    "v_mfma_f32_16x16x32_bf16 v[152:155], a[8:11], v[212:215], v[152:155]\n"
    "ds_read_b128 v[208:211], v14 offset:22528\n"
    "v_fma_f32 v193, v176, v195, v141\n"
    "v_mfma_f32_16x16x32_bf16 v[156:159], a[12:15], v[212:215], v[156:159]\n"
    "s_waitcnt lgkmcnt(5)\n"
    "v_mfma_f32_16x16x32_bf16 v[152:155], a[16:19], v[16:19], v[152:155]\n"
    "ds_read_b128 v[212:215], v15 offset:22528\n"
    "v_fma_f32 v190, -v177, v195, v192\n"
    "v_mfma_f32_16x16x32_bf16 v[156:159], a[20:23], v[16:19], v[156:159]\n"
    "buffer_load_dwordx4 v185, s[36:39], s52 offen lds\n"
    "s_add_u32 m0, m0, 1024\n"
    "v_mfma_f32_16x16x32_bf16 v[152:155], a[24:27], v[20:23], v[152:155]\n"
    "ds_read_b128 v[16:19], v14 offset:23552\n"
    "v_fma_f32 v191, v177, v194, v193\n"
    "v_mfma_f32_16x16x32_bf16 v[156:159], a[28:31], v[20:23], v[156:159]\n"
    "s_waitcnt lgkmcnt(5)\n"
    "v_mfma_f32_16x16x32_bf16 v[152:155], a[32:35], v[24:27], v[152:155]\n"
    "ds_read_b128 v[20:23], v15 offset:23552\n"
    "v_fma_f32 v192, v176, v190, v134\n"
    "v_mfma_f32_16x16x32_bf16 v[156:159], a[36:39], v[24:27], v[156:159]\n"
    "v_mfma_f32_16x16x32_bf16 v[152:155], a[40:43], v[28:31], v[152:155]\n"
    "ds_read_b128 v[24:27], v14 offset:24576\n"
    "v_fma_f32 v193, v176, v191, v142\n"
    "v_mfma_f32_16x16x32_bf16 v[156:159], a[44:47], v[28:31], v[156:159]\n"
    "s_waitcnt lgkmcnt(5)\n"
    "v_mfma_f32_16x16x32_bf16 v[152:155], a[48:51], v[200:203], v[152:155]\n"
    "ds_read_b128 v[28:31], v15 offset:24576\n"
    "v_fma_f32 v194, -v177, v191, v192\n"
    "v_mfma_f32_16x16x32_bf16 v[156:159], a[52:55], v[200:203], v[156:159]\n"
    "buffer_load_dwordx4 v186, s[36:39], s52 offen lds\n"
    "s_add_u32 m0, m0, 1024\n"
    "v_mfma_f32_16x16x32_bf16 v[152:155], a[56:59], v[204:207], v[152:155]\n"
    "ds_read_b128 v[200:203], v14 offset:25600\n"
    "v_fma_f32 v195, v177, v190, v193\n"
    "v_mfma_f32_16x16x32_bf16 v[156:159], a[60:63], v[204:207], v[156:159]\n"
    "s_waitcnt lgkmcnt(5)\n"
    "v_mfma_f32_16x16x32_bf16 v[152:155], a[64:67], v[208:211], v[152:155]\n"
    "ds_read_b128 v[204:207], v15 offset:25600\n"
    "v_fma_f32 v192, v176, v194, v135\n"
    "v_mfma_f32_16x16x32_bf16 v[156:159], a[68:71], v[208:211], v[156:159]\n"
    "v_mfma_f32_16x16x32_bf16 v[152:155], a[72:75], v[212:215], v[152:155]\n"
    "ds_read_b128 v[208:211], v14 offset:26624\n"
    "v_fma_f32 v193, v176, v195, v143\n"
    "v_mfma_f32_16x16x32_bf16 v[156:159], a[76:79], v[212:215], v[156:159]\n"
    "s_waitcnt lgkmcnt(5)\n"
    "v_mfma_f32_16x16x32_bf16 v[152:155], a[80:83], v[16:19], v[152:155]\n"
    "ds_read_b128 v[212:215], v15 offset:26624\n"
    "v_fma_f32 v196, -v177, v195, v192\n"
    "v_mfma_f32_16x16x32_bf16 v[156:159], a[84:87], v[16:19], v[156:159]\n"
    "buffer_load_dwordx4 v187, s[36:39], s52 offen lds\n"
    "s_add_u32 m0, m0, 1024\n"
    "v_mfma_f32_16x16x32_bf16 v[152:155], a[88:91], v[20:23], v[152:155]\n"
    "v_fma_f32 v197, v177, v194, v193\n"
    "v_mfma_f32_16x16x32_bf16 v[156:159], a[92:95], v[20:23], v[156:159]\n"
    "s_waitcnt lgkmcnt(4)\n"
    "v_mfma_f32_16x16x32_bf16 v[152:155], a[96:99], v[24:27], v[152:155]\n"
    "ds_write_b64 v10, v[196:197] offset:768\n"
    "v_mfma_f32_16x16x32_bf16 v[156:159], a[100:103], v[24:27], v[156:159]\n"
    "v_mfma_f32_16x16x32_bf16 v[152:155], a[104:107], v[28:31], v[152:155]\n"
    "v_mfma_f32_16x16x32_bf16 v[156:159], a[108:111], v[28:31], v[156:159]\n"
    "s_waitcnt lgkmcnt(3)\n"
    "s_waitcnt lgkmcnt(0)\n"
    "s_waitcnt vmcnt(8)\n"
    "s_barrier\n"
    "v_mfma_f32_16x16x32_bf16 v[152:155], a[112:115], v[200:203], v[152:155]\n"
    "ds_read_b128 v[16:19], v3 offset:0\n"
    "v_mfma_f32_16x16x32_bf16 v[156:159], a[116:119], v[200:203], v[156:159]\n"
    "ds_read_b128 v[20:23], v4 offset:0\n"
    "v_mfma_f32_16x16x32_bf16 v[152:155], a[120:123], v[204:207], v[152:155]\n"
    "buffer_load_dwordx4 v188, s[36:39], s52 offen lds\n"
    "ds_read_b128 v[24:27], v3 offset:1024\n"
    "v_mfma_f32_16x16x32_bf16 v[156:159], a[124:127], v[204:207], v[156:159]\n"
    "ds_read_b128 v[28:31], v4 offset:1024\n"
    "v_mfma_f32_16x16x32_bf16 v[152:155], a[128:131], v[208:211], v[152:155]\n"
    "ds_read_b128 v[200:203], v3 offset:2048\n"
    "v_mfma_f32_16x16x32_bf16 v[156:159], a[132:135], v[208:211], v[156:159]\n"
    "ds_read_b128 v[204:207], v4 offset:2048\n"
    "v_mfma_f32_16x16x32_bf16 v[152:155], a[136:139], v[212:215], v[152:155]\n"
    "ds_read_b128 v[208:211], v3 offset:3072\n"
    "v_mfma_f32_16x16x32_bf16 v[156:159], a[140:143], v[212:215], v[156:159]\n"
    "s_waitcnt lgkmcnt(5)\n"
    "v_mfma_f32_16x16x32_bf16 v[160:163], a[0:3], v[16:19], 0\n"
    "ds_read_b128 v[212:215], v4 offset:3072\n"
    "v_mfma_f32_16x16x32_bf16 v[164:167], a[4:7], v[16:19], 0\n"
    "s_add_u32 s40, s15, 1\n"
    "s_cmp_ge_u32 s40, 9\n"
    "s_cselect_b32 s41, 9, 0\n"
    "s_sub_u32 s40, s40, s41\n"
    "s_lshl_b32 s40, s40, 6\n"
    "s_lshl_b32 s41, s13, 5\n"
    "s_add_u32 s40, s40, s41\n"
    "s_mul_i32 s40, s40, 1152\n"
    "s_mul_i32 s41, s11, 288\n"
    "s_add_u32 s52, s40, s41\n"
    "s_mul_i32 s53, s9, 9216\n"
    "s_add_u32 m0, s53, 36864\n"
    "s_nop 0\n"
    "buffer_load_dwordx4 v180, s[36:39], s52 offen lds\n"
    "s_add_u32 m0, m0, 1024\n"
    "v_mfma_f32_16x16x32_bf16 v[160:163], a[8:11], v[20:23], v[160:163]\n"
    "ds_read_b128 v[16:19], v3 offset:4096\n"
    "v_mfma_f32_16x16x32_bf16 v[164:167], a[12:15], v[20:23], v[164:167]\n"
    "s_waitcnt lgkmcnt(5)\n"
    "v_mfma_f32_16x16x32_bf16 v[160:163], a[16:19], v[24:27], v[160:163]\n"
    "ds_read_b128 v[20:23], v4 offset:4096\n"
    "ds_read_b64 v[176:177], v9 offset:1792\n"
    "v_mfma_f32_16x16x32_bf16 v[164:167], a[20:23], v[24:27], v[164:167]\n"
    "v_mfma_f32_16x16x32_bf16 v[160:163], a[24:27], v[28:31], v[160:163]\n"
    "ds_read_b128 v[24:27], v3 offset:5120\n"
    "s_waitcnt lgkmcnt(1)\n"
    "v_mfma_f32_16x16x32_bf16 v[164:167], a[28:31], v[28:31], v[164:167]\n"
    "v_fma_f32 v192, v176, v144, v145\n"
    "v_mfma_f32_16x16x32_bf16 v[160:163], a[32:35], v[200:203], v[160:163]\n"
    "ds_read_b128 v[28:31], v4 offset:5120\n"
    "v_fma_f32 v193, v176, v152, v153\n"
    "v_mfma_f32_16x16x32_bf16 v[164:167], a[36:39], v[200:203], v[164:167]\n"
    "buffer_load_dwordx4 v181, s[36:39], s52 offen lds\n"
    "s_add_u32 m0, m0, 1024\n"
    "v_fma_f32 v190, -v177, v152, v192\n"
    "v_mfma_f32_16x16x32_bf16 v[160:163], a[40:43], v[204:207], v[160:163]\n"
    "ds_read_b128 v[200:203], v3 offset:6144\n"
    "v_fma_f32 v191, v177, v144, v193\n"
    "v_mfma_f32_16x16x32_bf16 v[164:167], a[44:47], v[204:207], v[164:167]\n"
    "v_mfma_f32_16x16x32_bf16 v[160:163], a[48:51], v[208:211], v[160:163]\n"
    "ds_read_b128 v[204:207], v4 offset:6144\n"
    "v_fma_f32 v192, v176, v190, v146\n"
    "v_mfma_f32_16x16x32_bf16 v[164:167], a[52:55], v[208:211], v[164:167]\n"
    "v_mfma_f32_16x16x32_bf16 v[160:163], a[56:59], v[212:215], v[160:163]\n"
    "ds_read_b128 v[208:211], v3 offset:7168\n"
    "v_fma_f32 v193, v176, v191, v154\n"
    "v_mfma_f32_16x16x32_bf16 v[164:167], a[60:63], v[212:215], v[164:167]\n"
    "v_mfma_f32_16x16x32_bf16 v[160:163], a[64:67], v[16:19], v[160:163]\n"
    "ds_read_b128 v[212:215], v4 offset:7168\n"
    "v_fma_f32 v194, -v177, v191, v192\n"
    "v_mfma_f32_16x16x32_bf16 v[164:167], a[68:71], v[16:19], v[164:167]\n"
    "buffer_load_dwordx4 v182, s[36:39], s52 offen lds\n"
    "s_add_u32 m0, m0, 1024\n"
    "v_mfma_f32_16x16x32_bf16 v[160:163], a[72:75], v[20:23], v[160:163]\n"
    "ds_read_b128 v[16:19], v3 offset:8192\n"
    "v_fma_f32 v195, v177, v190, v193\n"
    "v_mfma_f32_16x16x32_bf16 v[164:167], a[76:79], v[20:23], v[164:167]\n"
    "s_waitcnt lgkmcnt(5)\n"
    "v_mfma_f32_16x16x32_bf16 v[160:163], a[80:83], v[24:27], v[160:163]\n"
    "ds_read_b128 v[20:23], v4 offset:8192\n"
    "v_fma_f32 v192, v176, v194, v147\n"
    "v_mfma_f32_16x16x32_bf16 v[164:167], a[84:87], v[24:27], v[164:167]\n"
    "v_mfma_f32_16x16x32_bf16 v[160:163], a[88:91], v[28:31], v[160:163]\n"
    "ds_read_b128 v[24:27], v3 offset:18432\n"
    "v_fma_f32 v193, v176, v195, v155\n"
    "v_mfma_f32_16x16x32_bf16 v[164:167], a[92:95], v[28:31], v[164:167]\n"
    "s_waitcnt lgkmcnt(5)\n"
    "v_mfma_f32_16x16x32_bf16 v[160:163], a[96:99], v[200:203], v[160:163]\n"
    "ds_read_b128 v[28:31], v4 offset:18432\n"
    "v_fma_f32 v190, -v177, v195, v192\n"
    "v_mfma_f32_16x16x32_bf16 v[164:167], a[100:103], v[200:203], v[164:167]\n"
    "buffer_load_dwordx4 v183, s[36:39], s52 offen lds\n"
    "s_add_u32 m0, m0, 1024\n"
    "v_mfma_f32_16x16x32_bf16 v[160:163], a[104:107], v[204:207], v[160:163]\n"
    "ds_read_b128 v[200:203], v3 offset:19456\n"
    "v_fma_f32 v191, v177, v194, v193\n"
    "v_mfma_f32_16x16x32_bf16 v[164:167], a[108:111], v[204:207], v[164:167]\n"
    "s_waitcnt lgkmcnt(5)\n"
    "v_mfma_f32_16x16x32_bf16 v[160:163], a[112:115], v[208:211], v[160:163]\n"
    "ds_read_b128 v[204:207], v4 offset:19456\n"
    "v_fma_f32 v192, v176, v190, v148\n"
    "v_mfma_f32_16x16x32_bf16 v[164:167], a[116:119], v[208:211], v[164:167]\n"
    "v_mfma_f32_16x16x32_bf16 v[160:163], a[120:123], v[212:215], v[160:163]\n"
    "ds_read_b128 v[208:211], v3 offset:20480\n"
    "v_fma_f32 v193, v176, v191, v156\n"
    "v_mfma_f32_16x16x32_bf16 v[164:167], a[124:127], v[212:215], v[164:167]\n"
    "s_waitcnt lgkmcnt(5)\n"
    "v_mfma_f32_16x16x32_bf16 v[160:163], a[128:131], v[16:19], v[160:163]\n"
    "ds_read_b128 v[212:215], v4 offset:20480\n"
    "v_fma_f32 v194, -v177, v191, v192\n"
    "v_mfma_f32_16x16x32_bf16 v[164:167], a[132:135], v[16:19], v[164:167]\n"
    "buffer_load_dwordx4 v184, s[36:39], s52 offen lds\n"
    "s_add_u32 m0, m0, 1024\n"
    "v_mfma_f32_16x16x32_bf16 v[160:163], a[136:139], v[20:23], v[160:163]\n"
    "ds_read_b128 v[16:19], v3 offset:21504\n"
    "v_fma_f32 v195, v177, v190, v193\n"
    "v_mfma_f32_16x16x32_bf16 v[164:167], a[140:143], v[20:23], v[164:167]\n"
    "s_waitcnt lgkmcnt(5)\n"
    "v_mfma_f32_16x16x32_bf16 v[168:171], a[0:3], v[24:27], 0\n"
    "ds_read_b128 v[20:23], v4 offset:21504\n"
    "v_fma_f32 v192, v176, v194, v149\n"
    "v_mfma_f32_16x16x32_bf16 v[172:175], a[4:7], v[24:27], 0\n"
    "v_mfma_f32_16x16x32_bf16 v[168:171], a[8:11], v[28:31], v[168:171]\n"
    "ds_read_b128 v[24:27], v3 offset:22528\n"
    "v_fma_f32 v193, v176, v195, v157\n"
    "v_mfma_f32_16x16x32_bf16 v[172:175], a[12:15], v[28:31], v[172:175]\n"
    "s_waitcnt lgkmcnt(5)\n"
    "v_mfma_f32_16x16x32_bf16 v[168:171], a[16:19], v[200:203], v[168:171]\n"
    "ds_read_b128 v[28:31], v4 offset:22528\n"
    "v_fma_f32 v190, -v177, v195, v192\n"
    "v_mfma_f32_16x16x32_bf16 v[172:175], a[20:23], v[200:203], v[172:175]\n"
    "buffer_load_dwordx4 v185, s[36:39], s52 offen lds\n"
    "s_add_u32 m0, m0, 1024\n"
    "v_mfma_f32_16x16x32_bf16 v[168:171], a[24:27], v[204:207], v[168:171]\n"
    "ds_read_b128 v[200:203], v3 offset:23552\n"
    "v_fma_f32 v191, v177, v194, v193\n"
    "v_mfma_f32_16x16x32_bf16 v[172:175], a[28:31], v[204:207], v[172:175]\n"
    "s_waitcnt lgkmcnt(5)\n"
    "v_mfma_f32_16x16x32_bf16 v[168:171], a[32:35], v[208:211], v[168:171]\n"
    "ds_read_b128 v[204:207], v4 offset:23552\n"
    "v_fma_f32 v192, v176, v190, v150\n"
    "v_mfma_f32_16x16x32_bf16 v[172:175], a[36:39], v[208:211], v[172:175]\n"
    "v_mfma_f32_16x16x32_bf16 v[168:171], a[40:43], v[212:215], v[168:171]\n"
    "ds_read_b128 v[208:211], v3 offset:24576\n"
    "v_fma_f32 v193, v176, v191, v158\n"
    "v_mfma_f32_16x16x32_bf16 v[172:175], a[44:47], v[212:215], v[172:175]\n"
    "s_waitcnt lgkmcnt(5)\n"
    "v_mfma_f32_16x16x32_bf16 v[168:171], a[48:51], v[16:19], v[168:171]\n"
    "ds_read_b128 v[212:215], v4 offset:24576\n"
    "v_fma_f32 v194, -v177, v191, v192\n"
    "v_mfma_f32_16x16x32_bf16 v[172:175], a[52:55], v[16:19], v[172:175]\n"
    "buffer_load_dwordx4 v186, s[36:39], s52 offen lds\n"
    "s_add_u32 m0, m0, 1024\n"
    "v_mfma_f32_16x16x32_bf16 v[168:171], a[56:59], v[20:23], v[168:171]\n"
    "ds_read_b128 v[16:19], v3 offset:25600\n"
    "v_fma_f32 v195, v177, v190, v193\n"
    "v_mfma_f32_16x16x32_bf16 v[172:175], a[60:63], v[20:23], v[172:175]\n"
    "s_waitcnt lgkmcnt(5)\n"
    "v_mfma_f32_16x16x32_bf16 v[168:171], a[64:67], v[24:27], v[168:171]\n"
    "ds_read_b128 v[20:23], v4 offset:25600\n"
    "v_fma_f32 v192, v176, v194, v151\n"
    "v_mfma_f32_16x16x32_bf16 v[172:175], a[68:71], v[24:27], v[172:175]\n"
    "v_mfma_f32_16x16x32_bf16 v[168:171], a[72:75], v[28:31], v[168:171]\n"
    "ds_read_b128 v[24:27], v3 offset:26624\n"
    "v_fma_f32 v193, v176, v195, v159\n"
    "v_mfma_f32_16x16x32_bf16 v[172:175], a[76:79], v[28:31], v[172:175]\n"
    "s_waitcnt lgkmcnt(5)\n"
    "v_mfma_f32_16x16x32_bf16 v[168:171], a[80:83], v[200:203], v[168:171]\n"
    "ds_read_b128 v[28:31], v4 offset:26624\n"
    "v_fma_f32 v196, -v177, v195, v192\n"
    "v_mfma_f32_16x16x32_bf16 v[172:175], a[84:87], v[200:203], v[172:175]\n"
    "buffer_load_dwordx4 v187, s[36:39], s52 offen lds\n"
    "s_add_u32 m0, m0, 1024\n"
    "v_mfma_f32_16x16x32_bf16 v[168:171], a[88:91], v[204:207], v[168:171]\n"
    "v_fma_f32 v197, v177, v194, v193\n"
    "v_mfma_f32_16x16x32_bf16 v[172:175], a[92:95], v[204:207], v[172:175]\n"
    "s_waitcnt lgkmcnt(4)\n"
    "v_mfma_f32_16x16x32_bf16 v[168:171], a[96:99], v[208:211], v[168:171]\n"
    "ds_write_b64 v10, v[196:197] offset:896\n"
    "v_mfma_f32_16x16x32_bf16 v[172:175], a[100:103], v[208:211], v[172:175]\n"
    "v_mfma_f32_16x16x32_bf16 v[168:171], a[104:107], v[212:215], v[168:171]\n"
    "v_mfma_f32_16x16x32_bf16 v[172:175], a[108:111], v[212:215], v[172:175]\n"
    "s_waitcnt lgkmcnt(3)\n"
    "v_mfma_f32_16x16x32_bf16 v[168:171], a[112:115], v[16:19], v[168:171]\n"
    "v_mfma_f32_16x16x32_bf16 v[172:175], a[116:119], v[16:19], v[172:175]\n"
    "v_mfma_f32_16x16x32_bf16 v[168:171], a[120:123], v[20:23], v[168:171]\n"
    "buffer_load_dwordx4 v188, s[36:39], s52 offen lds\n"
    "v_mfma_f32_16x16x32_bf16 v[172:175], a[124:127], v[20:23], v[172:175]\n"
    "s_waitcnt lgkmcnt(1)\n"
    "v_mfma_f32_16x16x32_bf16 v[168:171], a[128:131], v[24:27], v[168:171]\n"
    "v_mfma_f32_16x16x32_bf16 v[172:175], a[132:135], v[24:27], v[172:175]\n"
    "v_mfma_f32_16x16x32_bf16 v[168:171], a[136:139], v[28:31], v[168:171]\n"
    "v_mfma_f32_16x16x32_bf16 v[172:175], a[140:143], v[28:31], v[172:175]\n"
    "ds_read_b64 v[176:177], v9 offset:2048\n"
    "s_waitcnt lgkmcnt(0)\n"
    "v_fma_f32 v192, v176, v160, v161\n"
    "s_nop 3\n"
    "v_fma_f32 v193, v176, v168, v169\n"
    "v_fma_f32 v190, -v177, v168, v192\n"
    "v_fma_f32 v191, v177, v160, v193\n"
    "v_fma_f32 v192, v176, v190, v162\n"
    "v_fma_f32 v193, v176, v191, v170\n"
    "v_fma_f32 v194, -v177, v191, v192\n"
    "v_fma_f32 v195, v177, v190, v193\n"
    "v_fma_f32 v192, v176, v194, v163\n"
    "v_fma_f32 v193, v176, v195, v171\n"
    "v_fma_f32 v190, -v177, v195, v192\n"
    "v_fma_f32 v191, v177, v194, v193\n"
    "v_fma_f32 v192, v176, v190, v164\n"
    "v_fma_f32 v193, v176, v191, v172\n"
    "v_fma_f32 v194, -v177, v191, v192\n"
    "v_fma_f32 v195, v177, v190, v193\n"
    "v_fma_f32 v192, v176, v194, v165\n"
    "v_fma_f32 v193, v176, v195, v173\n"
    "v_fma_f32 v190, -v177, v195, v192\n"
    "v_fma_f32 v191, v177, v194, v193\n"
    "v_fma_f32 v192, v176, v190, v166\n"
    "v_fma_f32 v193, v176, v191, v174\n"
    "v_fma_f32 v194, -v177, v191, v192\n"
    "v_fma_f32 v195, v177, v190, v193\n"
    "v_fma_f32 v192, v176, v194, v167\n"
    "v_fma_f32 v193, v176, v195, v175\n"
    "v_fma_f32 v196, -v177, v195, v192\n"
    "v_fma_f32 v197, v177, v194, v193\n"
    "ds_write_b64 v10, v[196:197] offset:1024\n"
    "s_waitcnt lgkmcnt(0)\n"
    "s_barrier\n"
    "s_cmp_eq_u32 s9, 3\n"
    "s_cbranch_scc1 CSK_13\n"
    "s_lshl_b32 s40, s9, 6\n"
    "v_add_u32 v235, s40, v0\n"
    "v_cmp_gt_u32 vcc, 0x90, v235\n"
    "s_nop 1\n"
    "s_mov_b64 exec, vcc\n"
    "v_lshlrev_b32 v232, 3, v189\n"
    "v_add_u32 v233, 129024, v232\n"
    "v_add_u32 v232, 110592, v232\n"
    "ds_read_b64 v[200:201], v232 offset:0\n"
    "ds_read_b64 v[202:203], v232 offset:1152\n"
    "ds_read_b64 v[204:205], v232 offset:2304\n"
    "ds_read_b64 v[206:207], v232 offset:3456\n"
    "ds_read_b64 v[208:209], v232 offset:4608\n"
    "ds_read_b64 v[210:211], v232 offset:5760\n"
    "ds_read_b64 v[212:213], v232 offset:6912\n"
    "ds_read_b64 v[214:215], v232 offset:8064\n"
    "ds_read_b64 v[216:217], v232 offset:9216\n"
    "ds_read_b64 v[218:219], v232 offset:10368\n"
    "ds_read_b64 v[220:221], v232 offset:11520\n"
    "ds_read_b64 v[222:223], v232 offset:12672\n"
    "s_waitcnt lgkmcnt(11)\n"
    "ds_read_b64 v[224:225], v232 offset:13824\n"
    "ds_read_b64 v[226:227], v232 offset:14976\n"
    "ds_read_b64 v[228:229], v232 offset:16128\n"
    "ds_read_b64 v[230:231], v232 offset:17280\n"
    "v_mov_b32 v244, 0\n"
    "v_mov_b32 v245, 0\n"
    "ds_write_b64 v233, v[244:245] offset:0\n"
    "v_fma_f32 v235, v248, v244, v200\n"
    "v_fma_f32 v236, v248, v245, v201\n"
    "v_fma_f32 v246, -v249, v245, v235\n"
    "v_fma_f32 v247, v249, v244, v236\n"
    "s_waitcnt lgkmcnt(15)\n"
    "ds_write_b64 v233, v[246:247] offset:1152\n"
    "v_fma_f32 v235, v248, v246, v202\n"
    "v_fma_f32 v236, v248, v247, v203\n"
    "v_fma_f32 v244, -v249, v247, v235\n"
    "v_fma_f32 v245, v249, v246, v236\n"
    "s_waitcnt lgkmcnt(15)\n"
    "ds_write_b64 v233, v[244:245] offset:2304\n"
    "v_fma_f32 v235, v248, v244, v204\n"
    "v_fma_f32 v236, v248, v245, v205\n"
    "v_fma_f32 v246, -v249, v245, v235\n"
    "v_fma_f32 v247, v249, v244, v236\n"
    "s_waitcnt lgkmcnt(15)\n"
    "ds_write_b64 v233, v[246:247] offset:3456\n"
    "v_fma_f32 v235, v248, v246, v206\n"
    "v_fma_f32 v236, v248, v247, v207\n"
    "v_fma_f32 v244, -v249, v247, v235\n"
    "v_fma_f32 v245, v249, v246, v236\n"
    "s_waitcnt lgkmcnt(15)\n"
    "ds_write_b64 v233, v[244:245] offset:4608\n"
    "v_fma_f32 v235, v248, v244, v208\n"
    "v_fma_f32 v236, v248, v245, v209\n"
    "v_fma_f32 v246, -v249, v245, v235\n"
    "v_fma_f32 v247, v249, v244, v236\n"
    "s_waitcnt lgkmcnt(15)\n"
    "ds_write_b64 v233, v[246:247] offset:5760\n"
    "v_fma_f32 v235, v248, v246, v210\n"
    "v_fma_f32 v236, v248, v247, v211\n"
    "v_fma_f32 v244, -v249, v247, v235\n"
    "v_fma_f32 v245, v249, v246, v236\n"
    "s_waitcnt lgkmcnt(15)\n"
    "ds_write_b64 v233, v[244:245] offset:6912\n"
    "v_fma_f32 v235, v248, v244, v212\n"
    "v_fma_f32 v236, v248, v245, v213\n"
    "v_fma_f32 v246, -v249, v245, v235\n"
    "v_fma_f32 v247, v249, v244, v236\n"
    "s_waitcnt lgkmcnt(15)\n"
    "ds_write_b64 v233, v[246:247] offset:8064\n"
    "v_fma_f32 v235, v248, v246, v214\n"
    "v_fma_f32 v236, v248, v247, v215\n"
    "v_fma_f32 v244, -v249, v247, v235\n"
    "v_fma_f32 v245, v249, v246, v236\n"
    "s_waitcnt lgkmcnt(15)\n"
    "ds_write_b64 v233, v[244:245] offset:9216\n"
    "v_fma_f32 v235, v248, v244, v216\n"
    "v_fma_f32 v236, v248, v245, v217\n"
    "v_fma_f32 v246, -v249, v245, v235\n"
    "v_fma_f32 v247, v249, v244, v236\n"
    "s_waitcnt lgkmcnt(15)\n"
    "ds_write_b64 v233, v[246:247] offset:10368\n"
    "v_fma_f32 v235, v248, v246, v218\n"
    "v_fma_f32 v236, v248, v247, v219\n"
    "v_fma_f32 v244, -v249, v247, v235\n"
    "v_fma_f32 v245, v249, v246, v236\n"
    "s_waitcnt lgkmcnt(0)\n"
    "ds_write_b64 v233, v[244:245] offset:11520\n"
    "v_fma_f32 v235, v248, v244, v220\n"
    "v_fma_f32 v236, v248, v245, v221\n"
    "v_fma_f32 v246, -v249, v245, v235\n"
    "v_fma_f32 v247, v249, v244, v236\n"
    "ds_write_b64 v233, v[246:247] offset:12672\n"
    "v_fma_f32 v235, v248, v246, v222\n"
    "v_fma_f32 v236, v248, v247, v223\n"
    "v_fma_f32 v244, -v249, v247, v235\n"
    "v_fma_f32 v245, v249, v246, v236\n"
    "ds_write_b64 v233, v[244:245] offset:13824\n"
    "v_fma_f32 v235, v248, v244, v224\n"
    "v_fma_f32 v236, v248, v245, v225\n"
    "v_fma_f32 v246, -v249, v245, v235\n"
    "v_fma_f32 v247, v249, v244, v236\n"
    "ds_write_b64 v233, v[246:247] offset:14976\n"
    "v_fma_f32 v235, v248, v246, v226\n"
    "v_fma_f32 v236, v248, v247, v227\n"
    "v_fma_f32 v244, -v249, v247, v235\n"
    "v_fma_f32 v245, v249, v246, v236\n"
    "ds_write_b64 v233, v[244:245] offset:16128\n"
    "v_fma_f32 v235, v248, v244, v228\n"
    "v_fma_f32 v236, v248, v245, v229\n"
    "v_fma_f32 v246, -v249, v245, v235\n"
    "v_fma_f32 v247, v249, v244, v236\n"
    "ds_write_b64 v233, v[246:247] offset:17280\n"
    "v_fma_f32 v235, v248, v246, v230\n"
    "v_fma_f32 v236, v248, v247, v231\n"
    "v_fma_f32 v244, -v249, v247, v235\n"
    "v_fma_f32 v245, v249, v246, v236\n"
    "s_lshl_b32 s40, s11, 4\n"
    "s_add_u32 s40, s40, s10\n"
    "s_mul_i32 s40, s40, 2304\n"
    "s_add_u32 s40, s40, 524288\n"
    "v_lshlrev_b32 v234, 4, v189\n"
    "v_mov_b32 v240, v244\n"
    "v_mov_b32 v241, s84\n"
    "v_mov_b32 v242, v245\n"
    "v_mov_b32 v243, s85\n"
    "buffer_store_dwordx4 v[240:243], v234, s[72:75], s40 offen sc1\n"
    "s_waitcnt lgkmcnt(0)\n"
    "v_mov_b32 v244, 1.0\n"
    "v_mov_b32 v245, 0\n"
    "ds_write_b64 v232, v[244:245] offset:0\n"
    "v_mul_f32 v235, v248, v244\n"
    "v_mul_f32 v236, v248, v245\n"
    "v_fma_f32 v246, -v249, v245, v235\n"
    "v_fma_f32 v247, v249, v244, v236\n"
    "ds_write_b64 v232, v[246:247] offset:1152\n"
    "v_mul_f32 v235, v248, v246\n"
    "v_mul_f32 v236, v248, v247\n"
    "v_fma_f32 v244, -v249, v247, v235\n"
    "v_fma_f32 v245, v249, v246, v236\n"
    "ds_write_b64 v232, v[244:245] offset:2304\n"
    "v_mul_f32 v235, v248, v244\n"
    "v_mul_f32 v236, v248, v245\n"
    "v_fma_f32 v246, -v249, v245, v235\n"
    "v_fma_f32 v247, v249, v244, v236\n"
    "ds_write_b64 v232, v[246:247] offset:3456\n"
    "v_mul_f32 v235, v248, v246\n"
    "v_mul_f32 v236, v248, v247\n"
    "v_fma_f32 v244, -v249, v247, v235\n"
    "v_fma_f32 v245, v249, v246, v236\n"
    "ds_write_b64 v232, v[244:245] offset:4608\n"
    "v_mul_f32 v235, v248, v244\n"
    "v_mul_f32 v236, v248, v245\n"
    "v_fma_f32 v246, -v249, v245, v235\n"
    "v_fma_f32 v247, v249, v244, v236\n"
    "ds_write_b64 v232, v[246:247] offset:5760\n"
    "v_mul_f32 v235, v248, v246\n"
    "v_mul_f32 v236, v248, v247\n"
    "v_fma_f32 v244, -v249, v247, v235\n"
    "v_fma_f32 v245, v249, v246, v236\n"
    "ds_write_b64 v232, v[244:245] offset:6912\n"
    "v_mul_f32 v235, v248, v244\n"
    "v_mul_f32 v236, v248, v245\n"
    "v_fma_f32 v246, -v249, v245, v235\n"
    "v_fma_f32 v247, v249, v244, v236\n"
    "ds_write_b64 v232, v[246:247] offset:8064\n"
    "v_mul_f32 v235, v248, v246\n"
    "v_mul_f32 v236, v248, v247\n"
    "v_fma_f32 v244, -v249, v247, v235\n"
    "v_fma_f32 v245, v249, v246, v236\n"
    "ds_write_b64 v232, v[244:245] offset:9216\n"
    "v_mul_f32 v235, v248, v244\n"
    "v_mul_f32 v236, v248, v245\n"
    "v_fma_f32 v246, -v249, v245, v235\n"
    "v_fma_f32 v247, v249, v244, v236\n"
    "ds_write_b64 v232, v[246:247] offset:10368\n"
    "v_mul_f32 v235, v248, v246\n"
    "v_mul_f32 v236, v248, v247\n"
    "v_fma_f32 v244, -v249, v247, v235\n"
    "v_fma_f32 v245, v249, v246, v236\n"
    "s_waitcnt lgkmcnt(0)\n"
    "ds_write_b64 v232, v[244:245] offset:11520\n"
    "v_mul_f32 v235, v248, v244\n"
    "v_mul_f32 v236, v248, v245\n"
    "v_fma_f32 v246, -v249, v245, v235\n"
    "v_fma_f32 v247, v249, v244, v236\n"
    "ds_write_b64 v232, v[246:247] offset:12672\n"
    "v_mul_f32 v235, v248, v246\n"
    "v_mul_f32 v236, v248, v247\n"
    "v_fma_f32 v244, -v249, v247, v235\n"
    "v_fma_f32 v245, v249, v246, v236\n"
    "ds_write_b64 v232, v[244:245] offset:13824\n"
    "v_mul_f32 v235, v248, v244\n"
    "v_mul_f32 v236, v248, v245\n"
    "v_fma_f32 v246, -v249, v245, v235\n"
    "v_fma_f32 v247, v249, v244, v236\n"
    "ds_write_b64 v232, v[246:247] offset:14976\n"
    "v_mul_f32 v235, v248, v246\n"
    "v_mul_f32 v236, v248, v247\n"
    "v_fma_f32 v244, -v249, v247, v235\n"
    "v_fma_f32 v245, v249, v246, v236\n"
    "ds_write_b64 v232, v[244:245] offset:16128\n"
    "v_mul_f32 v235, v248, v244\n"
    "v_mul_f32 v236, v248, v245\n"
    "v_fma_f32 v246, -v249, v245, v235\n"
    "v_fma_f32 v247, v249, v244, v236\n"
    "ds_write_b64 v232, v[246:247] offset:17280\n"
    "s_mov_b64 s[46:47], exec\n"
    "s_mov_b32 s55, 0\n"
    "SP_14:\n"
    "s_lshl_b32 s40, s11, 4\n"
    "s_mul_i32 s40, s40, 2304\n"
    "s_add_u32 s40, s40, 524288\n"
    "s_cmp_le_u32 s10, 0\n"
    "s_cbranch_scc1 SL_15\n"
    "buffer_load_dwordx4 a[144:147], v234, s[72:75], s40 offen sc1\n"
    "s_add_u32 s40, s40, 2304\n"
    "s_cmp_le_u32 s10, 1\n"
    "s_cbranch_scc1 SL_15\n"
    "buffer_load_dwordx4 a[148:151], v234, s[72:75], s40 offen sc1\n"
    "s_add_u32 s40, s40, 2304\n"
    "s_cmp_le_u32 s10, 2\n"
    "s_cbranch_scc1 SL_15\n"
    "buffer_load_dwordx4 a[152:155], v234, s[72:75], s40 offen sc1\n"
    "s_add_u32 s40, s40, 2304\n"
    "s_cmp_le_u32 s10, 3\n"
    "s_cbranch_scc1 SL_15\n"
    "buffer_load_dwordx4 a[156:159], v234, s[72:75], s40 offen sc1\n"
    "s_add_u32 s40, s40, 2304\n"
    "s_cmp_le_u32 s10, 4\n"
    "s_cbranch_scc1 SL_15\n"
    "buffer_load_dwordx4 a[160:163], v234, s[72:75], s40 offen sc1\n"
    "s_add_u32 s40, s40, 2304\n"
    "s_cmp_le_u32 s10, 5\n"
    "s_cbranch_scc1 SL_15\n"
    "buffer_load_dwordx4 a[164:167], v234, s[72:75], s40 offen sc1\n"
    "s_add_u32 s40, s40, 2304\n"
    "s_cmp_le_u32 s10, 6\n"
    "s_cbranch_scc1 SL_15\n"
    "buffer_load_dwordx4 a[168:171], v234, s[72:75], s40 offen sc1\n"
    "s_add_u32 s40, s40, 2304\n"
    "s_cmp_le_u32 s10, 7\n"
    "s_cbranch_scc1 SL_15\n"
    "buffer_load_dwordx4 a[172:175], v234, s[72:75], s40 offen sc1\n"
    "s_add_u32 s40, s40, 2304\n"
    "s_cmp_le_u32 s10, 8\n"
    "s_cbranch_scc1 SL_15\n"
    "buffer_load_dwordx4 a[176:179], v234, s[72:75], s40 offen sc1\n"
    "s_add_u32 s40, s40, 2304\n"
    "s_cmp_le_u32 s10, 9\n"
    "s_cbranch_scc1 SL_15\n"
    "buffer_load_dwordx4 a[180:183], v234, s[72:75], s40 offen sc1\n"
    "s_add_u32 s40, s40, 2304\n"
    "s_cmp_le_u32 s10, 10\n"
    "s_cbranch_scc1 SL_15\n"
    "buffer_load_dwordx4 a[184:187], v234, s[72:75], s40 offen sc1\n"
    "s_add_u32 s40, s40, 2304\n"
    "s_cmp_le_u32 s10, 11\n"
    "s_cbranch_scc1 SL_15\n"
    "buffer_load_dwordx4 a[188:191], v234, s[72:75], s40 offen sc1\n"
    "s_add_u32 s40, s40, 2304\n"
    "s_cmp_le_u32 s10, 12\n"
    "s_cbranch_scc1 SL_15\n"
    "buffer_load_dwordx4 a[192:195], v234, s[72:75], s40 offen sc1\n"
    "s_add_u32 s40, s40, 2304\n"
    "s_cmp_le_u32 s10, 13\n"
    "s_cbranch_scc1 SL_15\n"
    "buffer_load_dwordx4 a[196:199], v234, s[72:75], s40 offen sc1\n"
    "s_add_u32 s40, s40, 2304\n"
    "s_cmp_le_u32 s10, 14\n"
    "s_cbranch_scc1 SL_15\n"
    "buffer_load_dwordx4 a[200:203], v234, s[72:75], s40 offen sc1\n"
    "s_add_u32 s40, s40, 2304\n"
    "SL_15:\n"
    "s_waitcnt vmcnt(0)\n"
    "v_mov_b32 v244, 0\n"
    "v_mov_b32 v245, 0\n"
    "v_mov_b32 v237, 0\n"
    "s_cmp_le_u32 s10, 0\n"
    "s_cbranch_scc1 SC_16\n"
    "v_accvgpr_read_b32 v240, a144\n"
    "v_accvgpr_read_b32 v241, a146\n"
    "v_accvgpr_read_b32 v238, a145\n"
    "v_accvgpr_read_b32 v239, a147\n"
    "v_fma_f32 v235, v250, v244, v240\n"
    "v_fma_f32 v236, v250, v245, v241\n"
    "v_xor_b32 v238, s84, v238\n"
    "v_xor_b32 v239, s85, v239\n"
    "v_fma_f32 v242, -v251, v245, v235\n"
    "v_fma_f32 v245, v251, v244, v236\n"
    "v_or3_b32 v237, v237, v238, v239\n"
    "v_mov_b32 v244, v242\n"
    "s_cmp_le_u32 s10, 1\n"
    "s_cbranch_scc1 SC_16\n"
    "v_accvgpr_read_b32 v240, a148\n"
    "v_accvgpr_read_b32 v241, a150\n"
    "v_accvgpr_read_b32 v238, a149\n"
    "v_accvgpr_read_b32 v239, a151\n"
    "v_fma_f32 v235, v250, v244, v240\n"
    "v_fma_f32 v236, v250, v245, v241\n"
    "v_xor_b32 v238, s84, v238\n"
    "v_xor_b32 v239, s85, v239\n"
    "v_fma_f32 v242, -v251, v245, v235\n"
    "v_fma_f32 v245, v251, v244, v236\n"
    "v_or3_b32 v237, v237, v238, v239\n"
    "v_mov_b32 v244, v242\n"
    "s_cmp_le_u32 s10, 2\n"
    "s_cbranch_scc1 SC_16\n"
    "v_accvgpr_read_b32 v240, a152\n"
    "v_accvgpr_read_b32 v241, a154\n"
    "v_accvgpr_read_b32 v238, a153\n"
    "v_accvgpr_read_b32 v239, a155\n"
    "v_fma_f32 v235, v250, v244, v240\n"
    "v_fma_f32 v236, v250, v245, v241\n"
    "v_xor_b32 v238, s84, v238\n"
    "v_xor_b32 v239, s85, v239\n"
    "v_fma_f32 v242, -v251, v245, v235\n"
    "v_fma_f32 v245, v251, v244, v236\n"
    "v_or3_b32 v237, v237, v238, v239\n"
    "v_mov_b32 v244, v242\n"
    "s_cmp_le_u32 s10, 3\n"
    "s_cbranch_scc1 SC_16\n"
    "v_accvgpr_read_b32 v240, a156\n"
    "v_accvgpr_read_b32 v241, a158\n"
    "v_accvgpr_read_b32 v238, a157\n"
    "v_accvgpr_read_b32 v239, a159\n"
    "v_fma_f32 v235, v250, v244, v240\n"
    "v_fma_f32 v236, v250, v245, v241\n"
    "v_xor_b32 v238, s84, v238\n"
    "v_xor_b32 v239, s85, v239\n"
    "v_fma_f32 v242, -v251, v245, v235\n"
    "v_fma_f32 v245, v251, v244, v236\n"
    "v_or3_b32 v237, v237, v238, v239\n"
    "v_mov_b32 v244, v242\n"
    "s_cmp_le_u32 s10, 4\n"
    "s_cbranch_scc1 SC_16\n"
    "v_accvgpr_read_b32 v240, a160\n"
    "v_accvgpr_read_b32 v241, a162\n"
    "v_accvgpr_read_b32 v238, a161\n"
    "v_accvgpr_read_b32 v239, a163\n"
    "v_fma_f32 v235, v250, v244, v240\n"
    "v_fma_f32 v236, v250, v245, v241\n"
    "v_xor_b32 v238, s84, v238\n"
    "v_xor_b32 v239, s85, v239\n"
    "v_fma_f32 v242, -v251, v245, v235\n"
    "v_fma_f32 v245, v251, v244, v236\n"
    "v_or3_b32 v237, v237, v238, v239\n"
    "v_mov_b32 v244, v242\n"
    "s_cmp_le_u32 s10, 5\n"
    "s_cbranch_scc1 SC_16\n"
    "v_accvgpr_read_b32 v240, a164\n"
    "v_accvgpr_read_b32 v241, a166\n"
    "v_accvgpr_read_b32 v238, a165\n"
    "v_accvgpr_read_b32 v239, a167\n"
    "v_fma_f32 v235, v250, v244, v240\n"
    "v_fma_f32 v236, v250, v245, v241\n"
    "v_xor_b32 v238, s84, v238\n"
    "v_xor_b32 v239, s85, v239\n"
    "v_fma_f32 v242, -v251, v245, v235\n"
    "v_fma_f32 v245, v251, v244, v236\n"
    "v_or3_b32 v237, v237, v238, v239\n"
    "v_mov_b32 v244, v242\n"
    "s_cmp_le_u32 s10, 6\n"
    "s_cbranch_scc1 SC_16\n"
    "v_accvgpr_read_b32 v240, a168\n"
    "v_accvgpr_read_b32 v241, a170\n"
    "v_accvgpr_read_b32 v238, a169\n"
    "v_accvgpr_read_b32 v239, a171\n"
    "v_fma_f32 v235, v250, v244, v240\n"
    "v_fma_f32 v236, v250, v245, v241\n"
    "v_xor_b32 v238, s84, v238\n"
    "v_xor_b32 v239, s85, v239\n"
    "v_fma_f32 v242, -v251, v245, v235\n"
    "v_fma_f32 v245, v251, v244, v236\n"
    "v_or3_b32 v237, v237, v238, v239\n"
    "v_mov_b32 v244, v242\n"
    "s_cmp_le_u32 s10, 7\n"
    "s_cbranch_scc1 SC_16\n"
    "v_accvgpr_read_b32 v240, a172\n"
    "v_accvgpr_read_b32 v241, a174\n"
    "v_accvgpr_read_b32 v238, a173\n"
    "v_accvgpr_read_b32 v239, a175\n"
    "v_fma_f32 v235, v250, v244, v240\n"
    "v_fma_f32 v236, v250, v245, v241\n"
    "v_xor_b32 v238, s84, v238\n"
    "v_xor_b32 v239, s85, v239\n"
    "v_fma_f32 v242, -v251, v245, v235\n"
    "v_fma_f32 v245, v251, v244, v236\n"
    "v_or3_b32 v237, v237, v238, v239\n"
    "v_mov_b32 v244, v242\n"
    "s_cmp_le_u32 s10, 8\n"
    "s_cbranch_scc1 SC_16\n"
    "v_accvgpr_read_b32 v240, a176\n"
    "v_accvgpr_read_b32 v241, a178\n"
    "v_accvgpr_read_b32 v238, a177\n"
    "v_accvgpr_read_b32 v239, a179\n"
    "v_fma_f32 v235, v250, v244, v240\n"
    "v_fma_f32 v236, v250, v245, v241\n"
    "v_xor_b32 v238, s84, v238\n"
    "v_xor_b32 v239, s85, v239\n"
    "v_fma_f32 v242, -v251, v245, v235\n"
    "v_fma_f32 v245, v251, v244, v236\n"
    "v_or3_b32 v237, v237, v238, v239\n"
    "v_mov_b32 v244, v242\n"
    "s_cmp_le_u32 s10, 9\n"
    "s_cbranch_scc1 SC_16\n"
    "v_accvgpr_read_b32 v240, a180\n"
    "v_accvgpr_read_b32 v241, a182\n"
    "v_accvgpr_read_b32 v238, a181\n"
    "v_accvgpr_read_b32 v239, a183\n"
    "v_fma_f32 v235, v250, v244, v240\n"
    "v_fma_f32 v236, v250, v245, v241\n"
    "v_xor_b32 v238, s84, v238\n"
    "v_xor_b32 v239, s85, v239\n"
    "v_fma_f32 v242, -v251, v245, v235\n"
    "v_fma_f32 v245, v251, v244, v236\n"
    "v_or3_b32 v237, v237, v238, v239\n"
    "v_mov_b32 v244, v242\n"
    "s_cmp_le_u32 s10, 10\n"
    "s_cbranch_scc1 SC_16\n"
    "v_accvgpr_read_b32 v240, a184\n"
    "v_accvgpr_read_b32 v241, a186\n"
    "v_accvgpr_read_b32 v238, a185\n"
    "v_accvgpr_read_b32 v239, a187\n"
    "v_fma_f32 v235, v250, v244, v240\n"
    "v_fma_f32 v236, v250, v245, v241\n"
    "v_xor_b32 v238, s84, v238\n"
    "v_xor_b32 v239, s85, v239\n"
    "v_fma_f32 v242, -v251, v245, v235\n"
    "v_fma_f32 v245, v251, v244, v236\n"
    "v_or3_b32 v237, v237, v238, v239\n"
    "v_mov_b32 v244, v242\n"
    "s_cmp_le_u32 s10, 11\n"
    "s_cbranch_scc1 SC_16\n"
    "v_accvgpr_read_b32 v240, a188\n"
    "v_accvgpr_read_b32 v241, a190\n"
    "v_accvgpr_read_b32 v238, a189\n"
    "v_accvgpr_read_b32 v239, a191\n"
    "v_fma_f32 v235, v250, v244, v240\n"
    "v_fma_f32 v236, v250, v245, v241\n"
    "v_xor_b32 v238, s84, v238\n"
    "v_xor_b32 v239, s85, v239\n"
    "v_fma_f32 v242, -v251, v245, v235\n"
    "v_fma_f32 v245, v251, v244, v236\n"
    "v_or3_b32 v237, v237, v238, v239\n"
    "v_mov_b32 v244, v242\n"
    "s_cmp_le_u32 s10, 12\n"
    "s_cbranch_scc1 SC_16\n"
    "v_accvgpr_read_b32 v240, a192\n"
    "v_accvgpr_read_b32 v241, a194\n"
    "v_accvgpr_read_b32 v238, a193\n"
    "v_accvgpr_read_b32 v239, a195\n"
    "v_fma_f32 v235, v250, v244, v240\n"
    "v_fma_f32 v236, v250, v245, v241\n"
    "v_xor_b32 v238, s84, v238\n"
    "v_xor_b32 v239, s85, v239\n"
    "v_fma_f32 v242, -v251, v245, v235\n"
    "v_fma_f32 v245, v251, v244, v236\n"
    "v_or3_b32 v237, v237, v238, v239\n"
    "v_mov_b32 v244, v242\n"
    "s_cmp_le_u32 s10, 13\n"
    "s_cbranch_scc1 SC_16\n"
    "v_accvgpr_read_b32 v240, a196\n"
    "v_accvgpr_read_b32 v241, a198\n"
    "v_accvgpr_read_b32 v238, a197\n"
    "v_accvgpr_read_b32 v239, a199\n"
    "v_fma_f32 v235, v250, v244, v240\n"
    "v_fma_f32 v236, v250, v245, v241\n"
    "v_xor_b32 v238, s84, v238\n"
    "v_xor_b32 v239, s85, v239\n"
    "v_fma_f32 v242, -v251, v245, v235\n"
    "v_fma_f32 v245, v251, v244, v236\n"
    "v_or3_b32 v237, v237, v238, v239\n"
    "v_mov_b32 v244, v242\n"
    "s_cmp_le_u32 s10, 14\n"
    "s_cbranch_scc1 SC_16\n"
    "v_accvgpr_read_b32 v240, a200\n"
    "v_accvgpr_read_b32 v241, a202\n"
    "v_accvgpr_read_b32 v238, a201\n"
    "v_accvgpr_read_b32 v239, a203\n"
    "v_fma_f32 v235, v250, v244, v240\n"
    "v_fma_f32 v236, v250, v245, v241\n"
    "v_xor_b32 v238, s84, v238\n"
    "v_xor_b32 v239, s85, v239\n"
    "v_fma_f32 v242, -v251, v245, v235\n"
    "v_fma_f32 v245, v251, v244, v236\n"
    "v_or3_b32 v237, v237, v238, v239\n"
    "v_mov_b32 v244, v242\n"
    "SC_16:\n"
    "v_cmp_eq_u32 vcc, 0, v237\n"
    "s_nop 1\n"
    "s_cmp_eq_u64 vcc, s[46:47]\n"
    "s_cbranch_scc1 SG_17\n"
    "s_add_u32 s55, s55, 1\n"
    "s_cmp_gt_u32 s55, 20000\n"
    "s_cbranch_scc1 SG_17\n"
    "s_sleep 1\n"
    "s_branch SP_14\n"
    "SG_17:\n"
    "v_lshlrev_b32 v235, 3, v189\n"
    "v_add_u32 v235, 149760, v235\n"
    "ds_write_b64 v235, v[244:245]\n"
    "s_mov_b64 exec, -1\n"
    "s_waitcnt lgkmcnt(0)\n"
    "CSK_13:\n"
    "s_or_b32 s40, s10, s11\n"
    "s_xor_b32 s41, s9, 3\n"
    "s_or_b32 s40, s40, s41\n"
    "s_cmp_lg_u32 s40, 0\n"
    "s_cbranch_scc1 NB_19\n"
    "s_mov_b32 s55, 0\n"
    "BL_20:\n"
    "v_lshlrev_b32 v240, 3, v0\n"
    "s_mov_b32 s40, 4096\n"
    "s_mov_b64 s[56:57], -1\n"
    "buffer_load_dwordx2 a[144:145], v240, s[72:75], s40 offen sc1\n"
    "s_add_u32 s40, s40, 512\n"
    "buffer_load_dwordx2 a[146:147], v240, s[72:75], s40 offen sc1\n"
    "s_add_u32 s40, s40, 512\n"
    "buffer_load_dwordx2 a[148:149], v240, s[72:75], s40 offen sc1\n"
    "s_add_u32 s40, s40, 512\n"
    "buffer_load_dwordx2 a[150:151], v240, s[72:75], s40 offen sc1\n"
    "s_add_u32 s40, s40, 512\n"
    "buffer_load_dwordx2 a[152:153], v240, s[72:75], s40 offen sc1\n"
    "s_add_u32 s40, s40, 512\n"
    "buffer_load_dwordx2 a[154:155], v240, s[72:75], s40 offen sc1\n"
    "s_add_u32 s40, s40, 512\n"
    "buffer_load_dwordx2 a[156:157], v240, s[72:75], s40 offen sc1\n"
    "s_add_u32 s40, s40, 512\n"
    "buffer_load_dwordx2 a[158:159], v240, s[72:75], s40 offen sc1\n"
    "s_add_u32 s40, s40, 512\n"
    "buffer_load_dwordx2 a[160:161], v240, s[72:75], s40 offen sc1\n"
    "s_add_u32 s40, s40, 512\n"
    "buffer_load_dwordx2 a[162:163], v240, s[72:75], s40 offen sc1\n"
    "s_add_u32 s40, s40, 512\n"
    "buffer_load_dwordx2 a[164:165], v240, s[72:75], s40 offen sc1\n"
    "s_add_u32 s40, s40, 512\n"
    "buffer_load_dwordx2 a[166:167], v240, s[72:75], s40 offen sc1\n"
    "s_add_u32 s40, s40, 512\n"
    "buffer_load_dwordx2 a[168:169], v240, s[72:75], s40 offen sc1\n"
    "s_add_u32 s40, s40, 512\n"
    "buffer_load_dwordx2 a[170:171], v240, s[72:75], s40 offen sc1\n"
    "s_add_u32 s40, s40, 512\n"
    "buffer_load_dwordx2 a[172:173], v240, s[72:75], s40 offen sc1\n"
    "s_add_u32 s40, s40, 512\n"
    "buffer_load_dwordx2 a[174:175], v240, s[72:75], s40 offen sc1\n"
    "s_add_u32 s40, s40, 512\n"
    "s_waitcnt vmcnt(0)\n"
    "v_accvgpr_read_b32 v235, a144\n"
    "v_accvgpr_read_b32 v236, a145\n"
    "v_cmp_eq_u32 vcc, s84, v235\n"
    "v_cmp_eq_u32 s[42:43], s85, v236\n"
    "s_nop 0\n"
    "s_and_b64 s[56:57], s[56:57], vcc\n"
    "s_and_b64 s[56:57], s[56:57], s[42:43]\n"
    "v_accvgpr_read_b32 v235, a146\n"
    "v_accvgpr_read_b32 v236, a147\n"
    "v_cmp_eq_u32 vcc, s84, v235\n"
    "v_cmp_eq_u32 s[42:43], s85, v236\n"
    "s_nop 0\n"
    "s_and_b64 s[56:57], s[56:57], vcc\n"
    "s_and_b64 s[56:57], s[56:57], s[42:43]\n"
    "v_accvgpr_read_b32 v235, a148\n"
    "v_accvgpr_read_b32 v236, a149\n"
    "v_cmp_eq_u32 vcc, s84, v235\n"
    "v_cmp_eq_u32 s[42:43], s85, v236\n"
    "s_nop 0\n"
    "s_and_b64 s[56:57], s[56:57], vcc\n"
    "s_and_b64 s[56:57], s[56:57], s[42:43]\n"
    "v_accvgpr_read_b32 v235, a150\n"
    "v_accvgpr_read_b32 v236, a151\n"
    "v_cmp_eq_u32 vcc, s84, v235\n"
    "v_cmp_eq_u32 s[42:43], s85, v236\n"
    "s_nop 0\n"
    "s_and_b64 s[56:57], s[56:57], vcc\n"
    "s_and_b64 s[56:57], s[56:57], s[42:43]\n"
    "v_accvgpr_read_b32 v235, a152\n"
    "v_accvgpr_read_b32 v236, a153\n"
    "v_cmp_eq_u32 vcc, s84, v235\n"
    "v_cmp_eq_u32 s[42:43], s85, v236\n"
    "s_nop 0\n"
    "s_and_b64 s[56:57], s[56:57], vcc\n"
    "s_and_b64 s[56:57], s[56:57], s[42:43]\n"
    "v_accvgpr_read_b32 v235, a154\n"
    "v_accvgpr_read_b32 v236, a155\n"
    "v_cmp_eq_u32 vcc, s84, v235\n"
    "v_cmp_eq_u32 s[42:43], s85, v236\n"
    "s_nop 0\n"
    "s_and_b64 s[56:57], s[56:57], vcc\n"
    "s_and_b64 s[56:57], s[56:57], s[42:43]\n"
    "v_accvgpr_read_b32 v235, a156\n"
    "v_accvgpr_read_b32 v236, a157\n"
    "v_cmp_eq_u32 vcc, s84, v235\n"
    "v_cmp_eq_u32 s[42:43], s85, v236\n"
    "s_nop 0\n"
    "s_and_b64 s[56:57], s[56:57], vcc\n"
    "s_and_b64 s[56:57], s[56:57], s[42:43]\n"
    "v_accvgpr_read_b32 v235, a158\n"
    "v_accvgpr_read_b32 v236, a159\n"
    "v_cmp_eq_u32 vcc, s84, v235\n"
    "v_cmp_eq_u32 s[42:43], s85, v236\n"
    "s_nop 0\n"
    "s_and_b64 s[56:57], s[56:57], vcc\n"
    "s_and_b64 s[56:57], s[56:57], s[42:43]\n"
    "v_accvgpr_read_b32 v235, a160\n"
    "v_accvgpr_read_b32 v236, a161\n"
    "v_cmp_eq_u32 vcc, s84, v235\n"
    "v_cmp_eq_u32 s[42:43], s85, v236\n"
    "s_nop 0\n"
    "s_and_b64 s[56:57], s[56:57], vcc\n"
    "s_and_b64 s[56:57], s[56:57], s[42:43]\n"
    "v_accvgpr_read_b32 v235, a162\n"
    "v_accvgpr_read_b32 v236, a163\n"
    "v_cmp_eq_u32 vcc, s84, v235\n"
    "v_cmp_eq_u32 s[42:43], s85, v236\n"
    "s_nop 0\n"
    "s_and_b64 s[56:57], s[56:57], vcc\n"
    "s_and_b64 s[56:57], s[56:57], s[42:43]\n"
    "v_accvgpr_read_b32 v235, a164\n"
    "v_accvgpr_read_b32 v236, a165\n"
    "v_cmp_eq_u32 vcc, s84, v235\n"
    "v_cmp_eq_u32 s[42:43], s85, v236\n"
    "s_nop 0\n"
    "s_and_b64 s[56:57], s[56:57], vcc\n"
    "s_and_b64 s[56:57], s[56:57], s[42:43]\n"
    "v_accvgpr_read_b32 v235, a166\n"
    "v_accvgpr_read_b32 v236, a167\n"
    "v_cmp_eq_u32 vcc, s84, v235\n"
    "v_cmp_eq_u32 s[42:43], s85, v236\n"
    "s_nop 0\n"
    "s_and_b64 s[56:57], s[56:57], vcc\n"
    "s_and_b64 s[56:57], s[56:57], s[42:43]\n"
    "v_accvgpr_read_b32 v235, a168\n"
    "v_accvgpr_read_b32 v236, a169\n"
    "v_cmp_eq_u32 vcc, s84, v235\n"
    "v_cmp_eq_u32 s[42:43], s85, v236\n"
    "s_nop 0\n"
    "s_and_b64 s[56:57], s[56:57], vcc\n"
    "s_and_b64 s[56:57], s[56:57], s[42:43]\n"
    "v_accvgpr_read_b32 v235, a170\n"
    "v_accvgpr_read_b32 v236, a171\n"
    "v_cmp_eq_u32 vcc, s84, v235\n"
    "v_cmp_eq_u32 s[42:43], s85, v236\n"
    "s_nop 0\n"
    "s_and_b64 s[56:57], s[56:57], vcc\n"
    "s_and_b64 s[56:57], s[56:57], s[42:43]\n"
    "v_accvgpr_read_b32 v235, a172\n"
    "v_accvgpr_read_b32 v236, a173\n"
    "v_cmp_eq_u32 vcc, s84, v235\n"
    "v_cmp_eq_u32 s[42:43], s85, v236\n"
    "s_nop 0\n"
    "s_and_b64 s[56:57], s[56:57], vcc\n"
    "s_and_b64 s[56:57], s[56:57], s[42:43]\n"
    "v_accvgpr_read_b32 v235, a174\n"
    "v_accvgpr_read_b32 v236, a175\n"
    "v_cmp_eq_u32 vcc, s84, v235\n"
    "v_cmp_eq_u32 s[42:43], s85, v236\n"
    "s_nop 0\n"
    "s_and_b64 s[56:57], s[56:57], vcc\n"
    "s_and_b64 s[56:57], s[56:57], s[42:43]\n"
    "s_cmp_eq_u64 s[56:57], -1\n"
    "s_cbranch_scc1 BO_21\n"
    "s_add_u32 s55, s55, 1\n"
    "s_cmp_gt_u32 s55, 20000\n"
    "s_cbranch_scc1 NB_19\n"
    "s_sleep 1\n"
    "s_branch BL_20\n"
    "BO_21:\n"
    "s_mov_b32 s40, 0\n"
    "v_mov_b32 v241, 0x7fffffff\n"
    "v_cmp_eq_u32 vcc, 0, v0\n"
    "s_nop 1\n"
    "v_cndmask_b32 v241, v241, 0, vcc\n"
    "v_mov_b32 v242, s84\n"
    "v_mov_b32 v243, s85\n"
    "buffer_store_dwordx2 v[242:243], v241, s[72:75], s40 offen sc1\n"
    "NB_19:\n"
    "s_waitcnt lgkmcnt(0)\n"
    "s_barrier\n"
    "v_and_b32 v190, 15, v0\n"
    "v_lshrrev_b32 v191, 4, v0\n"
    "s_lshl_b32 s40, s9, 11\n"
    "s_add_u32 s40, s40, 152064\n"
    "v_lshlrev_b32 v192, 8, v191\n"
    "v_lshrrev_b32 v193, 3, v190\n"
    "v_lshl_add_u32 v192, v193, 7, v192\n"
    "v_lshrrev_b32 v193, 2, v190\n"
    "v_xor_b32 v193, v193, v191\n"
    "v_and_b32 v193, 1, v193\n"
    "v_lshl_add_u32 v192, v193, 6, v192\n"
    "v_and_b32 v193, 3, v190\n"
    "v_lshl_add_u32 v192, v193, 4, v192\n"
    "v_add_u32 v3, s40, v192\n"
    "v_lshrrev_b32 v192, 1, v191\n"
    "v_lshlrev_b32 v192, 10, v192\n"
    "v_and_b32 v193, 1, v191\n"
    "v_lshl_add_u32 v192, v193, 7, v192\n"
    "v_bfe_u32 v193, v0, 1, 1\n"
    "v_mul_u32_u24 v193, 320, v193\n"
    "v_add_u32 v192, v192, v193\n"
    "v_bfe_u32 v193, v0, 2, 2\n"
    "v_lshl_add_u32 v192, v193, 4, v192\n"
    "v_and_b32 v193, 1, v0\n"
    "v_lshl_add_u32 v192, v193, 3, v192\n"
    "v_add_u32 v4, s40, v192\n"
    "v_xor_b32 v14, 64, v4\n"
    "v_subrev_u32 v5, 18432, v11\n"
    "v_and_b32 v15, 15, v0\n"
    "v_lshlrev_b32 v15, 3, v15\n"
    "v_add_u32 v15, 149760, v15\n"
    "ds_read_b128 v[176:179], v9 offset:0\n"
    "ds_read_b64 v[232:233], v5 offset:0\n"
    "ds_read_b64 v[234:235], v15 offset:0\n"
    "ds_read_b64 v[190:191], v11 offset:0\n"
    "ds_read_b128 v[200:203], v9 offset:256\n"
    "ds_read_b64 v[236:237], v5 offset:128\n"
    "ds_read_b64 v[238:239], v15 offset:128\n"
    "ds_read_b64 v[204:205], v11 offset:128\n"
    "s_waitcnt lgkmcnt(4)\n"
    "v_fma_f32 v190, v232, v234, v190\n"
    "v_fma_f32 v191, v232, v235, v191\n"
    "v_fma_f32 v190, -v233, v235, v190\n"
    "v_fma_f32 v191, v233, v234, v191\n"
    "v_fma_f32 v194, v176, v190, v32\n"
    "v_fma_f32 v195, v176, v191, v40\n"
    "v_fma_f32 v192, -v177, v191, v194\n"
    "v_fma_f32 v193, v177, v190, v195\n"
    "v_mul_f32 v32, v178, v192\n"
    "v_mul_f32 v40, -v178, v193\n"
    "v_fma_f32 v32, -v179, v193, v32\n"
    "v_fma_f32 v40, -v179, v192, v40\n"
    "v_fma_f32 v194, v176, v192, v33\n"
    "v_fma_f32 v195, v176, v193, v41\n"
    "v_fma_f32 v196, -v177, v193, v194\n"
    "v_fma_f32 v197, v177, v192, v195\n"
    "v_mul_f32 v33, v178, v196\n"
    "v_mul_f32 v41, -v178, v197\n"
    "v_fma_f32 v33, -v179, v197, v33\n"
    "v_fma_f32 v41, -v179, v196, v41\n"
    "v_fma_f32 v194, v176, v196, v34\n"
    "v_fma_f32 v195, v176, v197, v42\n"
    "v_fma_f32 v192, -v177, v197, v194\n"
    "v_fma_f32 v193, v177, v196, v195\n"
    "v_mul_f32 v34, v178, v192\n"
    "v_mul_f32 v42, -v178, v193\n"
    "v_fma_f32 v34, -v179, v193, v34\n"
    "v_fma_f32 v42, -v179, v192, v42\n"
    "v_fma_f32 v194, v176, v192, v35\n"
    "v_fma_f32 v195, v176, v193, v43\n"
    "v_fma_f32 v196, -v177, v193, v194\n"
    "v_fma_f32 v197, v177, v192, v195\n"
    "v_mul_f32 v35, v178, v196\n"
    "v_mul_f32 v43, -v178, v197\n"
    "v_fma_f32 v35, -v179, v197, v35\n"
    "v_fma_f32 v43, -v179, v196, v43\n"
    "v_fma_f32 v194, v176, v196, v36\n"
    "v_fma_f32 v195, v176, v197, v44\n"
    "v_fma_f32 v192, -v177, v197, v194\n"
    "v_fma_f32 v193, v177, v196, v195\n"
    "v_mul_f32 v36, v178, v192\n"
    "v_mul_f32 v44, -v178, v193\n"
    "v_fma_f32 v36, -v179, v193, v36\n"
    "v_fma_f32 v44, -v179, v192, v44\n"
    "v_fma_f32 v194, v176, v192, v37\n"
    "v_fma_f32 v195, v176, v193, v45\n"
    "v_fma_f32 v196, -v177, v193, v194\n"
    "v_fma_f32 v197, v177, v192, v195\n"
    "v_mul_f32 v37, v178, v196\n"
    "v_mul_f32 v45, -v178, v197\n"
    "v_fma_f32 v37, -v179, v197, v37\n"
    "v_fma_f32 v45, -v179, v196, v45\n"
    "v_fma_f32 v194, v176, v196, v38\n"
    "v_fma_f32 v195, v176, v197, v46\n"
    "v_fma_f32 v192, -v177, v197, v194\n"
    "v_fma_f32 v193, v177, v196, v195\n"
    "v_mul_f32 v38, v178, v192\n"
    "v_mul_f32 v46, -v178, v193\n"
    "v_fma_f32 v38, -v179, v193, v38\n"
    "v_fma_f32 v46, -v179, v192, v46\n"
    "v_fma_f32 v194, v176, v192, v39\n"
    "v_fma_f32 v195, v176, v193, v47\n"
    "v_fma_f32 v196, -v177, v193, v194\n"
    "v_fma_f32 v197, v177, v192, v195\n"
    "v_mul_f32 v39, v178, v196\n"
    "v_mul_f32 v47, -v178, v197\n"
    "v_fma_f32 v39, -v179, v197, v39\n"
    "v_fma_f32 v47, -v179, v196, v47\n"
    "v_cvt_pk_bf16_f32 v216, v32, v33\n"
    "v_cvt_pk_bf16_f32 v217, v34, v35\n"
    "v_cvt_pk_bf16_f32 v218, v36, v37\n"
    "v_cvt_pk_bf16_f32 v219, v38, v39\n"
    "ds_write_b128 v3, v[216:219] offset:0\n"
    "v_cvt_pk_bf16_f32 v220, v40, v41\n"
    "v_cvt_pk_bf16_f32 v221, v42, v43\n"
    "v_cvt_pk_bf16_f32 v222, v44, v45\n"
    "v_cvt_pk_bf16_f32 v223, v46, v47\n"
    "ds_write_b128 v3, v[220:223] offset:1024\n"
    "ds_read_b64_tr_b16 v[32:33], v4 offset:0\n"
    "ds_read_b64_tr_b16 v[34:35], v14 offset:0\n"
    "ds_read_b64_tr_b16 v[36:37], v4 offset:512\n"
    "ds_read_b64_tr_b16 v[38:39], v14 offset:512\n"
    "ds_read_b128 v[176:179], v9 offset:512\n"
    "ds_read_b64 v[232:233], v5 offset:256\n"
    "ds_read_b64 v[234:235], v15 offset:256\n"
    "ds_read_b64 v[190:191], v11 offset:256\n"
    "s_waitcnt lgkmcnt(10)\n"
    "v_fma_f32 v204, v236, v238, v204\n"
    "v_fma_f32 v205, v236, v239, v205\n"
    "v_fma_f32 v204, -v237, v239, v204\n"
    "v_fma_f32 v205, v237, v238, v205\n"
    "v_fma_f32 v194, v200, v204, v48\n"
    "v_fma_f32 v195, v200, v205, v56\n"
    "v_fma_f32 v192, -v201, v205, v194\n"
    "v_fma_f32 v193, v201, v204, v195\n"
    "v_mul_f32 v48, v202, v192\n"
    "v_mul_f32 v56, -v202, v193\n"
    "v_fma_f32 v48, -v203, v193, v48\n"
    "v_fma_f32 v56, -v203, v192, v56\n"
    "v_fma_f32 v194, v200, v192, v49\n"
    "v_fma_f32 v195, v200, v193, v57\n"
    "v_fma_f32 v196, -v201, v193, v194\n"
    "v_fma_f32 v197, v201, v192, v195\n"
    "v_mul_f32 v49, v202, v196\n"
    "v_mul_f32 v57, -v202, v197\n"
    "v_fma_f32 v49, -v203, v197, v49\n"
    "v_fma_f32 v57, -v203, v196, v57\n"
    "v_fma_f32 v194, v200, v196, v50\n"
    "v_fma_f32 v195, v200, v197, v58\n"
    "v_fma_f32 v192, -v201, v197, v194\n"
    "v_fma_f32 v193, v201, v196, v195\n"
    "v_mul_f32 v50, v202, v192\n"
    "v_mul_f32 v58, -v202, v193\n"
    "v_fma_f32 v50, -v203, v193, v50\n"
    "v_fma_f32 v58, -v203, v192, v58\n"
    "v_fma_f32 v194, v200, v192, v51\n"
    "v_fma_f32 v195, v200, v193, v59\n"
    "v_fma_f32 v196, -v201, v193, v194\n"
    "v_fma_f32 v197, v201, v192, v195\n"
    "v_mul_f32 v51, v202, v196\n"
    "v_mul_f32 v59, -v202, v197\n"
    "v_fma_f32 v51, -v203, v197, v51\n"
    "v_fma_f32 v59, -v203, v196, v59\n"
    "v_fma_f32 v194, v200, v196, v52\n"
    "v_fma_f32 v195, v200, v197, v60\n"
    "v_fma_f32 v192, -v201, v197, v194\n"
    "v_fma_f32 v193, v201, v196, v195\n"
    "v_mul_f32 v52, v202, v192\n"
    "v_mul_f32 v60, -v202, v193\n"
    "v_fma_f32 v52, -v203, v193, v52\n"
    "v_fma_f32 v60, -v203, v192, v60\n"
    "v_fma_f32 v194, v200, v192, v53\n"
    "v_fma_f32 v195, v200, v193, v61\n"
    "v_fma_f32 v196, -v201, v193, v194\n"
    "v_fma_f32 v197, v201, v192, v195\n"
    "v_mul_f32 v53, v202, v196\n"
    "v_mul_f32 v61, -v202, v197\n"
    "v_fma_f32 v53, -v203, v197, v53\n"
    "v_fma_f32 v61, -v203, v196, v61\n"
    "v_fma_f32 v194, v200, v196, v54\n"
    "v_fma_f32 v195, v200, v197, v62\n"
    "v_fma_f32 v192, -v201, v197, v194\n"
    "v_fma_f32 v193, v201, v196, v195\n"
    "v_mul_f32 v54, v202, v192\n"
    "v_mul_f32 v62, -v202, v193\n"
    "v_fma_f32 v54, -v203, v193, v54\n"
    "v_fma_f32 v62, -v203, v192, v62\n"
    "v_fma_f32 v194, v200, v192, v55\n"
    "v_fma_f32 v195, v200, v193, v63\n"
    "v_fma_f32 v196, -v201, v193, v194\n"
    "v_fma_f32 v197, v201, v192, v195\n"
    "v_mul_f32 v55, v202, v196\n"
    "v_mul_f32 v63, -v202, v197\n"
    "v_fma_f32 v55, -v203, v197, v55\n"
    "v_fma_f32 v63, -v203, v196, v63\n"
    "v_cvt_pk_bf16_f32 v224, v48, v49\n"
    "v_cvt_pk_bf16_f32 v225, v50, v51\n"
    "v_cvt_pk_bf16_f32 v226, v52, v53\n"
    "v_cvt_pk_bf16_f32 v227, v54, v55\n"
    "ds_write_b128 v3, v[224:227] offset:0\n"
    "v_cvt_pk_bf16_f32 v228, v56, v57\n"
    "v_cvt_pk_bf16_f32 v229, v58, v59\n"
    "v_cvt_pk_bf16_f32 v230, v60, v61\n"
    "v_cvt_pk_bf16_f32 v231, v62, v63\n"
    "ds_write_b128 v3, v[228:231] offset:1024\n"
    "ds_read_b64_tr_b16 v[48:49], v4 offset:0\n"
    "ds_read_b64_tr_b16 v[50:51], v14 offset:0\n"
    "ds_read_b64_tr_b16 v[52:53], v4 offset:512\n"
    "ds_read_b64_tr_b16 v[54:55], v14 offset:512\n"
    "ds_read_b128 v[200:203], v9 offset:768\n"
    "ds_read_b64 v[236:237], v5 offset:384\n"
    "ds_read_b64 v[238:239], v15 offset:384\n"
    "ds_read_b64 v[204:205], v11 offset:384\n"
    "s_waitcnt lgkmcnt(10)\n"
    "v_fma_f32 v190, v232, v234, v190\n"
    "v_fma_f32 v191, v232, v235, v191\n"
    "v_fma_f32 v190, -v233, v235, v190\n"
    "v_fma_f32 v191, v233, v234, v191\n"
    "v_fma_f32 v194, v176, v190, v64\n"
    "v_fma_f32 v195, v176, v191, v72\n"
    "v_fma_f32 v192, -v177, v191, v194\n"
    "v_fma_f32 v193, v177, v190, v195\n"
    "v_mul_f32 v64, v178, v192\n"
    "v_mul_f32 v72, -v178, v193\n"
    "v_fma_f32 v64, -v179, v193, v64\n"
    "v_fma_f32 v72, -v179, v192, v72\n"
    "v_fma_f32 v194, v176, v192, v65\n"
    "v_fma_f32 v195, v176, v193, v73\n"
    "v_fma_f32 v196, -v177, v193, v194\n"
    "v_fma_f32 v197, v177, v192, v195\n"
    "v_mul_f32 v65, v178, v196\n"
    "v_mul_f32 v73, -v178, v197\n"
    "v_fma_f32 v65, -v179, v197, v65\n"
    "v_fma_f32 v73, -v179, v196, v73\n"
    "v_fma_f32 v194, v176, v196, v66\n"
    "v_fma_f32 v195, v176, v197, v74\n"
    "v_fma_f32 v192, -v177, v197, v194\n"
    "v_fma_f32 v193, v177, v196, v195\n"
    "v_mul_f32 v66, v178, v192\n"
    "v_mul_f32 v74, -v178, v193\n"
    "v_fma_f32 v66, -v179, v193, v66\n"
    "v_fma_f32 v74, -v179, v192, v74\n"
    "v_fma_f32 v194, v176, v192, v67\n"
    "v_fma_f32 v195, v176, v193, v75\n"
    "v_fma_f32 v196, -v177, v193, v194\n"
    "v_fma_f32 v197, v177, v192, v195\n"
    "v_mul_f32 v67, v178, v196\n"
    "v_mul_f32 v75, -v178, v197\n"
    "v_fma_f32 v67, -v179, v197, v67\n"
    "v_fma_f32 v75, -v179, v196, v75\n"
    "v_fma_f32 v194, v176, v196, v68\n"
    "v_fma_f32 v195, v176, v197, v76\n"
    "v_fma_f32 v192, -v177, v197, v194\n"
    "v_fma_f32 v193, v177, v196, v195\n"
    "v_mul_f32 v68, v178, v192\n"
    "v_mul_f32 v76, -v178, v193\n"
    "v_fma_f32 v68, -v179, v193, v68\n"
    "v_fma_f32 v76, -v179, v192, v76\n"
    "v_fma_f32 v194, v176, v192, v69\n"
    "v_fma_f32 v195, v176, v193, v77\n"
    "v_fma_f32 v196, -v177, v193, v194\n"
    "v_fma_f32 v197, v177, v192, v195\n"
    "v_mul_f32 v69, v178, v196\n"
    "v_mul_f32 v77, -v178, v197\n"
    "v_fma_f32 v69, -v179, v197, v69\n"
    "v_fma_f32 v77, -v179, v196, v77\n"
    "v_fma_f32 v194, v176, v196, v70\n"
    "v_fma_f32 v195, v176, v197, v78\n"
    "v_fma_f32 v192, -v177, v197, v194\n"
    "v_fma_f32 v193, v177, v196, v195\n"
    "v_mul_f32 v70, v178, v192\n"
    "v_mul_f32 v78, -v178, v193\n"
    "v_fma_f32 v70, -v179, v193, v70\n"
    "v_fma_f32 v78, -v179, v192, v78\n"
    "v_fma_f32 v194, v176, v192, v71\n"
    "v_fma_f32 v195, v176, v193, v79\n"
    "v_fma_f32 v196, -v177, v193, v194\n"
    "v_fma_f32 v197, v177, v192, v195\n"
    "v_mul_f32 v71, v178, v196\n"
    "v_mul_f32 v79, -v178, v197\n"
    "v_fma_f32 v71, -v179, v197, v71\n"
    "v_fma_f32 v79, -v179, v196, v79\n"
    "v_cvt_pk_bf16_f32 v216, v64, v65\n"
    "v_cvt_pk_bf16_f32 v217, v66, v67\n"
    "v_cvt_pk_bf16_f32 v218, v68, v69\n"
    "v_cvt_pk_bf16_f32 v219, v70, v71\n"
    "ds_write_b128 v3, v[216:219] offset:0\n"
    "v_cvt_pk_bf16_f32 v220, v72, v73\n"
    "v_cvt_pk_bf16_f32 v221, v74, v75\n"
    "v_cvt_pk_bf16_f32 v222, v76, v77\n"
    "v_cvt_pk_bf16_f32 v223, v78, v79\n"
    "ds_write_b128 v3, v[220:223] offset:1024\n"
    "ds_read_b64_tr_b16 v[64:65], v4 offset:0\n"
    "ds_read_b64_tr_b16 v[66:67], v14 offset:0\n"
    "ds_read_b64_tr_b16 v[68:69], v4 offset:512\n"
    "ds_read_b64_tr_b16 v[70:71], v14 offset:512\n"
    "ds_read_b128 v[176:179], v9 offset:1024\n"
    "ds_read_b64 v[232:233], v5 offset:512\n"
    "ds_read_b64 v[234:235], v15 offset:512\n"
    "ds_read_b64 v[190:191], v11 offset:512\n"
    "s_waitcnt lgkmcnt(10)\n"
    "v_fma_f32 v204, v236, v238, v204\n"
    "v_fma_f32 v205, v236, v239, v205\n"
    "v_fma_f32 v204, -v237, v239, v204\n"
    "v_fma_f32 v205, v237, v238, v205\n"
    "v_fma_f32 v194, v200, v204, v80\n"
    "v_fma_f32 v195, v200, v205, v88\n"
    "v_fma_f32 v192, -v201, v205, v194\n"
    "v_fma_f32 v193, v201, v204, v195\n"
    "v_mul_f32 v80, v202, v192\n"
    "v_mul_f32 v88, -v202, v193\n"
    "v_fma_f32 v80, -v203, v193, v80\n"
    "v_fma_f32 v88, -v203, v192, v88\n"
    "v_fma_f32 v194, v200, v192, v81\n"
    "v_fma_f32 v195, v200, v193, v89\n"
    "v_fma_f32 v196, -v201, v193, v194\n"
    "v_fma_f32 v197, v201, v192, v195\n"
    "v_mul_f32 v81, v202, v196\n"
    "v_mul_f32 v89, -v202, v197\n"
    "v_fma_f32 v81, -v203, v197, v81\n"
    "v_fma_f32 v89, -v203, v196, v89\n"
    "v_fma_f32 v194, v200, v196, v82\n"
    "v_fma_f32 v195, v200, v197, v90\n"
    "v_fma_f32 v192, -v201, v197, v194\n"
    "v_fma_f32 v193, v201, v196, v195\n"
    "v_mul_f32 v82, v202, v192\n"
    "v_mul_f32 v90, -v202, v193\n"
    "v_fma_f32 v82, -v203, v193, v82\n"
    "v_fma_f32 v90, -v203, v192, v90\n"
    "v_fma_f32 v194, v200, v192, v83\n"
    "v_fma_f32 v195, v200, v193, v91\n"
    "v_fma_f32 v196, -v201, v193, v194\n"
    "v_fma_f32 v197, v201, v192, v195\n"
    "v_mul_f32 v83, v202, v196\n"
    "v_mul_f32 v91, -v202, v197\n"
    "v_fma_f32 v83, -v203, v197, v83\n"
    "v_fma_f32 v91, -v203, v196, v91\n"
    "v_fma_f32 v194, v200, v196, v84\n"
    "v_fma_f32 v195, v200, v197, v92\n"
    "v_fma_f32 v192, -v201, v197, v194\n"
    "v_fma_f32 v193, v201, v196, v195\n"
    "v_mul_f32 v84, v202, v192\n"
    "v_mul_f32 v92, -v202, v193\n"
    "v_fma_f32 v84, -v203, v193, v84\n"
    "v_fma_f32 v92, -v203, v192, v92\n"
    "v_fma_f32 v194, v200, v192, v85\n"
    "v_fma_f32 v195, v200, v193, v93\n"
    "v_fma_f32 v196, -v201, v193, v194\n"
    "v_fma_f32 v197, v201, v192, v195\n"
    "v_mul_f32 v85, v202, v196\n"
    "v_mul_f32 v93, -v202, v197\n"
    "v_fma_f32 v85, -v203, v197, v85\n"
    "v_fma_f32 v93, -v203, v196, v93\n"
    "v_fma_f32 v194, v200, v196, v86\n"
    "v_fma_f32 v195, v200, v197, v94\n"
    "v_fma_f32 v192, -v201, v197, v194\n"
    "v_fma_f32 v193, v201, v196, v195\n"
    "v_mul_f32 v86, v202, v192\n"
    "v_mul_f32 v94, -v202, v193\n"
    "v_fma_f32 v86, -v203, v193, v86\n"
    "v_fma_f32 v94, -v203, v192, v94\n"
    "v_fma_f32 v194, v200, v192, v87\n"
    "v_fma_f32 v195, v200, v193, v95\n"
    "v_fma_f32 v196, -v201, v193, v194\n"
    "v_fma_f32 v197, v201, v192, v195\n"
    "v_mul_f32 v87, v202, v196\n"
    "v_mul_f32 v95, -v202, v197\n"
    "v_fma_f32 v87, -v203, v197, v87\n"
    "v_fma_f32 v95, -v203, v196, v95\n"
    "v_cvt_pk_bf16_f32 v224, v80, v81\n"
    "v_cvt_pk_bf16_f32 v225, v82, v83\n"
    "v_cvt_pk_bf16_f32 v226, v84, v85\n"
    "v_cvt_pk_bf16_f32 v227, v86, v87\n"
    "ds_write_b128 v3, v[224:227] offset:0\n"
    "v_cvt_pk_bf16_f32 v228, v88, v89\n"
    "v_cvt_pk_bf16_f32 v229, v90, v91\n"
    "v_cvt_pk_bf16_f32 v230, v92, v93\n"
    "v_cvt_pk_bf16_f32 v231, v94, v95\n"
    "ds_write_b128 v3, v[228:231] offset:1024\n"
    "ds_read_b64_tr_b16 v[80:81], v4 offset:0\n"
    "ds_read_b64_tr_b16 v[82:83], v14 offset:0\n"
    "ds_read_b64_tr_b16 v[84:85], v4 offset:512\n"
    "ds_read_b64_tr_b16 v[86:87], v14 offset:512\n"
    "ds_read_b128 v[200:203], v9 offset:1280\n"
    "ds_read_b64 v[236:237], v5 offset:640\n"
    "ds_read_b64 v[238:239], v15 offset:640\n"
    "ds_read_b64 v[204:205], v11 offset:640\n"
    "s_waitcnt lgkmcnt(10)\n"
    "v_fma_f32 v190, v232, v234, v190\n"
    "v_fma_f32 v191, v232, v235, v191\n"
    "v_fma_f32 v190, -v233, v235, v190\n"
    "v_fma_f32 v191, v233, v234, v191\n"
    "v_fma_f32 v194, v176, v190, v96\n"
    "v_fma_f32 v195, v176, v191, v104\n"
    "v_fma_f32 v192, -v177, v191, v194\n"
    "v_fma_f32 v193, v177, v190, v195\n"
    "v_mul_f32 v96, v178, v192\n"
    "v_mul_f32 v104, -v178, v193\n"
    "v_fma_f32 v96, -v179, v193, v96\n"
    "v_fma_f32 v104, -v179, v192, v104\n"
    "v_fma_f32 v194, v176, v192, v97\n"
    "v_fma_f32 v195, v176, v193, v105\n"
    "v_fma_f32 v196, -v177, v193, v194\n"
    "v_fma_f32 v197, v177, v192, v195\n"
    "v_mul_f32 v97, v178, v196\n"
    "v_mul_f32 v105, -v178, v197\n"
    "v_fma_f32 v97, -v179, v197, v97\n"
    "v_fma_f32 v105, -v179, v196, v105\n"
    "v_fma_f32 v194, v176, v196, v98\n"
    "v_fma_f32 v195, v176, v197, v106\n"
    "v_fma_f32 v192, -v177, v197, v194\n"
    "v_fma_f32 v193, v177, v196, v195\n"
    "v_mul_f32 v98, v178, v192\n"
    "v_mul_f32 v106, -v178, v193\n"
    "v_fma_f32 v98, -v179, v193, v98\n"
    "v_fma_f32 v106, -v179, v192, v106\n"
    "v_fma_f32 v194, v176, v192, v99\n"
    "v_fma_f32 v195, v176, v193, v107\n"
    "v_fma_f32 v196, -v177, v193, v194\n"
    "v_fma_f32 v197, v177, v192, v195\n"
    "v_mul_f32 v99, v178, v196\n"
    "v_mul_f32 v107, -v178, v197\n"
    "v_fma_f32 v99, -v179, v197, v99\n"
    "v_fma_f32 v107, -v179, v196, v107\n"
    "v_fma_f32 v194, v176, v196, v100\n"
    "v_fma_f32 v195, v176, v197, v108\n"
    "v_fma_f32 v192, -v177, v197, v194\n"
    "v_fma_f32 v193, v177, v196, v195\n"
    "v_mul_f32 v100, v178, v192\n"
    "v_mul_f32 v108, -v178, v193\n"
    "v_fma_f32 v100, -v179, v193, v100\n"
    "v_fma_f32 v108, -v179, v192, v108\n"
    "v_fma_f32 v194, v176, v192, v101\n"
    "v_fma_f32 v195, v176, v193, v109\n"
    "v_fma_f32 v196, -v177, v193, v194\n"
    "v_fma_f32 v197, v177, v192, v195\n"
    "v_mul_f32 v101, v178, v196\n"
    "v_mul_f32 v109, -v178, v197\n"
    "v_fma_f32 v101, -v179, v197, v101\n"
    "v_fma_f32 v109, -v179, v196, v109\n"
    "v_fma_f32 v194, v176, v196, v102\n"
    "v_fma_f32 v195, v176, v197, v110\n"
    "v_fma_f32 v192, -v177, v197, v194\n"
    "v_fma_f32 v193, v177, v196, v195\n"
    "v_mul_f32 v102, v178, v192\n"
    "v_mul_f32 v110, -v178, v193\n"
    "v_fma_f32 v102, -v179, v193, v102\n"
    "v_fma_f32 v110, -v179, v192, v110\n"
    "v_fma_f32 v194, v176, v192, v103\n"
    "v_fma_f32 v195, v176, v193, v111\n"
    "v_fma_f32 v196, -v177, v193, v194\n"
    "v_fma_f32 v197, v177, v192, v195\n"
    "v_mul_f32 v103, v178, v196\n"
    "v_mul_f32 v111, -v178, v197\n"
    "v_fma_f32 v103, -v179, v197, v103\n"
    "v_fma_f32 v111, -v179, v196, v111\n"
    "v_cvt_pk_bf16_f32 v216, v96, v97\n"
    "v_cvt_pk_bf16_f32 v217, v98, v99\n"
    "v_cvt_pk_bf16_f32 v218, v100, v101\n"
    "v_cvt_pk_bf16_f32 v219, v102, v103\n"
    "ds_write_b128 v3, v[216:219] offset:0\n"
    "v_cvt_pk_bf16_f32 v220, v104, v105\n"
    "v_cvt_pk_bf16_f32 v221, v106, v107\n"
    "v_cvt_pk_bf16_f32 v222, v108, v109\n"
    "v_cvt_pk_bf16_f32 v223, v110, v111\n"
    "ds_write_b128 v3, v[220:223] offset:1024\n"
    "ds_read_b64_tr_b16 v[96:97], v4 offset:0\n"
    "ds_read_b64_tr_b16 v[98:99], v14 offset:0\n"
    "ds_read_b64_tr_b16 v[100:101], v4 offset:512\n"
    "ds_read_b64_tr_b16 v[102:103], v14 offset:512\n"
    "ds_read_b128 v[176:179], v9 offset:1536\n"
    "ds_read_b64 v[232:233], v5 offset:768\n"
    "ds_read_b64 v[234:235], v15 offset:768\n"
    "ds_read_b64 v[190:191], v11 offset:768\n"
    "s_waitcnt lgkmcnt(10)\n"
    "v_fma_f32 v204, v236, v238, v204\n"
    "v_fma_f32 v205, v236, v239, v205\n"
    "v_fma_f32 v204, -v237, v239, v204\n"
    "v_fma_f32 v205, v237, v238, v205\n"
    "v_fma_f32 v194, v200, v204, v112\n"
    "v_fma_f32 v195, v200, v205, v120\n"
    "v_fma_f32 v192, -v201, v205, v194\n"
    "v_fma_f32 v193, v201, v204, v195\n"
    "v_mul_f32 v112, v202, v192\n"
    "v_mul_f32 v120, -v202, v193\n"
    "v_fma_f32 v112, -v203, v193, v112\n"
    "v_fma_f32 v120, -v203, v192, v120\n"
    "v_fma_f32 v194, v200, v192, v113\n"
    "v_fma_f32 v195, v200, v193, v121\n"
    "v_fma_f32 v196, -v201, v193, v194\n"
    "v_fma_f32 v197, v201, v192, v195\n"
    "v_mul_f32 v113, v202, v196\n"
    "v_mul_f32 v121, -v202, v197\n"
    "v_fma_f32 v113, -v203, v197, v113\n"
    "v_fma_f32 v121, -v203, v196, v121\n"
    "v_fma_f32 v194, v200, v196, v114\n"
    "v_fma_f32 v195, v200, v197, v122\n"
    "v_fma_f32 v192, -v201, v197, v194\n"
    "v_fma_f32 v193, v201, v196, v195\n"
    "v_mul_f32 v114, v202, v192\n"
    "v_mul_f32 v122, -v202, v193\n"
    "v_fma_f32 v114, -v203, v193, v114\n"
    "v_fma_f32 v122, -v203, v192, v122\n"
    "v_fma_f32 v194, v200, v192, v115\n"
    "v_fma_f32 v195, v200, v193, v123\n"
    "v_fma_f32 v196, -v201, v193, v194\n"
    "v_fma_f32 v197, v201, v192, v195\n"
    "v_mul_f32 v115, v202, v196\n"
    "v_mul_f32 v123, -v202, v197\n"
    "v_fma_f32 v115, -v203, v197, v115\n"
    "v_fma_f32 v123, -v203, v196, v123\n"
    "v_fma_f32 v194, v200, v196, v116\n"
    "v_fma_f32 v195, v200, v197, v124\n"
    "v_fma_f32 v192, -v201, v197, v194\n"
    "v_fma_f32 v193, v201, v196, v195\n"
    "v_mul_f32 v116, v202, v192\n"
    "v_mul_f32 v124, -v202, v193\n"
    "v_fma_f32 v116, -v203, v193, v116\n"
    "v_fma_f32 v124, -v203, v192, v124\n"
    "v_fma_f32 v194, v200, v192, v117\n"
    "v_fma_f32 v195, v200, v193, v125\n"
    "v_fma_f32 v196, -v201, v193, v194\n"
    "v_fma_f32 v197, v201, v192, v195\n"
    "v_mul_f32 v117, v202, v196\n"
    "v_mul_f32 v125, -v202, v197\n"
    "v_fma_f32 v117, -v203, v197, v117\n"
    "v_fma_f32 v125, -v203, v196, v125\n"
    "v_fma_f32 v194, v200, v196, v118\n"
    "v_fma_f32 v195, v200, v197, v126\n"
    "v_fma_f32 v192, -v201, v197, v194\n"
    "v_fma_f32 v193, v201, v196, v195\n"
    "v_mul_f32 v118, v202, v192\n"
    "v_mul_f32 v126, -v202, v193\n"
    "v_fma_f32 v118, -v203, v193, v118\n"
    "v_fma_f32 v126, -v203, v192, v126\n"
    "v_fma_f32 v194, v200, v192, v119\n"
    "v_fma_f32 v195, v200, v193, v127\n"
    "v_fma_f32 v196, -v201, v193, v194\n"
    "v_fma_f32 v197, v201, v192, v195\n"
    "v_mul_f32 v119, v202, v196\n"
    "v_mul_f32 v127, -v202, v197\n"
    "v_fma_f32 v119, -v203, v197, v119\n"
    "v_fma_f32 v127, -v203, v196, v127\n"
    "v_cvt_pk_bf16_f32 v224, v112, v113\n"
    "v_cvt_pk_bf16_f32 v225, v114, v115\n"
    "v_cvt_pk_bf16_f32 v226, v116, v117\n"
    "v_cvt_pk_bf16_f32 v227, v118, v119\n"
    "ds_write_b128 v3, v[224:227] offset:0\n"
    "v_cvt_pk_bf16_f32 v228, v120, v121\n"
    "v_cvt_pk_bf16_f32 v229, v122, v123\n"
    "v_cvt_pk_bf16_f32 v230, v124, v125\n"
    "v_cvt_pk_bf16_f32 v231, v126, v127\n"
    "ds_write_b128 v3, v[228:231] offset:1024\n"
    "ds_read_b64_tr_b16 v[112:113], v4 offset:0\n"
    "ds_read_b64_tr_b16 v[114:115], v14 offset:0\n"
    "ds_read_b64_tr_b16 v[116:117], v4 offset:512\n"
    "ds_read_b64_tr_b16 v[118:119], v14 offset:512\n"
    "ds_read_b128 v[200:203], v9 offset:1792\n"
    "ds_read_b64 v[236:237], v5 offset:896\n"
    "ds_read_b64 v[238:239], v15 offset:896\n"
    "ds_read_b64 v[204:205], v11 offset:896\n"
    "s_waitcnt lgkmcnt(10)\n"
    "v_fma_f32 v190, v232, v234, v190\n"
    "v_fma_f32 v191, v232, v235, v191\n"
    "v_fma_f32 v190, -v233, v235, v190\n"
    "v_fma_f32 v191, v233, v234, v191\n"
    "v_fma_f32 v194, v176, v190, v128\n"
    "v_fma_f32 v195, v176, v191, v136\n"
    "v_fma_f32 v192, -v177, v191, v194\n"
    "v_fma_f32 v193, v177, v190, v195\n"
    "v_mul_f32 v128, v178, v192\n"
    "v_mul_f32 v136, -v178, v193\n"
    "v_fma_f32 v128, -v179, v193, v128\n"
    "v_fma_f32 v136, -v179, v192, v136\n"
    "v_fma_f32 v194, v176, v192, v129\n"
    "v_fma_f32 v195, v176, v193, v137\n"
    "v_fma_f32 v196, -v177, v193, v194\n"
    "v_fma_f32 v197, v177, v192, v195\n"
    "v_mul_f32 v129, v178, v196\n"
    "v_mul_f32 v137, -v178, v197\n"
    "v_fma_f32 v129, -v179, v197, v129\n"
    "v_fma_f32 v137, -v179, v196, v137\n"
    "v_fma_f32 v194, v176, v196, v130\n"
    "v_fma_f32 v195, v176, v197, v138\n"
    "v_fma_f32 v192, -v177, v197, v194\n"
    "v_fma_f32 v193, v177, v196, v195\n"
    "v_mul_f32 v130, v178, v192\n"
    "v_mul_f32 v138, -v178, v193\n"
    "v_fma_f32 v130, -v179, v193, v130\n"
    "v_fma_f32 v138, -v179, v192, v138\n"
    "v_fma_f32 v194, v176, v192, v131\n"
    "v_fma_f32 v195, v176, v193, v139\n"
    "v_fma_f32 v196, -v177, v193, v194\n"
    "v_fma_f32 v197, v177, v192, v195\n"
    "v_mul_f32 v131, v178, v196\n"
    "v_mul_f32 v139, -v178, v197\n"
    "v_fma_f32 v131, -v179, v197, v131\n"
    "v_fma_f32 v139, -v179, v196, v139\n"
    "v_fma_f32 v194, v176, v196, v132\n"
    "v_fma_f32 v195, v176, v197, v140\n"
    "v_fma_f32 v192, -v177, v197, v194\n"
    "v_fma_f32 v193, v177, v196, v195\n"
    "v_mul_f32 v132, v178, v192\n"
    "v_mul_f32 v140, -v178, v193\n"
    "v_fma_f32 v132, -v179, v193, v132\n"
    "v_fma_f32 v140, -v179, v192, v140\n"
    "v_fma_f32 v194, v176, v192, v133\n"
    "v_fma_f32 v195, v176, v193, v141\n"
    "v_fma_f32 v196, -v177, v193, v194\n"
    "v_fma_f32 v197, v177, v192, v195\n"
    "v_mul_f32 v133, v178, v196\n"
    "v_mul_f32 v141, -v178, v197\n"
    "v_fma_f32 v133, -v179, v197, v133\n"
    "v_fma_f32 v141, -v179, v196, v141\n"
    "v_fma_f32 v194, v176, v196, v134\n"
    "v_fma_f32 v195, v176, v197, v142\n"
    "v_fma_f32 v192, -v177, v197, v194\n"
    "v_fma_f32 v193, v177, v196, v195\n"
    "v_mul_f32 v134, v178, v192\n"
    "v_mul_f32 v142, -v178, v193\n"
    "v_fma_f32 v134, -v179, v193, v134\n"
    "v_fma_f32 v142, -v179, v192, v142\n"
    "v_fma_f32 v194, v176, v192, v135\n"
    "v_fma_f32 v195, v176, v193, v143\n"
    "v_fma_f32 v196, -v177, v193, v194\n"
    "v_fma_f32 v197, v177, v192, v195\n"
    "v_mul_f32 v135, v178, v196\n"
    "v_mul_f32 v143, -v178, v197\n"
    "v_fma_f32 v135, -v179, v197, v135\n"
    "v_fma_f32 v143, -v179, v196, v143\n"
    "v_cvt_pk_bf16_f32 v216, v128, v129\n"
    "v_cvt_pk_bf16_f32 v217, v130, v131\n"
    "v_cvt_pk_bf16_f32 v218, v132, v133\n"
    "v_cvt_pk_bf16_f32 v219, v134, v135\n"
    "ds_write_b128 v3, v[216:219] offset:0\n"
    "v_cvt_pk_bf16_f32 v220, v136, v137\n"
    "v_cvt_pk_bf16_f32 v221, v138, v139\n"
    "v_cvt_pk_bf16_f32 v222, v140, v141\n"
    "v_cvt_pk_bf16_f32 v223, v142, v143\n"
    "ds_write_b128 v3, v[220:223] offset:1024\n"
    "ds_read_b64_tr_b16 v[128:129], v4 offset:0\n"
    "ds_read_b64_tr_b16 v[130:131], v14 offset:0\n"
    "ds_read_b64_tr_b16 v[132:133], v4 offset:512\n"
    "ds_read_b64_tr_b16 v[134:135], v14 offset:512\n"
    "ds_read_b128 v[176:179], v9 offset:2048\n"
    "ds_read_b64 v[232:233], v5 offset:1024\n"
    "ds_read_b64 v[234:235], v15 offset:1024\n"
    "ds_read_b64 v[190:191], v11 offset:1024\n"
    "s_waitcnt lgkmcnt(10)\n"
    "v_fma_f32 v204, v236, v238, v204\n"
    "v_fma_f32 v205, v236, v239, v205\n"
    "v_fma_f32 v204, -v237, v239, v204\n"
    "v_fma_f32 v205, v237, v238, v205\n"
    "v_fma_f32 v194, v200, v204, v144\n"
    "v_fma_f32 v195, v200, v205, v152\n"
    "v_fma_f32 v192, -v201, v205, v194\n"
    "v_fma_f32 v193, v201, v204, v195\n"
    "v_mul_f32 v144, v202, v192\n"
    "v_mul_f32 v152, -v202, v193\n"
    "v_fma_f32 v144, -v203, v193, v144\n"
    "v_fma_f32 v152, -v203, v192, v152\n"
    "v_fma_f32 v194, v200, v192, v145\n"
    "v_fma_f32 v195, v200, v193, v153\n"
    "v_fma_f32 v196, -v201, v193, v194\n"
    "v_fma_f32 v197, v201, v192, v195\n"
    "v_mul_f32 v145, v202, v196\n"
    "v_mul_f32 v153, -v202, v197\n"
    "v_fma_f32 v145, -v203, v197, v145\n"
    "v_fma_f32 v153, -v203, v196, v153\n"
    "v_fma_f32 v194, v200, v196, v146\n"
    "v_fma_f32 v195, v200, v197, v154\n"
    "v_fma_f32 v192, -v201, v197, v194\n"
    "v_fma_f32 v193, v201, v196, v195\n"
    "v_mul_f32 v146, v202, v192\n"
    "v_mul_f32 v154, -v202, v193\n"
    "v_fma_f32 v146, -v203, v193, v146\n"
    "v_fma_f32 v154, -v203, v192, v154\n"
    "v_fma_f32 v194, v200, v192, v147\n"
    "v_fma_f32 v195, v200, v193, v155\n"
    "v_fma_f32 v196, -v201, v193, v194\n"
    "v_fma_f32 v197, v201, v192, v195\n"
    "v_mul_f32 v147, v202, v196\n"
    "v_mul_f32 v155, -v202, v197\n"
    "v_fma_f32 v147, -v203, v197, v147\n"
    "v_fma_f32 v155, -v203, v196, v155\n"
    "v_fma_f32 v194, v200, v196, v148\n"
    "v_fma_f32 v195, v200, v197, v156\n"
    "v_fma_f32 v192, -v201, v197, v194\n"
    "v_fma_f32 v193, v201, v196, v195\n"
    "v_mul_f32 v148, v202, v192\n"
    "v_mul_f32 v156, -v202, v193\n"
    "v_fma_f32 v148, -v203, v193, v148\n"
    "v_fma_f32 v156, -v203, v192, v156\n"
    "v_fma_f32 v194, v200, v192, v149\n"
    "v_fma_f32 v195, v200, v193, v157\n"
    "v_fma_f32 v196, -v201, v193, v194\n"
    "v_fma_f32 v197, v201, v192, v195\n"
    "v_mul_f32 v149, v202, v196\n"
    "v_mul_f32 v157, -v202, v197\n"
    "v_fma_f32 v149, -v203, v197, v149\n"
    "v_fma_f32 v157, -v203, v196, v157\n"
    "v_fma_f32 v194, v200, v196, v150\n"
    "v_fma_f32 v195, v200, v197, v158\n"
    "v_fma_f32 v192, -v201, v197, v194\n"
    "v_fma_f32 v193, v201, v196, v195\n"
    "v_mul_f32 v150, v202, v192\n"
    "v_mul_f32 v158, -v202, v193\n"
    "v_fma_f32 v150, -v203, v193, v150\n"
    "v_fma_f32 v158, -v203, v192, v158\n"
    "v_fma_f32 v194, v200, v192, v151\n"
    "v_fma_f32 v195, v200, v193, v159\n"
    "v_fma_f32 v196, -v201, v193, v194\n"
    "v_fma_f32 v197, v201, v192, v195\n"
    "v_mul_f32 v151, v202, v196\n"
    "v_mul_f32 v159, -v202, v197\n"
    "v_fma_f32 v151, -v203, v197, v151\n"
    "v_fma_f32 v159, -v203, v196, v159\n"
    "v_cvt_pk_bf16_f32 v224, v144, v145\n"
    "v_cvt_pk_bf16_f32 v225, v146, v147\n"
    "v_cvt_pk_bf16_f32 v226, v148, v149\n"
    "v_cvt_pk_bf16_f32 v227, v150, v151\n"
    "ds_write_b128 v3, v[224:227] offset:0\n"
    "v_cvt_pk_bf16_f32 v228, v152, v153\n"
    "v_cvt_pk_bf16_f32 v229, v154, v155\n"
    "v_cvt_pk_bf16_f32 v230, v156, v157\n"
    "v_cvt_pk_bf16_f32 v231, v158, v159\n"
    "ds_write_b128 v3, v[228:231] offset:1024\n"
    "ds_read_b64_tr_b16 v[144:145], v4 offset:0\n"
    "ds_read_b64_tr_b16 v[146:147], v14 offset:0\n"
    "ds_read_b64_tr_b16 v[148:149], v4 offset:512\n"
    "ds_read_b64_tr_b16 v[150:151], v14 offset:512\n"
    "s_waitcnt lgkmcnt(6)\n"
    "v_fma_f32 v190, v232, v234, v190\n"
    "v_fma_f32 v191, v232, v235, v191\n"
    "v_fma_f32 v190, -v233, v235, v190\n"
    "v_fma_f32 v191, v233, v234, v191\n"
    "v_fma_f32 v194, v176, v190, v160\n"
    "v_fma_f32 v195, v176, v191, v168\n"
    "v_fma_f32 v192, -v177, v191, v194\n"
    "v_fma_f32 v193, v177, v190, v195\n"
    "v_mul_f32 v160, v178, v192\n"
    "v_mul_f32 v168, -v178, v193\n"
    "v_fma_f32 v160, -v179, v193, v160\n"
    "v_fma_f32 v168, -v179, v192, v168\n"
    "v_fma_f32 v194, v176, v192, v161\n"
    "v_fma_f32 v195, v176, v193, v169\n"
    "v_fma_f32 v196, -v177, v193, v194\n"
    "v_fma_f32 v197, v177, v192, v195\n"
    "v_mul_f32 v161, v178, v196\n"
    "v_mul_f32 v169, -v178, v197\n"
    "v_fma_f32 v161, -v179, v197, v161\n"
    "v_fma_f32 v169, -v179, v196, v169\n"
    "v_fma_f32 v194, v176, v196, v162\n"
    "v_fma_f32 v195, v176, v197, v170\n"
    "v_fma_f32 v192, -v177, v197, v194\n"
    "v_fma_f32 v193, v177, v196, v195\n"
    "v_mul_f32 v162, v178, v192\n"
    "v_mul_f32 v170, -v178, v193\n"
    "v_fma_f32 v162, -v179, v193, v162\n"
    "v_fma_f32 v170, -v179, v192, v170\n"
    "v_fma_f32 v194, v176, v192, v163\n"
    "v_fma_f32 v195, v176, v193, v171\n"
    "v_fma_f32 v196, -v177, v193, v194\n"
    "v_fma_f32 v197, v177, v192, v195\n"
    "v_mul_f32 v163, v178, v196\n"
    "v_mul_f32 v171, -v178, v197\n"
    "v_fma_f32 v163, -v179, v197, v163\n"
    "v_fma_f32 v171, -v179, v196, v171\n"
    "v_fma_f32 v194, v176, v196, v164\n"
    "v_fma_f32 v195, v176, v197, v172\n"
    "v_fma_f32 v192, -v177, v197, v194\n"
    "v_fma_f32 v193, v177, v196, v195\n"
    "v_mul_f32 v164, v178, v192\n"
    "v_mul_f32 v172, -v178, v193\n"
    "v_fma_f32 v164, -v179, v193, v164\n"
    "v_fma_f32 v172, -v179, v192, v172\n"
    "v_fma_f32 v194, v176, v192, v165\n"
    "v_fma_f32 v195, v176, v193, v173\n"
    "v_fma_f32 v196, -v177, v193, v194\n"
    "v_fma_f32 v197, v177, v192, v195\n"
    "v_mul_f32 v165, v178, v196\n"
    "v_mul_f32 v173, -v178, v197\n"
    "v_fma_f32 v165, -v179, v197, v165\n"
    "v_fma_f32 v173, -v179, v196, v173\n"
    "v_fma_f32 v194, v176, v196, v166\n"
    "v_fma_f32 v195, v176, v197, v174\n"
    "v_fma_f32 v192, -v177, v197, v194\n"
    "v_fma_f32 v193, v177, v196, v195\n"
    "v_mul_f32 v166, v178, v192\n"
    "v_mul_f32 v174, -v178, v193\n"
    "v_fma_f32 v166, -v179, v193, v166\n"
    "v_fma_f32 v174, -v179, v192, v174\n"
    "v_fma_f32 v194, v176, v192, v167\n"
    "v_fma_f32 v195, v176, v193, v175\n"
    "v_fma_f32 v196, -v177, v193, v194\n"
    "v_fma_f32 v197, v177, v192, v195\n"
    "v_mul_f32 v167, v178, v196\n"
    "v_mul_f32 v175, -v178, v197\n"
    "v_fma_f32 v167, -v179, v197, v167\n"
    "v_fma_f32 v175, -v179, v196, v175\n"
    "v_cvt_pk_bf16_f32 v216, v160, v161\n"
    "v_cvt_pk_bf16_f32 v217, v162, v163\n"
    "v_cvt_pk_bf16_f32 v218, v164, v165\n"
    "v_cvt_pk_bf16_f32 v219, v166, v167\n"
    "ds_write_b128 v3, v[216:219] offset:0\n"
    "v_cvt_pk_bf16_f32 v220, v168, v169\n"
    "v_cvt_pk_bf16_f32 v221, v170, v171\n"
    "v_cvt_pk_bf16_f32 v222, v172, v173\n"
    "v_cvt_pk_bf16_f32 v223, v174, v175\n"
    "ds_write_b128 v3, v[220:223] offset:1024\n"
    "ds_read_b64_tr_b16 v[160:161], v4 offset:0\n"
    "ds_read_b64_tr_b16 v[162:163], v14 offset:0\n"
    "ds_read_b64_tr_b16 v[164:165], v4 offset:512\n"
    "ds_read_b64_tr_b16 v[166:167], v14 offset:512\n"
    "s_waitcnt lgkmcnt(0)\n"
    "v_lshrrev_b32 v190, 5, v0\n"
    "v_mul_u32_u24 v190, 18432, v190\n"
    "v_and_b32 v191, 15, v0\n"
    "v_mul_u32_u24 v191, 288, v191\n"
    "v_add_u32 v190, v190, v191\n"
    "v_bfe_u32 v191, v0, 4, 1\n"
    "v_lshl_add_u32 v1, v191, 4, v190\n"
    "s_mul_i32 s40, s10, 18\n"
    "s_add_u32 s40, s40, s12\n"
    "s_lshl_b32 s40, s40, 5\n"
    "s_add_u32 s40, s40, s61\n"
    "s_lshl_b32 s40, s40, 2\n"
    "s_lshl_b32 s41, s13, 1\n"
    "s_add_u32 s40, s40, s41\n"
    "s_lshl_b32 s60, s40, 10\n"
    "s_mul_i32 s40, s10, 36\n"
    "s_add_u32 s40, s40, s9\n"
    "s_lshl_b32 s40, s40, 7\n"
    "s_lshl_b32 s41, s61, 3\n"
    "s_add_u32 s40, s40, s41\n"
    "s_add_u32 s57, s40, 16384\n"
    "v_mov_b32 v190, 0x7fffffff\n"
    "v_cmp_eq_u32 vcc, 0, v0\n"
    "s_nop 1\n"
    "v_cndmask_b32 v190, v190, 0, vcc\n"
    "v_mov_b32 v192, s84\n"
    "v_mov_b32 v193, s85\n"
    "s_waitcnt vmcnt(9)\n"
    "s_barrier\n"
    "v_add_u32 v3, 0, v1\n"
    "ds_read_b128 v[16:19], v3 offset:0\n"
    "ds_read_b128 v[20:23], v3 offset:4608\n"
    "ds_read_b128 v[24:27], v3 offset:9216\n"
    "ds_read_b128 v[28:31], v3 offset:13824\n"
    "ds_read_b128 v[40:43], v3 offset:32\n"
    "ds_read_b128 v[44:47], v3 offset:4640\n"
    "ds_read_b128 v[56:59], v3 offset:9248\n"
    "s_waitcnt lgkmcnt(5)\n"
    "v_mfma_f32_16x16x32_bf16 v[200:203], v[16:19], v[32:35], 0\n"
    "ds_read_b128 v[60:63], v3 offset:13856\n"
    "v_add_u32 v14, 36864, v1\n"
    "v_mfma_f32_16x16x32_bf16 v[204:207], v[16:19], v[36:39], 0\n"
    "v_mfma_f32_16x16x32_bf16 v[208:211], v[20:23], v[32:35], 0\n"
    "ds_read_b128 v[16:19], v3 offset:64\n"
    "v_mfma_f32_16x16x32_bf16 v[212:215], v[20:23], v[36:39], 0\n"
    "s_waitcnt lgkmcnt(5)\n"
    "v_mfma_f32_16x16x32_bf16 v[216:219], v[24:27], v[32:35], 0\n"
    "ds_read_b128 v[20:23], v3 offset:4672\n"
    "v_mfma_f32_16x16x32_bf16 v[220:223], v[24:27], v[36:39], 0\n"
    "v_mfma_f32_16x16x32_bf16 v[224:227], v[28:31], v[32:35], 0\n"
    "ds_read_b128 v[24:27], v3 offset:9280\n"
    "v_mfma_f32_16x16x32_bf16 v[228:231], v[28:31], v[36:39], 0\n"
    "s_waitcnt lgkmcnt(5)\n"
    "v_mfma_f32_16x16x32_bf16 v[200:203], v[40:43], v[48:51], v[200:203]\n"
    "ds_read_b128 v[28:31], v3 offset:13888\n"
    "v_mfma_f32_16x16x32_bf16 v[204:207], v[40:43], v[52:55], v[204:207]\n"
    "v_mfma_f32_16x16x32_bf16 v[208:211], v[44:47], v[48:51], v[208:211]\n"
    "ds_read_b128 v[40:43], v3 offset:96\n"
    "v_mfma_f32_16x16x32_bf16 v[212:215], v[44:47], v[52:55], v[212:215]\n"
    "s_waitcnt lgkmcnt(5)\n"
    "v_mfma_f32_16x16x32_bf16 v[216:219], v[56:59], v[48:51], v[216:219]\n"
    "ds_read_b128 v[44:47], v3 offset:4704\n"
    "v_mfma_f32_16x16x32_bf16 v[220:223], v[56:59], v[52:55], v[220:223]\n"
    "v_mfma_f32_16x16x32_bf16 v[224:227], v[60:63], v[48:51], v[224:227]\n"
    "ds_read_b128 v[56:59], v3 offset:9312\n"
    "v_mfma_f32_16x16x32_bf16 v[228:231], v[60:63], v[52:55], v[228:231]\n"
    "s_waitcnt lgkmcnt(5)\n"
    "v_mfma_f32_16x16x32_bf16 v[200:203], v[16:19], v[64:67], v[200:203]\n"
    "ds_read_b128 v[60:63], v3 offset:13920\n"
    "v_mfma_f32_16x16x32_bf16 v[204:207], v[16:19], v[68:71], v[204:207]\n"
    "v_mfma_f32_16x16x32_bf16 v[208:211], v[20:23], v[64:67], v[208:211]\n"
    "ds_read_b128 v[16:19], v3 offset:128\n"
    "v_mfma_f32_16x16x32_bf16 v[212:215], v[20:23], v[68:71], v[212:215]\n"
    "s_waitcnt lgkmcnt(5)\n"
    "v_mfma_f32_16x16x32_bf16 v[216:219], v[24:27], v[64:67], v[216:219]\n"
    "ds_read_b128 v[20:23], v3 offset:4736\n"
    "v_mfma_f32_16x16x32_bf16 v[220:223], v[24:27], v[68:71], v[220:223]\n"
    "v_mfma_f32_16x16x32_bf16 v[224:227], v[28:31], v[64:67], v[224:227]\n"
    "s_add_u32 s40, s15, 2\n"
    "s_cmp_ge_u32 s40, 9\n"
    "s_cselect_b32 s41, 9, 0\n"
    "s_sub_u32 s40, s40, s41\n"
    "s_lshl_b32 s40, s40, 6\n"
    "s_lshl_b32 s41, s13, 5\n"
    "s_add_u32 s40, s40, s41\n"
    "s_mul_i32 s40, s40, 1152\n"
    "s_mul_i32 s41, s11, 288\n"
    "s_add_u32 s52, s40, s41\n"
    "s_mul_i32 s53, s9, 9216\n"
    "s_add_u32 m0, s53, 73728\n"
    "s_nop 0\n"
    "buffer_load_dwordx4 v180, s[36:39], s52 offen lds\n"
    "s_add_u32 m0, m0, 1024\n"
    "ds_read_b128 v[24:27], v3 offset:9344\n"
    "v_mfma_f32_16x16x32_bf16 v[228:231], v[28:31], v[68:71], v[228:231]\n"
    "s_waitcnt lgkmcnt(5)\n"
    "v_mfma_f32_16x16x32_bf16 v[200:203], v[40:43], v[80:83], v[200:203]\n"
    "ds_read_b128 v[28:31], v3 offset:13952\n"
    "v_mfma_f32_16x16x32_bf16 v[204:207], v[40:43], v[84:87], v[204:207]\n"
    "v_mfma_f32_16x16x32_bf16 v[208:211], v[44:47], v[80:83], v[208:211]\n"
    "ds_read_b128 v[40:43], v3 offset:160\n"
    "v_mfma_f32_16x16x32_bf16 v[212:215], v[44:47], v[84:87], v[212:215]\n"
    "s_waitcnt lgkmcnt(5)\n"
    "v_mfma_f32_16x16x32_bf16 v[216:219], v[56:59], v[80:83], v[216:219]\n"
    "buffer_load_dwordx4 v181, s[36:39], s52 offen lds\n"
    "s_add_u32 m0, m0, 1024\n"
    "ds_read_b128 v[44:47], v3 offset:4768\n"
    "v_mfma_f32_16x16x32_bf16 v[220:223], v[56:59], v[84:87], v[220:223]\n"
    "v_mfma_f32_16x16x32_bf16 v[224:227], v[60:63], v[80:83], v[224:227]\n"
    "ds_read_b128 v[56:59], v3 offset:9376\n"
    "v_mfma_f32_16x16x32_bf16 v[228:231], v[60:63], v[84:87], v[228:231]\n"
    "s_waitcnt lgkmcnt(5)\n"
    "v_mfma_f32_16x16x32_bf16 v[200:203], v[16:19], v[96:99], v[200:203]\n"
    "ds_read_b128 v[60:63], v3 offset:13984\n"
    "v_mfma_f32_16x16x32_bf16 v[204:207], v[16:19], v[100:103], v[204:207]\n"
    "v_mfma_f32_16x16x32_bf16 v[208:211], v[20:23], v[96:99], v[208:211]\n"
    "buffer_load_dwordx4 v182, s[36:39], s52 offen lds\n"
    "s_add_u32 m0, m0, 1024\n"
    "ds_read_b128 v[16:19], v3 offset:192\n"
    "v_mfma_f32_16x16x32_bf16 v[212:215], v[20:23], v[100:103], v[212:215]\n"
    "s_waitcnt lgkmcnt(5)\n"
    "v_mfma_f32_16x16x32_bf16 v[216:219], v[24:27], v[96:99], v[216:219]\n"
    "ds_read_b128 v[20:23], v3 offset:4800\n"
    "v_mfma_f32_16x16x32_bf16 v[220:223], v[24:27], v[100:103], v[220:223]\n"
    "v_mfma_f32_16x16x32_bf16 v[224:227], v[28:31], v[96:99], v[224:227]\n"
    "ds_read_b128 v[24:27], v3 offset:9408\n"
    "v_mfma_f32_16x16x32_bf16 v[228:231], v[28:31], v[100:103], v[228:231]\n"
    "s_waitcnt lgkmcnt(5)\n"
    "v_mfma_f32_16x16x32_bf16 v[200:203], v[40:43], v[112:115], v[200:203]\n"
    "buffer_load_dwordx4 v183, s[36:39], s52 offen lds\n"
    "s_add_u32 m0, m0, 1024\n"
    "ds_read_b128 v[28:31], v3 offset:14016\n"
    "v_mfma_f32_16x16x32_bf16 v[204:207], v[40:43], v[116:119], v[204:207]\n"
    "v_mfma_f32_16x16x32_bf16 v[208:211], v[44:47], v[112:115], v[208:211]\n"
    "ds_read_b128 v[40:43], v3 offset:224\n"
    "v_mfma_f32_16x16x32_bf16 v[212:215], v[44:47], v[116:119], v[212:215]\n"
    "s_waitcnt lgkmcnt(5)\n"
    "v_mfma_f32_16x16x32_bf16 v[216:219], v[56:59], v[112:115], v[216:219]\n"
    "ds_read_b128 v[44:47], v3 offset:4832\n"
    "v_mfma_f32_16x16x32_bf16 v[220:223], v[56:59], v[116:119], v[220:223]\n"
    "v_mfma_f32_16x16x32_bf16 v[224:227], v[60:63], v[112:115], v[224:227]\n"
    "buffer_load_dwordx4 v184, s[36:39], s52 offen lds\n"
    "s_add_u32 m0, m0, 1024\n"
    "ds_read_b128 v[56:59], v3 offset:9440\n"
    "v_mfma_f32_16x16x32_bf16 v[228:231], v[60:63], v[116:119], v[228:231]\n"
    "s_waitcnt lgkmcnt(5)\n"
    "v_mfma_f32_16x16x32_bf16 v[200:203], v[16:19], v[128:131], v[200:203]\n"
    "ds_read_b128 v[60:63], v3 offset:14048\n"
    "v_mfma_f32_16x16x32_bf16 v[204:207], v[16:19], v[132:135], v[204:207]\n"
    "v_mfma_f32_16x16x32_bf16 v[208:211], v[20:23], v[128:131], v[208:211]\n"
    "ds_read_b128 v[16:19], v3 offset:256\n"
    "v_mfma_f32_16x16x32_bf16 v[212:215], v[20:23], v[132:135], v[212:215]\n"
    "s_waitcnt lgkmcnt(5)\n"
    "v_mfma_f32_16x16x32_bf16 v[216:219], v[24:27], v[128:131], v[216:219]\n"
    "buffer_load_dwordx4 v185, s[36:39], s52 offen lds\n"
    "s_add_u32 m0, m0, 1024\n"
    "ds_read_b128 v[20:23], v3 offset:4864\n"
    "v_mfma_f32_16x16x32_bf16 v[220:223], v[24:27], v[132:135], v[220:223]\n"
    "v_mfma_f32_16x16x32_bf16 v[224:227], v[28:31], v[128:131], v[224:227]\n"
    "ds_read_b128 v[24:27], v3 offset:9472\n"
    "v_mfma_f32_16x16x32_bf16 v[228:231], v[28:31], v[132:135], v[228:231]\n"
    "s_waitcnt lgkmcnt(5)\n"
    "v_mfma_f32_16x16x32_bf16 v[200:203], v[40:43], v[144:147], v[200:203]\n"
    "ds_read_b128 v[28:31], v3 offset:14080\n"
    "v_mfma_f32_16x16x32_bf16 v[204:207], v[40:43], v[148:151], v[204:207]\n"
    "v_mfma_f32_16x16x32_bf16 v[208:211], v[44:47], v[144:147], v[208:211]\n"
    "buffer_load_dwordx4 v186, s[36:39], s52 offen lds\n"
    "s_add_u32 m0, m0, 1024\n"
    "v_mfma_f32_16x16x32_bf16 v[212:215], v[44:47], v[148:151], v[212:215]\n"
    "s_waitcnt lgkmcnt(4)\n"
    "v_mfma_f32_16x16x32_bf16 v[216:219], v[56:59], v[144:147], v[216:219]\n"
    "v_mfma_f32_16x16x32_bf16 v[220:223], v[56:59], v[148:151], v[220:223]\n"
    "v_mfma_f32_16x16x32_bf16 v[224:227], v[60:63], v[144:147], v[224:227]\n"
    "v_mfma_f32_16x16x32_bf16 v[228:231], v[60:63], v[148:151], v[228:231]\n"
    "s_waitcnt lgkmcnt(2)\n"
    "s_waitcnt lgkmcnt(0)\n"
    "s_waitcnt vmcnt(7)\n"
    "s_barrier\n"
    "v_mfma_f32_16x16x32_bf16 v[200:203], v[16:19], v[160:163], v[200:203]\n"
    "buffer_load_dwordx4 v187, s[36:39], s52 offen lds\n"
    "s_add_u32 m0, m0, 1024\n"
    "ds_read_b128 v[40:43], v14 offset:0\n"
    "v_mfma_f32_16x16x32_bf16 v[204:207], v[16:19], v[164:167], v[204:207]\n"
    "ds_read_b128 v[44:47], v14 offset:4608\n"
    "v_mfma_f32_16x16x32_bf16 v[208:211], v[20:23], v[160:163], v[208:211]\n"
    "ds_read_b128 v[56:59], v14 offset:9216\n"
    "v_mfma_f32_16x16x32_bf16 v[212:215], v[20:23], v[164:167], v[212:215]\n"
    "ds_read_b128 v[60:63], v14 offset:13824\n"
    "v_mfma_f32_16x16x32_bf16 v[216:219], v[24:27], v[160:163], v[216:219]\n"
    "ds_read_b128 v[16:19], v14 offset:32\n"
    "v_mfma_f32_16x16x32_bf16 v[220:223], v[24:27], v[164:167], v[220:223]\n"
    "ds_read_b128 v[20:23], v14 offset:4640\n"
    "v_mfma_f32_16x16x32_bf16 v[224:227], v[28:31], v[160:163], v[224:227]\n"
    "buffer_load_dwordx4 v188, s[36:39], s52 offen lds\n"
    "ds_read_b128 v[24:27], v14 offset:9248\n"
    "v_mfma_f32_16x16x32_bf16 v[228:231], v[28:31], v[164:167], v[228:231]\n"
    "s_waitcnt lgkmcnt(5)\n"
    "v_cvt_pk_bf16_f32 v232, v200, v201\n"
    "v_cvt_pk_bf16_f32 v233, v202, v203\n"
    "v_mfma_f32_16x16x32_bf16 v[200:203], v[40:43], v[32:35], 0\n"
    "ds_read_b128 v[28:31], v14 offset:13856\n"
    "v_add_u32 v3, 73728, v1\n"
    "v_cvt_pk_bf16_f32 v236, v204, v205\n"
    "v_cvt_pk_bf16_f32 v237, v206, v207\n"
    "v_mfma_f32_16x16x32_bf16 v[204:207], v[40:43], v[36:39], 0\n"
    "v_cvt_pk_bf16_f32 v234, v208, v209\n"
    "v_cvt_pk_bf16_f32 v235, v210, v211\n"
    "v_mfma_f32_16x16x32_bf16 v[208:211], v[44:47], v[32:35], 0\n"
    "ds_read_b128 v[40:43], v14 offset:64\n"
    "v_cvt_pk_bf16_f32 v238, v212, v213\n"
    "v_cvt_pk_bf16_f32 v239, v214, v215\n"
    "v_mfma_f32_16x16x32_bf16 v[212:215], v[44:47], v[36:39], 0\n"
    "s_waitcnt lgkmcnt(5)\n"
    "v_cvt_pk_bf16_f32 v240, v216, v217\n"
    "v_cvt_pk_bf16_f32 v241, v218, v219\n"
    "v_mfma_f32_16x16x32_bf16 v[216:219], v[56:59], v[32:35], 0\n"
    "ds_read_b128 v[44:47], v14 offset:4672\n"
    "v_cvt_pk_bf16_f32 v244, v220, v221\n"
    "v_cvt_pk_bf16_f32 v245, v222, v223\n"
    "v_mfma_f32_16x16x32_bf16 v[220:223], v[56:59], v[36:39], 0\n"
    "v_cvt_pk_bf16_f32 v242, v224, v225\n"
    "v_cvt_pk_bf16_f32 v243, v226, v227\n"
    "v_mfma_f32_16x16x32_bf16 v[224:227], v[60:63], v[32:35], 0\n"
    "ds_read_b128 v[56:59], v14 offset:9280\n"
    "v_cvt_pk_bf16_f32 v246, v228, v229\n"
    "v_cvt_pk_bf16_f32 v247, v230, v231\n"
    "v_mfma_f32_16x16x32_bf16 v[228:231], v[60:63], v[36:39], 0\n"
    "s_waitcnt lgkmcnt(5)\n"
    "v_mfma_f32_16x16x32_bf16 v[200:203], v[16:19], v[48:51], v[200:203]\n"
    "ds_read_b128 v[60:63], v14 offset:13888\n"
    "v_mfma_f32_16x16x32_bf16 v[204:207], v[16:19], v[52:55], v[204:207]\n"
    "s_add_u32 s59, s60, 65536\n"
    "buffer_store_dwordx4 v[232:235], v13, s[28:31], s60 offen offset:0 sc1\n"
    "v_mfma_f32_16x16x32_bf16 v[208:211], v[20:23], v[48:51], v[208:211]\n"
    "ds_read_b128 v[16:19], v14 offset:96\n"
    "v_mfma_f32_16x16x32_bf16 v[212:215], v[20:23], v[52:55], v[212:215]\n"
    "s_waitcnt lgkmcnt(5)\n"
    "v_mfma_f32_16x16x32_bf16 v[216:219], v[24:27], v[48:51], v[216:219]\n"
    "buffer_store_dwordx4 v[236:239], v13, s[28:31], s60 offen offset:1024 sc1\n"
    "ds_read_b128 v[20:23], v14 offset:4704\n"
    "v_mfma_f32_16x16x32_bf16 v[220:223], v[24:27], v[52:55], v[220:223]\n"
    "v_mfma_f32_16x16x32_bf16 v[224:227], v[28:31], v[48:51], v[224:227]\n"
    "ds_read_b128 v[24:27], v14 offset:9312\n"
    "v_mfma_f32_16x16x32_bf16 v[228:231], v[28:31], v[52:55], v[228:231]\n"
    "s_waitcnt lgkmcnt(5)\n"
    "v_mfma_f32_16x16x32_bf16 v[200:203], v[40:43], v[64:67], v[200:203]\n"
    "buffer_store_dwordx4 v[240:243], v13, s[28:31], s59 offen offset:0 sc1\n"
    "ds_read_b128 v[28:31], v14 offset:13920\n"
    "v_mfma_f32_16x16x32_bf16 v[204:207], v[40:43], v[68:71], v[204:207]\n"
    "v_mfma_f32_16x16x32_bf16 v[208:211], v[44:47], v[64:67], v[208:211]\n"
    "ds_read_b128 v[40:43], v14 offset:128\n"
    "v_mfma_f32_16x16x32_bf16 v[212:215], v[44:47], v[68:71], v[212:215]\n"
    "s_waitcnt lgkmcnt(5)\n"
    "v_mfma_f32_16x16x32_bf16 v[216:219], v[56:59], v[64:67], v[216:219]\n"
    "buffer_store_dwordx4 v[244:247], v13, s[28:31], s59 offen offset:1024 sc1\n"
    "s_add_u32 s60, s60, 262144\n"
    "ds_read_b128 v[44:47], v14 offset:4736\n"
    "v_mfma_f32_16x16x32_bf16 v[220:223], v[56:59], v[68:71], v[220:223]\n"
    "v_mfma_f32_16x16x32_bf16 v[224:227], v[60:63], v[64:67], v[224:227]\n"
    "s_add_u32 s40, s15, 3\n"
    "s_cmp_ge_u32 s40, 9\n"
    "s_cselect_b32 s41, 9, 0\n"
    "s_sub_u32 s40, s40, s41\n"
    "s_lshl_b32 s40, s40, 6\n"
    "s_lshl_b32 s41, s13, 5\n"
    "s_add_u32 s40, s40, s41\n"
    "s_mul_i32 s40, s40, 1152\n"
    "s_mul_i32 s41, s11, 288\n"
    "s_add_u32 s52, s40, s41\n"
    "s_mul_i32 s53, s9, 9216\n"
    "s_add_u32 m0, s53, 0\n"
    "s_nop 0\n"
    "buffer_load_dwordx4 v180, s[36:39], s52 offen lds\n"
    "s_add_u32 m0, m0, 1024\n"
    "ds_read_b128 v[56:59], v14 offset:9344\n"
    "v_mfma_f32_16x16x32_bf16 v[228:231], v[60:63], v[68:71], v[228:231]\n"
    "s_waitcnt lgkmcnt(5)\n"
    "v_mfma_f32_16x16x32_bf16 v[200:203], v[16:19], v[80:83], v[200:203]\n"
    "ds_read_b128 v[60:63], v14 offset:13952\n"
    "v_mfma_f32_16x16x32_bf16 v[204:207], v[16:19], v[84:87], v[204:207]\n"
    "v_mfma_f32_16x16x32_bf16 v[208:211], v[20:23], v[80:83], v[208:211]\n"
    "ds_read_b128 v[16:19], v14 offset:160\n"
    "v_mfma_f32_16x16x32_bf16 v[212:215], v[20:23], v[84:87], v[212:215]\n"
    "s_waitcnt lgkmcnt(5)\n"
    "v_mfma_f32_16x16x32_bf16 v[216:219], v[24:27], v[80:83], v[216:219]\n"
    "buffer_load_dwordx4 v181, s[36:39], s52 offen lds\n"
    "s_add_u32 m0, m0, 1024\n"
    "ds_read_b128 v[20:23], v14 offset:4768\n"
    "v_mfma_f32_16x16x32_bf16 v[220:223], v[24:27], v[84:87], v[220:223]\n"
    "v_mfma_f32_16x16x32_bf16 v[224:227], v[28:31], v[80:83], v[224:227]\n"
    "ds_read_b128 v[24:27], v14 offset:9376\n"
    "v_mfma_f32_16x16x32_bf16 v[228:231], v[28:31], v[84:87], v[228:231]\n"
    "s_waitcnt lgkmcnt(5)\n"
    "v_mfma_f32_16x16x32_bf16 v[200:203], v[40:43], v[96:99], v[200:203]\n"
    "ds_read_b128 v[28:31], v14 offset:13984\n"
    "v_mfma_f32_16x16x32_bf16 v[204:207], v[40:43], v[100:103], v[204:207]\n"
    "v_mfma_f32_16x16x32_bf16 v[208:211], v[44:47], v[96:99], v[208:211]\n"
    "buffer_load_dwordx4 v182, s[36:39], s52 offen lds\n"
    "s_add_u32 m0, m0, 1024\n"
    "ds_read_b128 v[40:43], v14 offset:192\n"
    "v_mfma_f32_16x16x32_bf16 v[212:215], v[44:47], v[100:103], v[212:215]\n"
    "s_waitcnt lgkmcnt(5)\n"
    "v_mfma_f32_16x16x32_bf16 v[216:219], v[56:59], v[96:99], v[216:219]\n"
    "ds_read_b128 v[44:47], v14 offset:4800\n"
    "v_mfma_f32_16x16x32_bf16 v[220:223], v[56:59], v[100:103], v[220:223]\n"
    "v_mfma_f32_16x16x32_bf16 v[224:227], v[60:63], v[96:99], v[224:227]\n"
    "ds_read_b128 v[56:59], v14 offset:9408\n"
    "v_mfma_f32_16x16x32_bf16 v[228:231], v[60:63], v[100:103], v[228:231]\n"
    "s_waitcnt lgkmcnt(5)\n"
    "v_mfma_f32_16x16x32_bf16 v[200:203], v[16:19], v[112:115], v[200:203]\n"
    "buffer_load_dwordx4 v183, s[36:39], s52 offen lds\n"
    "s_add_u32 m0, m0, 1024\n"
    "ds_read_b128 v[60:63], v14 offset:14016\n"
    "v_mfma_f32_16x16x32_bf16 v[204:207], v[16:19], v[116:119], v[204:207]\n"
    "v_mfma_f32_16x16x32_bf16 v[208:211], v[20:23], v[112:115], v[208:211]\n"
    "ds_read_b128 v[16:19], v14 offset:224\n"
    "v_mfma_f32_16x16x32_bf16 v[212:215], v[20:23], v[116:119], v[212:215]\n"
    "s_waitcnt lgkmcnt(5)\n"
    "v_mfma_f32_16x16x32_bf16 v[216:219], v[24:27], v[112:115], v[216:219]\n"
    "ds_read_b128 v[20:23], v14 offset:4832\n"
    "v_mfma_f32_16x16x32_bf16 v[220:223], v[24:27], v[116:119], v[220:223]\n"
    "v_mfma_f32_16x16x32_bf16 v[224:227], v[28:31], v[112:115], v[224:227]\n"
    "buffer_load_dwordx4 v184, s[36:39], s52 offen lds\n"
    "s_add_u32 m0, m0, 1024\n"
    "ds_read_b128 v[24:27], v14 offset:9440\n"
    "v_mfma_f32_16x16x32_bf16 v[228:231], v[28:31], v[116:119], v[228:231]\n"
    "s_waitcnt lgkmcnt(5)\n"
    "v_mfma_f32_16x16x32_bf16 v[200:203], v[40:43], v[128:131], v[200:203]\n"
    "ds_read_b128 v[28:31], v14 offset:14048\n"
    "v_mfma_f32_16x16x32_bf16 v[204:207], v[40:43], v[132:135], v[204:207]\n"
    "v_mfma_f32_16x16x32_bf16 v[208:211], v[44:47], v[128:131], v[208:211]\n"
    "ds_read_b128 v[40:43], v14 offset:256\n"
    "v_mfma_f32_16x16x32_bf16 v[212:215], v[44:47], v[132:135], v[212:215]\n"
    "s_waitcnt lgkmcnt(5)\n"
    "v_mfma_f32_16x16x32_bf16 v[216:219], v[56:59], v[128:131], v[216:219]\n"
    "buffer_load_dwordx4 v185, s[36:39], s52 offen lds\n"
    "s_add_u32 m0, m0, 1024\n"
    "ds_read_b128 v[44:47], v14 offset:4864\n"
    "v_mfma_f32_16x16x32_bf16 v[220:223], v[56:59], v[132:135], v[220:223]\n"
    "v_mfma_f32_16x16x32_bf16 v[224:227], v[60:63], v[128:131], v[224:227]\n"
    "ds_read_b128 v[56:59], v14 offset:9472\n"
    "v_mfma_f32_16x16x32_bf16 v[228:231], v[60:63], v[132:135], v[228:231]\n"
    "s_waitcnt lgkmcnt(5)\n"
    "v_mfma_f32_16x16x32_bf16 v[200:203], v[16:19], v[144:147], v[200:203]\n"
    "ds_read_b128 v[60:63], v14 offset:14080\n"
    "v_mfma_f32_16x16x32_bf16 v[204:207], v[16:19], v[148:151], v[204:207]\n"
    "v_mfma_f32_16x16x32_bf16 v[208:211], v[20:23], v[144:147], v[208:211]\n"
    "buffer_load_dwordx4 v186, s[36:39], s52 offen lds\n"
    "s_add_u32 m0, m0, 1024\n"
    "v_mfma_f32_16x16x32_bf16 v[212:215], v[20:23], v[148:151], v[212:215]\n"
    "s_waitcnt lgkmcnt(4)\n"
    "v_mfma_f32_16x16x32_bf16 v[216:219], v[24:27], v[144:147], v[216:219]\n"
    "v_mfma_f32_16x16x32_bf16 v[220:223], v[24:27], v[148:151], v[220:223]\n"
    "v_mfma_f32_16x16x32_bf16 v[224:227], v[28:31], v[144:147], v[224:227]\n"
    "v_mfma_f32_16x16x32_bf16 v[228:231], v[28:31], v[148:151], v[228:231]\n"
    "s_waitcnt lgkmcnt(2)\n"
    "s_waitcnt lgkmcnt(0)\n"
    "s_waitcnt vmcnt(11)\n"
    "s_barrier\n"
    "v_mfma_f32_16x16x32_bf16 v[200:203], v[40:43], v[160:163], v[200:203]\n"
    "buffer_load_dwordx4 v187, s[36:39], s52 offen lds\n"
    "s_add_u32 m0, m0, 1024\n"
    "ds_read_b128 v[16:19], v3 offset:0\n"
    "v_mfma_f32_16x16x32_bf16 v[204:207], v[40:43], v[164:167], v[204:207]\n"
    "ds_read_b128 v[20:23], v3 offset:4608\n"
    "v_mfma_f32_16x16x32_bf16 v[208:211], v[44:47], v[160:163], v[208:211]\n"
    "ds_read_b128 v[24:27], v3 offset:9216\n"
    "v_mfma_f32_16x16x32_bf16 v[212:215], v[44:47], v[164:167], v[212:215]\n"
    "ds_read_b128 v[28:31], v3 offset:13824\n"
    "v_mfma_f32_16x16x32_bf16 v[216:219], v[56:59], v[160:163], v[216:219]\n"
    "ds_read_b128 v[40:43], v3 offset:32\n"
    "v_mfma_f32_16x16x32_bf16 v[220:223], v[56:59], v[164:167], v[220:223]\n"
    "ds_read_b128 v[44:47], v3 offset:4640\n"
    "v_mfma_f32_16x16x32_bf16 v[224:227], v[60:63], v[160:163], v[224:227]\n"
    "buffer_load_dwordx4 v188, s[36:39], s52 offen lds\n"
    "ds_read_b128 v[56:59], v3 offset:9248\n"
    "v_mfma_f32_16x16x32_bf16 v[228:231], v[60:63], v[164:167], v[228:231]\n"
    "s_waitcnt lgkmcnt(5)\n"
    "v_cvt_pk_bf16_f32 v232, v200, v201\n"
    "v_cvt_pk_bf16_f32 v233, v202, v203\n"
    "v_mfma_f32_16x16x32_bf16 v[200:203], v[16:19], v[32:35], 0\n"
    "ds_read_b128 v[60:63], v3 offset:13856\n"
    "v_add_u32 v14, 0, v1\n"
    "v_cvt_pk_bf16_f32 v236, v204, v205\n"
    "v_cvt_pk_bf16_f32 v237, v206, v207\n"
    "v_mfma_f32_16x16x32_bf16 v[204:207], v[16:19], v[36:39], 0\n"
    "v_cvt_pk_bf16_f32 v234, v208, v209\n"
    "v_cvt_pk_bf16_f32 v235, v210, v211\n"
    "v_mfma_f32_16x16x32_bf16 v[208:211], v[20:23], v[32:35], 0\n"
    "ds_read_b128 v[16:19], v3 offset:64\n"
    "v_cvt_pk_bf16_f32 v238, v212, v213\n"
    "v_cvt_pk_bf16_f32 v239, v214, v215\n"
    "v_mfma_f32_16x16x32_bf16 v[212:215], v[20:23], v[36:39], 0\n"
    "s_waitcnt lgkmcnt(5)\n"
    "v_cvt_pk_bf16_f32 v240, v216, v217\n"
    "v_cvt_pk_bf16_f32 v241, v218, v219\n"
    "v_mfma_f32_16x16x32_bf16 v[216:219], v[24:27], v[32:35], 0\n"
    "ds_read_b128 v[20:23], v3 offset:4672\n"
    "v_cvt_pk_bf16_f32 v244, v220, v221\n"
    "v_cvt_pk_bf16_f32 v245, v222, v223\n"
    "v_mfma_f32_16x16x32_bf16 v[220:223], v[24:27], v[36:39], 0\n"
    "v_cvt_pk_bf16_f32 v242, v224, v225\n"
    "v_cvt_pk_bf16_f32 v243, v226, v227\n"
    "v_mfma_f32_16x16x32_bf16 v[224:227], v[28:31], v[32:35], 0\n"
    "ds_read_b128 v[24:27], v3 offset:9280\n"
    "v_cvt_pk_bf16_f32 v246, v228, v229\n"
    "v_cvt_pk_bf16_f32 v247, v230, v231\n"
    "v_mfma_f32_16x16x32_bf16 v[228:231], v[28:31], v[36:39], 0\n"
    "s_waitcnt lgkmcnt(5)\n"
    "v_mfma_f32_16x16x32_bf16 v[200:203], v[40:43], v[48:51], v[200:203]\n"
    "ds_read_b128 v[28:31], v3 offset:13888\n"
    "v_mfma_f32_16x16x32_bf16 v[204:207], v[40:43], v[52:55], v[204:207]\n"
    "s_add_u32 s59, s60, 65536\n"
    "buffer_store_dwordx4 v[232:235], v13, s[28:31], s60 offen offset:0 sc1\n"
    "v_mfma_f32_16x16x32_bf16 v[208:211], v[44:47], v[48:51], v[208:211]\n"
    "ds_read_b128 v[40:43], v3 offset:96\n"
    "v_mfma_f32_16x16x32_bf16 v[212:215], v[44:47], v[52:55], v[212:215]\n"
    "s_waitcnt lgkmcnt(5)\n"
    "v_mfma_f32_16x16x32_bf16 v[216:219], v[56:59], v[48:51], v[216:219]\n"
    "buffer_store_dwordx4 v[236:239], v13, s[28:31], s60 offen offset:1024 sc1\n"
    "ds_read_b128 v[44:47], v3 offset:4704\n"
    "v_mfma_f32_16x16x32_bf16 v[220:223], v[56:59], v[52:55], v[220:223]\n"
    "v_mfma_f32_16x16x32_bf16 v[224:227], v[60:63], v[48:51], v[224:227]\n"
    "ds_read_b128 v[56:59], v3 offset:9312\n"
    "v_mfma_f32_16x16x32_bf16 v[228:231], v[60:63], v[52:55], v[228:231]\n"
    "s_waitcnt lgkmcnt(5)\n"
    "v_mfma_f32_16x16x32_bf16 v[200:203], v[16:19], v[64:67], v[200:203]\n"
    "buffer_store_dwordx4 v[240:243], v13, s[28:31], s59 offen offset:0 sc1\n"
    "ds_read_b128 v[60:63], v3 offset:13920\n"
    "v_mfma_f32_16x16x32_bf16 v[204:207], v[16:19], v[68:71], v[204:207]\n"
    "v_mfma_f32_16x16x32_bf16 v[208:211], v[20:23], v[64:67], v[208:211]\n"
    "ds_read_b128 v[16:19], v3 offset:128\n"
    "v_mfma_f32_16x16x32_bf16 v[212:215], v[20:23], v[68:71], v[212:215]\n"
    "s_waitcnt lgkmcnt(5)\n"
    "v_mfma_f32_16x16x32_bf16 v[216:219], v[24:27], v[64:67], v[216:219]\n"
    "buffer_store_dwordx4 v[244:247], v13, s[28:31], s59 offen offset:1024 sc1\n"
    "s_add_u32 s60, s60, 262144\n"
    "ds_read_b128 v[20:23], v3 offset:4736\n"
    "v_mfma_f32_16x16x32_bf16 v[220:223], v[24:27], v[68:71], v[220:223]\n"
    "v_mfma_f32_16x16x32_bf16 v[224:227], v[28:31], v[64:67], v[224:227]\n"
    "s_add_u32 s40, s15, 4\n"
    "s_cmp_ge_u32 s40, 9\n"
    "s_cselect_b32 s41, 9, 0\n"
    "s_sub_u32 s40, s40, s41\n"
    "s_lshl_b32 s40, s40, 6\n"
    "s_lshl_b32 s41, s13, 5\n"
    "s_add_u32 s40, s40, s41\n"
    "s_mul_i32 s40, s40, 1152\n"
    "s_mul_i32 s41, s11, 288\n"
    "s_add_u32 s52, s40, s41\n"
    "s_mul_i32 s53, s9, 9216\n"
    "s_add_u32 m0, s53, 36864\n"
    "s_nop 0\n"
    "buffer_load_dwordx4 v180, s[36:39], s52 offen lds\n"
    "s_add_u32 m0, m0, 1024\n"
    "ds_read_b128 v[24:27], v3 offset:9344\n"
    "v_mfma_f32_16x16x32_bf16 v[228:231], v[28:31], v[68:71], v[228:231]\n"
    "s_waitcnt lgkmcnt(5)\n"
    "v_mfma_f32_16x16x32_bf16 v[200:203], v[40:43], v[80:83], v[200:203]\n"
    "ds_read_b128 v[28:31], v3 offset:13952\n"
    "v_mfma_f32_16x16x32_bf16 v[204:207], v[40:43], v[84:87], v[204:207]\n"
    "v_mfma_f32_16x16x32_bf16 v[208:211], v[44:47], v[80:83], v[208:211]\n"
    "ds_read_b128 v[40:43], v3 offset:160\n"
    "v_mfma_f32_16x16x32_bf16 v[212:215], v[44:47], v[84:87], v[212:215]\n"
    "s_waitcnt lgkmcnt(5)\n"
    "v_mfma_f32_16x16x32_bf16 v[216:219], v[56:59], v[80:83], v[216:219]\n"
    "buffer_load_dwordx4 v181, s[36:39], s52 offen lds\n"
    "s_add_u32 m0, m0, 1024\n"
    "ds_read_b128 v[44:47], v3 offset:4768\n"
    "v_mfma_f32_16x16x32_bf16 v[220:223], v[56:59], v[84:87], v[220:223]\n"
    "v_mfma_f32_16x16x32_bf16 v[224:227], v[60:63], v[80:83], v[224:227]\n"
    "ds_read_b128 v[56:59], v3 offset:9376\n"
    "v_mfma_f32_16x16x32_bf16 v[228:231], v[60:63], v[84:87], v[228:231]\n"
    "s_waitcnt lgkmcnt(5)\n"
    "v_mfma_f32_16x16x32_bf16 v[200:203], v[16:19], v[96:99], v[200:203]\n"
    "ds_read_b128 v[60:63], v3 offset:13984\n"
    "v_mfma_f32_16x16x32_bf16 v[204:207], v[16:19], v[100:103], v[204:207]\n"
    "v_mfma_f32_16x16x32_bf16 v[208:211], v[20:23], v[96:99], v[208:211]\n"
    "buffer_load_dwordx4 v182, s[36:39], s52 offen lds\n"
    "s_add_u32 m0, m0, 1024\n"
    "ds_read_b128 v[16:19], v3 offset:192\n"
    "v_mfma_f32_16x16x32_bf16 v[212:215], v[20:23], v[100:103], v[212:215]\n"
    "s_waitcnt lgkmcnt(5)\n"
    "v_mfma_f32_16x16x32_bf16 v[216:219], v[24:27], v[96:99], v[216:219]\n"
    "ds_read_b128 v[20:23], v3 offset:4800\n"
    "v_mfma_f32_16x16x32_bf16 v[220:223], v[24:27], v[100:103], v[220:223]\n"
    "v_mfma_f32_16x16x32_bf16 v[224:227], v[28:31], v[96:99], v[224:227]\n"
    "ds_read_b128 v[24:27], v3 offset:9408\n"
    "v_mfma_f32_16x16x32_bf16 v[228:231], v[28:31], v[100:103], v[228:231]\n"
    "s_waitcnt lgkmcnt(5)\n"
    "v_mfma_f32_16x16x32_bf16 v[200:203], v[40:43], v[112:115], v[200:203]\n"
    "buffer_load_dwordx4 v183, s[36:39], s52 offen lds\n"
    "s_add_u32 m0, m0, 1024\n"
    "ds_read_b128 v[28:31], v3 offset:14016\n"
    "v_mfma_f32_16x16x32_bf16 v[204:207], v[40:43], v[116:119], v[204:207]\n"
    "v_mfma_f32_16x16x32_bf16 v[208:211], v[44:47], v[112:115], v[208:211]\n"
    "ds_read_b128 v[40:43], v3 offset:224\n"
    "v_mfma_f32_16x16x32_bf16 v[212:215], v[44:47], v[116:119], v[212:215]\n"
    "s_waitcnt lgkmcnt(5)\n"
    "v_mfma_f32_16x16x32_bf16 v[216:219], v[56:59], v[112:115], v[216:219]\n"
    "ds_read_b128 v[44:47], v3 offset:4832\n"
    "v_mfma_f32_16x16x32_bf16 v[220:223], v[56:59], v[116:119], v[220:223]\n"
    "v_mfma_f32_16x16x32_bf16 v[224:227], v[60:63], v[112:115], v[224:227]\n"
    "buffer_load_dwordx4 v184, s[36:39], s52 offen lds\n"
    "s_add_u32 m0, m0, 1024\n"
    "ds_read_b128 v[56:59], v3 offset:9440\n"
    "v_mfma_f32_16x16x32_bf16 v[228:231], v[60:63], v[116:119], v[228:231]\n"
    "s_waitcnt lgkmcnt(5)\n"
    "v_mfma_f32_16x16x32_bf16 v[200:203], v[16:19], v[128:131], v[200:203]\n"
    "ds_read_b128 v[60:63], v3 offset:14048\n"
    "v_mfma_f32_16x16x32_bf16 v[204:207], v[16:19], v[132:135], v[204:207]\n"
    "v_mfma_f32_16x16x32_bf16 v[208:211], v[20:23], v[128:131], v[208:211]\n"
    "s_waitcnt vmcnt(18)\n"
    "buffer_store_dwordx2 v[192:193], v190, s[72:75], s57 offen sc1\n"
    "s_add_u32 s57, s57, 512\n"
    "ds_read_b128 v[16:19], v3 offset:256\n"
    "v_mfma_f32_16x16x32_bf16 v[212:215], v[20:23], v[132:135], v[212:215]\n"
    "s_waitcnt lgkmcnt(5)\n"
    "v_mfma_f32_16x16x32_bf16 v[216:219], v[24:27], v[128:131], v[216:219]\n"
    "buffer_load_dwordx4 v185, s[36:39], s52 offen lds\n"
    "s_add_u32 m0, m0, 1024\n"
    "ds_read_b128 v[20:23], v3 offset:4864\n"
    "v_mfma_f32_16x16x32_bf16 v[220:223], v[24:27], v[132:135], v[220:223]\n"
    "v_mfma_f32_16x16x32_bf16 v[224:227], v[28:31], v[128:131], v[224:227]\n"
    "ds_read_b128 v[24:27], v3 offset:9472\n"
    "v_mfma_f32_16x16x32_bf16 v[228:231], v[28:31], v[132:135], v[228:231]\n"
    "s_waitcnt lgkmcnt(5)\n"
    "v_mfma_f32_16x16x32_bf16 v[200:203], v[40:43], v[144:147], v[200:203]\n"
    "ds_read_b128 v[28:31], v3 offset:14080\n"
    "v_mfma_f32_16x16x32_bf16 v[204:207], v[40:43], v[148:151], v[204:207]\n"
    "v_mfma_f32_16x16x32_bf16 v[208:211], v[44:47], v[144:147], v[208:211]\n"
    "buffer_load_dwordx4 v186, s[36:39], s52 offen lds\n"
    "s_add_u32 m0, m0, 1024\n"
    "v_mfma_f32_16x16x32_bf16 v[212:215], v[44:47], v[148:151], v[212:215]\n"
    "s_waitcnt lgkmcnt(4)\n"
    "v_mfma_f32_16x16x32_bf16 v[216:219], v[56:59], v[144:147], v[216:219]\n"
    "v_mfma_f32_16x16x32_bf16 v[220:223], v[56:59], v[148:151], v[220:223]\n"
    "v_mfma_f32_16x16x32_bf16 v[224:227], v[60:63], v[144:147], v[224:227]\n"
    "v_mfma_f32_16x16x32_bf16 v[228:231], v[60:63], v[148:151], v[228:231]\n"
    "s_waitcnt lgkmcnt(2)\n"
    "s_waitcnt lgkmcnt(0)\n"
    "s_waitcnt vmcnt(12)\n"
    "s_barrier\n"
    "v_mfma_f32_16x16x32_bf16 v[200:203], v[16:19], v[160:163], v[200:203]\n"
    "buffer_load_dwordx4 v187, s[36:39], s52 offen lds\n"
    "s_add_u32 m0, m0, 1024\n"
    "ds_read_b128 v[40:43], v14 offset:0\n"
    "v_mfma_f32_16x16x32_bf16 v[204:207], v[16:19], v[164:167], v[204:207]\n"
    "ds_read_b128 v[44:47], v14 offset:4608\n"
    "v_mfma_f32_16x16x32_bf16 v[208:211], v[20:23], v[160:163], v[208:211]\n"
    "ds_read_b128 v[56:59], v14 offset:9216\n"
    "v_mfma_f32_16x16x32_bf16 v[212:215], v[20:23], v[164:167], v[212:215]\n"
    "ds_read_b128 v[60:63], v14 offset:13824\n"
    "v_mfma_f32_16x16x32_bf16 v[216:219], v[24:27], v[160:163], v[216:219]\n"
    "ds_read_b128 v[16:19], v14 offset:32\n"
    "v_mfma_f32_16x16x32_bf16 v[220:223], v[24:27], v[164:167], v[220:223]\n"
    "ds_read_b128 v[20:23], v14 offset:4640\n"
    "v_mfma_f32_16x16x32_bf16 v[224:227], v[28:31], v[160:163], v[224:227]\n"
    "buffer_load_dwordx4 v188, s[36:39], s52 offen lds\n"
    "ds_read_b128 v[24:27], v14 offset:9248\n"
    "v_mfma_f32_16x16x32_bf16 v[228:231], v[28:31], v[164:167], v[228:231]\n"
    "s_waitcnt lgkmcnt(5)\n"
    "v_cvt_pk_bf16_f32 v232, v200, v201\n"
    "v_cvt_pk_bf16_f32 v233, v202, v203\n"
    "v_mfma_f32_16x16x32_bf16 v[200:203], v[40:43], v[32:35], 0\n"
    "ds_read_b128 v[28:31], v14 offset:13856\n"
    "v_add_u32 v3, 36864, v1\n"
    "v_cvt_pk_bf16_f32 v236, v204, v205\n"
    "v_cvt_pk_bf16_f32 v237, v206, v207\n"
    "v_mfma_f32_16x16x32_bf16 v[204:207], v[40:43], v[36:39], 0\n"
    "v_cvt_pk_bf16_f32 v234, v208, v209\n"
    "v_cvt_pk_bf16_f32 v235, v210, v211\n"
    "v_mfma_f32_16x16x32_bf16 v[208:211], v[44:47], v[32:35], 0\n"
    "ds_read_b128 v[40:43], v14 offset:64\n"
    "v_cvt_pk_bf16_f32 v238, v212, v213\n"
    "v_cvt_pk_bf16_f32 v239, v214, v215\n"
    "v_mfma_f32_16x16x32_bf16 v[212:215], v[44:47], v[36:39], 0\n"
    "s_waitcnt lgkmcnt(5)\n"
    "v_cvt_pk_bf16_f32 v240, v216, v217\n"
    "v_cvt_pk_bf16_f32 v241, v218, v219\n"
    "v_mfma_f32_16x16x32_bf16 v[216:219], v[56:59], v[32:35], 0\n"
    "ds_read_b128 v[44:47], v14 offset:4672\n"
    "v_cvt_pk_bf16_f32 v244, v220, v221\n"
    "v_cvt_pk_bf16_f32 v245, v222, v223\n"
    "v_mfma_f32_16x16x32_bf16 v[220:223], v[56:59], v[36:39], 0\n"
    "v_cvt_pk_bf16_f32 v242, v224, v225\n"
    "v_cvt_pk_bf16_f32 v243, v226, v227\n"
    "v_mfma_f32_16x16x32_bf16 v[224:227], v[60:63], v[32:35], 0\n"
    "ds_read_b128 v[56:59], v14 offset:9280\n"
    "v_cvt_pk_bf16_f32 v246, v228, v229\n"
    "v_cvt_pk_bf16_f32 v247, v230, v231\n"
    "v_mfma_f32_16x16x32_bf16 v[228:231], v[60:63], v[36:39], 0\n"
    "s_waitcnt lgkmcnt(5)\n"
    "v_mfma_f32_16x16x32_bf16 v[200:203], v[16:19], v[48:51], v[200:203]\n"
    "ds_read_b128 v[60:63], v14 offset:13888\n"
    "v_mfma_f32_16x16x32_bf16 v[204:207], v[16:19], v[52:55], v[204:207]\n"
    "s_add_u32 s59, s60, 65536\n"
    "buffer_store_dwordx4 v[232:235], v13, s[28:31], s60 offen offset:0 sc1\n"
    "v_mfma_f32_16x16x32_bf16 v[208:211], v[20:23], v[48:51], v[208:211]\n"
    "ds_read_b128 v[16:19], v14 offset:96\n"
    "v_mfma_f32_16x16x32_bf16 v[212:215], v[20:23], v[52:55], v[212:215]\n"
    "s_waitcnt lgkmcnt(5)\n"
    "v_mfma_f32_16x16x32_bf16 v[216:219], v[24:27], v[48:51], v[216:219]\n"
    "buffer_store_dwordx4 v[236:239], v13, s[28:31], s60 offen offset:1024 sc1\n"
    "ds_read_b128 v[20:23], v14 offset:4704\n"
    "v_mfma_f32_16x16x32_bf16 v[220:223], v[24:27], v[52:55], v[220:223]\n"
    "v_mfma_f32_16x16x32_bf16 v[224:227], v[28:31], v[48:51], v[224:227]\n"
    "ds_read_b128 v[24:27], v14 offset:9312\n"
    "v_mfma_f32_16x16x32_bf16 v[228:231], v[28:31], v[52:55], v[228:231]\n"
    "s_waitcnt lgkmcnt(5)\n"
    "v_mfma_f32_16x16x32_bf16 v[200:203], v[40:43], v[64:67], v[200:203]\n"
    "buffer_store_dwordx4 v[240:243], v13, s[28:31], s59 offen offset:0 sc1\n"
    "ds_read_b128 v[28:31], v14 offset:13920\n"
    "v_mfma_f32_16x16x32_bf16 v[204:207], v[40:43], v[68:71], v[204:207]\n"
    "v_mfma_f32_16x16x32_bf16 v[208:211], v[44:47], v[64:67], v[208:211]\n"
    "ds_read_b128 v[40:43], v14 offset:128\n"
    "v_mfma_f32_16x16x32_bf16 v[212:215], v[44:47], v[68:71], v[212:215]\n"
    "s_waitcnt lgkmcnt(5)\n"
    "v_mfma_f32_16x16x32_bf16 v[216:219], v[56:59], v[64:67], v[216:219]\n"
    "buffer_store_dwordx4 v[244:247], v13, s[28:31], s59 offen offset:1024 sc1\n"
    "s_add_u32 s60, s60, 262144\n"
    "ds_read_b128 v[44:47], v14 offset:4736\n"
    "v_mfma_f32_16x16x32_bf16 v[220:223], v[56:59], v[68:71], v[220:223]\n"
    "v_mfma_f32_16x16x32_bf16 v[224:227], v[60:63], v[64:67], v[224:227]\n"
    "s_add_u32 s40, s15, 5\n"
    "s_cmp_ge_u32 s40, 9\n"
    "s_cselect_b32 s41, 9, 0\n"
    "s_sub_u32 s40, s40, s41\n"
    "s_lshl_b32 s40, s40, 6\n"
    "s_lshl_b32 s41, s13, 5\n"
    "s_add_u32 s40, s40, s41\n"
    "s_mul_i32 s40, s40, 1152\n"
    "s_mul_i32 s41, s11, 288\n"
    "s_add_u32 s52, s40, s41\n"
    "s_mul_i32 s53, s9, 9216\n"
    "s_add_u32 m0, s53, 73728\n"
    "s_nop 0\n"
    "buffer_load_dwordx4 v180, s[36:39], s52 offen lds\n"
    "s_add_u32 m0, m0, 1024\n"
    "ds_read_b128 v[56:59], v14 offset:9344\n"
    "v_mfma_f32_16x16x32_bf16 v[228:231], v[60:63], v[68:71], v[228:231]\n"
    "s_waitcnt lgkmcnt(5)\n"
    "v_mfma_f32_16x16x32_bf16 v[200:203], v[16:19], v[80:83], v[200:203]\n"
    "ds_read_b128 v[60:63], v14 offset:13952\n"
    "v_mfma_f32_16x16x32_bf16 v[204:207], v[16:19], v[84:87], v[204:207]\n"
    "v_mfma_f32_16x16x32_bf16 v[208:211], v[20:23], v[80:83], v[208:211]\n"
    "ds_read_b128 v[16:19], v14 offset:160\n"
    "v_mfma_f32_16x16x32_bf16 v[212:215], v[20:23], v[84:87], v[212:215]\n"
    "s_waitcnt lgkmcnt(5)\n"
    "v_mfma_f32_16x16x32_bf16 v[216:219], v[24:27], v[80:83], v[216:219]\n"
    "buffer_load_dwordx4 v181, s[36:39], s52 offen lds\n"
    "s_add_u32 m0, m0, 1024\n"
    "ds_read_b128 v[20:23], v14 offset:4768\n"
    "v_mfma_f32_16x16x32_bf16 v[220:223], v[24:27], v[84:87], v[220:223]\n"
    "v_mfma_f32_16x16x32_bf16 v[224:227], v[28:31], v[80:83], v[224:227]\n"
    "ds_read_b128 v[24:27], v14 offset:9376\n"
    "v_mfma_f32_16x16x32_bf16 v[228:231], v[28:31], v[84:87], v[228:231]\n"
    "s_waitcnt lgkmcnt(5)\n"
    "v_mfma_f32_16x16x32_bf16 v[200:203], v[40:43], v[96:99], v[200:203]\n"
    "ds_read_b128 v[28:31], v14 offset:13984\n"
    "v_mfma_f32_16x16x32_bf16 v[204:207], v[40:43], v[100:103], v[204:207]\n"
    "v_mfma_f32_16x16x32_bf16 v[208:211], v[44:47], v[96:99], v[208:211]\n"
    "buffer_load_dwordx4 v182, s[36:39], s52 offen lds\n"
    "s_add_u32 m0, m0, 1024\n"
    "ds_read_b128 v[40:43], v14 offset:192\n"
    "v_mfma_f32_16x16x32_bf16 v[212:215], v[44:47], v[100:103], v[212:215]\n"
    "s_waitcnt lgkmcnt(5)\n"
    "v_mfma_f32_16x16x32_bf16 v[216:219], v[56:59], v[96:99], v[216:219]\n"
    "ds_read_b128 v[44:47], v14 offset:4800\n"
    "v_mfma_f32_16x16x32_bf16 v[220:223], v[56:59], v[100:103], v[220:223]\n"
    "v_mfma_f32_16x16x32_bf16 v[224:227], v[60:63], v[96:99], v[224:227]\n"
    "ds_read_b128 v[56:59], v14 offset:9408\n"
    "v_mfma_f32_16x16x32_bf16 v[228:231], v[60:63], v[100:103], v[228:231]\n"
    "s_waitcnt lgkmcnt(5)\n"
    "v_mfma_f32_16x16x32_bf16 v[200:203], v[16:19], v[112:115], v[200:203]\n"
    "buffer_load_dwordx4 v183, s[36:39], s52 offen lds\n"
    "s_add_u32 m0, m0, 1024\n"
    "ds_read_b128 v[60:63], v14 offset:14016\n"
    "v_mfma_f32_16x16x32_bf16 v[204:207], v[16:19], v[116:119], v[204:207]\n"
    "v_mfma_f32_16x16x32_bf16 v[208:211], v[20:23], v[112:115], v[208:211]\n"
    "ds_read_b128 v[16:19], v14 offset:224\n"
    "v_mfma_f32_16x16x32_bf16 v[212:215], v[20:23], v[116:119], v[212:215]\n"
    "s_waitcnt lgkmcnt(5)\n"
    "v_mfma_f32_16x16x32_bf16 v[216:219], v[24:27], v[112:115], v[216:219]\n"
    "ds_read_b128 v[20:23], v14 offset:4832\n"
    "v_mfma_f32_16x16x32_bf16 v[220:223], v[24:27], v[116:119], v[220:223]\n"
    "v_mfma_f32_16x16x32_bf16 v[224:227], v[28:31], v[112:115], v[224:227]\n"
    "buffer_load_dwordx4 v184, s[36:39], s52 offen lds\n"
    "s_add_u32 m0, m0, 1024\n"
    "ds_read_b128 v[24:27], v14 offset:9440\n"
    "v_mfma_f32_16x16x32_bf16 v[228:231], v[28:31], v[116:119], v[228:231]\n"
    "s_waitcnt lgkmcnt(5)\n"
    "v_mfma_f32_16x16x32_bf16 v[200:203], v[40:43], v[128:131], v[200:203]\n"
    "ds_read_b128 v[28:31], v14 offset:14048\n"
    "v_mfma_f32_16x16x32_bf16 v[204:207], v[40:43], v[132:135], v[204:207]\n"
    "v_mfma_f32_16x16x32_bf16 v[208:211], v[44:47], v[128:131], v[208:211]\n"
    "s_waitcnt vmcnt(19)\n"
    "buffer_store_dwordx2 v[192:193], v190, s[72:75], s57 offen sc1\n"
    "s_add_u32 s57, s57, 512\n"
    "ds_read_b128 v[40:43], v14 offset:256\n"
    "v_mfma_f32_16x16x32_bf16 v[212:215], v[44:47], v[132:135], v[212:215]\n"
    "s_waitcnt lgkmcnt(5)\n"
    "v_mfma_f32_16x16x32_bf16 v[216:219], v[56:59], v[128:131], v[216:219]\n"
    "buffer_load_dwordx4 v185, s[36:39], s52 offen lds\n"
    "s_add_u32 m0, m0, 1024\n"
    "ds_read_b128 v[44:47], v14 offset:4864\n"
    "v_mfma_f32_16x16x32_bf16 v[220:223], v[56:59], v[132:135], v[220:223]\n"
    "v_mfma_f32_16x16x32_bf16 v[224:227], v[60:63], v[128:131], v[224:227]\n"
    "ds_read_b128 v[56:59], v14 offset:9472\n"
    "v_mfma_f32_16x16x32_bf16 v[228:231], v[60:63], v[132:135], v[228:231]\n"
    "s_waitcnt lgkmcnt(5)\n"
    "v_mfma_f32_16x16x32_bf16 v[200:203], v[16:19], v[144:147], v[200:203]\n"
    "ds_read_b128 v[60:63], v14 offset:14080\n"
    "v_mfma_f32_16x16x32_bf16 v[204:207], v[16:19], v[148:151], v[204:207]\n"
    "v_mfma_f32_16x16x32_bf16 v[208:211], v[20:23], v[144:147], v[208:211]\n"
    "buffer_load_dwordx4 v186, s[36:39], s52 offen lds\n"
    "s_add_u32 m0, m0, 1024\n"
    "v_mfma_f32_16x16x32_bf16 v[212:215], v[20:23], v[148:151], v[212:215]\n"
    "s_waitcnt lgkmcnt(4)\n"
    "v_mfma_f32_16x16x32_bf16 v[216:219], v[24:27], v[144:147], v[216:219]\n"
    "v_mfma_f32_16x16x32_bf16 v[220:223], v[24:27], v[148:151], v[220:223]\n"
    "v_mfma_f32_16x16x32_bf16 v[224:227], v[28:31], v[144:147], v[224:227]\n"
    "v_mfma_f32_16x16x32_bf16 v[228:231], v[28:31], v[148:151], v[228:231]\n"
    "s_waitcnt lgkmcnt(2)\n"
    "s_waitcnt lgkmcnt(0)\n"
    "s_waitcnt vmcnt(12)\n"
    "s_barrier\n"
    "v_mfma_f32_16x16x32_bf16 v[200:203], v[40:43], v[160:163], v[200:203]\n"
    "buffer_load_dwordx4 v187, s[36:39], s52 offen lds\n"
    "s_add_u32 m0, m0, 1024\n"
    "ds_read_b128 v[16:19], v3 offset:0\n"
    "v_mfma_f32_16x16x32_bf16 v[204:207], v[40:43], v[164:167], v[204:207]\n"
    "ds_read_b128 v[20:23], v3 offset:4608\n"
    "v_mfma_f32_16x16x32_bf16 v[208:211], v[44:47], v[160:163], v[208:211]\n"
    "ds_read_b128 v[24:27], v3 offset:9216\n"
    "v_mfma_f32_16x16x32_bf16 v[212:215], v[44:47], v[164:167], v[212:215]\n"
    "ds_read_b128 v[28:31], v3 offset:13824\n"
    "v_mfma_f32_16x16x32_bf16 v[216:219], v[56:59], v[160:163], v[216:219]\n"
    "ds_read_b128 v[40:43], v3 offset:32\n"
    "v_mfma_f32_16x16x32_bf16 v[220:223], v[56:59], v[164:167], v[220:223]\n"
    "ds_read_b128 v[44:47], v3 offset:4640\n"
    "v_mfma_f32_16x16x32_bf16 v[224:227], v[60:63], v[160:163], v[224:227]\n"
    "buffer_load_dwordx4 v188, s[36:39], s52 offen lds\n"
    "ds_read_b128 v[56:59], v3 offset:9248\n"
    "v_mfma_f32_16x16x32_bf16 v[228:231], v[60:63], v[164:167], v[228:231]\n"
    "s_waitcnt lgkmcnt(5)\n"
    "v_cvt_pk_bf16_f32 v232, v200, v201\n"
    "v_cvt_pk_bf16_f32 v233, v202, v203\n"
    "v_mfma_f32_16x16x32_bf16 v[200:203], v[16:19], v[32:35], 0\n"
    "ds_read_b128 v[60:63], v3 offset:13856\n"
    "v_add_u32 v14, 73728, v1\n"
    "v_cvt_pk_bf16_f32 v236, v204, v205\n"
    "v_cvt_pk_bf16_f32 v237, v206, v207\n"
    "v_mfma_f32_16x16x32_bf16 v[204:207], v[16:19], v[36:39], 0\n"
    "v_cvt_pk_bf16_f32 v234, v208, v209\n"
    "v_cvt_pk_bf16_f32 v235, v210, v211\n"
    "v_mfma_f32_16x16x32_bf16 v[208:211], v[20:23], v[32:35], 0\n"
    "ds_read_b128 v[16:19], v3 offset:64\n"
    "v_cvt_pk_bf16_f32 v238, v212, v213\n"
    "v_cvt_pk_bf16_f32 v239, v214, v215\n"
    "v_mfma_f32_16x16x32_bf16 v[212:215], v[20:23], v[36:39], 0\n"
    "s_waitcnt lgkmcnt(5)\n"
    "v_cvt_pk_bf16_f32 v240, v216, v217\n"
    "v_cvt_pk_bf16_f32 v241, v218, v219\n"
    "v_mfma_f32_16x16x32_bf16 v[216:219], v[24:27], v[32:35], 0\n"
    "ds_read_b128 v[20:23], v3 offset:4672\n"
    "v_cvt_pk_bf16_f32 v244, v220, v221\n"
    "v_cvt_pk_bf16_f32 v245, v222, v223\n"
    "v_mfma_f32_16x16x32_bf16 v[220:223], v[24:27], v[36:39], 0\n"
    "v_cvt_pk_bf16_f32 v242, v224, v225\n"
    "v_cvt_pk_bf16_f32 v243, v226, v227\n"
    "v_mfma_f32_16x16x32_bf16 v[224:227], v[28:31], v[32:35], 0\n"
    "ds_read_b128 v[24:27], v3 offset:9280\n"
    "v_cvt_pk_bf16_f32 v246, v228, v229\n"
    "v_cvt_pk_bf16_f32 v247, v230, v231\n"
    "v_mfma_f32_16x16x32_bf16 v[228:231], v[28:31], v[36:39], 0\n"
    "s_waitcnt lgkmcnt(5)\n"
    "v_mfma_f32_16x16x32_bf16 v[200:203], v[40:43], v[48:51], v[200:203]\n"
    "ds_read_b128 v[28:31], v3 offset:13888\n"
    "v_mfma_f32_16x16x32_bf16 v[204:207], v[40:43], v[52:55], v[204:207]\n"
    "s_add_u32 s59, s60, 65536\n"
    "buffer_store_dwordx4 v[232:235], v13, s[28:31], s60 offen offset:0 sc1\n"
    "v_mfma_f32_16x16x32_bf16 v[208:211], v[44:47], v[48:51], v[208:211]\n"
    "ds_read_b128 v[40:43], v3 offset:96\n"
    "v_mfma_f32_16x16x32_bf16 v[212:215], v[44:47], v[52:55], v[212:215]\n"
    "s_waitcnt lgkmcnt(5)\n"
    "v_mfma_f32_16x16x32_bf16 v[216:219], v[56:59], v[48:51], v[216:219]\n"
    "buffer_store_dwordx4 v[236:239], v13, s[28:31], s60 offen offset:1024 sc1\n"
    "ds_read_b128 v[44:47], v3 offset:4704\n"
    "v_mfma_f32_16x16x32_bf16 v[220:223], v[56:59], v[52:55], v[220:223]\n"
    "v_mfma_f32_16x16x32_bf16 v[224:227], v[60:63], v[48:51], v[224:227]\n"
    "ds_read_b128 v[56:59], v3 offset:9312\n"
    "v_mfma_f32_16x16x32_bf16 v[228:231], v[60:63], v[52:55], v[228:231]\n"
    "s_waitcnt lgkmcnt(5)\n"
    "v_mfma_f32_16x16x32_bf16 v[200:203], v[16:19], v[64:67], v[200:203]\n"
    "buffer_store_dwordx4 v[240:243], v13, s[28:31], s59 offen offset:0 sc1\n"
    "ds_read_b128 v[60:63], v3 offset:13920\n"
    "v_mfma_f32_16x16x32_bf16 v[204:207], v[16:19], v[68:71], v[204:207]\n"
    "v_mfma_f32_16x16x32_bf16 v[208:211], v[20:23], v[64:67], v[208:211]\n"
    "ds_read_b128 v[16:19], v3 offset:128\n"
    "v_mfma_f32_16x16x32_bf16 v[212:215], v[20:23], v[68:71], v[212:215]\n"
    "s_waitcnt lgkmcnt(5)\n"
    "v_mfma_f32_16x16x32_bf16 v[216:219], v[24:27], v[64:67], v[216:219]\n"
    "buffer_store_dwordx4 v[244:247], v13, s[28:31], s59 offen offset:1024 sc1\n"
    "s_add_u32 s60, s60, 262144\n"
    "ds_read_b128 v[20:23], v3 offset:4736\n"
    "v_mfma_f32_16x16x32_bf16 v[220:223], v[24:27], v[68:71], v[220:223]\n"
    "v_mfma_f32_16x16x32_bf16 v[224:227], v[28:31], v[64:67], v[224:227]\n"
    "s_add_u32 s40, s15, 6\n"
    "s_cmp_ge_u32 s40, 9\n"
    "s_cselect_b32 s41, 9, 0\n"
    "s_sub_u32 s40, s40, s41\n"
    "s_lshl_b32 s40, s40, 6\n"
    "s_lshl_b32 s41, s13, 5\n"
    "s_add_u32 s40, s40, s41\n"
    "s_mul_i32 s40, s40, 1152\n"
    "s_mul_i32 s41, s11, 288\n"
    "s_add_u32 s52, s40, s41\n"
    "s_mul_i32 s53, s9, 9216\n"
    "s_add_u32 m0, s53, 0\n"
    "s_nop 0\n"
    "buffer_load_dwordx4 v180, s[36:39], s52 offen lds\n"
    "s_add_u32 m0, m0, 1024\n"
    "ds_read_b128 v[24:27], v3 offset:9344\n"
    "v_mfma_f32_16x16x32_bf16 v[228:231], v[28:31], v[68:71], v[228:231]\n"
    "s_waitcnt lgkmcnt(5)\n"
    "v_mfma_f32_16x16x32_bf16 v[200:203], v[40:43], v[80:83], v[200:203]\n"
    "ds_read_b128 v[28:31], v3 offset:13952\n"
    "v_mfma_f32_16x16x32_bf16 v[204:207], v[40:43], v[84:87], v[204:207]\n"
    "v_mfma_f32_16x16x32_bf16 v[208:211], v[44:47], v[80:83], v[208:211]\n"
    "ds_read_b128 v[40:43], v3 offset:160\n"
    "v_mfma_f32_16x16x32_bf16 v[212:215], v[44:47], v[84:87], v[212:215]\n"
    "s_waitcnt lgkmcnt(5)\n"
    "v_mfma_f32_16x16x32_bf16 v[216:219], v[56:59], v[80:83], v[216:219]\n"
    "buffer_load_dwordx4 v181, s[36:39], s52 offen lds\n"
    "s_add_u32 m0, m0, 1024\n"
    "ds_read_b128 v[44:47], v3 offset:4768\n"
    "v_mfma_f32_16x16x32_bf16 v[220:223], v[56:59], v[84:87], v[220:223]\n"
    "v_mfma_f32_16x16x32_bf16 v[224:227], v[60:63], v[80:83], v[224:227]\n"
    "ds_read_b128 v[56:59], v3 offset:9376\n"
    "v_mfma_f32_16x16x32_bf16 v[228:231], v[60:63], v[84:87], v[228:231]\n"
    "s_waitcnt lgkmcnt(5)\n"
    "v_mfma_f32_16x16x32_bf16 v[200:203], v[16:19], v[96:99], v[200:203]\n"
    "ds_read_b128 v[60:63], v3 offset:13984\n"
    "v_mfma_f32_16x16x32_bf16 v[204:207], v[16:19], v[100:103], v[204:207]\n"
    "v_mfma_f32_16x16x32_bf16 v[208:211], v[20:23], v[96:99], v[208:211]\n"
    "buffer_load_dwordx4 v182, s[36:39], s52 offen lds\n"
    "s_add_u32 m0, m0, 1024\n"
    "ds_read_b128 v[16:19], v3 offset:192\n"
    "v_mfma_f32_16x16x32_bf16 v[212:215], v[20:23], v[100:103], v[212:215]\n"
    "s_waitcnt lgkmcnt(5)\n"
    "v_mfma_f32_16x16x32_bf16 v[216:219], v[24:27], v[96:99], v[216:219]\n"
    "ds_read_b128 v[20:23], v3 offset:4800\n"
    "v_mfma_f32_16x16x32_bf16 v[220:223], v[24:27], v[100:103], v[220:223]\n"
    "v_mfma_f32_16x16x32_bf16 v[224:227], v[28:31], v[96:99], v[224:227]\n"
    "ds_read_b128 v[24:27], v3 offset:9408\n"
    "v_mfma_f32_16x16x32_bf16 v[228:231], v[28:31], v[100:103], v[228:231]\n"
    "s_waitcnt lgkmcnt(5)\n"
    "v_mfma_f32_16x16x32_bf16 v[200:203], v[40:43], v[112:115], v[200:203]\n"
    "buffer_load_dwordx4 v183, s[36:39], s52 offen lds\n"
    "s_add_u32 m0, m0, 1024\n"
    "ds_read_b128 v[28:31], v3 offset:14016\n"
    "v_mfma_f32_16x16x32_bf16 v[204:207], v[40:43], v[116:119], v[204:207]\n"
    "v_mfma_f32_16x16x32_bf16 v[208:211], v[44:47], v[112:115], v[208:211]\n"
    "ds_read_b128 v[40:43], v3 offset:224\n"
    "v_mfma_f32_16x16x32_bf16 v[212:215], v[44:47], v[116:119], v[212:215]\n"
    "s_waitcnt lgkmcnt(5)\n"
    "v_mfma_f32_16x16x32_bf16 v[216:219], v[56:59], v[112:115], v[216:219]\n"
    "ds_read_b128 v[44:47], v3 offset:4832\n"
    "v_mfma_f32_16x16x32_bf16 v[220:223], v[56:59], v[116:119], v[220:223]\n"
    "v_mfma_f32_16x16x32_bf16 v[224:227], v[60:63], v[112:115], v[224:227]\n"
    "buffer_load_dwordx4 v184, s[36:39], s52 offen lds\n"
    "s_add_u32 m0, m0, 1024\n"
    "ds_read_b128 v[56:59], v3 offset:9440\n"
    "v_mfma_f32_16x16x32_bf16 v[228:231], v[60:63], v[116:119], v[228:231]\n"
    "s_waitcnt lgkmcnt(5)\n"
    "v_mfma_f32_16x16x32_bf16 v[200:203], v[16:19], v[128:131], v[200:203]\n"
    "ds_read_b128 v[60:63], v3 offset:14048\n"
    "v_mfma_f32_16x16x32_bf16 v[204:207], v[16:19], v[132:135], v[204:207]\n"
    "v_mfma_f32_16x16x32_bf16 v[208:211], v[20:23], v[128:131], v[208:211]\n"
    "s_waitcnt vmcnt(19)\n"
    "buffer_store_dwordx2 v[192:193], v190, s[72:75], s57 offen sc1\n"
    "s_add_u32 s57, s57, 512\n"
    "ds_read_b128 v[16:19], v3 offset:256\n"
    "v_mfma_f32_16x16x32_bf16 v[212:215], v[20:23], v[132:135], v[212:215]\n"
    "s_waitcnt lgkmcnt(5)\n"
    "v_mfma_f32_16x16x32_bf16 v[216:219], v[24:27], v[128:131], v[216:219]\n"
    "buffer_load_dwordx4 v185, s[36:39], s52 offen lds\n"
    "s_add_u32 m0, m0, 1024\n"
    "ds_read_b128 v[20:23], v3 offset:4864\n"
    "v_mfma_f32_16x16x32_bf16 v[220:223], v[24:27], v[132:135], v[220:223]\n"
    "v_mfma_f32_16x16x32_bf16 v[224:227], v[28:31], v[128:131], v[224:227]\n"
    "ds_read_b128 v[24:27], v3 offset:9472\n"
    "v_mfma_f32_16x16x32_bf16 v[228:231], v[28:31], v[132:135], v[228:231]\n"
    "s_waitcnt lgkmcnt(5)\n"
    "v_mfma_f32_16x16x32_bf16 v[200:203], v[40:43], v[144:147], v[200:203]\n"
    "ds_read_b128 v[28:31], v3 offset:14080\n"
    "v_mfma_f32_16x16x32_bf16 v[204:207], v[40:43], v[148:151], v[204:207]\n"
    "v_mfma_f32_16x16x32_bf16 v[208:211], v[44:47], v[144:147], v[208:211]\n"
    "buffer_load_dwordx4 v186, s[36:39], s52 offen lds\n"
    "s_add_u32 m0, m0, 1024\n"
    "v_mfma_f32_16x16x32_bf16 v[212:215], v[44:47], v[148:151], v[212:215]\n"
    "s_waitcnt lgkmcnt(4)\n"
    "v_mfma_f32_16x16x32_bf16 v[216:219], v[56:59], v[144:147], v[216:219]\n"
    "v_mfma_f32_16x16x32_bf16 v[220:223], v[56:59], v[148:151], v[220:223]\n"
    "v_mfma_f32_16x16x32_bf16 v[224:227], v[60:63], v[144:147], v[224:227]\n"
    "v_mfma_f32_16x16x32_bf16 v[228:231], v[60:63], v[148:151], v[228:231]\n"
    "s_waitcnt lgkmcnt(2)\n"
    "s_waitcnt lgkmcnt(0)\n"
    "s_waitcnt vmcnt(12)\n"
    "s_barrier\n"
    "v_mfma_f32_16x16x32_bf16 v[200:203], v[16:19], v[160:163], v[200:203]\n"
    "buffer_load_dwordx4 v187, s[36:39], s52 offen lds\n"
    "s_add_u32 m0, m0, 1024\n"
    "ds_read_b128 v[40:43], v14 offset:0\n"
    "v_mfma_f32_16x16x32_bf16 v[204:207], v[16:19], v[164:167], v[204:207]\n"
    "ds_read_b128 v[44:47], v14 offset:4608\n"
    "v_mfma_f32_16x16x32_bf16 v[208:211], v[20:23], v[160:163], v[208:211]\n"
    "ds_read_b128 v[56:59], v14 offset:9216\n"
    "v_mfma_f32_16x16x32_bf16 v[212:215], v[20:23], v[164:167], v[212:215]\n"
    "ds_read_b128 v[60:63], v14 offset:13824\n"
    "v_mfma_f32_16x16x32_bf16 v[216:219], v[24:27], v[160:163], v[216:219]\n"
    "ds_read_b128 v[16:19], v14 offset:32\n"
    "v_mfma_f32_16x16x32_bf16 v[220:223], v[24:27], v[164:167], v[220:223]\n"
    "ds_read_b128 v[20:23], v14 offset:4640\n"
    "v_mfma_f32_16x16x32_bf16 v[224:227], v[28:31], v[160:163], v[224:227]\n"
    "buffer_load_dwordx4 v188, s[36:39], s52 offen lds\n"
    "ds_read_b128 v[24:27], v14 offset:9248\n"
    "v_mfma_f32_16x16x32_bf16 v[228:231], v[28:31], v[164:167], v[228:231]\n"
    "s_waitcnt lgkmcnt(5)\n"
    "v_cvt_pk_bf16_f32 v232, v200, v201\n"
    "v_cvt_pk_bf16_f32 v233, v202, v203\n"
    "v_mfma_f32_16x16x32_bf16 v[200:203], v[40:43], v[32:35], 0\n"
    "ds_read_b128 v[28:31], v14 offset:13856\n"
    "v_add_u32 v3, 0, v1\n"
    "v_cvt_pk_bf16_f32 v236, v204, v205\n"
    "v_cvt_pk_bf16_f32 v237, v206, v207\n"
    "v_mfma_f32_16x16x32_bf16 v[204:207], v[40:43], v[36:39], 0\n"
    "v_cvt_pk_bf16_f32 v234, v208, v209\n"
    "v_cvt_pk_bf16_f32 v235, v210, v211\n"
    "v_mfma_f32_16x16x32_bf16 v[208:211], v[44:47], v[32:35], 0\n"
    "ds_read_b128 v[40:43], v14 offset:64\n"
    "v_cvt_pk_bf16_f32 v238, v212, v213\n"
    "v_cvt_pk_bf16_f32 v239, v214, v215\n"
    "v_mfma_f32_16x16x32_bf16 v[212:215], v[44:47], v[36:39], 0\n"
    "s_waitcnt lgkmcnt(5)\n"
    "v_cvt_pk_bf16_f32 v240, v216, v217\n"
    "v_cvt_pk_bf16_f32 v241, v218, v219\n"
    "v_mfma_f32_16x16x32_bf16 v[216:219], v[56:59], v[32:35], 0\n"
    "ds_read_b128 v[44:47], v14 offset:4672\n"
    "v_cvt_pk_bf16_f32 v244, v220, v221\n"
    "v_cvt_pk_bf16_f32 v245, v222, v223\n"
    "v_mfma_f32_16x16x32_bf16 v[220:223], v[56:59], v[36:39], 0\n"
    "v_cvt_pk_bf16_f32 v242, v224, v225\n"
    "v_cvt_pk_bf16_f32 v243, v226, v227\n"
    "v_mfma_f32_16x16x32_bf16 v[224:227], v[60:63], v[32:35], 0\n"
    "ds_read_b128 v[56:59], v14 offset:9280\n"
    "v_cvt_pk_bf16_f32 v246, v228, v229\n"
    "v_cvt_pk_bf16_f32 v247, v230, v231\n"
    "v_mfma_f32_16x16x32_bf16 v[228:231], v[60:63], v[36:39], 0\n"
    "s_waitcnt lgkmcnt(5)\n"
    "v_mfma_f32_16x16x32_bf16 v[200:203], v[16:19], v[48:51], v[200:203]\n"
    "ds_read_b128 v[60:63], v14 offset:13888\n"
    "v_mfma_f32_16x16x32_bf16 v[204:207], v[16:19], v[52:55], v[204:207]\n"
    "s_add_u32 s59, s60, 65536\n"
    "buffer_store_dwordx4 v[232:235], v13, s[28:31], s60 offen offset:0 sc1\n"
    "v_mfma_f32_16x16x32_bf16 v[208:211], v[20:23], v[48:51], v[208:211]\n"
    "ds_read_b128 v[16:19], v14 offset:96\n"
    "v_mfma_f32_16x16x32_bf16 v[212:215], v[20:23], v[52:55], v[212:215]\n"
    "s_waitcnt lgkmcnt(5)\n"
    "v_mfma_f32_16x16x32_bf16 v[216:219], v[24:27], v[48:51], v[216:219]\n"
    "buffer_store_dwordx4 v[236:239], v13, s[28:31], s60 offen offset:1024 sc1\n"
    "ds_read_b128 v[20:23], v14 offset:4704\n"
    "v_mfma_f32_16x16x32_bf16 v[220:223], v[24:27], v[52:55], v[220:223]\n"
    "v_mfma_f32_16x16x32_bf16 v[224:227], v[28:31], v[48:51], v[224:227]\n"
    "ds_read_b128 v[24:27], v14 offset:9312\n"
    "v_mfma_f32_16x16x32_bf16 v[228:231], v[28:31], v[52:55], v[228:231]\n"
    "s_waitcnt lgkmcnt(5)\n"
    "v_mfma_f32_16x16x32_bf16 v[200:203], v[40:43], v[64:67], v[200:203]\n"
    "buffer_store_dwordx4 v[240:243], v13, s[28:31], s59 offen offset:0 sc1\n"
    "ds_read_b128 v[28:31], v14 offset:13920\n"
    "v_mfma_f32_16x16x32_bf16 v[204:207], v[40:43], v[68:71], v[204:207]\n"
    "v_mfma_f32_16x16x32_bf16 v[208:211], v[44:47], v[64:67], v[208:211]\n"
    "ds_read_b128 v[40:43], v14 offset:128\n"
    "v_mfma_f32_16x16x32_bf16 v[212:215], v[44:47], v[68:71], v[212:215]\n"
    "s_waitcnt lgkmcnt(5)\n"
    "v_mfma_f32_16x16x32_bf16 v[216:219], v[56:59], v[64:67], v[216:219]\n"
    "buffer_store_dwordx4 v[244:247], v13, s[28:31], s59 offen offset:1024 sc1\n"
    "s_add_u32 s60, s60, 262144\n"
    "ds_read_b128 v[44:47], v14 offset:4736\n"
    "v_mfma_f32_16x16x32_bf16 v[220:223], v[56:59], v[68:71], v[220:223]\n"
    "v_mfma_f32_16x16x32_bf16 v[224:227], v[60:63], v[64:67], v[224:227]\n"
    "s_add_u32 s40, s15, 7\n"
    "s_cmp_ge_u32 s40, 9\n"
    "s_cselect_b32 s41, 9, 0\n"
    "s_sub_u32 s40, s40, s41\n"
    "s_lshl_b32 s40, s40, 6\n"
    "s_lshl_b32 s41, s13, 5\n"
    "s_add_u32 s40, s40, s41\n"
    "s_mul_i32 s40, s40, 1152\n"
    "s_mul_i32 s41, s11, 288\n"
    "s_add_u32 s52, s40, s41\n"
    "s_mul_i32 s53, s9, 9216\n"
    "s_add_u32 m0, s53, 36864\n"
    "s_nop 0\n"
    "buffer_load_dwordx4 v180, s[36:39], s52 offen lds\n"
    "s_add_u32 m0, m0, 1024\n"
    "ds_read_b128 v[56:59], v14 offset:9344\n"
    "v_mfma_f32_16x16x32_bf16 v[228:231], v[60:63], v[68:71], v[228:231]\n"
    "s_waitcnt lgkmcnt(5)\n"
    "v_mfma_f32_16x16x32_bf16 v[200:203], v[16:19], v[80:83], v[200:203]\n"
    "ds_read_b128 v[60:63], v14 offset:13952\n"
    "v_mfma_f32_16x16x32_bf16 v[204:207], v[16:19], v[84:87], v[204:207]\n"
    "v_mfma_f32_16x16x32_bf16 v[208:211], v[20:23], v[80:83], v[208:211]\n"
    "ds_read_b128 v[16:19], v14 offset:160\n"
    "v_mfma_f32_16x16x32_bf16 v[212:215], v[20:23], v[84:87], v[212:215]\n"
    "s_waitcnt lgkmcnt(5)\n"
    "v_mfma_f32_16x16x32_bf16 v[216:219], v[24:27], v[80:83], v[216:219]\n"
    "buffer_load_dwordx4 v181, s[36:39], s52 offen lds\n"
    "s_add_u32 m0, m0, 1024\n"
    "ds_read_b128 v[20:23], v14 offset:4768\n"
    "v_mfma_f32_16x16x32_bf16 v[220:223], v[24:27], v[84:87], v[220:223]\n"
    "v_mfma_f32_16x16x32_bf16 v[224:227], v[28:31], v[80:83], v[224:227]\n"
    "ds_read_b128 v[24:27], v14 offset:9376\n"
    "v_mfma_f32_16x16x32_bf16 v[228:231], v[28:31], v[84:87], v[228:231]\n"
    "s_waitcnt lgkmcnt(5)\n"
    "v_mfma_f32_16x16x32_bf16 v[200:203], v[40:43], v[96:99], v[200:203]\n"
    "ds_read_b128 v[28:31], v14 offset:13984\n"
    "v_mfma_f32_16x16x32_bf16 v[204:207], v[40:43], v[100:103], v[204:207]\n"
    "v_mfma_f32_16x16x32_bf16 v[208:211], v[44:47], v[96:99], v[208:211]\n"
    "buffer_load_dwordx4 v182, s[36:39], s52 offen lds\n"
    "s_add_u32 m0, m0, 1024\n"
    "ds_read_b128 v[40:43], v14 offset:192\n"
    "v_mfma_f32_16x16x32_bf16 v[212:215], v[44:47], v[100:103], v[212:215]\n"
    "s_waitcnt lgkmcnt(5)\n"
    "v_mfma_f32_16x16x32_bf16 v[216:219], v[56:59], v[96:99], v[216:219]\n"
    "ds_read_b128 v[44:47], v14 offset:4800\n"
    "v_mfma_f32_16x16x32_bf16 v[220:223], v[56:59], v[100:103], v[220:223]\n"
    "v_mfma_f32_16x16x32_bf16 v[224:227], v[60:63], v[96:99], v[224:227]\n"
    "ds_read_b128 v[56:59], v14 offset:9408\n"
    "v_mfma_f32_16x16x32_bf16 v[228:231], v[60:63], v[100:103], v[228:231]\n"
    "s_waitcnt lgkmcnt(5)\n"
    "v_mfma_f32_16x16x32_bf16 v[200:203], v[16:19], v[112:115], v[200:203]\n"
    "buffer_load_dwordx4 v183, s[36:39], s52 offen lds\n"
    "s_add_u32 m0, m0, 1024\n"
    "ds_read_b128 v[60:63], v14 offset:14016\n"
    "v_mfma_f32_16x16x32_bf16 v[204:207], v[16:19], v[116:119], v[204:207]\n"
    "v_mfma_f32_16x16x32_bf16 v[208:211], v[20:23], v[112:115], v[208:211]\n"
    "ds_read_b128 v[16:19], v14 offset:224\n"
    "v_mfma_f32_16x16x32_bf16 v[212:215], v[20:23], v[116:119], v[212:215]\n"
    "s_waitcnt lgkmcnt(5)\n"
    "v_mfma_f32_16x16x32_bf16 v[216:219], v[24:27], v[112:115], v[216:219]\n"
    "ds_read_b128 v[20:23], v14 offset:4832\n"
    "v_mfma_f32_16x16x32_bf16 v[220:223], v[24:27], v[116:119], v[220:223]\n"
    "v_mfma_f32_16x16x32_bf16 v[224:227], v[28:31], v[112:115], v[224:227]\n"
    "buffer_load_dwordx4 v184, s[36:39], s52 offen lds\n"
    "s_add_u32 m0, m0, 1024\n"
    "ds_read_b128 v[24:27], v14 offset:9440\n"
    "v_mfma_f32_16x16x32_bf16 v[228:231], v[28:31], v[116:119], v[228:231]\n"
    "s_waitcnt lgkmcnt(5)\n"
    "v_mfma_f32_16x16x32_bf16 v[200:203], v[40:43], v[128:131], v[200:203]\n"
    "ds_read_b128 v[28:31], v14 offset:14048\n"
    "v_mfma_f32_16x16x32_bf16 v[204:207], v[40:43], v[132:135], v[204:207]\n"
    "v_mfma_f32_16x16x32_bf16 v[208:211], v[44:47], v[128:131], v[208:211]\n"
    "s_waitcnt vmcnt(19)\n"
    "buffer_store_dwordx2 v[192:193], v190, s[72:75], s57 offen sc1\n"
    "s_add_u32 s57, s57, 512\n"
    "ds_read_b128 v[40:43], v14 offset:256\n"
    "v_mfma_f32_16x16x32_bf16 v[212:215], v[44:47], v[132:135], v[212:215]\n"
    "s_waitcnt lgkmcnt(5)\n"
    "v_mfma_f32_16x16x32_bf16 v[216:219], v[56:59], v[128:131], v[216:219]\n"
    "buffer_load_dwordx4 v185, s[36:39], s52 offen lds\n"
    "s_add_u32 m0, m0, 1024\n"
    "ds_read_b128 v[44:47], v14 offset:4864\n"
    "v_mfma_f32_16x16x32_bf16 v[220:223], v[56:59], v[132:135], v[220:223]\n"
    "v_mfma_f32_16x16x32_bf16 v[224:227], v[60:63], v[128:131], v[224:227]\n"
    "ds_read_b128 v[56:59], v14 offset:9472\n"
    "v_mfma_f32_16x16x32_bf16 v[228:231], v[60:63], v[132:135], v[228:231]\n"
    "s_waitcnt lgkmcnt(5)\n"
    "v_mfma_f32_16x16x32_bf16 v[200:203], v[16:19], v[144:147], v[200:203]\n"
    "ds_read_b128 v[60:63], v14 offset:14080\n"
    "v_mfma_f32_16x16x32_bf16 v[204:207], v[16:19], v[148:151], v[204:207]\n"
    "v_mfma_f32_16x16x32_bf16 v[208:211], v[20:23], v[144:147], v[208:211]\n"
    "buffer_load_dwordx4 v186, s[36:39], s52 offen lds\n"
    "s_add_u32 m0, m0, 1024\n"
    "v_mfma_f32_16x16x32_bf16 v[212:215], v[20:23], v[148:151], v[212:215]\n"
    "s_waitcnt lgkmcnt(4)\n"
    "v_mfma_f32_16x16x32_bf16 v[216:219], v[24:27], v[144:147], v[216:219]\n"
    "v_mfma_f32_16x16x32_bf16 v[220:223], v[24:27], v[148:151], v[220:223]\n"
    "v_mfma_f32_16x16x32_bf16 v[224:227], v[28:31], v[144:147], v[224:227]\n"
    "v_mfma_f32_16x16x32_bf16 v[228:231], v[28:31], v[148:151], v[228:231]\n"
    "s_waitcnt lgkmcnt(2)\n"
    "s_waitcnt lgkmcnt(0)\n"
    "s_waitcnt vmcnt(12)\n"
    "s_barrier\n"
    "v_mfma_f32_16x16x32_bf16 v[200:203], v[40:43], v[160:163], v[200:203]\n"
    "buffer_load_dwordx4 v187, s[36:39], s52 offen lds\n"
    "s_add_u32 m0, m0, 1024\n"
    "ds_read_b128 v[16:19], v3 offset:0\n"
    "v_mfma_f32_16x16x32_bf16 v[204:207], v[40:43], v[164:167], v[204:207]\n"
    "ds_read_b128 v[20:23], v3 offset:4608\n"
    "v_mfma_f32_16x16x32_bf16 v[208:211], v[44:47], v[160:163], v[208:211]\n"
    "ds_read_b128 v[24:27], v3 offset:9216\n"
    "v_mfma_f32_16x16x32_bf16 v[212:215], v[44:47], v[164:167], v[212:215]\n"
    "ds_read_b128 v[28:31], v3 offset:13824\n"
    "v_mfma_f32_16x16x32_bf16 v[216:219], v[56:59], v[160:163], v[216:219]\n"
    "ds_read_b128 v[40:43], v3 offset:32\n"
    "v_mfma_f32_16x16x32_bf16 v[220:223], v[56:59], v[164:167], v[220:223]\n"
    "ds_read_b128 v[44:47], v3 offset:4640\n"
    "v_mfma_f32_16x16x32_bf16 v[224:227], v[60:63], v[160:163], v[224:227]\n"
    "buffer_load_dwordx4 v188, s[36:39], s52 offen lds\n"
    "ds_read_b128 v[56:59], v3 offset:9248\n"
    "v_mfma_f32_16x16x32_bf16 v[228:231], v[60:63], v[164:167], v[228:231]\n"
    "s_waitcnt lgkmcnt(5)\n"
    "v_cvt_pk_bf16_f32 v232, v200, v201\n"
    "v_cvt_pk_bf16_f32 v233, v202, v203\n"
    "v_mfma_f32_16x16x32_bf16 v[200:203], v[16:19], v[32:35], 0\n"
    "ds_read_b128 v[60:63], v3 offset:13856\n"
    "v_add_u32 v14, 36864, v1\n"
    "v_cvt_pk_bf16_f32 v236, v204, v205\n"
    "v_cvt_pk_bf16_f32 v237, v206, v207\n"
    "v_mfma_f32_16x16x32_bf16 v[204:207], v[16:19], v[36:39], 0\n"
    "v_cvt_pk_bf16_f32 v234, v208, v209\n"
    "v_cvt_pk_bf16_f32 v235, v210, v211\n"
    "v_mfma_f32_16x16x32_bf16 v[208:211], v[20:23], v[32:35], 0\n"
    "ds_read_b128 v[16:19], v3 offset:64\n"
    "v_cvt_pk_bf16_f32 v238, v212, v213\n"
    "v_cvt_pk_bf16_f32 v239, v214, v215\n"
    "v_mfma_f32_16x16x32_bf16 v[212:215], v[20:23], v[36:39], 0\n"
    "s_waitcnt lgkmcnt(5)\n"
    "v_cvt_pk_bf16_f32 v240, v216, v217\n"
    "v_cvt_pk_bf16_f32 v241, v218, v219\n"
    "v_mfma_f32_16x16x32_bf16 v[216:219], v[24:27], v[32:35], 0\n"
    "ds_read_b128 v[20:23], v3 offset:4672\n"
    "v_cvt_pk_bf16_f32 v244, v220, v221\n"
    "v_cvt_pk_bf16_f32 v245, v222, v223\n"
    "v_mfma_f32_16x16x32_bf16 v[220:223], v[24:27], v[36:39], 0\n"
    "v_cvt_pk_bf16_f32 v242, v224, v225\n"
    "v_cvt_pk_bf16_f32 v243, v226, v227\n"
    "v_mfma_f32_16x16x32_bf16 v[224:227], v[28:31], v[32:35], 0\n"
    "ds_read_b128 v[24:27], v3 offset:9280\n"
    "v_cvt_pk_bf16_f32 v246, v228, v229\n"
    "v_cvt_pk_bf16_f32 v247, v230, v231\n"
    "v_mfma_f32_16x16x32_bf16 v[228:231], v[28:31], v[36:39], 0\n"
    "s_waitcnt lgkmcnt(5)\n"
    "v_mfma_f32_16x16x32_bf16 v[200:203], v[40:43], v[48:51], v[200:203]\n"
    "ds_read_b128 v[28:31], v3 offset:13888\n"
    "v_mfma_f32_16x16x32_bf16 v[204:207], v[40:43], v[52:55], v[204:207]\n"
    "s_add_u32 s59, s60, 65536\n"
    "buffer_store_dwordx4 v[232:235], v13, s[28:31], s60 offen offset:0 sc1\n"
    "v_mfma_f32_16x16x32_bf16 v[208:211], v[44:47], v[48:51], v[208:211]\n"
    "ds_read_b128 v[40:43], v3 offset:96\n"
    "v_mfma_f32_16x16x32_bf16 v[212:215], v[44:47], v[52:55], v[212:215]\n"
    "s_waitcnt lgkmcnt(5)\n"
    "v_mfma_f32_16x16x32_bf16 v[216:219], v[56:59], v[48:51], v[216:219]\n"
    "buffer_store_dwordx4 v[236:239], v13, s[28:31], s60 offen offset:1024 sc1\n"
    "ds_read_b128 v[44:47], v3 offset:4704\n"
    "v_mfma_f32_16x16x32_bf16 v[220:223], v[56:59], v[52:55], v[220:223]\n"
    "v_mfma_f32_16x16x32_bf16 v[224:227], v[60:63], v[48:51], v[224:227]\n"
    "ds_read_b128 v[56:59], v3 offset:9312\n"
    "v_mfma_f32_16x16x32_bf16 v[228:231], v[60:63], v[52:55], v[228:231]\n"
    "s_waitcnt lgkmcnt(5)\n"
    "v_mfma_f32_16x16x32_bf16 v[200:203], v[16:19], v[64:67], v[200:203]\n"
    "buffer_store_dwordx4 v[240:243], v13, s[28:31], s59 offen offset:0 sc1\n"
    "ds_read_b128 v[60:63], v3 offset:13920\n"
    "v_mfma_f32_16x16x32_bf16 v[204:207], v[16:19], v[68:71], v[204:207]\n"
    "v_mfma_f32_16x16x32_bf16 v[208:211], v[20:23], v[64:67], v[208:211]\n"
    "ds_read_b128 v[16:19], v3 offset:128\n"
    "v_mfma_f32_16x16x32_bf16 v[212:215], v[20:23], v[68:71], v[212:215]\n"
    "s_waitcnt lgkmcnt(5)\n"
    "v_mfma_f32_16x16x32_bf16 v[216:219], v[24:27], v[64:67], v[216:219]\n"
    "buffer_store_dwordx4 v[244:247], v13, s[28:31], s59 offen offset:1024 sc1\n"
    "s_add_u32 s60, s60, 262144\n"
    "ds_read_b128 v[20:23], v3 offset:4736\n"
    "v_mfma_f32_16x16x32_bf16 v[220:223], v[24:27], v[68:71], v[220:223]\n"
    "v_mfma_f32_16x16x32_bf16 v[224:227], v[28:31], v[64:67], v[224:227]\n"
    "s_add_u32 s40, s15, 8\n"
    "s_cmp_ge_u32 s40, 9\n"
    "s_cselect_b32 s41, 9, 0\n"
    "s_sub_u32 s40, s40, s41\n"
    "s_lshl_b32 s40, s40, 6\n"
    "s_lshl_b32 s41, s13, 5\n"
    "s_add_u32 s40, s40, s41\n"
    "s_mul_i32 s40, s40, 1152\n"
    "s_mul_i32 s41, s11, 288\n"
    "s_add_u32 s52, s40, s41\n"
    "s_mul_i32 s53, s9, 9216\n"
    "s_add_u32 m0, s53, 73728\n"
    "s_nop 0\n"
    "buffer_load_dwordx4 v180, s[36:39], s52 offen lds\n"
    "s_add_u32 m0, m0, 1024\n"
    "ds_read_b128 v[24:27], v3 offset:9344\n"
    "v_mfma_f32_16x16x32_bf16 v[228:231], v[28:31], v[68:71], v[228:231]\n"
    "s_waitcnt lgkmcnt(5)\n"
    "v_mfma_f32_16x16x32_bf16 v[200:203], v[40:43], v[80:83], v[200:203]\n"
    "ds_read_b128 v[28:31], v3 offset:13952\n"
    "v_mfma_f32_16x16x32_bf16 v[204:207], v[40:43], v[84:87], v[204:207]\n"
    "v_mfma_f32_16x16x32_bf16 v[208:211], v[44:47], v[80:83], v[208:211]\n"
    "ds_read_b128 v[40:43], v3 offset:160\n"
    "v_mfma_f32_16x16x32_bf16 v[212:215], v[44:47], v[84:87], v[212:215]\n"
    "s_waitcnt lgkmcnt(5)\n"
    "v_mfma_f32_16x16x32_bf16 v[216:219], v[56:59], v[80:83], v[216:219]\n"
    "buffer_load_dwordx4 v181, s[36:39], s52 offen lds\n"
    "s_add_u32 m0, m0, 1024\n"
    "ds_read_b128 v[44:47], v3 offset:4768\n"
    "v_mfma_f32_16x16x32_bf16 v[220:223], v[56:59], v[84:87], v[220:223]\n"
    "v_mfma_f32_16x16x32_bf16 v[224:227], v[60:63], v[80:83], v[224:227]\n"
    "ds_read_b128 v[56:59], v3 offset:9376\n"
    "v_mfma_f32_16x16x32_bf16 v[228:231], v[60:63], v[84:87], v[228:231]\n"
    "s_waitcnt lgkmcnt(5)\n"
    "v_mfma_f32_16x16x32_bf16 v[200:203], v[16:19], v[96:99], v[200:203]\n"
    "ds_read_b128 v[60:63], v3 offset:13984\n"
    "v_mfma_f32_16x16x32_bf16 v[204:207], v[16:19], v[100:103], v[204:207]\n"
    "v_mfma_f32_16x16x32_bf16 v[208:211], v[20:23], v[96:99], v[208:211]\n"
    "buffer_load_dwordx4 v182, s[36:39], s52 offen lds\n"
    "s_add_u32 m0, m0, 1024\n"
    "ds_read_b128 v[16:19], v3 offset:192\n"
    "v_mfma_f32_16x16x32_bf16 v[212:215], v[20:23], v[100:103], v[212:215]\n"
    "s_waitcnt lgkmcnt(5)\n"
    "v_mfma_f32_16x16x32_bf16 v[216:219], v[24:27], v[96:99], v[216:219]\n"
    "ds_read_b128 v[20:23], v3 offset:4800\n"
    "v_mfma_f32_16x16x32_bf16 v[220:223], v[24:27], v[100:103], v[220:223]\n"
    "v_mfma_f32_16x16x32_bf16 v[224:227], v[28:31], v[96:99], v[224:227]\n"
    "ds_read_b128 v[24:27], v3 offset:9408\n"
    "v_mfma_f32_16x16x32_bf16 v[228:231], v[28:31], v[100:103], v[228:231]\n"
    "s_waitcnt lgkmcnt(5)\n"
    "v_mfma_f32_16x16x32_bf16 v[200:203], v[40:43], v[112:115], v[200:203]\n"
    "buffer_load_dwordx4 v183, s[36:39], s52 offen lds\n"
    "s_add_u32 m0, m0, 1024\n"
    "ds_read_b128 v[28:31], v3 offset:14016\n"
    "v_mfma_f32_16x16x32_bf16 v[204:207], v[40:43], v[116:119], v[204:207]\n"
    "v_mfma_f32_16x16x32_bf16 v[208:211], v[44:47], v[112:115], v[208:211]\n"
    "ds_read_b128 v[40:43], v3 offset:224\n"
    "v_mfma_f32_16x16x32_bf16 v[212:215], v[44:47], v[116:119], v[212:215]\n"
    "s_waitcnt lgkmcnt(5)\n"
    "v_mfma_f32_16x16x32_bf16 v[216:219], v[56:59], v[112:115], v[216:219]\n"
    "ds_read_b128 v[44:47], v3 offset:4832\n"
    "v_mfma_f32_16x16x32_bf16 v[220:223], v[56:59], v[116:119], v[220:223]\n"
    "v_mfma_f32_16x16x32_bf16 v[224:227], v[60:63], v[112:115], v[224:227]\n"
    "buffer_load_dwordx4 v184, s[36:39], s52 offen lds\n"
    "s_add_u32 m0, m0, 1024\n"
    "ds_read_b128 v[56:59], v3 offset:9440\n"
    "v_mfma_f32_16x16x32_bf16 v[228:231], v[60:63], v[116:119], v[228:231]\n"
    "s_waitcnt lgkmcnt(5)\n"
    "v_mfma_f32_16x16x32_bf16 v[200:203], v[16:19], v[128:131], v[200:203]\n"
    "ds_read_b128 v[60:63], v3 offset:14048\n"
    "v_mfma_f32_16x16x32_bf16 v[204:207], v[16:19], v[132:135], v[204:207]\n"
    "v_mfma_f32_16x16x32_bf16 v[208:211], v[20:23], v[128:131], v[208:211]\n"
    "s_waitcnt vmcnt(19)\n"
    "buffer_store_dwordx2 v[192:193], v190, s[72:75], s57 offen sc1\n"
    "s_add_u32 s57, s57, 512\n"
    "ds_read_b128 v[16:19], v3 offset:256\n"
    "v_mfma_f32_16x16x32_bf16 v[212:215], v[20:23], v[132:135], v[212:215]\n"
    "s_waitcnt lgkmcnt(5)\n"
    "v_mfma_f32_16x16x32_bf16 v[216:219], v[24:27], v[128:131], v[216:219]\n"
    "buffer_load_dwordx4 v185, s[36:39], s52 offen lds\n"
    "s_add_u32 m0, m0, 1024\n"
    "ds_read_b128 v[20:23], v3 offset:4864\n"
    "v_mfma_f32_16x16x32_bf16 v[220:223], v[24:27], v[132:135], v[220:223]\n"
    "v_mfma_f32_16x16x32_bf16 v[224:227], v[28:31], v[128:131], v[224:227]\n"
    "ds_read_b128 v[24:27], v3 offset:9472\n"
    "v_mfma_f32_16x16x32_bf16 v[228:231], v[28:31], v[132:135], v[228:231]\n"
    "s_waitcnt lgkmcnt(5)\n"
    "v_mfma_f32_16x16x32_bf16 v[200:203], v[40:43], v[144:147], v[200:203]\n"
    "ds_read_b128 v[28:31], v3 offset:14080\n"
    "v_mfma_f32_16x16x32_bf16 v[204:207], v[40:43], v[148:151], v[204:207]\n"
    "v_mfma_f32_16x16x32_bf16 v[208:211], v[44:47], v[144:147], v[208:211]\n"
    "buffer_load_dwordx4 v186, s[36:39], s52 offen lds\n"
    "s_add_u32 m0, m0, 1024\n"
    "v_mfma_f32_16x16x32_bf16 v[212:215], v[44:47], v[148:151], v[212:215]\n"
    "s_waitcnt lgkmcnt(4)\n"
    "v_mfma_f32_16x16x32_bf16 v[216:219], v[56:59], v[144:147], v[216:219]\n"
    "v_mfma_f32_16x16x32_bf16 v[220:223], v[56:59], v[148:151], v[220:223]\n"
    "v_mfma_f32_16x16x32_bf16 v[224:227], v[60:63], v[144:147], v[224:227]\n"
    "v_mfma_f32_16x16x32_bf16 v[228:231], v[60:63], v[148:151], v[228:231]\n"
    "s_waitcnt lgkmcnt(2)\n"
    "s_waitcnt lgkmcnt(0)\n"
    "s_waitcnt vmcnt(12)\n"
    "s_barrier\n"
    "v_mfma_f32_16x16x32_bf16 v[200:203], v[16:19], v[160:163], v[200:203]\n"
    "buffer_load_dwordx4 v187, s[36:39], s52 offen lds\n"
    "s_add_u32 m0, m0, 1024\n"
    "ds_read_b128 v[40:43], v14 offset:0\n"
    "v_mfma_f32_16x16x32_bf16 v[204:207], v[16:19], v[164:167], v[204:207]\n"
    "ds_read_b128 v[44:47], v14 offset:4608\n"
    "v_mfma_f32_16x16x32_bf16 v[208:211], v[20:23], v[160:163], v[208:211]\n"
    "ds_read_b128 v[56:59], v14 offset:9216\n"
    "v_mfma_f32_16x16x32_bf16 v[212:215], v[20:23], v[164:167], v[212:215]\n"
    "ds_read_b128 v[60:63], v14 offset:13824\n"
    "v_mfma_f32_16x16x32_bf16 v[216:219], v[24:27], v[160:163], v[216:219]\n"
    "ds_read_b128 v[16:19], v14 offset:32\n"
    "v_mfma_f32_16x16x32_bf16 v[220:223], v[24:27], v[164:167], v[220:223]\n"
    "ds_read_b128 v[20:23], v14 offset:4640\n"
    "v_mfma_f32_16x16x32_bf16 v[224:227], v[28:31], v[160:163], v[224:227]\n"
    "buffer_load_dwordx4 v188, s[36:39], s52 offen lds\n"
    "ds_read_b128 v[24:27], v14 offset:9248\n"
    "v_mfma_f32_16x16x32_bf16 v[228:231], v[28:31], v[164:167], v[228:231]\n"
    "s_waitcnt lgkmcnt(5)\n"
    "v_cvt_pk_bf16_f32 v232, v200, v201\n"
    "v_cvt_pk_bf16_f32 v233, v202, v203\n"
    "v_mfma_f32_16x16x32_bf16 v[200:203], v[40:43], v[32:35], 0\n"
    "ds_read_b128 v[28:31], v14 offset:13856\n"
    "v_add_u32 v3, 73728, v1\n"
    "v_cvt_pk_bf16_f32 v236, v204, v205\n"
    "v_cvt_pk_bf16_f32 v237, v206, v207\n"
    "v_mfma_f32_16x16x32_bf16 v[204:207], v[40:43], v[36:39], 0\n"
    "v_cvt_pk_bf16_f32 v234, v208, v209\n"
    "v_cvt_pk_bf16_f32 v235, v210, v211\n"
    "v_mfma_f32_16x16x32_bf16 v[208:211], v[44:47], v[32:35], 0\n"
    "ds_read_b128 v[40:43], v14 offset:64\n"
    "v_cvt_pk_bf16_f32 v238, v212, v213\n"
    "v_cvt_pk_bf16_f32 v239, v214, v215\n"
    "v_mfma_f32_16x16x32_bf16 v[212:215], v[44:47], v[36:39], 0\n"
    "s_waitcnt lgkmcnt(5)\n"
    "v_cvt_pk_bf16_f32 v240, v216, v217\n"
    "v_cvt_pk_bf16_f32 v241, v218, v219\n"
    "v_mfma_f32_16x16x32_bf16 v[216:219], v[56:59], v[32:35], 0\n"
    "ds_read_b128 v[44:47], v14 offset:4672\n"
    "v_cvt_pk_bf16_f32 v244, v220, v221\n"
    "v_cvt_pk_bf16_f32 v245, v222, v223\n"
    "v_mfma_f32_16x16x32_bf16 v[220:223], v[56:59], v[36:39], 0\n"
    "v_cvt_pk_bf16_f32 v242, v224, v225\n"
    "v_cvt_pk_bf16_f32 v243, v226, v227\n"
    "v_mfma_f32_16x16x32_bf16 v[224:227], v[60:63], v[32:35], 0\n"
    "ds_read_b128 v[56:59], v14 offset:9280\n"
    "v_cvt_pk_bf16_f32 v246, v228, v229\n"
    "v_cvt_pk_bf16_f32 v247, v230, v231\n"
    "v_mfma_f32_16x16x32_bf16 v[228:231], v[60:63], v[36:39], 0\n"
    "s_waitcnt lgkmcnt(5)\n"
    "v_mfma_f32_16x16x32_bf16 v[200:203], v[16:19], v[48:51], v[200:203]\n"
    "ds_read_b128 v[60:63], v14 offset:13888\n"
    "v_mfma_f32_16x16x32_bf16 v[204:207], v[16:19], v[52:55], v[204:207]\n"
    "s_add_u32 s59, s60, 65536\n"
    "buffer_store_dwordx4 v[232:235], v13, s[28:31], s60 offen offset:0 sc1\n"
    "v_mfma_f32_16x16x32_bf16 v[208:211], v[20:23], v[48:51], v[208:211]\n"
    "ds_read_b128 v[16:19], v14 offset:96\n"
    "v_mfma_f32_16x16x32_bf16 v[212:215], v[20:23], v[52:55], v[212:215]\n"
    "s_waitcnt lgkmcnt(5)\n"
    "v_mfma_f32_16x16x32_bf16 v[216:219], v[24:27], v[48:51], v[216:219]\n"
    "buffer_store_dwordx4 v[236:239], v13, s[28:31], s60 offen offset:1024 sc1\n"
    "ds_read_b128 v[20:23], v14 offset:4704\n"
    "v_mfma_f32_16x16x32_bf16 v[220:223], v[24:27], v[52:55], v[220:223]\n"
    "v_mfma_f32_16x16x32_bf16 v[224:227], v[28:31], v[48:51], v[224:227]\n"
    "ds_read_b128 v[24:27], v14 offset:9312\n"
    "v_mfma_f32_16x16x32_bf16 v[228:231], v[28:31], v[52:55], v[228:231]\n"
    "s_waitcnt lgkmcnt(5)\n"
    "v_mfma_f32_16x16x32_bf16 v[200:203], v[40:43], v[64:67], v[200:203]\n"
    "buffer_store_dwordx4 v[240:243], v13, s[28:31], s59 offen offset:0 sc1\n"
    "ds_read_b128 v[28:31], v14 offset:13920\n"
    "v_mfma_f32_16x16x32_bf16 v[204:207], v[40:43], v[68:71], v[204:207]\n"
    "v_mfma_f32_16x16x32_bf16 v[208:211], v[44:47], v[64:67], v[208:211]\n"
    "ds_read_b128 v[40:43], v14 offset:128\n"
    "v_mfma_f32_16x16x32_bf16 v[212:215], v[44:47], v[68:71], v[212:215]\n"
    "s_waitcnt lgkmcnt(5)\n"
    "v_mfma_f32_16x16x32_bf16 v[216:219], v[56:59], v[64:67], v[216:219]\n"
    "buffer_store_dwordx4 v[244:247], v13, s[28:31], s59 offen offset:1024 sc1\n"
    "s_add_u32 s60, s60, 262144\n"
    "ds_read_b128 v[44:47], v14 offset:4736\n"
    "v_mfma_f32_16x16x32_bf16 v[220:223], v[56:59], v[68:71], v[220:223]\n"
    "v_mfma_f32_16x16x32_bf16 v[224:227], v[60:63], v[64:67], v[224:227]\n"
    "ds_read_b128 v[56:59], v14 offset:9344\n"
    "v_mfma_f32_16x16x32_bf16 v[228:231], v[60:63], v[68:71], v[228:231]\n"
    "s_waitcnt lgkmcnt(5)\n"
    "v_mfma_f32_16x16x32_bf16 v[200:203], v[16:19], v[80:83], v[200:203]\n"
    "ds_read_b128 v[60:63], v14 offset:13952\n"
    "v_mfma_f32_16x16x32_bf16 v[204:207], v[16:19], v[84:87], v[204:207]\n"
    "v_mfma_f32_16x16x32_bf16 v[208:211], v[20:23], v[80:83], v[208:211]\n"
    "ds_read_b128 v[16:19], v14 offset:160\n"
    "v_mfma_f32_16x16x32_bf16 v[212:215], v[20:23], v[84:87], v[212:215]\n"
    "s_waitcnt lgkmcnt(5)\n"
    "v_mfma_f32_16x16x32_bf16 v[216:219], v[24:27], v[80:83], v[216:219]\n"
    "ds_read_b128 v[20:23], v14 offset:4768\n"
    "v_mfma_f32_16x16x32_bf16 v[220:223], v[24:27], v[84:87], v[220:223]\n"
    "v_mfma_f32_16x16x32_bf16 v[224:227], v[28:31], v[80:83], v[224:227]\n"
    "ds_read_b128 v[24:27], v14 offset:9376\n"
    "v_mfma_f32_16x16x32_bf16 v[228:231], v[28:31], v[84:87], v[228:231]\n"
    "s_waitcnt lgkmcnt(5)\n"
    "v_mfma_f32_16x16x32_bf16 v[200:203], v[40:43], v[96:99], v[200:203]\n"
    "ds_read_b128 v[28:31], v14 offset:13984\n"
    "v_mfma_f32_16x16x32_bf16 v[204:207], v[40:43], v[100:103], v[204:207]\n"
    "v_mfma_f32_16x16x32_bf16 v[208:211], v[44:47], v[96:99], v[208:211]\n"
    "ds_read_b128 v[40:43], v14 offset:192\n"
    "v_mfma_f32_16x16x32_bf16 v[212:215], v[44:47], v[100:103], v[212:215]\n"
    "s_waitcnt lgkmcnt(5)\n"
    "v_mfma_f32_16x16x32_bf16 v[216:219], v[56:59], v[96:99], v[216:219]\n"
    "ds_read_b128 v[44:47], v14 offset:4800\n"
    "v_mfma_f32_16x16x32_bf16 v[220:223], v[56:59], v[100:103], v[220:223]\n"
    "v_mfma_f32_16x16x32_bf16 v[224:227], v[60:63], v[96:99], v[224:227]\n"
    "ds_read_b128 v[56:59], v14 offset:9408\n"
    "v_mfma_f32_16x16x32_bf16 v[228:231], v[60:63], v[100:103], v[228:231]\n"
    "s_waitcnt lgkmcnt(5)\n"
    "v_mfma_f32_16x16x32_bf16 v[200:203], v[16:19], v[112:115], v[200:203]\n"
    "ds_read_b128 v[60:63], v14 offset:14016\n"
    "v_mfma_f32_16x16x32_bf16 v[204:207], v[16:19], v[116:119], v[204:207]\n"
    "v_mfma_f32_16x16x32_bf16 v[208:211], v[20:23], v[112:115], v[208:211]\n"
    "ds_read_b128 v[16:19], v14 offset:224\n"
    "v_mfma_f32_16x16x32_bf16 v[212:215], v[20:23], v[116:119], v[212:215]\n"
    "s_waitcnt lgkmcnt(5)\n"
    "v_mfma_f32_16x16x32_bf16 v[216:219], v[24:27], v[112:115], v[216:219]\n"
    "ds_read_b128 v[20:23], v14 offset:4832\n"
    "v_mfma_f32_16x16x32_bf16 v[220:223], v[24:27], v[116:119], v[220:223]\n"
    "v_mfma_f32_16x16x32_bf16 v[224:227], v[28:31], v[112:115], v[224:227]\n"
    "ds_read_b128 v[24:27], v14 offset:9440\n"
    "v_mfma_f32_16x16x32_bf16 v[228:231], v[28:31], v[116:119], v[228:231]\n"
    "s_waitcnt lgkmcnt(5)\n"
    "v_mfma_f32_16x16x32_bf16 v[200:203], v[40:43], v[128:131], v[200:203]\n"
    "ds_read_b128 v[28:31], v14 offset:14048\n"
    "v_mfma_f32_16x16x32_bf16 v[204:207], v[40:43], v[132:135], v[204:207]\n"
    "v_mfma_f32_16x16x32_bf16 v[208:211], v[44:47], v[128:131], v[208:211]\n"
    "s_waitcnt vmcnt(14)\n"
    "buffer_store_dwordx2 v[192:193], v190, s[72:75], s57 offen sc1\n"
    "s_add_u32 s57, s57, 512\n"
    "ds_read_b128 v[40:43], v14 offset:256\n"
    "v_mfma_f32_16x16x32_bf16 v[212:215], v[44:47], v[132:135], v[212:215]\n"
    "s_waitcnt lgkmcnt(5)\n"
    "v_mfma_f32_16x16x32_bf16 v[216:219], v[56:59], v[128:131], v[216:219]\n"
    "ds_read_b128 v[44:47], v14 offset:4864\n"
    "v_mfma_f32_16x16x32_bf16 v[220:223], v[56:59], v[132:135], v[220:223]\n"
    "v_mfma_f32_16x16x32_bf16 v[224:227], v[60:63], v[128:131], v[224:227]\n"
    "ds_read_b128 v[56:59], v14 offset:9472\n"
    "v_mfma_f32_16x16x32_bf16 v[228:231], v[60:63], v[132:135], v[228:231]\n"
    "s_waitcnt lgkmcnt(5)\n"
    "v_mfma_f32_16x16x32_bf16 v[200:203], v[16:19], v[144:147], v[200:203]\n"
    "ds_read_b128 v[60:63], v14 offset:14080\n"
    "v_mfma_f32_16x16x32_bf16 v[204:207], v[16:19], v[148:151], v[204:207]\n"
    "v_mfma_f32_16x16x32_bf16 v[208:211], v[20:23], v[144:147], v[208:211]\n"
    "v_mfma_f32_16x16x32_bf16 v[212:215], v[20:23], v[148:151], v[212:215]\n"
    "s_waitcnt lgkmcnt(4)\n"
    "v_mfma_f32_16x16x32_bf16 v[216:219], v[24:27], v[144:147], v[216:219]\n"
    "v_mfma_f32_16x16x32_bf16 v[220:223], v[24:27], v[148:151], v[220:223]\n"
    "v_mfma_f32_16x16x32_bf16 v[224:227], v[28:31], v[144:147], v[224:227]\n"
    "v_mfma_f32_16x16x32_bf16 v[228:231], v[28:31], v[148:151], v[228:231]\n"
    "s_waitcnt lgkmcnt(2)\n"
    "s_waitcnt lgkmcnt(0)\n"
    "s_waitcnt vmcnt(5)\n"
    "s_barrier\n"
    "v_mfma_f32_16x16x32_bf16 v[200:203], v[40:43], v[160:163], v[200:203]\n"
    "ds_read_b128 v[16:19], v3 offset:0\n"
    "v_mfma_f32_16x16x32_bf16 v[204:207], v[40:43], v[164:167], v[204:207]\n"
    "ds_read_b128 v[20:23], v3 offset:4608\n"
    "v_mfma_f32_16x16x32_bf16 v[208:211], v[44:47], v[160:163], v[208:211]\n"
    "ds_read_b128 v[24:27], v3 offset:9216\n"
    "v_mfma_f32_16x16x32_bf16 v[212:215], v[44:47], v[164:167], v[212:215]\n"
    "ds_read_b128 v[28:31], v3 offset:13824\n"
    "v_mfma_f32_16x16x32_bf16 v[216:219], v[56:59], v[160:163], v[216:219]\n"
    "ds_read_b128 v[40:43], v3 offset:32\n"
    "v_mfma_f32_16x16x32_bf16 v[220:223], v[56:59], v[164:167], v[220:223]\n"
    "ds_read_b128 v[44:47], v3 offset:4640\n"
    "v_mfma_f32_16x16x32_bf16 v[224:227], v[60:63], v[160:163], v[224:227]\n"
    "ds_read_b128 v[56:59], v3 offset:9248\n"
    "v_mfma_f32_16x16x32_bf16 v[228:231], v[60:63], v[164:167], v[228:231]\n"
    "s_waitcnt lgkmcnt(5)\n"
    "v_cvt_pk_bf16_f32 v232, v200, v201\n"
    "v_cvt_pk_bf16_f32 v233, v202, v203\n"
    "v_mfma_f32_16x16x32_bf16 v[200:203], v[16:19], v[32:35], 0\n"
    "ds_read_b128 v[60:63], v3 offset:13856\n"
    "v_cvt_pk_bf16_f32 v236, v204, v205\n"
    "v_cvt_pk_bf16_f32 v237, v206, v207\n"
    "v_mfma_f32_16x16x32_bf16 v[204:207], v[16:19], v[36:39], 0\n"
    "v_cvt_pk_bf16_f32 v234, v208, v209\n"
    "v_cvt_pk_bf16_f32 v235, v210, v211\n"
    "v_mfma_f32_16x16x32_bf16 v[208:211], v[20:23], v[32:35], 0\n"
    "ds_read_b128 v[16:19], v3 offset:64\n"
    "v_cvt_pk_bf16_f32 v238, v212, v213\n"
    "v_cvt_pk_bf16_f32 v239, v214, v215\n"
    "v_mfma_f32_16x16x32_bf16 v[212:215], v[20:23], v[36:39], 0\n"
    "s_waitcnt lgkmcnt(5)\n"
    "v_cvt_pk_bf16_f32 v240, v216, v217\n"
    "v_cvt_pk_bf16_f32 v241, v218, v219\n"
    "v_mfma_f32_16x16x32_bf16 v[216:219], v[24:27], v[32:35], 0\n"
    "ds_read_b128 v[20:23], v3 offset:4672\n"
    "v_cvt_pk_bf16_f32 v244, v220, v221\n"
    "v_cvt_pk_bf16_f32 v245, v222, v223\n"
    "v_mfma_f32_16x16x32_bf16 v[220:223], v[24:27], v[36:39], 0\n"
    "v_cvt_pk_bf16_f32 v242, v224, v225\n"
    "v_cvt_pk_bf16_f32 v243, v226, v227\n"
    "v_mfma_f32_16x16x32_bf16 v[224:227], v[28:31], v[32:35], 0\n"
    "ds_read_b128 v[24:27], v3 offset:9280\n"
    "v_cvt_pk_bf16_f32 v246, v228, v229\n"
    "v_cvt_pk_bf16_f32 v247, v230, v231\n"
    "v_mfma_f32_16x16x32_bf16 v[228:231], v[28:31], v[36:39], 0\n"
    "s_waitcnt lgkmcnt(5)\n"
    "v_mfma_f32_16x16x32_bf16 v[200:203], v[40:43], v[48:51], v[200:203]\n"
    "ds_read_b128 v[28:31], v3 offset:13888\n"
    "v_mfma_f32_16x16x32_bf16 v[204:207], v[40:43], v[52:55], v[204:207]\n"
    "s_add_u32 s59, s60, 65536\n"
    "buffer_store_dwordx4 v[232:235], v13, s[28:31], s60 offen offset:0 sc1\n"
    "v_mfma_f32_16x16x32_bf16 v[208:211], v[44:47], v[48:51], v[208:211]\n"
    "ds_read_b128 v[40:43], v3 offset:96\n"
    "v_mfma_f32_16x16x32_bf16 v[212:215], v[44:47], v[52:55], v[212:215]\n"
    "s_waitcnt lgkmcnt(5)\n"
    "v_mfma_f32_16x16x32_bf16 v[216:219], v[56:59], v[48:51], v[216:219]\n"
    "buffer_store_dwordx4 v[236:239], v13, s[28:31], s60 offen offset:1024 sc1\n"
    "ds_read_b128 v[44:47], v3 offset:4704\n"
    "v_mfma_f32_16x16x32_bf16 v[220:223], v[56:59], v[52:55], v[220:223]\n"
    "v_mfma_f32_16x16x32_bf16 v[224:227], v[60:63], v[48:51], v[224:227]\n"
    "ds_read_b128 v[56:59], v3 offset:9312\n"
    "v_mfma_f32_16x16x32_bf16 v[228:231], v[60:63], v[52:55], v[228:231]\n"
    "s_waitcnt lgkmcnt(5)\n"
    "v_mfma_f32_16x16x32_bf16 v[200:203], v[16:19], v[64:67], v[200:203]\n"
    "buffer_store_dwordx4 v[240:243], v13, s[28:31], s59 offen offset:0 sc1\n"
    "ds_read_b128 v[60:63], v3 offset:13920\n"
    "v_mfma_f32_16x16x32_bf16 v[204:207], v[16:19], v[68:71], v[204:207]\n"
    "v_mfma_f32_16x16x32_bf16 v[208:211], v[20:23], v[64:67], v[208:211]\n"
    "ds_read_b128 v[16:19], v3 offset:128\n"
    "v_mfma_f32_16x16x32_bf16 v[212:215], v[20:23], v[68:71], v[212:215]\n"
    "s_waitcnt lgkmcnt(5)\n"
    "v_mfma_f32_16x16x32_bf16 v[216:219], v[24:27], v[64:67], v[216:219]\n"
    "buffer_store_dwordx4 v[244:247], v13, s[28:31], s59 offen offset:1024 sc1\n"
    "s_add_u32 s60, s60, 262144\n"
    "ds_read_b128 v[20:23], v3 offset:4736\n"
    "v_mfma_f32_16x16x32_bf16 v[220:223], v[24:27], v[68:71], v[220:223]\n"
    "v_mfma_f32_16x16x32_bf16 v[224:227], v[28:31], v[64:67], v[224:227]\n"
    "ds_read_b128 v[24:27], v3 offset:9344\n"
    "v_mfma_f32_16x16x32_bf16 v[228:231], v[28:31], v[68:71], v[228:231]\n"
    "s_waitcnt lgkmcnt(5)\n"
    "v_mfma_f32_16x16x32_bf16 v[200:203], v[40:43], v[80:83], v[200:203]\n"
    "ds_read_b128 v[28:31], v3 offset:13952\n"
    "v_mfma_f32_16x16x32_bf16 v[204:207], v[40:43], v[84:87], v[204:207]\n"
    "v_mfma_f32_16x16x32_bf16 v[208:211], v[44:47], v[80:83], v[208:211]\n"
    "ds_read_b128 v[40:43], v3 offset:160\n"
    "v_mfma_f32_16x16x32_bf16 v[212:215], v[44:47], v[84:87], v[212:215]\n"
    "s_waitcnt lgkmcnt(5)\n"
    "v_mfma_f32_16x16x32_bf16 v[216:219], v[56:59], v[80:83], v[216:219]\n"
    "ds_read_b128 v[44:47], v3 offset:4768\n"
    "v_mfma_f32_16x16x32_bf16 v[220:223], v[56:59], v[84:87], v[220:223]\n"
    "v_mfma_f32_16x16x32_bf16 v[224:227], v[60:63], v[80:83], v[224:227]\n"
    "ds_read_b128 v[56:59], v3 offset:9376\n"
    "v_mfma_f32_16x16x32_bf16 v[228:231], v[60:63], v[84:87], v[228:231]\n"
    "s_waitcnt lgkmcnt(5)\n"
    "v_mfma_f32_16x16x32_bf16 v[200:203], v[16:19], v[96:99], v[200:203]\n"
    "ds_read_b128 v[60:63], v3 offset:13984\n"
    "v_mfma_f32_16x16x32_bf16 v[204:207], v[16:19], v[100:103], v[204:207]\n"
    "v_mfma_f32_16x16x32_bf16 v[208:211], v[20:23], v[96:99], v[208:211]\n"
    "ds_read_b128 v[16:19], v3 offset:192\n"
    "v_mfma_f32_16x16x32_bf16 v[212:215], v[20:23], v[100:103], v[212:215]\n"
    "s_waitcnt lgkmcnt(5)\n"
    "v_mfma_f32_16x16x32_bf16 v[216:219], v[24:27], v[96:99], v[216:219]\n"
    "ds_read_b128 v[20:23], v3 offset:4800\n"
    "v_mfma_f32_16x16x32_bf16 v[220:223], v[24:27], v[100:103], v[220:223]\n"
    "v_mfma_f32_16x16x32_bf16 v[224:227], v[28:31], v[96:99], v[224:227]\n"
    "ds_read_b128 v[24:27], v3 offset:9408\n"
    "v_mfma_f32_16x16x32_bf16 v[228:231], v[28:31], v[100:103], v[228:231]\n"
    "s_waitcnt lgkmcnt(5)\n"
    "v_mfma_f32_16x16x32_bf16 v[200:203], v[40:43], v[112:115], v[200:203]\n"
    "ds_read_b128 v[28:31], v3 offset:14016\n"
    "v_mfma_f32_16x16x32_bf16 v[204:207], v[40:43], v[116:119], v[204:207]\n"
    "v_mfma_f32_16x16x32_bf16 v[208:211], v[44:47], v[112:115], v[208:211]\n"
    "ds_read_b128 v[40:43], v3 offset:224\n"
    "v_mfma_f32_16x16x32_bf16 v[212:215], v[44:47], v[116:119], v[212:215]\n"
    "s_waitcnt lgkmcnt(5)\n"
    "v_mfma_f32_16x16x32_bf16 v[216:219], v[56:59], v[112:115], v[216:219]\n"
    "ds_read_b128 v[44:47], v3 offset:4832\n"
    "v_mfma_f32_16x16x32_bf16 v[220:223], v[56:59], v[116:119], v[220:223]\n"
    "v_mfma_f32_16x16x32_bf16 v[224:227], v[60:63], v[112:115], v[224:227]\n"
    "ds_read_b128 v[56:59], v3 offset:9440\n"
    "v_mfma_f32_16x16x32_bf16 v[228:231], v[60:63], v[116:119], v[228:231]\n"
    "s_waitcnt lgkmcnt(5)\n"
    "v_mfma_f32_16x16x32_bf16 v[200:203], v[16:19], v[128:131], v[200:203]\n"
    "ds_read_b128 v[60:63], v3 offset:14048\n"
    "v_mfma_f32_16x16x32_bf16 v[204:207], v[16:19], v[132:135], v[204:207]\n"
    "v_mfma_f32_16x16x32_bf16 v[208:211], v[20:23], v[128:131], v[208:211]\n"
    "s_waitcnt vmcnt(5)\n"
    "buffer_store_dwordx2 v[192:193], v190, s[72:75], s57 offen sc1\n"
    "s_add_u32 s57, s57, 512\n"
    "ds_read_b128 v[16:19], v3 offset:256\n"
    "v_mfma_f32_16x16x32_bf16 v[212:215], v[20:23], v[132:135], v[212:215]\n"
    "s_waitcnt lgkmcnt(5)\n"
    "v_mfma_f32_16x16x32_bf16 v[216:219], v[24:27], v[128:131], v[216:219]\n"
    "ds_read_b128 v[20:23], v3 offset:4864\n"
    "v_mfma_f32_16x16x32_bf16 v[220:223], v[24:27], v[132:135], v[220:223]\n"
    "v_mfma_f32_16x16x32_bf16 v[224:227], v[28:31], v[128:131], v[224:227]\n"
    "ds_read_b128 v[24:27], v3 offset:9472\n"
    "v_mfma_f32_16x16x32_bf16 v[228:231], v[28:31], v[132:135], v[228:231]\n"
    "s_waitcnt lgkmcnt(5)\n"
    "v_mfma_f32_16x16x32_bf16 v[200:203], v[40:43], v[144:147], v[200:203]\n"
    "ds_read_b128 v[28:31], v3 offset:14080\n"
    "v_mfma_f32_16x16x32_bf16 v[204:207], v[40:43], v[148:151], v[204:207]\n"
    "v_mfma_f32_16x16x32_bf16 v[208:211], v[44:47], v[144:147], v[208:211]\n"
    "v_mfma_f32_16x16x32_bf16 v[212:215], v[44:47], v[148:151], v[212:215]\n"
    "s_waitcnt lgkmcnt(4)\n"
    "v_mfma_f32_16x16x32_bf16 v[216:219], v[56:59], v[144:147], v[216:219]\n"
    "v_mfma_f32_16x16x32_bf16 v[220:223], v[56:59], v[148:151], v[220:223]\n"
    "v_mfma_f32_16x16x32_bf16 v[224:227], v[60:63], v[144:147], v[224:227]\n"
    "v_mfma_f32_16x16x32_bf16 v[228:231], v[60:63], v[148:151], v[228:231]\n"
    "s_waitcnt lgkmcnt(2)\n"
    "v_mfma_f32_16x16x32_bf16 v[200:203], v[16:19], v[160:163], v[200:203]\n"
    "v_mfma_f32_16x16x32_bf16 v[204:207], v[16:19], v[164:167], v[204:207]\n"
    "v_mfma_f32_16x16x32_bf16 v[208:211], v[20:23], v[160:163], v[208:211]\n"
    "v_mfma_f32_16x16x32_bf16 v[212:215], v[20:23], v[164:167], v[212:215]\n"
    "s_waitcnt lgkmcnt(0)\n"
    "v_mfma_f32_16x16x32_bf16 v[216:219], v[24:27], v[160:163], v[216:219]\n"
    "v_mfma_f32_16x16x32_bf16 v[220:223], v[24:27], v[164:167], v[220:223]\n"
    "v_mfma_f32_16x16x32_bf16 v[224:227], v[28:31], v[160:163], v[224:227]\n"
    "v_mfma_f32_16x16x32_bf16 v[228:231], v[28:31], v[164:167], v[228:231]\n"
    "v_cvt_pk_bf16_f32 v232, v200, v201\n"
    "v_cvt_pk_bf16_f32 v233, v202, v203\n"
    "v_cvt_pk_bf16_f32 v236, v204, v205\n"
    "v_cvt_pk_bf16_f32 v237, v206, v207\n"
    "v_cvt_pk_bf16_f32 v234, v208, v209\n"
    "v_cvt_pk_bf16_f32 v235, v210, v211\n"
    "v_cvt_pk_bf16_f32 v238, v212, v213\n"
    "v_cvt_pk_bf16_f32 v239, v214, v215\n"
    "v_cvt_pk_bf16_f32 v240, v216, v217\n"
    "v_cvt_pk_bf16_f32 v241, v218, v219\n"
    "v_cvt_pk_bf16_f32 v244, v220, v221\n"
    "v_cvt_pk_bf16_f32 v245, v222, v223\n"
    "v_cvt_pk_bf16_f32 v242, v224, v225\n"
    "v_cvt_pk_bf16_f32 v243, v226, v227\n"
    "v_cvt_pk_bf16_f32 v246, v228, v229\n"
    "v_cvt_pk_bf16_f32 v247, v230, v231\n"
    "s_add_u32 s59, s60, 65536\n"
    "buffer_store_dwordx4 v[232:235], v13, s[28:31], s60 offen offset:0 sc1\n"
    "buffer_store_dwordx4 v[236:239], v13, s[28:31], s60 offen offset:1024 sc1\n"
    "buffer_store_dwordx4 v[240:243], v13, s[28:31], s59 offen offset:0 sc1\n"
    "buffer_store_dwordx4 v[244:247], v13, s[28:31], s59 offen offset:1024 sc1\n"
    "s_add_u32 s60, s60, 262144\n"
    "s_waitcnt vmcnt(5)\n"
    "buffer_store_dwordx2 v[192:193], v190, s[72:75], s57 offen sc1\n"
    "s_add_u32 s57, s57, 512\n"
    "s_load_dwordx2 s[86:87], %0, 96\n"
    "s_load_dwordx2 s[88:89], %0, 104\n"
    "s_load_dwordx2 s[90:91], %0, 64\n"
    "s_lshl_b32 s40, s11, 2\n"
    "s_add_u32 s40, s40, s9\n"
    "s_lshr_b32 s48, s40, 1\n"
    "s_and_b32 s49, s40, 1\n"
    "s_mul_i32 s40, s10, 18\n"
    "s_lshr_b32 s41, s48, 2\n"
    "s_add_u32 s40, s40, s41\n"
    "s_lshl_b32 s40, s40, 1\n"
    "s_add_u32 s40, s40, s49\n"
    "s_lshl_b32 s40, s40, 16\n"
    "s_and_b32 s41, s48, 3\n"
    "s_lshl_b32 s41, s41, 10\n"
    "s_add_u32 s51, s40, s41\n"
    "v_and_b32 v28, 15, v0\n"
    "s_lshr_b32 s40, s48, 2\n"
    "s_lshl_b32 s40, s40, 1\n"
    "s_add_u32 s40, s40, s49\n"
    "s_lshr_b32 s41, s48, 1\n"
    "v_mov_b32 v29, s41\n"
    "v_mov_b32 v30, s40\n"
    "v_cmp_gt_u32 vcc, 12, v28\n"
    "s_nop 1\n"
    "v_cndmask_b32 v29, v29, v30, vcc\n"
    "v_lshl_add_u32 v29, v29, 4, v28\n"
    "v_lshlrev_b32 v20, 3, v29\n"
    "s_lshl_b32 s40, s10, 7\n"
    "s_lshl_b32 s41, s48, 4\n"
    "s_add_u32 s40, s40, s41\n"
    "v_add_u32 v28, s40, v28\n"
    "v_lshrrev_b32 v29, 4, v0\n"
    "v_lshlrev_b32 v29, 2, v29\n"
    "s_lshl_b32 s40, s49, 5\n"
    "v_add_u32 v29, s40, v29\n"
    "v_mul_u32_u24 v21, 1152, v28\n"
    "v_lshl_add_u32 v21, v29, 1, v21\n"
    "v_lshlrev_b32 v22, 1, v21\n"
    "v_lshlrev_b32 v23, 1, v29\n"
    "v_lshlrev_b32 v28, 3, v28\n"
    "v_add_u32 v28, 131072, v28\n"
    "buffer_load_dwordx2 v[24:25], v28, s[72:75], 0 offen sc1\n"
    "v_lshrrev_b32 v28, 4, v0\n"
    "v_min_u32 v28, 2, v28\n"
    "v_lshlrev_b32 v28, 9, v28\n"
    "v_add_u32 v20, v28, v20\n"
    "s_mul_i32 s40, s10, 9\n"
    "s_lshl_b32 s40, s40, 9\n"
    "s_add_u32 s52, s40, 16384\n"
    "buffer_load_dwordx2 v[26:27], v20, s[72:75], s52 offen sc1\n"
    "s_waitcnt vmcnt(2)\n"
    "buffer_store_dwordx2 v[192:193], v190, s[72:75], s57 offen sc1\n"
    "s_add_u32 s57, s57, 512\n"
    "s_waitcnt lgkmcnt(0)\n"
    "s_mov_b32 s92, s86\n"
    "s_and_b32 s93, s87, 0xffff\n"
    "s_mov_b32 s94, 1152\n"
    "s_mov_b32 s95, 0x20000\n"
    "s_mov_b32 s80, s88\n"
    "s_and_b32 s81, s89, 0xffff\n"
    "s_mov_b32 s82, 4718592\n"
    "s_mov_b32 s83, 0x20000\n"
    "s_mov_b32 s96, s90\n"
    "s_and_b32 s97, s91, 0xffff\n"
    "s_mov_b32 s98, 1152\n"
    "s_mov_b32 s99, 0x20000\n"
    "s_mov_b32 s100, 0x00003f80\n"
    "s_mov_b32 s101, 0x3f800000\n"
    "s_barrier\n"
    "s_cmp_lt_u32 s9, 2\n"
    "s_cselect_b32 s44, s92, s96\n"
    "s_cselect_b32 s45, s93, s97\n"
    "s_cselect_b32 s46, s94, s98\n"
    "s_cselect_b32 s47, s95, s99\n"
    "s_and_b32 s40, s9, 1\n"
    "s_lshl_b32 s40, s40, 10\n"
    "s_lshl_b32 s41, s9, 10\n"
    "s_mov_b32 m0, s41\n"
    "s_nop 0\n"
    "buffer_load_dwordx4 v13, s[44:47], s40 offen lds\n"
    "s_add_u32 s40, s15, 0\n"
    "s_cmp_ge_u32 s40, 9\n"
    "s_cselect_b32 s41, 9, 0\n"
    "s_sub_u32 s40, s40, s41\n"
    "s_lshl_b32 s56, s40, 7\n"
    "buffer_load_dwordx2 a[0:1], v21, s[76:79], s56 offen offset:0\n"
    "buffer_load_dwordx2 a[2:3], v21, s[76:79], s56 offen offset:32\n"
    "s_add_u32 s40, s15, 1\n"
    "s_cmp_ge_u32 s40, 9\n"
    "s_cselect_b32 s41, 9, 0\n"
    "s_sub_u32 s40, s40, s41\n"
    "s_lshl_b32 s57, s40, 7\n"
    "buffer_load_dwordx2 a[4:5], v21, s[76:79], s57 offen offset:0\n"
    "buffer_load_dwordx2 a[6:7], v21, s[76:79], s57 offen offset:32\n"
    "s_add_u32 s40, s15, 2\n"
    "s_cmp_ge_u32 s40, 9\n"
    "s_cselect_b32 s41, 9, 0\n"
    "s_sub_u32 s40, s40, s41\n"
    "s_lshl_b32 s58, s40, 7\n"
    "buffer_load_dwordx2 a[8:9], v21, s[76:79], s58 offen offset:0\n"
    "buffer_load_dwordx2 a[10:11], v21, s[76:79], s58 offen offset:32\n"
    "s_waitcnt vmcnt(0)\n"
    "s_barrier\n"
    "v_cmp_eq_u32 vcc, s84, v26\n"
    "v_cmp_eq_u32 s[40:41], s85, v27\n"
    "s_nop 1\n"
    "s_and_b64 vcc, vcc, s[40:41]\n"
    "s_cmp_eq_u64 vcc, -1\n"
    "s_cbranch_scc1 JOK_22\n"
    "s_mov_b32 s42, 0\n"
    "JLP_23:\n"
    "s_sleep 1\n"
    "buffer_load_dwordx2 v[26:27], v20, s[72:75], s52 offen sc1\n"
    "s_waitcnt vmcnt(0)\n"
    "v_cmp_eq_u32 vcc, s84, v26\n"
    "v_cmp_eq_u32 s[40:41], s85, v27\n"
    "s_nop 1\n"
    "s_and_b64 vcc, vcc, s[40:41]\n"
    "s_cmp_eq_u64 vcc, -1\n"
    "s_cbranch_scc1 JOK_22\n"
    "s_add_u32 s42, s42, 1\n"
    "s_cmp_lt_u32 s42, 20000\n"
    "s_cbranch_scc1 JLP_23\n"
    "JOK_22:\n"
    "s_add_u32 s43, s51, 0\n"
    "buffer_load_dwordx4 v[32:35], v13, s[28:31], s43 offen sc1\n"
    "s_add_u32 s43, s43, 4096\n"
    "buffer_load_dwordx4 v[36:39], v13, s[28:31], s43 offen sc1\n"
    "s_add_u32 s43, s43, 4096\n"
    "buffer_load_dwordx4 v[40:43], v13, s[28:31], s43 offen sc1\n"
    "s_add_u32 s43, s43, 4096\n"
    "buffer_load_dwordx4 v[44:47], v13, s[28:31], s43 offen sc1\n"
    "s_add_u32 s43, s43, 4096\n"
    "buffer_load_dwordx4 v[48:51], v13, s[28:31], s43 offen sc1\n"
    "s_add_u32 s43, s43, 4096\n"
    "buffer_load_dwordx4 v[52:55], v13, s[28:31], s43 offen sc1\n"
    "s_add_u32 s43, s43, 4096\n"
    "buffer_load_dwordx4 v[56:59], v13, s[28:31], s43 offen sc1\n"
    "s_add_u32 s43, s43, 4096\n"
    "buffer_load_dwordx4 v[60:63], v13, s[28:31], s43 offen sc1\n"
    "s_add_u32 s43, s43, 4096\n"
    "buffer_load_dwordx4 v[64:67], v13, s[28:31], s43 offen sc1\n"
    "s_add_u32 s43, s43, 4096\n"
    "buffer_load_dwordx4 v[68:71], v13, s[28:31], s43 offen sc1\n"
    "s_add_u32 s43, s43, 4096\n"
    "buffer_load_dwordx4 v[72:75], v13, s[28:31], s43 offen sc1\n"
    "s_add_u32 s43, s43, 4096\n"
    "buffer_load_dwordx4 v[76:79], v13, s[28:31], s43 offen sc1\n"
    "s_add_u32 s43, s43, 4096\n"
    "buffer_load_dwordx4 v[80:83], v13, s[28:31], s43 offen sc1\n"
    "s_add_u32 s43, s43, 4096\n"
    "buffer_load_dwordx4 v[84:87], v13, s[28:31], s43 offen sc1\n"
    "s_add_u32 s43, s43, 4096\n"
    "buffer_load_dwordx4 v[88:91], v13, s[28:31], s43 offen sc1\n"
    "s_add_u32 s43, s43, 4096\n"
    "buffer_load_dwordx4 v[92:95], v13, s[28:31], s43 offen sc1\n"
    "s_add_u32 s43, s51, 262144\n"
    "buffer_load_dwordx4 v[96:99], v13, s[28:31], s43 offen sc1\n"
    "s_add_u32 s43, s43, 4096\n"
    "buffer_load_dwordx4 v[100:103], v13, s[28:31], s43 offen sc1\n"
    "s_add_u32 s43, s43, 4096\n"
    "buffer_load_dwordx4 v[104:107], v13, s[28:31], s43 offen sc1\n"
    "s_add_u32 s43, s43, 4096\n"
    "buffer_load_dwordx4 v[108:111], v13, s[28:31], s43 offen sc1\n"
    "s_add_u32 s43, s43, 4096\n"
    "buffer_load_dwordx4 v[112:115], v13, s[28:31], s43 offen sc1\n"
    "s_add_u32 s43, s43, 4096\n"
    "buffer_load_dwordx4 v[116:119], v13, s[28:31], s43 offen sc1\n"
    "s_add_u32 s43, s43, 4096\n"
    "buffer_load_dwordx4 v[120:123], v13, s[28:31], s43 offen sc1\n"
    "s_add_u32 s43, s43, 4096\n"
    "buffer_load_dwordx4 v[124:127], v13, s[28:31], s43 offen sc1\n"
    "s_add_u32 s43, s43, 4096\n"
    "buffer_load_dwordx4 v[128:131], v13, s[28:31], s43 offen sc1\n"
    "s_add_u32 s43, s43, 4096\n"
    "buffer_load_dwordx4 v[132:135], v13, s[28:31], s43 offen sc1\n"
    "s_add_u32 s43, s43, 4096\n"
    "buffer_load_dwordx4 v[136:139], v13, s[28:31], s43 offen sc1\n"
    "s_add_u32 s43, s43, 4096\n"
    "buffer_load_dwordx4 v[140:143], v13, s[28:31], s43 offen sc1\n"
    "s_add_u32 s43, s43, 4096\n"
    "buffer_load_dwordx4 v[144:147], v13, s[28:31], s43 offen sc1\n"
    "s_add_u32 s43, s43, 4096\n"
    "buffer_load_dwordx4 v[148:151], v13, s[28:31], s43 offen sc1\n"
    "s_add_u32 s43, s43, 4096\n"
    "buffer_load_dwordx4 v[152:155], v13, s[28:31], s43 offen sc1\n"
    "s_add_u32 s43, s43, 4096\n"
    "buffer_load_dwordx4 v[156:159], v13, s[28:31], s43 offen sc1\n"
    "s_waitcnt vmcnt(16)\n"
    "v_lshlrev_b32 v224, 16, v32\n"
    "s_add_u32 s43, s51, 524288\n"
    "buffer_load_dwordx4 v[160:163], v13, s[28:31], s43 offen sc1\n"
    "s_add_u32 s43, s43, 4096\n"
    "v_and_b32 v225, 0xffff0000, v32\n"
    "v_lshlrev_b32 v226, 16, v33\n"
    "v_and_b32 v227, 0xffff0000, v33\n"
    "v_lshlrev_b32 v228, 16, v34\n"
    "v_and_b32 v229, 0xffff0000, v34\n"
    "v_lshlrev_b32 v230, 16, v35\n"
    "v_and_b32 v231, 0xffff0000, v35\n"
    "v_dot2_f32_bf16 v224, v36, s100, v224\n"
    "v_dot2_f32_bf16 v225, v36, s101, v225\n"
    "v_dot2_f32_bf16 v226, v37, s100, v226\n"
    "v_dot2_f32_bf16 v227, v37, s101, v227\n"
    "v_dot2_f32_bf16 v228, v38, s100, v228\n"
    "v_dot2_f32_bf16 v229, v38, s101, v229\n"
    "buffer_load_dwordx4 v[164:167], v13, s[28:31], s43 offen sc1\n"
    "s_add_u32 s43, s43, 4096\n"
    "v_dot2_f32_bf16 v230, v39, s100, v230\n"
    "v_dot2_f32_bf16 v231, v39, s101, v231\n"
    "v_dot2_f32_bf16 v224, v40, s100, v224\n"
    "v_dot2_f32_bf16 v225, v40, s101, v225\n"
    "v_dot2_f32_bf16 v226, v41, s100, v226\n"
    "v_dot2_f32_bf16 v227, v41, s101, v227\n"
    "v_dot2_f32_bf16 v228, v42, s100, v228\n"
    "v_dot2_f32_bf16 v229, v42, s101, v229\n"
    "v_dot2_f32_bf16 v230, v43, s100, v230\n"
    "v_dot2_f32_bf16 v231, v43, s101, v231\n"
    "v_dot2_f32_bf16 v224, v44, s100, v224\n"
    "v_dot2_f32_bf16 v225, v44, s101, v225\n"
    "v_dot2_f32_bf16 v226, v45, s100, v226\n"
    "v_dot2_f32_bf16 v227, v45, s101, v227\n"
    "buffer_load_dwordx4 v[168:171], v13, s[28:31], s43 offen sc1\n"
    "s_add_u32 s43, s43, 4096\n"
    "v_dot2_f32_bf16 v228, v46, s100, v228\n"
    "v_dot2_f32_bf16 v229, v46, s101, v229\n"
    "v_dot2_f32_bf16 v230, v47, s100, v230\n"
    "v_dot2_f32_bf16 v231, v47, s101, v231\n"
    "v_dot2_f32_bf16 v224, v48, s100, v224\n"
    "v_dot2_f32_bf16 v225, v48, s101, v225\n"
    "v_dot2_f32_bf16 v226, v49, s100, v226\n"
    "v_dot2_f32_bf16 v227, v49, s101, v227\n"
    "v_dot2_f32_bf16 v228, v50, s100, v228\n"
    "v_dot2_f32_bf16 v229, v50, s101, v229\n"
    "v_dot2_f32_bf16 v230, v51, s100, v230\n"
    "v_dot2_f32_bf16 v231, v51, s101, v231\n"
    "v_dot2_f32_bf16 v224, v52, s100, v224\n"
    "v_dot2_f32_bf16 v225, v52, s101, v225\n"
    "buffer_load_dwordx4 v[172:175], v13, s[28:31], s43 offen sc1\n"
    "s_add_u32 s43, s43, 4096\n"
    "v_dot2_f32_bf16 v226, v53, s100, v226\n"
    "v_dot2_f32_bf16 v227, v53, s101, v227\n"
    "v_dot2_f32_bf16 v228, v54, s100, v228\n"
    "v_dot2_f32_bf16 v229, v54, s101, v229\n"
    "v_dot2_f32_bf16 v230, v55, s100, v230\n"
    "v_dot2_f32_bf16 v231, v55, s101, v231\n"
    "v_dot2_f32_bf16 v224, v56, s100, v224\n"
    "v_dot2_f32_bf16 v225, v56, s101, v225\n"
    "v_dot2_f32_bf16 v226, v57, s100, v226\n"
    "v_dot2_f32_bf16 v227, v57, s101, v227\n"
    "v_dot2_f32_bf16 v228, v58, s100, v228\n"
    "v_dot2_f32_bf16 v229, v58, s101, v229\n"
    "v_dot2_f32_bf16 v230, v59, s100, v230\n"
    "v_dot2_f32_bf16 v231, v59, s101, v231\n"
    "buffer_load_dwordx4 v[176:179], v13, s[28:31], s43 offen sc1\n"
    "s_add_u32 s43, s43, 4096\n"
    "v_dot2_f32_bf16 v224, v60, s100, v224\n"
    "v_dot2_f32_bf16 v225, v60, s101, v225\n"
    "v_dot2_f32_bf16 v226, v61, s100, v226\n"
    "v_dot2_f32_bf16 v227, v61, s101, v227\n"
    "v_dot2_f32_bf16 v228, v62, s100, v228\n"
    "v_dot2_f32_bf16 v229, v62, s101, v229\n"
    "v_dot2_f32_bf16 v230, v63, s100, v230\n"
    "v_dot2_f32_bf16 v231, v63, s101, v231\n"
    "v_dot2_f32_bf16 v224, v64, s100, v224\n"
    "v_dot2_f32_bf16 v225, v64, s101, v225\n"
    "v_dot2_f32_bf16 v226, v65, s100, v226\n"
    "v_dot2_f32_bf16 v227, v65, s101, v227\n"
    "v_dot2_f32_bf16 v228, v66, s100, v228\n"
    "v_dot2_f32_bf16 v229, v66, s101, v229\n"
    "buffer_load_dwordx4 v[180:183], v13, s[28:31], s43 offen sc1\n"
    "s_add_u32 s43, s43, 4096\n"
    "v_dot2_f32_bf16 v230, v67, s100, v230\n"
    "v_dot2_f32_bf16 v231, v67, s101, v231\n"
    "v_dot2_f32_bf16 v224, v68, s100, v224\n"
    "v_dot2_f32_bf16 v225, v68, s101, v225\n"
    "v_dot2_f32_bf16 v226, v69, s100, v226\n"
    "v_dot2_f32_bf16 v227, v69, s101, v227\n"
    "v_dot2_f32_bf16 v228, v70, s100, v228\n"
    "v_dot2_f32_bf16 v229, v70, s101, v229\n"
    "v_dot2_f32_bf16 v230, v71, s100, v230\n"
    "v_dot2_f32_bf16 v231, v71, s101, v231\n"
    "v_dot2_f32_bf16 v224, v72, s100, v224\n"
    "v_dot2_f32_bf16 v225, v72, s101, v225\n"
    "v_dot2_f32_bf16 v226, v73, s100, v226\n"
    "v_dot2_f32_bf16 v227, v73, s101, v227\n"
    "buffer_load_dwordx4 v[184:187], v13, s[28:31], s43 offen sc1\n"
    "s_add_u32 s43, s43, 4096\n"
    "v_dot2_f32_bf16 v228, v74, s100, v228\n"
    "v_dot2_f32_bf16 v229, v74, s101, v229\n"
    "v_dot2_f32_bf16 v230, v75, s100, v230\n"
    "v_dot2_f32_bf16 v231, v75, s101, v231\n"
    "v_dot2_f32_bf16 v224, v76, s100, v224\n"
    "v_dot2_f32_bf16 v225, v76, s101, v225\n"
    "v_dot2_f32_bf16 v226, v77, s100, v226\n"
    "v_dot2_f32_bf16 v227, v77, s101, v227\n"
    "v_dot2_f32_bf16 v228, v78, s100, v228\n"
    "v_dot2_f32_bf16 v229, v78, s101, v229\n"
    "v_dot2_f32_bf16 v230, v79, s100, v230\n"
    "v_dot2_f32_bf16 v231, v79, s101, v231\n"
    "v_lshlrev_b32 v232, 16, v80\n"
    "v_and_b32 v233, 0xffff0000, v80\n"
    "buffer_load_dwordx4 v[188:191], v13, s[28:31], s43 offen sc1\n"
    "s_add_u32 s43, s43, 4096\n"
    "v_lshlrev_b32 v234, 16, v81\n"
    "v_and_b32 v235, 0xffff0000, v81\n"
    "v_lshlrev_b32 v236, 16, v82\n"
    "v_and_b32 v237, 0xffff0000, v82\n"
    "v_lshlrev_b32 v238, 16, v83\n"
    "v_and_b32 v239, 0xffff0000, v83\n"
    "v_dot2_f32_bf16 v232, v84, s100, v232\n"
    "v_dot2_f32_bf16 v233, v84, s101, v233\n"
    "v_dot2_f32_bf16 v234, v85, s100, v234\n"
    "v_dot2_f32_bf16 v235, v85, s101, v235\n"
    "v_dot2_f32_bf16 v236, v86, s100, v236\n"
    "v_dot2_f32_bf16 v237, v86, s101, v237\n"
    "v_dot2_f32_bf16 v238, v87, s100, v238\n"
    "v_dot2_f32_bf16 v239, v87, s101, v239\n"
    "buffer_load_dwordx4 v[192:195], v13, s[28:31], s43 offen sc1\n"
    "s_add_u32 s43, s43, 4096\n"
    "v_dot2_f32_bf16 v232, v88, s100, v232\n"
    "v_dot2_f32_bf16 v233, v88, s101, v233\n"
    "v_dot2_f32_bf16 v234, v89, s100, v234\n"
    "v_dot2_f32_bf16 v235, v89, s101, v235\n"
    "v_dot2_f32_bf16 v236, v90, s100, v236\n"
    "v_dot2_f32_bf16 v237, v90, s101, v237\n"
    "v_dot2_f32_bf16 v238, v91, s100, v238\n"
    "v_dot2_f32_bf16 v239, v91, s101, v239\n"
    "v_dot2_f32_bf16 v232, v92, s100, v232\n"
    "v_dot2_f32_bf16 v233, v92, s101, v233\n"
    "v_dot2_f32_bf16 v234, v93, s100, v234\n"
    "v_dot2_f32_bf16 v235, v93, s101, v235\n"
    "v_dot2_f32_bf16 v236, v94, s100, v236\n"
    "buffer_load_dwordx4 v[196:199], v13, s[28:31], s43 offen sc1\n"
    "s_add_u32 s43, s43, 4096\n"
    "v_dot2_f32_bf16 v237, v94, s101, v237\n"
    "v_dot2_f32_bf16 v238, v95, s100, v238\n"
    "v_dot2_f32_bf16 v239, v95, s101, v239\n"
    "v_accvgpr_read_b32 v48, a0\n"
    "v_accvgpr_read_b32 v49, a1\n"
    "v_accvgpr_read_b32 v50, a2\n"
    "v_accvgpr_read_b32 v51, a3\n"
    "v_add_u32 v28, s56, v23\n"
    "ds_read_b64 v[52:53], v28 offset:0\n"
    "ds_read_b64 v[56:57], v28 offset:2048\n"
    "ds_read_b64 v[54:55], v28 offset:32\n"
    "ds_read_b64 v[58:59], v28 offset:2080\n"
    "s_waitcnt lgkmcnt(0)\n"
    "v_lshlrev_b32 v240, 16, v48\n"
    "buffer_load_dwordx4 v[200:203], v13, s[28:31], s43 offen sc1\n"
    "s_add_u32 s43, s43, 4096\n"
    "v_and_b32 v241, 0xffff0000, v48\n"
    "v_lshlrev_b32 v248, 16, v52\n"
    "v_and_b32 v249, 0xffff0000, v52\n"
    "v_lshlrev_b32 v242, 16, v49\n"
    "v_and_b32 v243, 0xffff0000, v49\n"
    "v_lshlrev_b32 v250, 16, v53\n"
    "v_and_b32 v251, 0xffff0000, v53\n"
    "v_lshlrev_b32 v244, 16, v50\n"
    "v_and_b32 v245, 0xffff0000, v50\n"
    "v_lshlrev_b32 v252, 16, v54\n"
    "v_and_b32 v253, 0xffff0000, v54\n"
    "v_lshlrev_b32 v246, 16, v51\n"
    "v_and_b32 v247, 0xffff0000, v51\n"
    "v_lshlrev_b32 v254, 16, v55\n"
    "buffer_load_dwordx4 v[204:207], v13, s[28:31], s43 offen sc1\n"
    "s_add_u32 s43, s43, 4096\n"
    "v_and_b32 v255, 0xffff0000, v55\n"
    "v_pk_add_f32 v[32:33], v[240:241], v[24:25] op_sel_hi:[1,0] neg_lo:[0,1] neg_hi:[0,1]\n"
    "v_pk_add_f32 v[34:35], v[242:243], v[24:25] op_sel_hi:[1,0] neg_lo:[0,1] neg_hi:[0,1]\n"
    "v_pk_add_f32 v[36:37], v[244:245], v[24:25] op_sel_hi:[1,0] neg_lo:[0,1] neg_hi:[0,1]\n"
    "v_pk_add_f32 v[38:39], v[246:247], v[24:25] op_sel_hi:[1,0] neg_lo:[0,1] neg_hi:[0,1]\n"
    "v_lshlrev_b32 v52, 16, v56\n"
    "v_and_b32 v53, 0xffff0000, v56\n"
    "v_pk_mul_f32 v[32:33], v[32:33], v[24:25] op_sel:[0,1] op_sel_hi:[1,1]\n"
    "v_pk_mul_f32 v[32:33], v[32:33], v[52:53]\n"
    "v_lshlrev_b32 v52, 16, v57\n"
    "v_and_b32 v53, 0xffff0000, v57\n"
    "v_pk_mul_f32 v[34:35], v[34:35], v[24:25] op_sel:[0,1] op_sel_hi:[1,1]\n"
    "v_pk_mul_f32 v[34:35], v[34:35], v[52:53]\n"
    "v_lshlrev_b32 v52, 16, v58\n"
    "buffer_load_dwordx4 v[208:211], v13, s[28:31], s43 offen sc1\n"
    "s_add_u32 s43, s43, 4096\n"
    "v_and_b32 v53, 0xffff0000, v58\n"
    "v_pk_mul_f32 v[36:37], v[36:37], v[24:25] op_sel:[0,1] op_sel_hi:[1,1]\n"
    "v_pk_mul_f32 v[36:37], v[36:37], v[52:53]\n"
    "v_lshlrev_b32 v52, 16, v59\n"
    "v_and_b32 v53, 0xffff0000, v59\n"
    "v_pk_mul_f32 v[38:39], v[38:39], v[24:25] op_sel:[0,1] op_sel_hi:[1,1]\n"
    "v_pk_mul_f32 v[38:39], v[38:39], v[52:53]\n"
    "v_pk_add_f32 v[224:225], v[224:225], v[248:249]\n"
    "v_pk_add_f32 v[226:227], v[226:227], v[250:251]\n"
    "v_pk_add_f32 v[228:229], v[228:229], v[252:253]\n"
    "v_pk_add_f32 v[230:231], v[230:231], v[254:255]\n"
    "v_mul_f32 v40, 0xbfb8aa3b, v224\n"
    "v_mul_f32 v41, 0xbfb8aa3b, v225\n"
    "v_mul_f32 v42, 0xbfb8aa3b, v226\n"
    "buffer_load_dwordx4 v[212:215], v13, s[28:31], s43 offen sc1\n"
    "s_add_u32 s43, s43, 4096\n"
    "v_mul_f32 v43, 0xbfb8aa3b, v227\n"
    "v_mul_f32 v44, 0xbfb8aa3b, v228\n"
    "v_mul_f32 v45, 0xbfb8aa3b, v229\n"
    "v_mul_f32 v46, 0xbfb8aa3b, v230\n"
    "v_mul_f32 v47, 0xbfb8aa3b, v231\n"
    "v_exp_f32 v40, v40\n"
    "v_exp_f32 v41, v41\n"
    "v_exp_f32 v42, v42\n"
    "v_exp_f32 v43, v43\n"
    "v_exp_f32 v44, v44\n"
    "v_exp_f32 v45, v45\n"
    "v_exp_f32 v46, v46\n"
    "v_exp_f32 v47, v47\n"
    "v_pk_add_f32 v[232:233], v[232:233], v[32:33]\n"
    "buffer_load_dwordx4 v[216:219], v13, s[28:31], s43 offen sc1\n"
    "s_add_u32 s43, s43, 4096\n"
    "v_pk_add_f32 v[234:235], v[234:235], v[34:35]\n"
    "v_pk_add_f32 v[236:237], v[236:237], v[36:37]\n"
    "v_pk_add_f32 v[238:239], v[238:239], v[38:39]\n"
    "v_add_f32 v40, 1.0, v40\n"
    "v_add_f32 v41, 1.0, v41\n"
    "v_add_f32 v42, 1.0, v42\n"
    "v_add_f32 v43, 1.0, v43\n"
    "v_add_f32 v44, 1.0, v44\n"
    "v_add_f32 v45, 1.0, v45\n"
    "v_add_f32 v46, 1.0, v46\n"
    "v_add_f32 v47, 1.0, v47\n"
    "v_rcp_f32 v40, v40\n"
    "v_rcp_f32 v41, v41\n"
    "v_rcp_f32 v42, v42\n"
    "buffer_load_dwordx4 v[220:223], v13, s[28:31], s43 offen sc1\n"
    "v_rcp_f32 v43, v43\n"
    "v_rcp_f32 v44, v44\n"
    "v_rcp_f32 v45, v45\n"
    "v_rcp_f32 v46, v46\n"
    "v_rcp_f32 v47, v47\n"
    "v_pk_mul_f32 v[232:233], v[232:233], v[224:225]\n"
    "v_pk_mul_f32 v[234:235], v[234:235], v[226:227]\n"
    "v_pk_mul_f32 v[236:237], v[236:237], v[228:229]\n"
    "v_pk_mul_f32 v[238:239], v[238:239], v[230:231]\n"
    "v_pk_fma_f32 v[240:241], v[232:233], v[40:41], v[240:241]\n"
    "v_pk_fma_f32 v[242:243], v[234:235], v[42:43], v[242:243]\n"
    "v_pk_fma_f32 v[244:245], v[236:237], v[44:45], v[244:245]\n"
    "v_pk_fma_f32 v[246:247], v[238:239], v[46:47], v[246:247]\n"
    "s_lshl_b32 s41, s56, 1\n"
    "buffer_store_dwordx4 v[240:243], v22, s[80:83], s41 offen offset:0\n"
    "buffer_store_dwordx4 v[244:247], v22, s[80:83], s41 offen offset:64\n"
    "s_waitcnt vmcnt(18)\n"
    "v_lshlrev_b32 v224, 16, v96\n"
    "v_and_b32 v225, 0xffff0000, v96\n"
    "v_lshlrev_b32 v226, 16, v97\n"
    "v_and_b32 v227, 0xffff0000, v97\n"
    "v_lshlrev_b32 v228, 16, v98\n"
    "v_and_b32 v229, 0xffff0000, v98\n"
    "v_lshlrev_b32 v230, 16, v99\n"
    "v_and_b32 v231, 0xffff0000, v99\n"
    "v_dot2_f32_bf16 v224, v100, s100, v224\n"
    "v_dot2_f32_bf16 v225, v100, s101, v225\n"
    "v_dot2_f32_bf16 v226, v101, s100, v226\n"
    "v_dot2_f32_bf16 v227, v101, s101, v227\n"
    "v_dot2_f32_bf16 v228, v102, s100, v228\n"
    "v_dot2_f32_bf16 v229, v102, s101, v229\n"
    "v_dot2_f32_bf16 v230, v103, s100, v230\n"
    "v_dot2_f32_bf16 v231, v103, s101, v231\n"
    "v_dot2_f32_bf16 v224, v104, s100, v224\n"
    "v_dot2_f32_bf16 v225, v104, s101, v225\n"
    "v_dot2_f32_bf16 v226, v105, s100, v226\n"
    "v_dot2_f32_bf16 v227, v105, s101, v227\n"
    "v_dot2_f32_bf16 v228, v106, s100, v228\n"
    "v_dot2_f32_bf16 v229, v106, s101, v229\n"
    "v_dot2_f32_bf16 v230, v107, s100, v230\n"
    "v_dot2_f32_bf16 v231, v107, s101, v231\n"
    "v_dot2_f32_bf16 v224, v108, s100, v224\n"
    "v_dot2_f32_bf16 v225, v108, s101, v225\n"
    "v_dot2_f32_bf16 v226, v109, s100, v226\n"
    "v_dot2_f32_bf16 v227, v109, s101, v227\n"
    "v_dot2_f32_bf16 v228, v110, s100, v228\n"
    "v_dot2_f32_bf16 v229, v110, s101, v229\n"
    "v_dot2_f32_bf16 v230, v111, s100, v230\n"
    "v_dot2_f32_bf16 v231, v111, s101, v231\n"
    "v_dot2_f32_bf16 v224, v112, s100, v224\n"
    "v_dot2_f32_bf16 v225, v112, s101, v225\n"
    "v_dot2_f32_bf16 v226, v113, s100, v226\n"
    "v_dot2_f32_bf16 v227, v113, s101, v227\n"
    "v_dot2_f32_bf16 v228, v114, s100, v228\n"
    "v_dot2_f32_bf16 v229, v114, s101, v229\n"
    "v_dot2_f32_bf16 v230, v115, s100, v230\n"
    "v_dot2_f32_bf16 v231, v115, s101, v231\n"
    "v_dot2_f32_bf16 v224, v116, s100, v224\n"
    "v_dot2_f32_bf16 v225, v116, s101, v225\n"
    "v_dot2_f32_bf16 v226, v117, s100, v226\n"
    "v_dot2_f32_bf16 v227, v117, s101, v227\n"
    "v_dot2_f32_bf16 v228, v118, s100, v228\n"
    "v_dot2_f32_bf16 v229, v118, s101, v229\n"
    "v_dot2_f32_bf16 v230, v119, s100, v230\n"
    "v_dot2_f32_bf16 v231, v119, s101, v231\n"
    "v_dot2_f32_bf16 v224, v120, s100, v224\n"
    "v_dot2_f32_bf16 v225, v120, s101, v225\n"
    "v_dot2_f32_bf16 v226, v121, s100, v226\n"
    "v_dot2_f32_bf16 v227, v121, s101, v227\n"
    "v_dot2_f32_bf16 v228, v122, s100, v228\n"
    "v_dot2_f32_bf16 v229, v122, s101, v229\n"
    "v_dot2_f32_bf16 v230, v123, s100, v230\n"
    "v_dot2_f32_bf16 v231, v123, s101, v231\n"
    "v_dot2_f32_bf16 v224, v124, s100, v224\n"
    "v_dot2_f32_bf16 v225, v124, s101, v225\n"
    "v_dot2_f32_bf16 v226, v125, s100, v226\n"
    "v_dot2_f32_bf16 v227, v125, s101, v227\n"
    "v_dot2_f32_bf16 v228, v126, s100, v228\n"
    "v_dot2_f32_bf16 v229, v126, s101, v229\n"
    "v_dot2_f32_bf16 v230, v127, s100, v230\n"
    "v_dot2_f32_bf16 v231, v127, s101, v231\n"
    "v_dot2_f32_bf16 v224, v128, s100, v224\n"
    "v_dot2_f32_bf16 v225, v128, s101, v225\n"
    "v_dot2_f32_bf16 v226, v129, s100, v226\n"
    "v_dot2_f32_bf16 v227, v129, s101, v227\n"
    "v_dot2_f32_bf16 v228, v130, s100, v228\n"
    "v_dot2_f32_bf16 v229, v130, s101, v229\n"
    "v_dot2_f32_bf16 v230, v131, s100, v230\n"
    "v_dot2_f32_bf16 v231, v131, s101, v231\n"
    "v_dot2_f32_bf16 v224, v132, s100, v224\n"
    "v_dot2_f32_bf16 v225, v132, s101, v225\n"
    "v_dot2_f32_bf16 v226, v133, s100, v226\n"
    "v_dot2_f32_bf16 v227, v133, s101, v227\n"
    "v_dot2_f32_bf16 v228, v134, s100, v228\n"
    "v_dot2_f32_bf16 v229, v134, s101, v229\n"
    "v_dot2_f32_bf16 v230, v135, s100, v230\n"
    "v_dot2_f32_bf16 v231, v135, s101, v231\n"
    "v_dot2_f32_bf16 v224, v136, s100, v224\n"
    "v_dot2_f32_bf16 v225, v136, s101, v225\n"
    "v_dot2_f32_bf16 v226, v137, s100, v226\n"
    "v_dot2_f32_bf16 v227, v137, s101, v227\n"
    "v_dot2_f32_bf16 v228, v138, s100, v228\n"
    "v_dot2_f32_bf16 v229, v138, s101, v229\n"
    "v_dot2_f32_bf16 v230, v139, s100, v230\n"
    "v_dot2_f32_bf16 v231, v139, s101, v231\n"
    "v_dot2_f32_bf16 v224, v140, s100, v224\n"
    "v_dot2_f32_bf16 v225, v140, s101, v225\n"
    "v_dot2_f32_bf16 v226, v141, s100, v226\n"
    "v_dot2_f32_bf16 v227, v141, s101, v227\n"
    "v_dot2_f32_bf16 v228, v142, s100, v228\n"
    "v_dot2_f32_bf16 v229, v142, s101, v229\n"
    "v_dot2_f32_bf16 v230, v143, s100, v230\n"
    "v_dot2_f32_bf16 v231, v143, s101, v231\n"
    "v_lshlrev_b32 v232, 16, v144\n"
    "v_and_b32 v233, 0xffff0000, v144\n"
    "v_lshlrev_b32 v234, 16, v145\n"
    "v_and_b32 v235, 0xffff0000, v145\n"
    "v_lshlrev_b32 v236, 16, v146\n"
    "v_and_b32 v237, 0xffff0000, v146\n"
    "v_lshlrev_b32 v238, 16, v147\n"
    "v_and_b32 v239, 0xffff0000, v147\n"
    "v_dot2_f32_bf16 v232, v148, s100, v232\n"
    "v_dot2_f32_bf16 v233, v148, s101, v233\n"
    "v_dot2_f32_bf16 v234, v149, s100, v234\n"
    "v_dot2_f32_bf16 v235, v149, s101, v235\n"
    "v_dot2_f32_bf16 v236, v150, s100, v236\n"
    "v_dot2_f32_bf16 v237, v150, s101, v237\n"
    "v_dot2_f32_bf16 v238, v151, s100, v238\n"
    "v_dot2_f32_bf16 v239, v151, s101, v239\n"
    "v_dot2_f32_bf16 v232, v152, s100, v232\n"
    "v_dot2_f32_bf16 v233, v152, s101, v233\n"
    "v_dot2_f32_bf16 v234, v153, s100, v234\n"
    "v_dot2_f32_bf16 v235, v153, s101, v235\n"
    "v_dot2_f32_bf16 v236, v154, s100, v236\n"
    "v_dot2_f32_bf16 v237, v154, s101, v237\n"
    "v_dot2_f32_bf16 v238, v155, s100, v238\n"
    "v_dot2_f32_bf16 v239, v155, s101, v239\n"
    "v_dot2_f32_bf16 v232, v156, s100, v232\n"
    "v_dot2_f32_bf16 v233, v156, s101, v233\n"
    "v_dot2_f32_bf16 v234, v157, s100, v234\n"
    "v_dot2_f32_bf16 v235, v157, s101, v235\n"
    "v_dot2_f32_bf16 v236, v158, s100, v236\n"
    "v_dot2_f32_bf16 v237, v158, s101, v237\n"
    "v_dot2_f32_bf16 v238, v159, s100, v238\n"
    "v_dot2_f32_bf16 v239, v159, s101, v239\n"
    "v_accvgpr_read_b32 v112, a4\n"
    "v_accvgpr_read_b32 v113, a5\n"
    "v_accvgpr_read_b32 v114, a6\n"
    "v_accvgpr_read_b32 v115, a7\n"
    "v_add_u32 v28, s57, v23\n"
    "ds_read_b64 v[116:117], v28 offset:0\n"
    "ds_read_b64 v[120:121], v28 offset:2048\n"
    "ds_read_b64 v[118:119], v28 offset:32\n"
    "ds_read_b64 v[122:123], v28 offset:2080\n"
    "s_waitcnt lgkmcnt(0)\n"
    "v_lshlrev_b32 v240, 16, v112\n"
    "v_and_b32 v241, 0xffff0000, v112\n"
    "v_lshlrev_b32 v248, 16, v116\n"
    "v_and_b32 v249, 0xffff0000, v116\n"
    "v_lshlrev_b32 v242, 16, v113\n"
    "v_and_b32 v243, 0xffff0000, v113\n"
    "v_lshlrev_b32 v250, 16, v117\n"
    "v_and_b32 v251, 0xffff0000, v117\n"
    "v_lshlrev_b32 v244, 16, v114\n"
    "v_and_b32 v245, 0xffff0000, v114\n"
    "v_lshlrev_b32 v252, 16, v118\n"
    "v_and_b32 v253, 0xffff0000, v118\n"
    "v_lshlrev_b32 v246, 16, v115\n"
    "v_and_b32 v247, 0xffff0000, v115\n"
    "v_lshlrev_b32 v254, 16, v119\n"
    "v_and_b32 v255, 0xffff0000, v119\n"
    "v_pk_add_f32 v[96:97], v[240:241], v[24:25] op_sel_hi:[1,0] neg_lo:[0,1] neg_hi:[0,1]\n"
    "v_pk_add_f32 v[98:99], v[242:243], v[24:25] op_sel_hi:[1,0] neg_lo:[0,1] neg_hi:[0,1]\n"
    "v_pk_add_f32 v[100:101], v[244:245], v[24:25] op_sel_hi:[1,0] neg_lo:[0,1] neg_hi:[0,1]\n"
    "v_pk_add_f32 v[102:103], v[246:247], v[24:25] op_sel_hi:[1,0] neg_lo:[0,1] neg_hi:[0,1]\n"
    "v_lshlrev_b32 v116, 16, v120\n"
    "v_and_b32 v117, 0xffff0000, v120\n"
    "v_pk_mul_f32 v[96:97], v[96:97], v[24:25] op_sel:[0,1] op_sel_hi:[1,1]\n"
    "v_pk_mul_f32 v[96:97], v[96:97], v[116:117]\n"
    "v_lshlrev_b32 v116, 16, v121\n"
    "v_and_b32 v117, 0xffff0000, v121\n"
    "v_pk_mul_f32 v[98:99], v[98:99], v[24:25] op_sel:[0,1] op_sel_hi:[1,1]\n"
    "v_pk_mul_f32 v[98:99], v[98:99], v[116:117]\n"
    "v_lshlrev_b32 v116, 16, v122\n"
    "v_and_b32 v117, 0xffff0000, v122\n"
    "v_pk_mul_f32 v[100:101], v[100:101], v[24:25] op_sel:[0,1] op_sel_hi:[1,1]\n"
    "v_pk_mul_f32 v[100:101], v[100:101], v[116:117]\n"
    "v_lshlrev_b32 v116, 16, v123\n"
    "v_and_b32 v117, 0xffff0000, v123\n"
    "v_pk_mul_f32 v[102:103], v[102:103], v[24:25] op_sel:[0,1] op_sel_hi:[1,1]\n"
    "v_pk_mul_f32 v[102:103], v[102:103], v[116:117]\n"
    "v_pk_add_f32 v[224:225], v[224:225], v[248:249]\n"
    "v_pk_add_f32 v[226:227], v[226:227], v[250:251]\n"
    "v_pk_add_f32 v[228:229], v[228:229], v[252:253]\n"
    "v_pk_add_f32 v[230:231], v[230:231], v[254:255]\n"
    "v_mul_f32 v104, 0xbfb8aa3b, v224\n"
    "v_mul_f32 v105, 0xbfb8aa3b, v225\n"
    "v_mul_f32 v106, 0xbfb8aa3b, v226\n"
    "v_mul_f32 v107, 0xbfb8aa3b, v227\n"
    "v_mul_f32 v108, 0xbfb8aa3b, v228\n"
    "v_mul_f32 v109, 0xbfb8aa3b, v229\n"
    "v_mul_f32 v110, 0xbfb8aa3b, v230\n"
    "v_mul_f32 v111, 0xbfb8aa3b, v231\n"
    "v_exp_f32 v104, v104\n"
    "v_exp_f32 v105, v105\n"
    "v_exp_f32 v106, v106\n"
    "v_exp_f32 v107, v107\n"
    "v_exp_f32 v108, v108\n"
    "v_exp_f32 v109, v109\n"
    "v_exp_f32 v110, v110\n"
    "v_exp_f32 v111, v111\n"
    "v_pk_add_f32 v[232:233], v[232:233], v[96:97]\n"
    "v_pk_add_f32 v[234:235], v[234:235], v[98:99]\n"
    "v_pk_add_f32 v[236:237], v[236:237], v[100:101]\n"
    "v_pk_add_f32 v[238:239], v[238:239], v[102:103]\n"
    "v_add_f32 v104, 1.0, v104\n"
    "v_add_f32 v105, 1.0, v105\n"
    "v_add_f32 v106, 1.0, v106\n"
    "v_add_f32 v107, 1.0, v107\n"
    "v_add_f32 v108, 1.0, v108\n"
    "v_add_f32 v109, 1.0, v109\n"
    "v_add_f32 v110, 1.0, v110\n"
    "v_add_f32 v111, 1.0, v111\n"
    "v_rcp_f32 v104, v104\n"
    "v_rcp_f32 v105, v105\n"
    "v_rcp_f32 v106, v106\n"
    "v_rcp_f32 v107, v107\n"
    "v_rcp_f32 v108, v108\n"
    "v_rcp_f32 v109, v109\n"
    "v_rcp_f32 v110, v110\n"
    "v_rcp_f32 v111, v111\n"
    "v_pk_mul_f32 v[232:233], v[232:233], v[224:225]\n"
    "v_pk_mul_f32 v[234:235], v[234:235], v[226:227]\n"
    "v_pk_mul_f32 v[236:237], v[236:237], v[228:229]\n"
    "v_pk_mul_f32 v[238:239], v[238:239], v[230:231]\n"
    "v_pk_fma_f32 v[240:241], v[232:233], v[104:105], v[240:241]\n"
    "v_pk_fma_f32 v[242:243], v[234:235], v[106:107], v[242:243]\n"
    "v_pk_fma_f32 v[244:245], v[236:237], v[108:109], v[244:245]\n"
    "v_pk_fma_f32 v[246:247], v[238:239], v[110:111], v[246:247]\n"
    "s_lshl_b32 s41, s57, 1\n"
    "buffer_store_dwordx4 v[240:243], v22, s[80:83], s41 offen offset:0\n"
    "buffer_store_dwordx4 v[244:247], v22, s[80:83], s41 offen offset:64\n"
    "s_waitcnt vmcnt(2)\n"
    "v_lshlrev_b32 v224, 16, v160\n"
    "v_and_b32 v225, 0xffff0000, v160\n"
    "v_lshlrev_b32 v226, 16, v161\n"
    "v_and_b32 v227, 0xffff0000, v161\n"
    "v_lshlrev_b32 v228, 16, v162\n"
    "v_and_b32 v229, 0xffff0000, v162\n"
    "v_lshlrev_b32 v230, 16, v163\n"
    "v_and_b32 v231, 0xffff0000, v163\n"
    "v_dot2_f32_bf16 v224, v164, s100, v224\n"
    "v_dot2_f32_bf16 v225, v164, s101, v225\n"
    "v_dot2_f32_bf16 v226, v165, s100, v226\n"
    "v_dot2_f32_bf16 v227, v165, s101, v227\n"
    "v_dot2_f32_bf16 v228, v166, s100, v228\n"
    "v_dot2_f32_bf16 v229, v166, s101, v229\n"
    "v_dot2_f32_bf16 v230, v167, s100, v230\n"
    "v_dot2_f32_bf16 v231, v167, s101, v231\n"
    "v_dot2_f32_bf16 v224, v168, s100, v224\n"
    "v_dot2_f32_bf16 v225, v168, s101, v225\n"
    "v_dot2_f32_bf16 v226, v169, s100, v226\n"
    "v_dot2_f32_bf16 v227, v169, s101, v227\n"
    "v_dot2_f32_bf16 v228, v170, s100, v228\n"
    "v_dot2_f32_bf16 v229, v170, s101, v229\n"
    "v_dot2_f32_bf16 v230, v171, s100, v230\n"
    "v_dot2_f32_bf16 v231, v171, s101, v231\n"
    "v_dot2_f32_bf16 v224, v172, s100, v224\n"
    "v_dot2_f32_bf16 v225, v172, s101, v225\n"
    "v_dot2_f32_bf16 v226, v173, s100, v226\n"
    "v_dot2_f32_bf16 v227, v173, s101, v227\n"
    "v_dot2_f32_bf16 v228, v174, s100, v228\n"
    "v_dot2_f32_bf16 v229, v174, s101, v229\n"
    "v_dot2_f32_bf16 v230, v175, s100, v230\n"
    "v_dot2_f32_bf16 v231, v175, s101, v231\n"
    "v_dot2_f32_bf16 v224, v176, s100, v224\n"
    "v_dot2_f32_bf16 v225, v176, s101, v225\n"
    "v_dot2_f32_bf16 v226, v177, s100, v226\n"
    "v_dot2_f32_bf16 v227, v177, s101, v227\n"
    "v_dot2_f32_bf16 v228, v178, s100, v228\n"
    "v_dot2_f32_bf16 v229, v178, s101, v229\n"
    "v_dot2_f32_bf16 v230, v179, s100, v230\n"
    "v_dot2_f32_bf16 v231, v179, s101, v231\n"
    "v_dot2_f32_bf16 v224, v180, s100, v224\n"
    "v_dot2_f32_bf16 v225, v180, s101, v225\n"
    "v_dot2_f32_bf16 v226, v181, s100, v226\n"
    "v_dot2_f32_bf16 v227, v181, s101, v227\n"
    "v_dot2_f32_bf16 v228, v182, s100, v228\n"
    "v_dot2_f32_bf16 v229, v182, s101, v229\n"
    "v_dot2_f32_bf16 v230, v183, s100, v230\n"
    "v_dot2_f32_bf16 v231, v183, s101, v231\n"
    "v_dot2_f32_bf16 v224, v184, s100, v224\n"
    "v_dot2_f32_bf16 v225, v184, s101, v225\n"
    "v_dot2_f32_bf16 v226, v185, s100, v226\n"
    "v_dot2_f32_bf16 v227, v185, s101, v227\n"
    "v_dot2_f32_bf16 v228, v186, s100, v228\n"
    "v_dot2_f32_bf16 v229, v186, s101, v229\n"
    "v_dot2_f32_bf16 v230, v187, s100, v230\n"
    "v_dot2_f32_bf16 v231, v187, s101, v231\n"
    "v_dot2_f32_bf16 v224, v188, s100, v224\n"
    "v_dot2_f32_bf16 v225, v188, s101, v225\n"
    "v_dot2_f32_bf16 v226, v189, s100, v226\n"
    "v_dot2_f32_bf16 v227, v189, s101, v227\n"
    "v_dot2_f32_bf16 v228, v190, s100, v228\n"
    "v_dot2_f32_bf16 v229, v190, s101, v229\n"
    "v_dot2_f32_bf16 v230, v191, s100, v230\n"
    "v_dot2_f32_bf16 v231, v191, s101, v231\n"
    "v_dot2_f32_bf16 v224, v192, s100, v224\n"
    "v_dot2_f32_bf16 v225, v192, s101, v225\n"
    "v_dot2_f32_bf16 v226, v193, s100, v226\n"
    "v_dot2_f32_bf16 v227, v193, s101, v227\n"
    "v_dot2_f32_bf16 v228, v194, s100, v228\n"
    "v_dot2_f32_bf16 v229, v194, s101, v229\n"
    "v_dot2_f32_bf16 v230, v195, s100, v230\n"
    "v_dot2_f32_bf16 v231, v195, s101, v231\n"
    "v_dot2_f32_bf16 v224, v196, s100, v224\n"
    "v_dot2_f32_bf16 v225, v196, s101, v225\n"
    "v_dot2_f32_bf16 v226, v197, s100, v226\n"
    "v_dot2_f32_bf16 v227, v197, s101, v227\n"
    "v_dot2_f32_bf16 v228, v198, s100, v228\n"
    "v_dot2_f32_bf16 v229, v198, s101, v229\n"
    "v_dot2_f32_bf16 v230, v199, s100, v230\n"
    "v_dot2_f32_bf16 v231, v199, s101, v231\n"
    "v_dot2_f32_bf16 v224, v200, s100, v224\n"
    "v_dot2_f32_bf16 v225, v200, s101, v225\n"
    "v_dot2_f32_bf16 v226, v201, s100, v226\n"
    "v_dot2_f32_bf16 v227, v201, s101, v227\n"
    "v_dot2_f32_bf16 v228, v202, s100, v228\n"
    "v_dot2_f32_bf16 v229, v202, s101, v229\n"
    "v_dot2_f32_bf16 v230, v203, s100, v230\n"
    "v_dot2_f32_bf16 v231, v203, s101, v231\n"
    "v_dot2_f32_bf16 v224, v204, s100, v224\n"
    "v_dot2_f32_bf16 v225, v204, s101, v225\n"
    "v_dot2_f32_bf16 v226, v205, s100, v226\n"
    "v_dot2_f32_bf16 v227, v205, s101, v227\n"
    "v_dot2_f32_bf16 v228, v206, s100, v228\n"
    "v_dot2_f32_bf16 v229, v206, s101, v229\n"
    "v_dot2_f32_bf16 v230, v207, s100, v230\n"
    "v_dot2_f32_bf16 v231, v207, s101, v231\n"
    "v_lshlrev_b32 v232, 16, v208\n"
    "v_and_b32 v233, 0xffff0000, v208\n"
    "v_lshlrev_b32 v234, 16, v209\n"
    "v_and_b32 v235, 0xffff0000, v209\n"
    "v_lshlrev_b32 v236, 16, v210\n"
    "v_and_b32 v237, 0xffff0000, v210\n"
    "v_lshlrev_b32 v238, 16, v211\n"
    "v_and_b32 v239, 0xffff0000, v211\n"
    "v_dot2_f32_bf16 v232, v212, s100, v232\n"
    "v_dot2_f32_bf16 v233, v212, s101, v233\n"
    "v_dot2_f32_bf16 v234, v213, s100, v234\n"
    "v_dot2_f32_bf16 v235, v213, s101, v235\n"
    "v_dot2_f32_bf16 v236, v214, s100, v236\n"
    "v_dot2_f32_bf16 v237, v214, s101, v237\n"
    "v_dot2_f32_bf16 v238, v215, s100, v238\n"
    "v_dot2_f32_bf16 v239, v215, s101, v239\n"
    "v_dot2_f32_bf16 v232, v216, s100, v232\n"
    "v_dot2_f32_bf16 v233, v216, s101, v233\n"
    "v_dot2_f32_bf16 v234, v217, s100, v234\n"
    "v_dot2_f32_bf16 v235, v217, s101, v235\n"
    "v_dot2_f32_bf16 v236, v218, s100, v236\n"
    "v_dot2_f32_bf16 v237, v218, s101, v237\n"
    "v_dot2_f32_bf16 v238, v219, s100, v238\n"
    "v_dot2_f32_bf16 v239, v219, s101, v239\n"
    "v_dot2_f32_bf16 v232, v220, s100, v232\n"
    "v_dot2_f32_bf16 v233, v220, s101, v233\n"
    "v_dot2_f32_bf16 v234, v221, s100, v234\n"
    "v_dot2_f32_bf16 v235, v221, s101, v235\n"
    "v_dot2_f32_bf16 v236, v222, s100, v236\n"
    "v_dot2_f32_bf16 v237, v222, s101, v237\n"
    "v_dot2_f32_bf16 v238, v223, s100, v238\n"
    "v_dot2_f32_bf16 v239, v223, s101, v239\n"
    "v_accvgpr_read_b32 v176, a8\n"
    "v_accvgpr_read_b32 v177, a9\n"
    "v_accvgpr_read_b32 v178, a10\n"
    "v_accvgpr_read_b32 v179, a11\n"
    "v_add_u32 v28, s58, v23\n"
    "ds_read_b64 v[180:181], v28 offset:0\n"
    "ds_read_b64 v[184:185], v28 offset:2048\n"
    "ds_read_b64 v[182:183], v28 offset:32\n"
    "ds_read_b64 v[186:187], v28 offset:2080\n"
    "s_waitcnt lgkmcnt(0)\n"
    "v_lshlrev_b32 v240, 16, v176\n"
    "v_and_b32 v241, 0xffff0000, v176\n"
    "v_lshlrev_b32 v248, 16, v180\n"
    "v_and_b32 v249, 0xffff0000, v180\n"
    "v_lshlrev_b32 v242, 16, v177\n"
    "v_and_b32 v243, 0xffff0000, v177\n"
    "v_lshlrev_b32 v250, 16, v181\n"
    "v_and_b32 v251, 0xffff0000, v181\n"
    "v_lshlrev_b32 v244, 16, v178\n"
    "v_and_b32 v245, 0xffff0000, v178\n"
    "v_lshlrev_b32 v252, 16, v182\n"
    "v_and_b32 v253, 0xffff0000, v182\n"
    "v_lshlrev_b32 v246, 16, v179\n"
    "v_and_b32 v247, 0xffff0000, v179\n"
    "v_lshlrev_b32 v254, 16, v183\n"
    "v_and_b32 v255, 0xffff0000, v183\n"
    "v_pk_add_f32 v[160:161], v[240:241], v[24:25] op_sel_hi:[1,0] neg_lo:[0,1] neg_hi:[0,1]\n"
    "v_pk_add_f32 v[162:163], v[242:243], v[24:25] op_sel_hi:[1,0] neg_lo:[0,1] neg_hi:[0,1]\n"
    "v_pk_add_f32 v[164:165], v[244:245], v[24:25] op_sel_hi:[1,0] neg_lo:[0,1] neg_hi:[0,1]\n"
    "v_pk_add_f32 v[166:167], v[246:247], v[24:25] op_sel_hi:[1,0] neg_lo:[0,1] neg_hi:[0,1]\n"
    "v_lshlrev_b32 v180, 16, v184\n"
    "v_and_b32 v181, 0xffff0000, v184\n"
    "v_pk_mul_f32 v[160:161], v[160:161], v[24:25] op_sel:[0,1] op_sel_hi:[1,1]\n"
    "v_pk_mul_f32 v[160:161], v[160:161], v[180:181]\n"
    "v_lshlrev_b32 v180, 16, v185\n"
    "v_and_b32 v181, 0xffff0000, v185\n"
    "v_pk_mul_f32 v[162:163], v[162:163], v[24:25] op_sel:[0,1] op_sel_hi:[1,1]\n"
    "v_pk_mul_f32 v[162:163], v[162:163], v[180:181]\n"
    "v_lshlrev_b32 v180, 16, v186\n"
    "v_and_b32 v181, 0xffff0000, v186\n"
    "v_pk_mul_f32 v[164:165], v[164:165], v[24:25] op_sel:[0,1] op_sel_hi:[1,1]\n"
    "v_pk_mul_f32 v[164:165], v[164:165], v[180:181]\n"
    "v_lshlrev_b32 v180, 16, v187\n"
    "v_and_b32 v181, 0xffff0000, v187\n"
    "v_pk_mul_f32 v[166:167], v[166:167], v[24:25] op_sel:[0,1] op_sel_hi:[1,1]\n"
    "v_pk_mul_f32 v[166:167], v[166:167], v[180:181]\n"
    "v_pk_add_f32 v[224:225], v[224:225], v[248:249]\n"
    "v_pk_add_f32 v[226:227], v[226:227], v[250:251]\n"
    "v_pk_add_f32 v[228:229], v[228:229], v[252:253]\n"
    "v_pk_add_f32 v[230:231], v[230:231], v[254:255]\n"
    "v_mul_f32 v168, 0xbfb8aa3b, v224\n"
    "v_mul_f32 v169, 0xbfb8aa3b, v225\n"
    "v_mul_f32 v170, 0xbfb8aa3b, v226\n"
    "v_mul_f32 v171, 0xbfb8aa3b, v227\n"
    "v_mul_f32 v172, 0xbfb8aa3b, v228\n"
    "v_mul_f32 v173, 0xbfb8aa3b, v229\n"
    "v_mul_f32 v174, 0xbfb8aa3b, v230\n"
    "v_mul_f32 v175, 0xbfb8aa3b, v231\n"
    "v_exp_f32 v168, v168\n"
    "v_exp_f32 v169, v169\n"
    "v_exp_f32 v170, v170\n"
    "v_exp_f32 v171, v171\n"
    "v_exp_f32 v172, v172\n"
    "v_exp_f32 v173, v173\n"
    "v_exp_f32 v174, v174\n"
    "v_exp_f32 v175, v175\n"
    "v_pk_add_f32 v[232:233], v[232:233], v[160:161]\n"
    "v_pk_add_f32 v[234:235], v[234:235], v[162:163]\n"
    "v_pk_add_f32 v[236:237], v[236:237], v[164:165]\n"
    "v_pk_add_f32 v[238:239], v[238:239], v[166:167]\n"
    "v_add_f32 v168, 1.0, v168\n"
    "v_add_f32 v169, 1.0, v169\n"
    "v_add_f32 v170, 1.0, v170\n"
    "v_add_f32 v171, 1.0, v171\n"
    "v_add_f32 v172, 1.0, v172\n"
    "v_add_f32 v173, 1.0, v173\n"
    "v_add_f32 v174, 1.0, v174\n"
    "v_add_f32 v175, 1.0, v175\n"
    "v_rcp_f32 v168, v168\n"
    "v_rcp_f32 v169, v169\n"
    "v_rcp_f32 v170, v170\n"
    "v_rcp_f32 v171, v171\n"
    "v_rcp_f32 v172, v172\n"
    "v_rcp_f32 v173, v173\n"
    "v_rcp_f32 v174, v174\n"
    "v_rcp_f32 v175, v175\n"
    "v_pk_mul_f32 v[232:233], v[232:233], v[224:225]\n"
    "v_pk_mul_f32 v[234:235], v[234:235], v[226:227]\n"
    "v_pk_mul_f32 v[236:237], v[236:237], v[228:229]\n"
    "v_pk_mul_f32 v[238:239], v[238:239], v[230:231]\n"
    "v_pk_fma_f32 v[240:241], v[232:233], v[168:169], v[240:241]\n"
    "v_pk_fma_f32 v[242:243], v[234:235], v[170:171], v[242:243]\n"
    "v_pk_fma_f32 v[244:245], v[236:237], v[172:173], v[244:245]\n"
    "v_pk_fma_f32 v[246:247], v[238:239], v[174:175], v[246:247]\n"
    "s_lshl_b32 s41, s58, 1\n"
    "buffer_store_dwordx4 v[240:243], v22, s[80:83], s41 offen offset:0\n"
    "buffer_store_dwordx4 v[244:247], v22, s[80:83], s41 offen offset:64\n"
    "s_waitcnt vmcnt(0)\n"
    "K_END:\n"
    :: "s"(ka), "s"(blockIdx.x), "s"(wid)
    : "memory", "vcc", "scc", "s8","s9","s10","s11","s12","s13","s14","s15","s16","s17","s18","s19","s20","s21","s22","s23","s24","s25","s26","s27","s28","s29","s30","s31","s34","s35","s36","s37","s38","s39","s40","s41","s42","s43","s44","s45","s46","s47","s48","s49","s50","s51","s52","s53","s54","s55","s56","s57","s58","s59","s60","s61","s62","s63","s64","s65","s66","s67","s68","s69","s70","s71","s72","s73","s74","s75","s76","s77","s78","s79","s80","s81","s82","s83","s84","s85","s86","s87","s88","s89","s90","s91","s92","s93","s94","s95","s96","s97","s98","s99","s100","s101","v0","v1","v2","v3","v4","v5","v6","v7","v8","v9","v10","v11","v12","v13","v14","v15","v16","v17","v18","v19","v20","v21","v22","v23","v24","v25","v26","v27","v28","v29","v30","v31","v32","v33","v34","v35","v36","v37","v38","v39","v40","v41","v42","v43","v44","v45","v46","v47","v48","v49","v50","v51","v52","v53","v54","v55","v56","v57","v58","v59","v60","v61","v62","v63","v64","v65","v66","v67","v68","v69","v70","v71","v72","v73","v74","v75","v76","v77","v78","v79","v80","v81","v82","v83","v84","v85","v86","v87","v88","v89","v90","v91","v92","v93","v94","v95","v96","v97","v98","v99","v100","v101","v102","v103","v104","v105","v106","v107","v108","v109","v110","v111","v112","v113","v114","v115","v116","v117","v118","v119","v120","v121","v122","v123","v124","v125","v126","v127","v128","v129","v130","v131","v132","v133","v134","v135","v136","v137","v138","v139","v140","v141","v142","v143","v144","v145","v146","v147","v148","v149","v150","v151","v152","v153","v154","v155","v156","v157","v158","v159","v160","v161","v162","v163","v164","v165","v166","v167","v168","v169","v170","v171","v172","v173","v174","v175","v176","v177","v178","v179","v180","v181","v182","v183","v184","v185","v186","v187","v188","v189","v190","v191","v192","v193","v194","v195","v196","v197","v198","v199","v200","v201","v202","v203","v204","v205","v206","v207","v208","v209","v210","v211","v212","v213","v214","v215","v216","v217","v218","v219","v220","v221","v222","v223","v224","v225","v226","v227","v228","v229","v230","v231","v232","v233","v234","v235","v236","v237","v238","v239","v240","v241","v242","v243","v244","v245","v246","v247","v248","v249","v250","v251","v252","v253","v254","v255","a0","a1","a2","a3","a4","a5","a6","a7","a8","a9","a10","a11","a12","a13","a14","a15","a16","a17","a18","a19","a20","a21","a22","a23","a24","a25","a26","a27","a28","a29","a30","a31","a32","a33","a34","a35","a36","a37","a38","a39","a40","a41","a42","a43","a44","a45","a46","a47","a48","a49","a50","a51","a52","a53","a54","a55","a56","a57","a58","a59","a60","a61","a62","a63","a64","a65","a66","a67","a68","a69","a70","a71","a72","a73","a74","a75","a76","a77","a78","a79","a80","a81","a82","a83","a84","a85","a86","a87","a88","a89","a90","a91","a92","a93","a94","a95","a96","a97","a98","a99","a100","a101","a102","a103","a104","a105","a106","a107","a108","a109","a110","a111","a112","a113","a114","a115","a116","a117","a118","a119","a120","a121","a122","a123","a124","a125","a126","a127","a128","a129","a130","a131","a132","a133","a134","a135","a136","a137","a138","a139","a140","a141","a142","a143","a144","a145","a146","a147","a148","a149","a150","a151","a152","a153","a154","a155","a156","a157","a158","a159","a160","a161","a162","a163","a164","a165","a166","a167","a168","a169","a170","a171","a172","a173","a174","a175","a176","a177","a178","a179","a180","a181","a182","a183","a184","a185","a186","a187","a188","a189","a190","a191","a192","a193","a194","a195","a196","a197","a198","a199","a200","a201","a202","a203","a204","a205","a206","a207","a208","a209","a210","a211","a212","a213","a214","a215","a216","a217","a218","a219","a220","a221","a222","a223","a224","a225","a226","a227","a228","a229","a230","a231","a232","a233","a234","a235","a236","a237","a238","a239","a240","a241","a242","a243","a244","a245","a246","a247","a248","a249","a250","a251","a252","a253","a254","a255");
}

extern "C" void kernel_launch(void* const* d_in, const int* in_sizes, int n_in,
                              void* d_out, int out_size, void* d_ws, size_t ws_size,
                              hipStream_t stream) {
  Params p;
  for (int i = 0; i < 9; ++i) p.p[i] = d_in[i];
  for (int i = 0; i < 4; ++i) p.p[9 + i] = d_in[10 + i];
  p.out = d_out; p.ws = d_ws;
  hipFuncSetAttribute((const void*)k_fused, hipFuncAttributeMaxDynamicSharedMemorySize, 163840);
  k_fused<<<256, 256, 163840, stream>>>(p);
}
```
